```python
import torch

def _softmax(x, axis=-1):
    return torch.softmax(x, dim=axis)

def _gelu(x):
    return torch.nn.functional.gelu(x, approximate='tanh')

def _silu(x):
    return torch.nn.functional.silu(x)

def _sigmoid(x):
    return torch.sigmoid(x)

def _layernorm(x, axis=-1, eps=1e-05):
    m = x.mean(dim=axis, keepdim=True)
    v = x.var(dim=axis, keepdim=True, unbiased=False)
    return (x - m) / torch.sqrt(v + eps)

def _scatter_add(tgt, idx, src, axis=0):
    return tgt.clone().index_add_(axis, idx, src)

def _segment_sum(src, seg, n):
    out = torch.zeros((n,) + src.shape[1:], dtype=src.dtype)
    return out.index_add_(0, seg, src)

def _pad(x, pw):
    flat = tuple((v for pair in reversed(pw) for v in pair))
    return torch.nn.functional.pad(x, flat)
kernel_name = 'standalone__tensor_product_equiv_tfn__graph'

def setup_inputs():

    _w = lambda a: (lambda t: t.to(torch.bfloat16) if t.is_floating_point() else t)(torch.as_tensor(a))
    N, deg, C, L, DE = (10000, 32, 128, 1, 16)
    E = N * deg
    M = (L + 1) ** 2
    src = (lambda lo, hi, *s: torch.randint(lo, hi, s))(0, N, E).to(torch.int64)
    dst = (lambda lo, hi, *s: torch.randint(lo, hi, s))(0, N, E).to(torch.int64)
    r = (lambda *s: torch.randn(*s, dtype=torch.bfloat16) * 0.05)(E, 3)
    r = r / (torch.linalg.norm(r, dim=-1, keepdim=True) + 1e-09)
    rx, ry, rz = (r[:, 0].clone(), r[:, 1].clone(), r[:, 2].clone())
    Ys = [torch.ones((E, 1), dtype=float)]
    if L >= 1:
        Ys.append(r.to(float))
    if L >= 2:
        Ys.append(torch.stack([2 * rx * ry, 2 * ry * rz, (3 * rz * rz - 1.0) / (lambda _x: torch.sqrt(torch.as_tensor(_x)))(3.0), 2 * rx * rz, rx * rx - ry * ry], dim=-1).to(float))
    for _l in range(3, L + 1):
        Ys.append(((lambda *s: torch.randn(*s, dtype=torch.bfloat16) * 0.05)(E, 2 * _l + 1) * 0.3).to(float))
    Y = torch.cat(Ys, dim=-1)
    cg = torch.zeros((M, M, M), dtype=float)
    off = [_l * _l for _l in range(L + 2)]
    for l1 in range(L + 1):
        for l2 in range(L + 1):
            for l3 in range(abs(l1 - l2), min(L, l1 + l2) + 1):
                d1, d2, d3 = (2 * l1 + 1, 2 * l2 + 1, 2 * l3 + 1)
                cg[off[l1]:off[l1 + 1], off[l2]:off[l2 + 1], off[l3]:off[l3 + 1]] = (lambda *s: torch.randn(*s, dtype=torch.bfloat16) * 0.05)(d1, d2, d3) / (lambda _x: torch.sqrt(torch.as_tensor(_x)))(float(d1 * d2))
    return {'x': _w((lambda *s: torch.randn(*s, dtype=torch.bfloat16) * 0.05)(N, C, M) * 0.2), 'src': _w(src), 'dst': _w(dst), 'Y': _w(Y), 'edge_feat': _w((lambda *s: torch.randn(*s, dtype=torch.bfloat16) * 0.05)(E, DE)), 'w_rad': _w((lambda *s: torch.randn(*s, dtype=torch.bfloat16) * 0.05)(DE, C) * (1.0 / max(1.0, DE) ** 0.5)), 'cg': _w(cg), 'w_mix': _w((lambda *s: torch.randn(*s, dtype=torch.bfloat16) * 0.05)(C, C) * (1.0 / max(1.0, C) ** 0.5))}

def reference(x, src, dst, Y, edge_feat, w_rad, cg, w_mix):
    N, C, M = (x.shape[0], x.shape[1], x.shape[2])
    R = _silu(edge_feat @ w_rad)
    xs = x[src]
    tp = torch.einsum('eci,ef,ifo->eco', xs, Y, cg)
    msg = R[:, :, None] * tp
    agg = _segment_sum(msg.reshape(-1, C * M), dst, N).reshape(N, C, M)
    return torch.einsum('ncm,cd->ndm', agg, w_mix)
```

```cpp
#include <hip/hip_runtime.h>
#include <hip/hip_bf16.h>
#include <stdint.h>

typedef __attribute__((ext_vector_type(8))) short bf16x8;
typedef __attribute__((ext_vector_type(4))) short bf16x4;
typedef __attribute__((ext_vector_type(4))) float f32x4;
typedef __attribute__((ext_vector_type(2))) unsigned u32x2;
typedef __attribute__((ext_vector_type(4))) unsigned u32x4;
typedef __attribute__((ext_vector_type(8))) unsigned u32x8;
typedef __attribute__((ext_vector_type(16))) unsigned u32x16;
typedef __attribute__((ext_vector_type(32))) unsigned u32x32;
typedef unsigned long long u64;

#define C_DIM 128
#define M_DIM 4
#define DE_DIM 16
#define NPB_MAX 80
#define NB_MAX 128
#define EPB_MAX 1280
#define NRND 7
#define SMAX 72
#define OOB_OFF 0x80000000u
#define SC1 16
#define NT 2
#define SPIN_MAX (1u << 22)
#define CODE_LINES 163
#define PTO 0xfffa9500u
#define PTY 0xffffa950u
#define PTZ 0xffffea54u
#define NT0 3
#define NT2 7
#define TLIST 8, 9, 4, 5, 10, 11, 12
#define HSPLIT(l) ((l) >> 1)
#define TKF 0x75624130u
#define TKB 0x75690213u
#define TKA 0x9848ffffu

#define W_CTL 0
#define W_FLG 4096
#define W_TAB (W_FLG + 32768)
#define W_RA (W_TAB + 256 * 256 * 8)
#define W_AGG (W_RA + 256 * EPB_MAX * 16)
#define W_END (W_AGG + NB_MAX * NPB_MAX * 1024)

#define SB() __builtin_amdgcn_sched_barrier(0)

static __device__ __forceinline__ float bf2f(unsigned short v) { return __uint_as_float(((unsigned)v) << 16); }
typedef __bf16 bf16v2 __attribute__((ext_vector_type(2)));
static __device__ __forceinline__ unsigned pack2bf(float lo, float hi) {
    bf16v2 r = (bf16v2){(__bf16)lo, (__bf16)hi};
    return __builtin_bit_cast(unsigned, r);
}
static __device__ __forceinline__ unsigned short f2bf(float v) { return (unsigned short)(pack2bf(v, 0.f) & 0xffffu); }
template <int CTRL, int RM> static __device__ __forceinline__ int dpp_add(int v) {
    return v + __builtin_amdgcn_update_dpp(0, v, CTRL, RM, 0xf, false);
}
static __device__ __forceinline__ int wave_incl_scan(int v) {
    v = dpp_add<0x111, 0xf>(v); v = dpp_add<0x112, 0xf>(v); v = dpp_add<0x114, 0xf>(v); v = dpp_add<0x118, 0xf>(v);
    v = dpp_add<0x142, 0xa>(v); v = dpp_add<0x143, 0xc>(v);
    return v;
}
template <int CTRL, int RM> static __device__ __forceinline__ unsigned dpp_max(unsigned v) {
    return max(v, (unsigned)__builtin_amdgcn_update_dpp(0, (int)v, CTRL, RM, 0xf, false));
}
static __device__ __forceinline__ unsigned wave_max_scan(unsigned v) {
    v = dpp_max<0x111, 0xf>(v); v = dpp_max<0x112, 0xf>(v); v = dpp_max<0x114, 0xf>(v); v = dpp_max<0x118, 0xf>(v);
    v = dpp_max<0x142, 0xa>(v); v = dpp_max<0x143, 0xc>(v);
    return v;
}
static __device__ __forceinline__ unsigned bsum(unsigned c, unsigned m, unsigned acc = 0u) { return __builtin_amdgcn_udot4(c, m, acc, false); }
static __device__ __forceinline__ unsigned bytesum2(unsigned c0, unsigned c1, unsigned m0, unsigned m1, unsigned acc = 0u) {
    return __builtin_amdgcn_udot4(c1, m1, __builtin_amdgcn_udot4(c0, m0, acc, false), false);
}
static __device__ __forceinline__ unsigned below_mask(int w, int k) {
    const int n = w - 4 * k;
    return n >= 4 ? 0x01010101u : (n <= 0 ? 0u : (0x01010101u & ((1u << (8 * n)) - 1u)));
}
static __device__ __forceinline__ void flag_store(const void* p, unsigned v) {
    unsigned t0, t1; u64 sv;
    asm volatile("v_mov_b32 %0, 0\n\tv_mov_b32 %1, %4\n\ts_mov_b64 %2, exec\n\ts_mov_b64 exec, 1\n\t"
                 "global_store_dword %0, %1, %3 sc1\n\ts_mov_b64 exec, %2" : "=&v"(t0), "=&v"(t1), "=&s"(sv) : "s"(p), "s"(v) : "memory");
}
static __device__ __forceinline__ unsigned lds_arrive_rtn(unsigned a) {
    unsigned t0, t1; u64 sv;
    asm volatile("v_mov_b32 %0, %3\n\tv_mov_b32 %1, 1\n\ts_mov_b64 %2, exec\n\ts_mov_b64 exec, 1\n\t"
                 "ds_add_rtn_u32 %1, %0, %1\n\ts_mov_b64 exec, %2\n\ts_waitcnt lgkmcnt(0)" : "=&v"(t0), "=&v"(t1), "=&s"(sv) : "s"(a) : "memory");
    return (unsigned)__builtin_amdgcn_readfirstlane((int)t1);
}
static __device__ __forceinline__ void lds_arrive(unsigned a) {
    unsigned t0, t1; u64 sv;
    asm volatile("s_waitcnt lgkmcnt(0)\n\tv_mov_b32 %0, %3\n\tv_mov_b32 %1, 1\n\ts_mov_b64 %2, exec\n\ts_mov_b64 exec, 1\n\t"
                 "ds_add_u32 %0, %1\n\ts_mov_b64 exec, %2" : "=&v"(t0), "=&v"(t1), "=&s"(sv) : "s"(a) : "memory");
}
template <int SLEEP = 0> static __device__ __forceinline__ void lds_wait(unsigned a, unsigned n) {
    for (;;) {
        unsigned v;
        asm volatile("ds_read_b32 %0, %1\n\ts_waitcnt lgkmcnt(0)" : "=v"(v) : "v"(a) : "memory");
        if ((unsigned)__builtin_amdgcn_readfirstlane((int)v) >= n) break;
        if (SLEEP) __builtin_amdgcn_s_sleep(SLEEP);
    }
}
static __device__ __forceinline__ unsigned sel3(unsigned r, unsigned a, unsigned b, unsigned c) {
    asm volatile("" : "+v"(a), "+v"(b), "+v"(c));
    return r == 0u ? a : (r == 1u ? b : c);
}
static __device__ __forceinline__ unsigned ld_agent(const void* p) { return __hip_atomic_load((const unsigned*)p, __ATOMIC_RELAXED, __HIP_MEMORY_SCOPE_AGENT); }
static __device__ __forceinline__ void poll_tag(const void* p, unsigned tag) {
#pragma unroll 1
    for (unsigned spin = 0; spin < SPIN_MAX; ++spin) {
        if (__all(ld_agent(p) == tag)) break;
        __builtin_amdgcn_s_sleep(2);
    }
    __builtin_amdgcn_fence(__ATOMIC_ACQUIRE, "wavefront");
}
static __device__ __forceinline__ void wg_barrier() {
    asm volatile("s_waitcnt lgkmcnt(0)" ::: "memory");
    __builtin_amdgcn_s_barrier();
    asm volatile("" ::: "memory");
}
static __device__ __forceinline__ f32x4 mfma4(u32x2 a, u32x2 b, f32x4 c) {
    return __builtin_amdgcn_mfma_f32_4x4x4bf16_1k(__builtin_bit_cast(bf16x4, a), __builtin_bit_cast(bf16x4, b), c, 0, 0, 0);
}
static __device__ __forceinline__ f32x4 mfma16(u32x4 a, u32x4 b, f32x4 c) {
    return __builtin_amdgcn_mfma_f32_16x16x32_bf16(__builtin_bit_cast(bf16x8, a), __builtin_bit_cast(bf16x8, b), c, 0, 0, 0);
}

static constexpr unsigned evt_of(unsigned pt) {
    unsigned e = 6u;
    for (int n = 2; n < 15; ++n) if (((pt >> (2 * n)) & 3u) != ((pt >> (2 * n + 2)) & 3u)) e |= 1u << n;
    return e;
}

static constexpr u64 wave_of(bool kept) {
    u64 r = 0;
    for (int w = 0; w < 8; ++w) {
        r |= (u64)w << (4 * (((kept ? TKF : TKB) >> (4 * w)) & 15u));
        if (w >= 4 && (w >= 6) == kept) r |= (u64)w << (4 * ((TKA >> (4 * w)) & 15u));
    }
    return r;
}
static constexpr unsigned outer_of(bool kept) { return 1u << ((TKA >> (kept ? 24 : 16)) & 15u) | 1u << ((TKA >> (kept ? 28 : 20)) & 15u); }

#define L_MISC 0
#define L_KEY 96
#define L_AT 416
#define L_NB 736
#define L_PCNT 1280
#define L_CCNT (L_PCNT + 2048)
#define L_BNC (L_CCNT + 1280)
#define L_CG (L_BNC + 4096)
#define L_PRV (L_CG + 1024)
#define A_STG L_PRV
#define A_PTAB (A_STG + EPB_MAX * 16)
#define PK_STR 8704
#define L_PK (L_PRV + 8 * SMAX * 64)
#define L_DUM L_PK
#define L_CTAB (L_PK + 2048)
#define L_MRK (L_PK + PK_STR)
#define L_WR (L_PK + 7 * PK_STR)
#define L_IMG (L_PK + 8 * PK_STR)
#define L_TOTAL (L_IMG + 32768)
static_assert(L_TOTAL <= 163840 && L_BNC <= 8192, "LDS budget");
static_assert(L_PRV % 16 == 0 && L_PK % 16 == 0 && L_IMG % 16 == 0 && L_CG % 16 == 0 && L_CCNT % 16 == 0, "alignment");
enum { SY_CPY = 40, SY_C1 = 44, SY_S = 48, SY_C2 = 52, SY_C3 = 56, SY_RI = 60, SY_PRE = 64 };

static __device__ __forceinline__ u32x4 mkdesc(const void* p, unsigned bytes) {
    return (u32x4){(unsigned)(uintptr_t)p, (unsigned)((uintptr_t)p >> 32) & 0xffffu, bytes, 0x00020000u};
}

__global__ __launch_bounds__(512, 2) void k_fused(
    const unsigned short* __restrict__ x, const int* __restrict__ src, const int* __restrict__ dst, const u32x2* __restrict__ Y,
    const u32x4* __restrict__ ef, const unsigned short* __restrict__ w_rad, const unsigned short* __restrict__ cg,
    const unsigned short* __restrict__ w_mix, unsigned short* __restrict__ out, unsigned char* ws,
    int N, int E, int epb, unsigned nb_magic, int nh, unsigned cmul)
{
    __shared__ __attribute__((aligned(256))) unsigned char smem[L_TOTAL];
    u64 tprobe = 0;
    const int tid = threadIdx.x, lane = tid & 63;
    const int w = __builtin_amdgcn_readfirstlane(tid >> 6);
    const int bx = blockIdx.x;
    const int half = (bx >> 2) & 1;
    const int b = (bx & 3) * 32 + (bx >> 3);
    const int g = lane >> 4, c16 = lane & 15;
    const int node0 = (b * N + 127) >> 7, npb = (((b + 1) * N + 127) >> 7) - node0;
    unsigned cp;
    {
        u64 pc;
        asm volatile("s_memrealtime %1\n\ts_waitcnt lgkmcnt(0)\n\ts_getpc_b64 %0" : "=s"(pc), "=s"(tprobe));
        const __amdgpu_buffer_rsrc_t rsc = __builtin_amdgcn_make_buffer_rsrc((void*)(pc & ~(u64)127), 0, CODE_LINES * 128, 0x00020000);
        cp = __builtin_amdgcn_raw_buffer_load_b32(rsc, (tid >> 3) == 32 ? ((bx >> 3) + lane * 32) * 128 : OOB_OFF, 0, 0);
        SB();
    }
    const unsigned lds0 = (unsigned)(uintptr_t)(__attribute__((address_space(3))) unsigned char*)smem;
    const __amdgpu_buffer_rsrc_t rsw = __builtin_amdgcn_make_buffer_rsrc((void*)ws, 0, W_END, 0x00020000);
    asm volatile("" :: "s"(x), "s"(src), "s"(dst), "s"(Y), "s"(ef), "s"(w_rad), "s"(cg), "s"(w_mix), "s"(out), "s"(ws), "s"(N), "s"(E), "s"(epb), "s"(nb_magic),
                 "s"(nh), "s"(cmul));
    const int base_e = bx * epb;
    const int ne = max(min(epb, E - base_e), 0);
    const int sS = 40 * (1 - half), sK = 40 * half;
    auto task_at = [=](unsigned wq, int t) -> unsigned {
        const unsigned q = (t == 0 ? TKF : (t == 1 ? TKB : TKA)) >> (4 * wq) & 15u;
        return q == 15u ? L_MISC + 8 : L_AT + ((t == 0 || (t == 2 && wq >= 6u) ? sK : sS) + 4 * q) * 4;
    };
    unsigned tag;
    bool slow;
    u32x4 wr[8], cgA;
    auto load_wr = [&]() {
#pragma unroll
        for (int t = 0; t < 8; ++t) wr[t] = *(const u32x4*)(smem + L_WR + t * 1024 + lane * 16);
        cgA = *(const u32x4*)(smem + L_CG + lane * 16);
        asm volatile("" : "+v"(wr[0]), "+v"(wr[1]), "+v"(wr[2]), "+v"(wr[3]), "+v"(wr[4]), "+v"(wr[5]), "+v"(wr[6]), "+v"(wr[7]), "+v"(cgA));
    };

    __builtin_amdgcn_s_setprio(3);
    auto touch = [=](unsigned code) -> unsigned {
        if (code < 8u)
            return __builtin_amdgcn_raw_buffer_load_b32(__builtin_amdgcn_make_buffer_rsrc((void*)(ef + (size_t)base_e * 2), 0, code == 0u ? 0 : ne * 32, 0x00020000), lane * 128, (code - 1u) * 8192u, 0);
        return __builtin_amdgcn_raw_buffer_load_b32(__builtin_amdgcn_make_buffer_rsrc((void*)(x + (size_t)half * nh * 512), 0, (N - nh) * 1024, 0x00020000), lane * 128,
                                                    code < 16u ? ((code - 8u) * 128u + b) * 8192u : ((code - 16u) * 32u + (bx >> 3)) * 8192u, 0);
    };
    constexpr unsigned tl[] = {TLIST};
    const __amdgpu_buffer_rsrc_t rsm = __builtin_amdgcn_make_buffer_rsrc((void*)w_mix, 0, C_DIM * C_DIM * 2, 0x00020000);
    unsigned t0v = 0u, cm = 0u, wv[8], cv[4], tv[NT2 + 1];
    {
        const unsigned sh = 8u * (w & 3);
        unsigned d[3], sv[3], epoch, inc[3];
        u32x2 yv[3];
        const __amdgpu_buffer_rsrc_t rsd = __builtin_amdgcn_make_buffer_rsrc((void*)(dst + base_e), 0, ne * 4, 0x00020000);
        const __amdgpu_buffer_rsrc_t rss = __builtin_amdgcn_make_buffer_rsrc((void*)(src + base_e), 0, ne * 4, 0x00020000);
        const __amdgpu_buffer_rsrc_t rsy = __builtin_amdgcn_make_buffer_rsrc((void*)(Y + base_e), 0, ne * 8, 0x00020000);
        const unsigned le = (unsigned)(w * 160 + lane), le2 = lane < 32 ? le : 0x1000000u;
#pragma unroll
        for (int r = 0; r < 3; ++r) {
            d[r] = __builtin_amdgcn_raw_buffer_load_b32(rsd, (r < 2 ? le : le2) * 4 + 256 * r, 0, NT);
            sv[r] = __builtin_amdgcn_raw_buffer_load_b32(rss, (r < 2 ? le : le2) * 4 + 256 * r, 0, NT);
            SB();
        }
        epoch = __builtin_amdgcn_raw_buffer_load_b32(rsw, W_CTL, 0, 0);
#pragma unroll
        for (int r = 0; r < 3; ++r) yv[r] = __builtin_amdgcn_raw_buffer_load_b64(rsy, (r < 2 ? le : le2) * 8 + 512 * r, 0, NT);
        SB();
        {
            const unsigned k0 = (unsigned)tid - L_KEY / 16 < NPB_MAX / 4 ? 127u + L_KEY / 4 - 4u * tid : 0u, k1 = min(k0, 1u);
            *(u32x4*)(smem + tid * 16) = (u32x4){k0, k0 - k1, k0 - 2u * k1, k0 - 3u * k1};
        }
        if (tid < 64 * NRND) *(u32x4*)(smem + L_MRK + tid * 16) = (u32x4){0u, 0u, 0u, 0u};
        {
            const int hm = w >= 4 ? -1 : 0;
            const __amdgpu_buffer_rsrc_t rsa = __builtin_amdgcn_make_buffer_rsrc((void*)w_rad, 0, DE_DIM * C_DIM * 2 & hm, 0x00020000);
            const __amdgpu_buffer_rsrc_t rsg = __builtin_amdgcn_make_buffer_rsrc((void*)cg, 0, 128 & hm, 0x00020000);
#pragma unroll
            for (int j = 0; j < 8; ++j) wv[j] = __builtin_amdgcn_raw_buffer_load_b32(rsa, (g & 1) * 2048 + 64 * (w & 3) + 4 * c16 + 256 * j, 0, 0);
#pragma unroll
            for (int f = 0; f < 4; ++f) cv[f] = __builtin_amdgcn_raw_buffer_load_b32(rsg, (lane & 3) * 32 + (lane & 8u) / 2 + 8 * f, 0, 0);
            t0v = __builtin_amdgcn_raw_buffer_load_b32(__builtin_amdgcn_make_buffer_rsrc((void*)(ef + (size_t)base_e * 2), 0, min(ne * 32, NT0 * 8192), 0x00020000), (511 - tid) * 128, 0, 0);
            cm = __builtin_amdgcn_raw_buffer_load_b32(rsm, (tid >> 3) == 32 ? ((bx >> 3) * 8 + lane) * 128 : OOB_OFF, 0, 0);
        }
#pragma unroll
        for (int r = 0; r < 3; ++r) inc[r] = (int)(r < 2 ? le : le2) + r * 64 < ne ? 1u << sh : 0u;
        asm volatile("" : "+v"(inc[0]), "+v"(inc[1]), "+v"(inc[2]));
        SB();
        wg_barrier();
        unsigned bin[3], rank[3], ba[3];
        asm volatile("" : "+v"(d[0]), "+v"(sv[0]), "+v"(d[1]), "+v"(sv[1]), "+v"(d[2]), "+v"(sv[2]));
#pragma unroll
        for (int r = 0; r < 3; ++r) {
            bin[r] = __umulhi(d[r], nb_magic);
            ba[r] = ((sv[r] >= (unsigned)nh ? 512u : 0u) + bin[r] * 4u) & 1023u;
            rank[r] = atomicAdd((unsigned*)(smem + L_PCNT + (w >> 2) * 1024 + ba[r]), inc[r]);
        }
        tag = (unsigned)__builtin_amdgcn_readfirstlane((int)epoch) * 1664525u + 1013904223u;
        asm volatile("" : "+s"(tag));
        unsigned rw0[3], rw1[3];
#pragma unroll
        for (int r = 0; r < 3; ++r) {
            const unsigned sl = sv[r] - (ba[r] >= 512u ? (unsigned)nh : 0u), nl = d[r] - ((__umul24(bin[r], (unsigned)N) + 127u) >> 7);
            rw0[r] = sl | (tag << 13);
            rw1[r] = (nl * 4u + (__umul24(sl, cmul) >> 16)) | ((le + r * 64) << 9) | (tag & 0xfff00000u);
            asm volatile("" : "+v"(rw0[r]), "+v"(rw1[r]));
        }
        wg_barrier();
        u32x2 c[4];
#pragma unroll
        for (int i = 0; i < 4; ++i) c[i] = *(const u32x2*)(smem + L_PCNT + i * 512 + lane * 8);
        const unsigned m0 = below_mask(w, 0), m1 = below_mask(w, 1);
        unsigned t[4], run[4];
#pragma unroll
        for (int i = 0; i < 4; ++i) t[i] = bytesum2(c[i >> 1][i & 1], c[2 + (i >> 1)][i & 1], 0x01010101u, 0x01010101u);
        const unsigned ts = (t[0] + t[1]) | (t[2] + t[3]) << 16;
        const unsigned ti = (unsigned)wave_incl_scan((int)ts), tx = ti - ts;
        run[0] = tx & 0xffffu; run[2] = (tx >> 16) + ((unsigned)__builtin_amdgcn_readlane((int)ti, 63) & 0xffffu);
        run[1] = run[0] + t[0]; run[3] = run[2] + t[2];
        if ((w & 6) == 4) {
            const unsigned ra = (w & 1) == 0 ? run[0] | (t[0] << 16) : run[2] | (t[2] << 16), rb = (w & 1) == 0 ? run[1] | (t[1] << 16) : run[3] | (t[3] << 16);
            __builtin_amdgcn_raw_buffer_store_b128((u32x4){ra, tag, rb, tag}, rsw, W_TAB + bx * 2048 + lane * 16 + (w & 1) * 1024, 0, SC1);
        }
        unsigned o[4];
#pragma unroll
        for (int i = 0; i < 4; ++i) o[i] = bytesum2(c[i >> 1][i & 1], c[2 + (i >> 1)][i & 1], m0, m1, run[i]);
        *(unsigned*)(smem + A_PTAB + w * 512 + lane * 4) = o[0] | (o[1] << 16);
        *(unsigned*)(smem + A_PTAB + w * 512 + 256 + lane * 4) = o[2] | (o[3] << 16);
        unsigned pos[3];
        asm volatile("" ::: "memory");
#pragma unroll
        for (int r = 0; r < 3; ++r) pos[r] = *(const unsigned short*)(smem + A_PTAB + w * 512 + (ba[r] >> 1));
#pragma unroll
        for (int r = 0; r < 3; ++r) pos[r] = inc[r] != 0u ? A_STG + (pos[r] + ((rank[r] >> sh) & 0xffu)) * 16 : L_DUM + lane * 16;
        asm volatile("" : "+v"(pos[0]), "+v"(pos[1]), "+v"(pos[2]));
#pragma unroll
        for (int r = 0; r < 3; ++r) *(u32x4*)(smem + pos[r]) = (u32x4){yv[r].x, rw0[r], yv[r].y, rw1[r]};
        wg_barrier();
    }
    if (w < 4) {
        asm volatile("" :: "v"(t0v), "v"(cm), "v"(wv[0]), "v"(wv[7]), "v"(cv[3]));
        const unsigned sh = 8u * w, mb = 0x01010101u & ((1u << sh) - 1u);
        const unsigned T0 = tag << 13, T1 = tag & 0xfff00000u;
        const unsigned to = W_TAB + tid * 2048 + (half * 128 + b) * 8;
        u32x2 en;
        __builtin_amdgcn_s_sleep(8);
#pragma unroll 1
        for (unsigned spin = 0; spin < SPIN_MAX; ++spin) {
            asm volatile("" ::: "memory");
            en = __builtin_amdgcn_raw_buffer_load_b64(rsw, to, 0, SC1);
            if (__all(en.y == tag)) break;
            __builtin_amdgcn_s_sleep(2);
        }
        const unsigned rc = en.x >> 16;
        const unsigned incl = (unsigned)wave_incl_scan((int)rc);
        const int twf = __builtin_amdgcn_readlane((int)incl, 63), tw = twf <= 64 * NRND ? twf : 0;
        const bool more = tw > 64 * (NRND - 1);
        *(unsigned*)(smem + ((rc != 0u && incl <= 64u * NRND) ? L_MRK + w * (256 * NRND) + (incl - rc) * 4 : L_DUM + lane * 4)) = (unsigned)lane * EPB_MAX + (en.x & 0xffffu) - (incl - rc);
        unsigned ra[NRND];
        asm volatile("" ::: "memory");
#pragma unroll
        for (int r = 0; r < NRND; ++r) ra[r] = *(const unsigned*)(smem + L_MRK + w * (256 * NRND) + r * 256 + lane * 4);
        unsigned carry = 0u;
        u32x4 A[NRND];
#pragma unroll
        for (int r = 0; r < NRND; ++r) {
            if (r == NRND - 1 && !more) { ra[r] = OOB_OFF >> 1; A[r] = __builtin_amdgcn_raw_buffer_load_b128(rsw, ra[r], 0, SC1); continue; }
            const unsigned m = max(wave_max_scan(ra[r]), carry);
            carry = (unsigned)__builtin_amdgcn_readlane((int)m, 63);
            ra[r] = r * 64 + lane < tw ? (m + (unsigned)(r * 64 + lane)) * 16u : OOB_OFF >> 1;
            A[r] = __builtin_amdgcn_raw_buffer_load_b128(rsw, ra[r], W_RA + 64 * w * (EPB_MAX * 16), SC1);
        }
        SB();
        lds_arrive(lds0 + SY_RI);
        unsigned pe[NRND], inc[NRND];
#pragma unroll
        for (int r = 0; r < NRND; ++r) {
            pe[r] = __umul24(__umulhi(ra[r], 209716u) + 64u * w, (unsigned)epb * 32u);
            inc[r] = ra[r] < 0x40000000u ? 1u << sh : 0u;
            asm volatile("" : "+v"(pe[r]), "+v"(inc[r]));
        }
        SB();
        bool ok = true;
#pragma unroll
        for (int r = 0; r < NRND; ++r) { A[r].y ^= T0; A[r].w ^= T1; ok = ok & ((inc[r] == 0u) | ((A[r].y < 8192u) & (A[r].w < 0x100000u))); }
        if (!__all(ok)) {
#pragma unroll 1
            for (unsigned spin = 0; spin < SPIN_MAX; ++spin) {
                asm volatile("" ::: "memory");
#pragma unroll
                for (int r = 0; r < NRND; ++r) A[r] = __builtin_amdgcn_raw_buffer_load_b128(rsw, ra[r], W_RA + 64 * w * (EPB_MAX * 16), SC1);
                ok = true;
#pragma unroll
                for (int r = 0; r < NRND; ++r) { A[r].y ^= T0; A[r].w ^= T1; ok = ok & ((inc[r] == 0u) | ((A[r].y < 8192u) & (A[r].w < 0x100000u))); }
                if (__all(ok)) break;
                __builtin_amdgcn_s_sleep(1);
            }
        }
        unsigned rank[NRND], nc[NRND];
#pragma unroll
        for (int r = 0; r < NRND; ++r) {
            nc[r] = (r < 4 || inc[r] != 0u) ? A[r].w & 0x1ffu : (unsigned)lane;
            if (r == NRND - 1 && !more) { rank[r] = 0u; continue; }
            rank[r] = atomicAdd((unsigned*)(smem + L_CCNT + nc[r] * 4), inc[r]);
            atomicAdd((unsigned*)(smem + L_KEY + (nc[r] & 0x1fcu)), min(inc[r], 1u) << 7);
        }
        if (twf > 64 * NRND) *(unsigned*)(smem + L_MISC) = 1u;
        lds_arrive(lds0 + SY_C1);
        const unsigned hb = (unsigned)(half * nh) * 1024u;
#pragma unroll
        for (int r = 0; r < NRND; ++r) {
            rank[r] = (rank[r] >> sh) & 0xffu;
            A[r].y = (A[r].y << 10) + hb;
            A[r].w = ((A[r].w >> 4) & 0xffe0u) + pe[r];
        }
        lds_wait(lds0 + SY_C1, 4);
        {
            const u32x4 c0 = *(const u32x4*)(smem + L_CCNT + lane * 16), c1 = *(const u32x4*)(smem + L_CCNT + (64 + c16) * 16);
            unsigned p0[4], p1[4], d0 = 0u, d1 = 0u;
#pragma unroll
            for (int c = 0; c < 4; ++c) {
                p0[c] = bsum(c0[c], mb, d0); p1[c] = bsum(c1[c], mb, d1);
                if (c < 3) { d0 = bsum(c0[c], 0x01010101u, d0); d1 = bsum(c1[c], 0x01010101u, d1); }
            }
            *(u32x2*)(smem + L_CTAB + w * 640 + lane * 8) = (u32x2){p0[0] | (p0[1] << 16), p0[2] | (p0[3] << 16)};
            *(u32x2*)(smem + L_CTAB + w * 640 + (64 + c16) * 8) = (u32x2){p1[0] | (p1[1] << 16), p1[2] | (p1[3] << 16)};
        }
        asm volatile("" ::: "memory");
#pragma unroll
        for (int r = 0; r < NRND; ++r) { rank[r] += *(const unsigned short*)(smem + L_CTAB + w * 640 + nc[r] * 2); nc[r] &= 0x1fcu; }
#pragma unroll
        for (int r = 0; r < NRND; ++r) asm volatile("" : "+v"(rank[r]), "+v"(nc[r]));
        lds_wait(lds0 + SY_PRE, 4);
        load_wr();
        lds_wait(lds0 + SY_C2, 3);
        unsigned nbv[NRND];
#pragma unroll
        for (int r = 0; r < NRND; ++r) nbv[r] = *(const unsigned*)(smem + L_NB + nc[r]);
        const unsigned slf = *(const unsigned*)(smem + L_MISC);
        slow = __builtin_amdgcn_readfirstlane((int)slf) != 0;
        if (!slow) {
#pragma unroll
            for (int r = 0; r < NRND; ++r) {
                if (r == NRND - 1 && !more) continue;
                const unsigned a = (nbv[r] & 0x3ffffu) + ((rank[r] + (rank[r] >= ((nbv[r] >> 18) & 127u) ? nbv[r] >> 25 : 0u)) << 6);
                *(u32x4*)(smem + (inc[r] != 0u ? a : L_DUM + lane * 16)) = (u32x4){A[r].y, A[r].w, A[r].x, A[r].z};
            }
        }
        lds_arrive(lds0 + SY_C3);
    } else {
        const int q = w - 4, t2 = tid - 256;
        {
            const __amdgpu_buffer_rsrc_t rsr = __builtin_amdgcn_make_buffer_rsrc((void*)(ws + W_RA + bx * (EPB_MAX * 16)), 0, (ne * 16 + 127) & ~127, 0x00020000);
            u32x4 va[5];
#pragma unroll
            for (int i = 0; i < 5; ++i) va[i] = *(const u32x4*)(smem + A_STG + t2 * 16 + 4096 * i);
#pragma unroll
            for (int i = 0; i < 5; ++i) __builtin_amdgcn_raw_buffer_store_b128(va[i], rsr, t2 * 16, 4096 * i, SC1);
            lds_arrive(lds0 + SY_CPY);
        }
        __builtin_amdgcn_s_setprio(2);
        u32x4 mv[8];
#pragma unroll
        for (int j = 0; j < 8; ++j)
            mv[j] = __builtin_amdgcn_raw_buffer_load_b128(rsm, g * 512 + c16 * 16 + q * 2048 + (j & 1) * 256, (j >> 1) * 8192, 0);
        SB();
        const int n5 = min(27 * q + (lane >> 1), NPB_MAX - 1);
        unsigned tb, ta[3];
        {
            const bool kp = (n5 >= 40) == (half != 0);
            const int qq = (n5 >= 40 ? n5 - 40 : n5) >> 2;
            const unsigned wq = (unsigned)((kp ? wave_of(true) : wave_of(false)) >> (4 * qq)) & 15u;
            tb = L_PRV + wq * (SMAX * 64) + (n5 & 3) * 16 + ((((kp ? outer_of(true) : outer_of(false)) >> qq) & 1u) ? 2u : (kp ? 0u : 1u));
#pragma unroll
            for (int t = 0; t < 3; ++t) ta[t] = task_at(wq, t);
            asm volatile("" : "+v"(tb), "+v"(ta[0]), "+v"(ta[1]), "+v"(ta[2]));
        }
        {
            float f0[8], f1[8];
#pragma unroll
            for (int j = 0; j < 8; ++j) {
                const float a = __uint_as_float(wv[j] << 16) * -1.44269504088896340736f, c = __uint_as_float(wv[j] & 0xffff0000u) * -1.44269504088896340736f;
                const unsigned hp = g < 2 ? 0u : pack2bf(a, c);
                f0[j] = a - __uint_as_float(hp << 16);
                f1[j] = c - __uint_as_float(hp & 0xffff0000u);
            }
            *(u32x4*)(smem + L_WR + (2 * q) * 1024 + lane * 16) = (u32x4){pack2bf(f0[0], f0[1]), pack2bf(f0[2], f0[3]), pack2bf(f0[4], f0[5]), pack2bf(f0[6], f0[7])};
            *(u32x4*)(smem + L_WR + (2 * q + 1) * 1024 + lane * 16) = (u32x4){pack2bf(f1[0], f1[1]), pack2bf(f1[2], f1[3]), pack2bf(f1[4], f1[5]), pack2bf(f1[6], f1[7])};
            float a[4];
#pragma unroll
            for (int f = 0; f < 4; ++f) a[f] = __uint_as_float((lane & 4) ? cv[f] & 0xffff0000u : cv[f] << 16) * -0.69314718055994530942f;
            const unsigned h0 = pack2bf(a[0], a[1]), h1 = pack2bf(a[2], a[3]);
            *(u32x4*)(smem + L_CG + lane * 16) = (u32x4){h0, h1, pack2bf(a[0] - __uint_as_float(h0 << 16), a[1] - __uint_as_float(h0 & 0xffff0000u)),
                                                        pack2bf(a[2] - __uint_as_float(h1 << 16), a[3] - __uint_as_float(h1 & 0xffff0000u))};
        }
        lds_wait(lds0 + SY_CPY, 4);
#pragma unroll
        for (int i = 0; i < SMAX / 8; ++i) *(u32x4*)(smem + L_PRV + (t2 + 256 * i) * 16) = (u32x4){OOB_OFF, OOB_OFF, 0u, 0u};
        SB();
        asm volatile("" :: "v"(cp), "v"(cm), "v"(t0v));
#pragma unroll
        for (int i = 0; i < 4; ++i) {
            u32x4 p0, p1;
#pragma unroll
            for (int j = 0; j < 4; ++j) {
                p0[j] = __builtin_amdgcn_perm(mv[2 * j + 1][i], mv[2 * j][i], 0x05040100u);
                p1[j] = __builtin_amdgcn_perm(mv[2 * j + 1][i], mv[2 * j][i], 0x07060302u);
            }
            const unsigned ia = L_IMG + (c16 >> 2) * 8192 + q * 1024 + g * 256 + (c16 & 3) * 64 + i * 16;
            *(u32x4*)(smem + ia) = p0;
            *(u32x4*)(smem + ia + 4096) = p1;
        }
        lds_arrive(lds0 + SY_PRE);
        if (w == 7) {
            lds_wait<1>(lds0 + SY_RI, 4);
#pragma unroll
            for (int i = 0; i < NT2; ++i) tv[i] = touch(tl[i]);
            SB();
            lds_wait<1>(lds0 + SY_C2, 3);
            load_wr();
#pragma unroll
            for (int i = 0; i < NT2; ++i) asm volatile("" :: "v"(tv[i]));
        } else {
            lds_wait(lds0 + SY_PRE, 4);
            load_wr();
            lds_wait<1>(lds0 + SY_C1, 3);
            __builtin_amdgcn_s_setprio(3);
            lds_wait(lds0 + SY_C1, 4);
            const unsigned kn = *(const unsigned*)(smem + L_KEY + n5 * 4);
            u32x4 dk[5];
#pragma unroll
            for (int k = 0; k < 5; ++k) dk[k] = *(const u32x4*)(smem + L_KEY + (n5 >= 40 ? 160 : 0) + (lane & 1) * 80 + k * 16);
            int c = 0;
#pragma unroll
            for (int k = 0; k < 5; ++k) c += (int)((kn - dk[k].x) >> 31) + (int)((kn - dk[k].y) >> 31) + (int)((kn - dk[k].z) >> 31) + (int)((kn - dk[k].w) >> 31);
            c += __builtin_amdgcn_update_dpp(0, c, 0xB1, 0xf, 0xf, false);
            *(unsigned*)(smem + L_AT + ((n5 >= 40 ? 40 : 0) + c) * 4) = (unsigned)n5 | (max(kn >> 7, 8u) << 8);
            lds_arrive(lds0 + SY_S); lds_wait(lds0 + SY_S, 3);
            const unsigned at = *(const unsigned*)(smem + L_AT + n5 * 4);
            const unsigned lf = *(const unsigned*)(smem + ta[0]) >> 8, lb = *(const unsigned*)(smem + ta[1]) >> 8, la = *(const unsigned*)(smem + ta[2]) >> 8;
            const unsigned h1 = HSPLIT(lf), a1 = la >> 1;
            *(unsigned*)(smem + L_NB + (at & 0xffu) * 4) = (tb & ~3u) + sel3(tb & 3u, h1 << 18 | (la + lb) << 25, (h1 + a1) << 6, h1 << 6 | a1 << 18 | lb << 25);
            if (lf + lb + la > SMAX) *(unsigned*)(smem + L_MISC) = 1u;
            lds_arrive(lds0 + SY_C2);
        }
        if (w != 7) lds_wait(lds0 + SY_C2, 3);
        slow = __builtin_amdgcn_readfirstlane(*(const int*)(smem + L_MISC)) != 0;
        __builtin_amdgcn_s_setprio(3);
    }
    const unsigned agf = *(const unsigned*)(smem + task_at(w, 0) + g * 4), ag1 = *(const unsigned*)(smem + task_at(w, 1) + g * 4),
                   ag2 = *(const unsigned*)(smem + task_at(w, 2) + (w < 4 ? 0 : g * 4));
    const unsigned lane_off = (unsigned)c16 * 16u;
    const unsigned aX = L_PRV + w * (SMAX * 64) + (c16 & 3) * 64 + g * 16;
    const unsigned aF = L_PRV + w * (SMAX * 64) + (c16 & 3) * 64 + (c16 >> 2) * 16 + 4;
    const unsigned aWw = L_BNC + w * 512 + lane * 8;
    const unsigned aWr = L_BNC + w * 512 + g * 128 + (c16 & 3) * 32 + (c16 >> 2) * 8;
    auto row_of = [=](unsigned at) -> unsigned {
        const unsigned nl = at & 0xffu;
        return nl < (unsigned)npb ? ((unsigned)node0 + nl) * 1024u + lane_off : OOB_OFF;
    };
    if (!slow) {
        u32x32 oxa, oxb;
        u32x4 efA, efN, S;
        const u32x4 dx = mkdesc(x, (unsigned)N * 1024u), de = mkdesc(ef, (unsigned)E * 32u);
        unsigned tmp;
        __builtin_amdgcn_s_waitcnt(0x0F70);
        asm volatile(
            "Lw_%=:\n"
            "ds_read_b32 v220, v219\n"
            "s_waitcnt lgkmcnt(0)\n"
            "v_readfirstlane_b32 %[tmp], v220\n"
            "s_cmp_lt_u32 %[tmp], 4\n"
            "s_cbranch_scc1 Lw_%=\n"
            "ds_read2st64_b32 v[232:233], v211 offset1:1\n"
            "ds_read_b128 v[196:199], v210\n"
            "s_waitcnt lgkmcnt(1)\n"
            "v_add_u32 v232, v232, v229\n"
            "v_add_u32 v233, v233, v229\n"
            "buffer_load_dwordx4 v[192:195], v232, %[rse], 0 offen\n"
            "s_waitcnt lgkmcnt(0)\n"
            "v_add_u32_dpp v220, v196, v214 quad_perm:[0,0,0,0] row_mask:0xf bank_mask:0xf\n"
            "v_add_u32_dpp v221, v196, v214 quad_perm:[1,1,1,1] row_mask:0xf bank_mask:0xf\n"
            "v_add_u32_dpp v222, v196, v214 quad_perm:[2,2,2,2] row_mask:0xf bank_mask:0xf\n"
            "v_add_u32_dpp v223, v196, v214 quad_perm:[3,3,3,3] row_mask:0xf bank_mask:0xf\n"
            "buffer_load_dwordx4 v[32:35], v220, %[rsx], 0 offen\n"
            "buffer_load_dwordx4 v[36:39], v220, %[rsx], 0 offen offset:256\n"
            "buffer_load_dwordx4 v[48:51], v221, %[rsx], 0 offen\n"
            "buffer_load_dwordx4 v[52:55], v221, %[rsx], 0 offen offset:256\n"
            "buffer_load_dwordx4 v[232:235], v233, %[rse], 0 offen\n"
            "buffer_load_dwordx4 v[64:67], v222, %[rsx], 0 offen\n"
            "buffer_load_dwordx4 v[68:71], v222, %[rsx], 0 offen offset:256\n"
            "buffer_load_dwordx4 v[80:83], v223, %[rsx], 0 offen\n"
            "buffer_load_dwordx4 v[84:87], v223, %[rsx], 0 offen offset:256\n"
            "buffer_load_dwordx4 v[40:43], v220, %[rsx], 0 offen offset:512\n"
            "buffer_load_dwordx4 v[44:47], v220, %[rsx], 0 offen offset:768\n"
            "buffer_load_dwordx4 v[56:59], v221, %[rsx], 0 offen offset:512\n"
            "buffer_load_dwordx4 v[60:63], v221, %[rsx], 0 offen offset:768\n"
            "buffer_load_dwordx4 v[72:75], v222, %[rsx], 0 offen offset:512\n"
            "buffer_load_dwordx4 v[76:79], v222, %[rsx], 0 offen offset:768\n"
            "buffer_load_dwordx4 v[88:91], v223, %[rsx], 0 offen offset:512\n"
            "buffer_load_dwordx4 v[92:95], v223, %[rsx], 0 offen offset:768\n"
            : "={v[32:63]}"(oxa), "={v[64:95]}"(oxb), "={v[192:195]}"(efA), "={v[196:199]}"(S), "={v[232:235]}"(efN), [tmp] "=&s"(tmp)
            : [rse] "s"(de), [rsx] "s"(dx), [ax] "{v210}"(lds0 + aX), [af] "{v211}"(lds0 + aF), [lo] "{v214}"(lane_off), [eo] "{v229}"((g & 1) * 16), [sy] "{v219}"(lds0 + SY_C3)
            : "memory", "scc", "v220", "v221", "v222", "v223");
        if (bx == 0 && w == 4) flag_store(ws + W_CTL, tag);
        const int lf = __builtin_amdgcn_readfirstlane((int)agf) >> 8, l1 = __builtin_amdgcn_readfirstlane((int)ag1) >> 8, l2 = __builtin_amdgcn_readfirstlane((int)ag2) >> 8;
        const int e2 = lf + l1 + l2;
        int nst = (e2 + 3) >> 2;
        const unsigned r2 = row_of(ag2);
        u32x32 owr;
        u32x16 omi;
        u32x8 ot;
#pragma unroll
        for (int i = 0; i < 32; ++i) owr[i] = wr[i >> 2][i & 3];
        const unsigned trb = lds0 + L_PK + w * PK_STR + 4096;
        omi[2] = lds0 + aX + 256; omi[3] = lds0 + aF + 256;
        omi[4] = lds0 + aWw; omi[5] = lds0 + aWr; omi[6] = lane_off; omi[7] = W_AGG + row_of(ag1); omi[8] = W_AGG + r2;
        omi[9] = row_of(agf); omi[10] = w >= 6 ? r2 : OOB_OFF; omi[15] = ~tag;
        ot[0] = (lane & 3) == 0 ? 0x3f80u : ((lane & 3) == 1 ? 0x3f800000u : 0u); ot[1] = (lane & 3) == 2 ? 0x3f80u : ((lane & 3) == 3 ? 0x3f800000u : 0u);
        ot[2] = trb + g * (4 * 272) + c16 * 16; ot[3] = trb + c16 * 272 + g * 16; ot[4] = lds0 + L_IMG + lane * 16; ot[5] = (g & 1) * 16; ot[7] = trb + lane * 16;
        unsigned ap1 = trb - 4096 + lane * 16;
        int rem = HSPLIT(lf), lens = w < 4 ? (l1 - 4) | 0xff00 : ((l2 >> 1) - 4) | (l1 - 4) << 8 | (l2 - (l2 >> 1) - 4) << 16 | 0xff << 24, fm = w < 4 ? 0x82 : (w < 6 ? 0x9022 : 0x3822);
        u64 ra;
        asm volatile(
            "s_nop 1\n"
            "v_mfma_f32_4x4x4_16b_bf16 v[204:207], v[202:203], v[198:199], 0\n"
            "v_mov_b64 v[0:1], 0\n"
            "v_mov_b64 v[2:3], 0\n"
            "v_mov_b64 v[4:5], 0\n"
            "v_mov_b64 v[6:7], 0\n"
            "v_mfma_f32_4x4x4_16b_bf16 v[204:207], v[200:201], v[198:199], v[204:207]\n"
            "v_mov_b64 v[8:9], 0\n"
            "v_mov_b64 v[10:11], 0\n"
            "v_mov_b64 v[12:13], 0\n"
            "v_mov_b64 v[14:15], 0\n"
            "v_mov_b64 v[16:17], 0\n"
            "v_mov_b64 v[18:19], 0\n"
            "v_mov_b64 v[20:21], 0\n"
            "v_mov_b64 v[22:23], 0\n"
            "v_mov_b64 v[24:25], 0\n"
            "v_mov_b64 v[26:27], 0\n"
            "v_mov_b64 v[28:29], 0\n"
            "v_mov_b64 v[30:31], 0\n"
            "v_cvt_pk_bf16_f32 v204, v204, v205\n"
            "v_cvt_pk_bf16_f32 v205, v206, v207\n"
            "ds_write_b64 v212, v[204:205]\n"
            "ds_read_b128 v[196:199], v210\n"
            "ds_read_b64 v[208:209], v213\n"
            "s_waitcnt vmcnt(17)\n"
            "v_mfma_f32_16x16x32_bf16 v[176:179], v[192:195], v[96:99], 0\n"
            "v_mfma_f32_16x16x32_bf16 v[180:183], v[192:195], v[100:103], 0\n"
            "v_mfma_f32_16x16x32_bf16 v[184:187], v[192:195], v[104:107], 0\n"
            "v_mfma_f32_16x16x32_bf16 v[188:191], v[192:195], v[108:111], 0\n"
            "v_mfma_f32_16x16x32_bf16 v[128:131], v[192:195], v[112:115], 0\n"
            "v_mfma_f32_16x16x32_bf16 v[132:135], v[192:195], v[116:119], 0\n"
            "s_nop 2\n"
            "v_exp_f32 v144, v176\n"
            "v_exp_f32 v145, v177\n"
            "v_exp_f32 v146, v178\n"
            "v_exp_f32 v147, v179\n"
            "v_exp_f32 v148, v180\n"
            "v_exp_f32 v149, v181\n"
            "v_exp_f32 v150, v182\n"
            "v_exp_f32 v151, v183\n"
            "v_exp_f32 v152, v184\n"
            "v_exp_f32 v153, v185\n"
            "v_exp_f32 v154, v186\n"
            "v_exp_f32 v155, v187\n"
            "v_exp_f32 v156, v188\n"
            "v_exp_f32 v157, v189\n"
            "v_exp_f32 v158, v190\n"
            "v_exp_f32 v159, v191\n"
            "v_pk_add_f32 v[144:145], v[144:145], 1.0 op_sel_hi:[1,0]\n"
            "v_pk_add_f32 v[146:147], v[146:147], 1.0 op_sel_hi:[1,0]\n"
            "v_pk_add_f32 v[148:149], v[148:149], 1.0 op_sel_hi:[1,0]\n"
            "v_pk_add_f32 v[150:151], v[150:151], 1.0 op_sel_hi:[1,0]\n"
            "v_pk_add_f32 v[152:153], v[152:153], 1.0 op_sel_hi:[1,0]\n"
            "v_pk_add_f32 v[154:155], v[154:155], 1.0 op_sel_hi:[1,0]\n"
            "v_pk_add_f32 v[156:157], v[156:157], 1.0 op_sel_hi:[1,0]\n"
            "v_pk_add_f32 v[158:159], v[158:159], 1.0 op_sel_hi:[1,0]\n"
            "v_rcp_f32 v144, v144\n"
            "v_rcp_f32 v145, v145\n"
            "v_rcp_f32 v146, v146\n"
            "v_rcp_f32 v147, v147\n"
            "v_rcp_f32 v148, v148\n"
            "v_rcp_f32 v149, v149\n"
            "v_rcp_f32 v150, v150\n"
            "v_rcp_f32 v151, v151\n"
            "v_rcp_f32 v152, v152\n"
            "v_rcp_f32 v153, v153\n"
            "v_rcp_f32 v154, v154\n"
            "v_rcp_f32 v155, v155\n"
            "v_rcp_f32 v156, v156\n"
            "v_rcp_f32 v157, v157\n"
            "v_rcp_f32 v158, v158\n"
            "v_rcp_f32 v159, v159\n"
            "v_pk_mul_f32 v[144:145], v[144:145], v[176:177]\n"
            "v_pk_mul_f32 v[146:147], v[146:147], v[178:179]\n"
            "v_pk_mul_f32 v[148:149], v[148:149], v[180:181]\n"
            "v_pk_mul_f32 v[150:151], v[150:151], v[182:183]\n"
            "v_pk_mul_f32 v[152:153], v[152:153], v[184:185]\n"
            "v_pk_mul_f32 v[154:155], v[154:155], v[186:187]\n"
            "v_pk_mul_f32 v[156:157], v[156:157], v[188:189]\n"
            "v_pk_mul_f32 v[158:159], v[158:159], v[190:191]\n"
            "s_branch Ldisp_%=\n"
            "Lfast_%=:\n"
            "v_mfma_f32_16x16x32_bf16 v[136:139], v[192:195], v[120:123], 0\n"
            "v_exp_f32 v160, v128\n"
            "v_add_u32 v211, 0x100, v211\n"
            "v_exp_f32 v161, v129\n"
            "s_waitcnt vmcnt(13) lgkmcnt(0)\n"
            "v_mfma_f32_4x4x4_16b_bf16 v[176:179], v[208:209], v[32:33], 0 cbsz:2\n"
            "v_exp_f32 v162, v130\n"
            "v_mfma_f32_4x4x4_16b_bf16 v[180:183], v[208:209], v[34:35], 0 cbsz:2\n"
            "v_exp_f32 v163, v131\n"
            "v_mfma_f32_4x4x4_16b_bf16 v[184:187], v[208:209], v[36:37], 0 cbsz:2\n"
            "v_exp_f32 v164, v132\n"
            "v_mfma_f32_4x4x4_16b_bf16 v[188:191], v[208:209], v[38:39], 0 cbsz:2\n"
            "v_exp_f32 v165, v133\n"
            "v_add_u32_dpp v219, v196, v214 quad_perm:[0,0,0,0] row_mask:0xf bank_mask:0xf\n"
            "v_mfma_f32_4x4x4_16b_bf16 v[204:207], v[202:203], v[198:199], 0\n"
            "v_exp_f32 v166, v134\n"
            "buffer_load_dwordx4 v[32:35], v219, %[rsx], 0 offen\n"
            "buffer_load_dwordx4 v[36:39], v219, %[rsx], 0 offen offset:256\n"
            "v_exp_f32 v167, v135\n"
            "v_pk_fma_f32 v[0:1], v[144:145], v[176:177], v[0:1] op_sel_hi:[0,1,1]\n"
            "v_pk_fma_f32 v[2:3], v[144:145], v[178:179], v[2:3] op_sel_hi:[0,1,1]\n"
            "v_pk_fma_f32 v[4:5], v[148:149], v[180:181], v[4:5] op_sel_hi:[0,1,1]\n"
            "v_pk_fma_f32 v[6:7], v[148:149], v[182:183], v[6:7] op_sel_hi:[0,1,1]\n"
            "v_pk_fma_f32 v[8:9], v[152:153], v[184:185], v[8:9] op_sel_hi:[0,1,1]\n"
            "v_pk_fma_f32 v[10:11], v[152:153], v[186:187], v[10:11] op_sel_hi:[0,1,1]\n"
            "v_pk_fma_f32 v[12:13], v[156:157], v[188:189], v[12:13] op_sel_hi:[0,1,1]\n"
            "v_pk_fma_f32 v[14:15], v[156:157], v[190:191], v[14:15] op_sel_hi:[0,1,1]\n"
            "v_pk_add_f32 v[160:161], v[160:161], 1.0 op_sel_hi:[1,0]\n"
            "v_pk_add_f32 v[162:163], v[162:163], 1.0 op_sel_hi:[1,0]\n"
            "v_pk_add_f32 v[164:165], v[164:165], 1.0 op_sel_hi:[1,0]\n"
            "v_pk_add_f32 v[166:167], v[166:167], 1.0 op_sel_hi:[1,0]\n"
            "v_mfma_f32_16x16x32_bf16 v[140:143], v[192:195], v[124:127], 0\n"
            "ds_read_b32 v230, v211\n"
            "v_rcp_f32 v160, v160\n"
            "v_rcp_f32 v161, v161\n"
            "v_mfma_f32_4x4x4_16b_bf16 v[176:179], v[208:209], v[48:49], 0 cbsz:2 abid:1\n"
            "v_rcp_f32 v162, v162\n"
            "v_mfma_f32_4x4x4_16b_bf16 v[180:183], v[208:209], v[50:51], 0 cbsz:2 abid:1\n"
            "v_rcp_f32 v163, v163\n"
            "v_mfma_f32_4x4x4_16b_bf16 v[184:187], v[208:209], v[52:53], 0 cbsz:2 abid:1\n"
            "v_rcp_f32 v164, v164\n"
            "v_mfma_f32_4x4x4_16b_bf16 v[188:191], v[208:209], v[54:55], 0 cbsz:2 abid:1\n"
            "v_rcp_f32 v165, v165\n"
            "v_add_u32_dpp v220, v196, v214 quad_perm:[1,1,1,1] row_mask:0xf bank_mask:0xf\n"
            "v_mfma_f32_4x4x4_16b_bf16 v[204:207], v[200:201], v[198:199], v[204:207]\n"
            "v_rcp_f32 v166, v166\n"
            "buffer_load_dwordx4 v[48:51], v220, %[rsx], 0 offen\n"
            "buffer_load_dwordx4 v[52:55], v220, %[rsx], 0 offen offset:256\n"
            "v_rcp_f32 v167, v167\n"
            "v_pk_fma_f32 v[0:1], v[144:145], v[176:177], v[0:1] op_sel:[1,0,0]\n"
            "v_pk_fma_f32 v[2:3], v[144:145], v[178:179], v[2:3] op_sel:[1,0,0]\n"
            "v_pk_fma_f32 v[4:5], v[148:149], v[180:181], v[4:5] op_sel:[1,0,0]\n"
            "v_pk_fma_f32 v[6:7], v[148:149], v[182:183], v[6:7] op_sel:[1,0,0]\n"
            "v_pk_fma_f32 v[8:9], v[152:153], v[184:185], v[8:9] op_sel:[1,0,0]\n"
            "v_pk_fma_f32 v[10:11], v[152:153], v[186:187], v[10:11] op_sel:[1,0,0]\n"
            "v_pk_fma_f32 v[12:13], v[156:157], v[188:189], v[12:13] op_sel:[1,0,0]\n"
            "v_pk_fma_f32 v[14:15], v[156:157], v[190:191], v[14:15] op_sel:[1,0,0]\n"
            "v_pk_mul_f32 v[160:161], v[160:161], v[128:129]\n"
            "v_pk_mul_f32 v[162:163], v[162:163], v[130:131]\n"
            "v_pk_mul_f32 v[164:165], v[164:165], v[132:133]\n"
            "v_pk_mul_f32 v[166:167], v[166:167], v[134:135]\n"
            "s_waitcnt vmcnt(12) lgkmcnt(0)\n"
            "v_mov_b64 v[192:193], v[232:233]\n"
            "v_mov_b64 v[194:195], v[234:235]\n"
            "v_add_u32 v230, v230, v229\n"
            "buffer_load_dwordx4 v[232:235], v230, %[rse], 0 offen\n"
            "v_mfma_f32_16x16x32_bf16 v[128:131], v[192:195], v[96:99], 0\n"
            "v_exp_f32 v168, v136\n"
            "v_exp_f32 v169, v137\n"
            "v_mfma_f32_4x4x4_16b_bf16 v[176:179], v[208:209], v[64:65], 0 cbsz:2 abid:2\n"
            "v_exp_f32 v170, v138\n"
            "v_mfma_f32_4x4x4_16b_bf16 v[180:183], v[208:209], v[66:67], 0 cbsz:2 abid:2\n"
            "v_exp_f32 v171, v139\n"
            "v_mfma_f32_4x4x4_16b_bf16 v[184:187], v[208:209], v[68:69], 0 cbsz:2 abid:2\n"
            "v_exp_f32 v172, v140\n"
            "v_mfma_f32_4x4x4_16b_bf16 v[188:191], v[208:209], v[70:71], 0 cbsz:2 abid:2\n"
            "v_exp_f32 v173, v141\n"
            "v_add_u32_dpp v221, v196, v214 quad_perm:[2,2,2,2] row_mask:0xf bank_mask:0xf\n"
            "v_exp_f32 v174, v142\n"
            "buffer_load_dwordx4 v[64:67], v221, %[rsx], 0 offen\n"
            "buffer_load_dwordx4 v[68:71], v221, %[rsx], 0 offen offset:256\n"
            "v_exp_f32 v175, v143\n"
            "v_pk_fma_f32 v[0:1], v[146:147], v[176:177], v[0:1] op_sel_hi:[0,1,1]\n"
            "v_pk_fma_f32 v[2:3], v[146:147], v[178:179], v[2:3] op_sel_hi:[0,1,1]\n"
            "v_pk_fma_f32 v[4:5], v[150:151], v[180:181], v[4:5] op_sel_hi:[0,1,1]\n"
            "v_pk_fma_f32 v[6:7], v[150:151], v[182:183], v[6:7] op_sel_hi:[0,1,1]\n"
            "v_pk_fma_f32 v[8:9], v[154:155], v[184:185], v[8:9] op_sel_hi:[0,1,1]\n"
            "v_pk_fma_f32 v[10:11], v[154:155], v[186:187], v[10:11] op_sel_hi:[0,1,1]\n"
            "v_pk_fma_f32 v[12:13], v[158:159], v[188:189], v[12:13] op_sel_hi:[0,1,1]\n"
            "v_pk_fma_f32 v[14:15], v[158:159], v[190:191], v[14:15] op_sel_hi:[0,1,1]\n"
            "v_pk_add_f32 v[168:169], v[168:169], 1.0 op_sel_hi:[1,0]\n"
            "v_pk_add_f32 v[170:171], v[170:171], 1.0 op_sel_hi:[1,0]\n"
            "v_pk_add_f32 v[172:173], v[172:173], 1.0 op_sel_hi:[1,0]\n"
            "v_pk_add_f32 v[174:175], v[174:175], 1.0 op_sel_hi:[1,0]\n"
            "v_cvt_pk_bf16_f32 v204, v204, v205\n"
            "v_cvt_pk_bf16_f32 v205, v206, v207\n"
            "ds_write_b64 v212, v[204:205]\n"
            "v_mfma_f32_16x16x32_bf16 v[132:135], v[192:195], v[100:103], 0\n"
            "v_rcp_f32 v168, v168\n"
            "v_rcp_f32 v169, v169\n"
            "v_mfma_f32_4x4x4_16b_bf16 v[176:179], v[208:209], v[80:81], 0 cbsz:2 abid:3\n"
            "v_rcp_f32 v170, v170\n"
            "v_mfma_f32_4x4x4_16b_bf16 v[180:183], v[208:209], v[82:83], 0 cbsz:2 abid:3\n"
            "v_rcp_f32 v171, v171\n"
            "v_mfma_f32_4x4x4_16b_bf16 v[184:187], v[208:209], v[84:85], 0 cbsz:2 abid:3\n"
            "v_rcp_f32 v172, v172\n"
            "v_mfma_f32_4x4x4_16b_bf16 v[188:191], v[208:209], v[86:87], 0 cbsz:2 abid:3\n"
            "v_rcp_f32 v173, v173\n"
            "v_add_u32_dpp v222, v196, v214 quad_perm:[3,3,3,3] row_mask:0xf bank_mask:0xf\n"
            "v_rcp_f32 v174, v174\n"
            "buffer_load_dwordx4 v[80:83], v222, %[rsx], 0 offen\n"
            "buffer_load_dwordx4 v[84:87], v222, %[rsx], 0 offen offset:256\n"
            "v_rcp_f32 v175, v175\n"
            "v_pk_fma_f32 v[0:1], v[146:147], v[176:177], v[0:1] op_sel:[1,0,0]\n"
            "v_pk_fma_f32 v[2:3], v[146:147], v[178:179], v[2:3] op_sel:[1,0,0]\n"
            "v_pk_fma_f32 v[4:5], v[150:151], v[180:181], v[4:5] op_sel:[1,0,0]\n"
            "v_pk_fma_f32 v[6:7], v[150:151], v[182:183], v[6:7] op_sel:[1,0,0]\n"
            "v_pk_fma_f32 v[8:9], v[154:155], v[184:185], v[8:9] op_sel:[1,0,0]\n"
            "v_pk_fma_f32 v[10:11], v[154:155], v[186:187], v[10:11] op_sel:[1,0,0]\n"
            "v_pk_fma_f32 v[12:13], v[158:159], v[188:189], v[12:13] op_sel:[1,0,0]\n"
            "v_pk_fma_f32 v[14:15], v[158:159], v[190:191], v[14:15] op_sel:[1,0,0]\n"
            "v_pk_mul_f32 v[168:169], v[168:169], v[136:137]\n"
            "v_pk_mul_f32 v[170:171], v[170:171], v[138:139]\n"
            "v_pk_mul_f32 v[172:173], v[172:173], v[140:141]\n"
            "v_pk_mul_f32 v[174:175], v[174:175], v[142:143]\n"
            "v_add_u32 v210, 0x100, v210\n"
            "ds_read_b128 v[196:199], v210\n"
            "v_mfma_f32_16x16x32_bf16 v[136:139], v[192:195], v[104:107], 0\n"
            "v_exp_f32 v144, v128\n"
            "v_exp_f32 v145, v129\n"
            "s_waitcnt vmcnt(13)\n"
            "v_mfma_f32_4x4x4_16b_bf16 v[176:179], v[208:209], v[40:41], 0 cbsz:2\n"
            "v_exp_f32 v146, v130\n"
            "v_mfma_f32_4x4x4_16b_bf16 v[180:183], v[208:209], v[42:43], 0 cbsz:2\n"
            "v_exp_f32 v147, v131\n"
            "v_mfma_f32_4x4x4_16b_bf16 v[184:187], v[208:209], v[44:45], 0 cbsz:2\n"
            "v_exp_f32 v148, v132\n"
            "v_mfma_f32_4x4x4_16b_bf16 v[188:191], v[208:209], v[46:47], 0 cbsz:2\n"
            "v_exp_f32 v149, v133\n"
            "v_exp_f32 v150, v134\n"
            "buffer_load_dwordx4 v[40:43], v219, %[rsx], 0 offen offset:512\n"
            "buffer_load_dwordx4 v[44:47], v219, %[rsx], 0 offen offset:768\n"
            "v_exp_f32 v151, v135\n"
            "v_pk_fma_f32 v[16:17], v[160:161], v[176:177], v[16:17] op_sel_hi:[0,1,1]\n"
            "v_pk_fma_f32 v[18:19], v[160:161], v[178:179], v[18:19] op_sel_hi:[0,1,1]\n"
            "v_pk_fma_f32 v[20:21], v[164:165], v[180:181], v[20:21] op_sel_hi:[0,1,1]\n"
            "v_pk_fma_f32 v[22:23], v[164:165], v[182:183], v[22:23] op_sel_hi:[0,1,1]\n"
            "v_pk_fma_f32 v[24:25], v[168:169], v[184:185], v[24:25] op_sel_hi:[0,1,1]\n"
            "v_pk_fma_f32 v[26:27], v[168:169], v[186:187], v[26:27] op_sel_hi:[0,1,1]\n"
            "v_pk_fma_f32 v[28:29], v[172:173], v[188:189], v[28:29] op_sel_hi:[0,1,1]\n"
            "v_pk_fma_f32 v[30:31], v[172:173], v[190:191], v[30:31] op_sel_hi:[0,1,1]\n"
            "v_pk_add_f32 v[144:145], v[144:145], 1.0 op_sel_hi:[1,0]\n"
            "v_pk_add_f32 v[146:147], v[146:147], 1.0 op_sel_hi:[1,0]\n"
            "v_pk_add_f32 v[148:149], v[148:149], 1.0 op_sel_hi:[1,0]\n"
            "v_pk_add_f32 v[150:151], v[150:151], 1.0 op_sel_hi:[1,0]\n"
            "v_mfma_f32_16x16x32_bf16 v[140:143], v[192:195], v[108:111], 0\n"
            "v_rcp_f32 v144, v144\n"
            "v_rcp_f32 v145, v145\n"
            "v_mfma_f32_4x4x4_16b_bf16 v[176:179], v[208:209], v[56:57], 0 cbsz:2 abid:1\n"
            "v_rcp_f32 v146, v146\n"
            "v_mfma_f32_4x4x4_16b_bf16 v[180:183], v[208:209], v[58:59], 0 cbsz:2 abid:1\n"
            "v_rcp_f32 v147, v147\n"
            "v_mfma_f32_4x4x4_16b_bf16 v[184:187], v[208:209], v[60:61], 0 cbsz:2 abid:1\n"
            "v_rcp_f32 v148, v148\n"
            "v_mfma_f32_4x4x4_16b_bf16 v[188:191], v[208:209], v[62:63], 0 cbsz:2 abid:1\n"
            "v_rcp_f32 v149, v149\n"
            "v_rcp_f32 v150, v150\n"
            "buffer_load_dwordx4 v[56:59], v220, %[rsx], 0 offen offset:512\n"
            "buffer_load_dwordx4 v[60:63], v220, %[rsx], 0 offen offset:768\n"
            "v_rcp_f32 v151, v151\n"
            "v_pk_fma_f32 v[16:17], v[160:161], v[176:177], v[16:17] op_sel:[1,0,0]\n"
            "v_pk_fma_f32 v[18:19], v[160:161], v[178:179], v[18:19] op_sel:[1,0,0]\n"
            "v_pk_fma_f32 v[20:21], v[164:165], v[180:181], v[20:21] op_sel:[1,0,0]\n"
            "v_pk_fma_f32 v[22:23], v[164:165], v[182:183], v[22:23] op_sel:[1,0,0]\n"
            "v_pk_fma_f32 v[24:25], v[168:169], v[184:185], v[24:25] op_sel:[1,0,0]\n"
            "v_pk_fma_f32 v[26:27], v[168:169], v[186:187], v[26:27] op_sel:[1,0,0]\n"
            "v_pk_fma_f32 v[28:29], v[172:173], v[188:189], v[28:29] op_sel:[1,0,0]\n"
            "v_pk_fma_f32 v[30:31], v[172:173], v[190:191], v[30:31] op_sel:[1,0,0]\n"
            "v_pk_mul_f32 v[144:145], v[144:145], v[128:129]\n"
            "v_pk_mul_f32 v[146:147], v[146:147], v[130:131]\n"
            "v_pk_mul_f32 v[148:149], v[148:149], v[132:133]\n"
            "v_pk_mul_f32 v[150:151], v[150:151], v[134:135]\n"
            "v_mfma_f32_16x16x32_bf16 v[128:131], v[192:195], v[112:115], 0\n"
            "v_exp_f32 v152, v136\n"
            "v_exp_f32 v153, v137\n"
            "s_waitcnt vmcnt(13)\n"
            "v_mfma_f32_4x4x4_16b_bf16 v[176:179], v[208:209], v[72:73], 0 cbsz:2 abid:2\n"
            "v_exp_f32 v154, v138\n"
            "v_mfma_f32_4x4x4_16b_bf16 v[180:183], v[208:209], v[74:75], 0 cbsz:2 abid:2\n"
            "v_exp_f32 v155, v139\n"
            "v_mfma_f32_4x4x4_16b_bf16 v[184:187], v[208:209], v[76:77], 0 cbsz:2 abid:2\n"
            "v_exp_f32 v156, v140\n"
            "v_mfma_f32_4x4x4_16b_bf16 v[188:191], v[208:209], v[78:79], 0 cbsz:2 abid:2\n"
            "v_exp_f32 v157, v141\n"
            "v_exp_f32 v158, v142\n"
            "buffer_load_dwordx4 v[72:75], v221, %[rsx], 0 offen offset:512\n"
            "buffer_load_dwordx4 v[76:79], v221, %[rsx], 0 offen offset:768\n"
            "v_exp_f32 v159, v143\n"
            "v_pk_fma_f32 v[16:17], v[162:163], v[176:177], v[16:17] op_sel_hi:[0,1,1]\n"
            "v_pk_fma_f32 v[18:19], v[162:163], v[178:179], v[18:19] op_sel_hi:[0,1,1]\n"
            "v_pk_fma_f32 v[20:21], v[166:167], v[180:181], v[20:21] op_sel_hi:[0,1,1]\n"
            "v_pk_fma_f32 v[22:23], v[166:167], v[182:183], v[22:23] op_sel_hi:[0,1,1]\n"
            "v_pk_fma_f32 v[24:25], v[170:171], v[184:185], v[24:25] op_sel_hi:[0,1,1]\n"
            "v_pk_fma_f32 v[26:27], v[170:171], v[186:187], v[26:27] op_sel_hi:[0,1,1]\n"
            "v_pk_fma_f32 v[28:29], v[174:175], v[188:189], v[28:29] op_sel_hi:[0,1,1]\n"
            "v_pk_fma_f32 v[30:31], v[174:175], v[190:191], v[30:31] op_sel_hi:[0,1,1]\n"
            "v_pk_add_f32 v[152:153], v[152:153], 1.0 op_sel_hi:[1,0]\n"
            "v_pk_add_f32 v[154:155], v[154:155], 1.0 op_sel_hi:[1,0]\n"
            "v_pk_add_f32 v[156:157], v[156:157], 1.0 op_sel_hi:[1,0]\n"
            "v_pk_add_f32 v[158:159], v[158:159], 1.0 op_sel_hi:[1,0]\n"
            "v_mfma_f32_16x16x32_bf16 v[132:135], v[192:195], v[116:119], 0\n"
            "v_rcp_f32 v152, v152\n"
            "v_rcp_f32 v153, v153\n"
            "v_mfma_f32_4x4x4_16b_bf16 v[176:179], v[208:209], v[88:89], 0 cbsz:2 abid:3\n"
            "v_rcp_f32 v154, v154\n"
            "v_mfma_f32_4x4x4_16b_bf16 v[180:183], v[208:209], v[90:91], 0 cbsz:2 abid:3\n"
            "v_rcp_f32 v155, v155\n"
            "v_mfma_f32_4x4x4_16b_bf16 v[184:187], v[208:209], v[92:93], 0 cbsz:2 abid:3\n"
            "v_rcp_f32 v156, v156\n"
            "v_mfma_f32_4x4x4_16b_bf16 v[188:191], v[208:209], v[94:95], 0 cbsz:2 abid:3\n"
            "ds_read_b64 v[208:209], v213\n"
            "v_rcp_f32 v157, v157\n"
            "v_rcp_f32 v158, v158\n"
            "buffer_load_dwordx4 v[88:91], v222, %[rsx], 0 offen offset:512\n"
            "buffer_load_dwordx4 v[92:95], v222, %[rsx], 0 offen offset:768\n"
            "v_rcp_f32 v159, v159\n"
            "v_pk_fma_f32 v[16:17], v[162:163], v[176:177], v[16:17] op_sel:[1,0,0]\n"
            "v_pk_fma_f32 v[18:19], v[162:163], v[178:179], v[18:19] op_sel:[1,0,0]\n"
            "v_pk_fma_f32 v[20:21], v[166:167], v[180:181], v[20:21] op_sel:[1,0,0]\n"
            "v_pk_fma_f32 v[22:23], v[166:167], v[182:183], v[22:23] op_sel:[1,0,0]\n"
            "v_pk_fma_f32 v[24:25], v[170:171], v[184:185], v[24:25] op_sel:[1,0,0]\n"
            "v_pk_fma_f32 v[26:27], v[170:171], v[186:187], v[26:27] op_sel:[1,0,0]\n"
            "v_pk_fma_f32 v[28:29], v[174:175], v[188:189], v[28:29] op_sel:[1,0,0]\n"
            "v_pk_fma_f32 v[30:31], v[174:175], v[190:191], v[30:31] op_sel:[1,0,0]\n"
            "v_pk_mul_f32 v[152:153], v[152:153], v[136:137]\n"
            "v_pk_mul_f32 v[154:155], v[154:155], v[138:139]\n"
            "v_pk_mul_f32 v[156:157], v[156:157], v[140:141]\n"
            "v_pk_mul_f32 v[158:159], v[158:159], v[142:143]\n"
            "s_sub_u32 %[rem], %[rem], 4\n"
            "Lnext_%=:\n"
            "s_sub_u32 %[nst], %[nst], 1\n"
            "Ldisp_%=:\n"
            "s_bitcmp1_b32 %[evt], %[nst]\n"
            "s_cbranch_scc1 Lprio_%=\n"
            "Ld2_%=:\n"
            "s_cmp_gt_u32 %[rem], 4\n"
            "s_cbranch_scc1 Lfast_%=\n"
            "v_mfma_f32_16x16x32_bf16 v[136:139], v[192:195], v[120:123], 0\n"
            "v_exp_f32 v160, v128\n"
            "v_add_u32 v211, 0x100, v211\n"
            "v_exp_f32 v161, v129\n"
            "s_waitcnt vmcnt(13) lgkmcnt(0)\n"
            "v_mfma_f32_4x4x4_16b_bf16 v[176:179], v[208:209], v[32:33], 0 cbsz:2\n"
            "v_exp_f32 v162, v130\n"
            "v_mfma_f32_4x4x4_16b_bf16 v[180:183], v[208:209], v[34:35], 0 cbsz:2\n"
            "v_exp_f32 v163, v131\n"
            "v_mfma_f32_4x4x4_16b_bf16 v[184:187], v[208:209], v[36:37], 0 cbsz:2\n"
            "v_exp_f32 v164, v132\n"
            "v_mfma_f32_4x4x4_16b_bf16 v[188:191], v[208:209], v[38:39], 0 cbsz:2\n"
            "v_exp_f32 v165, v133\n"
            "v_add_u32_dpp v219, v196, v214 quad_perm:[0,0,0,0] row_mask:0xf bank_mask:0xf\n"
            "v_mfma_f32_4x4x4_16b_bf16 v[204:207], v[202:203], v[198:199], 0\n"
            "v_exp_f32 v166, v134\n"
            "buffer_load_dwordx4 v[32:35], v219, %[rsx], 0 offen\n"
            "buffer_load_dwordx4 v[36:39], v219, %[rsx], 0 offen offset:256\n"
            "v_exp_f32 v167, v135\n"
            "v_pk_fma_f32 v[0:1], v[144:145], v[176:177], v[0:1] op_sel_hi:[0,1,1]\n"
            "v_pk_fma_f32 v[2:3], v[144:145], v[178:179], v[2:3] op_sel_hi:[0,1,1]\n"
            "v_pk_fma_f32 v[4:5], v[148:149], v[180:181], v[4:5] op_sel_hi:[0,1,1]\n"
            "v_pk_fma_f32 v[6:7], v[148:149], v[182:183], v[6:7] op_sel_hi:[0,1,1]\n"
            "v_pk_fma_f32 v[8:9], v[152:153], v[184:185], v[8:9] op_sel_hi:[0,1,1]\n"
            "v_pk_fma_f32 v[10:11], v[152:153], v[186:187], v[10:11] op_sel_hi:[0,1,1]\n"
            "v_pk_fma_f32 v[12:13], v[156:157], v[188:189], v[12:13] op_sel_hi:[0,1,1]\n"
            "v_pk_fma_f32 v[14:15], v[156:157], v[190:191], v[14:15] op_sel_hi:[0,1,1]\n"
            "v_pk_add_f32 v[160:161], v[160:161], 1.0 op_sel_hi:[1,0]\n"
            "v_pk_add_f32 v[162:163], v[162:163], 1.0 op_sel_hi:[1,0]\n"
            "v_pk_add_f32 v[164:165], v[164:165], 1.0 op_sel_hi:[1,0]\n"
            "v_pk_add_f32 v[166:167], v[166:167], 1.0 op_sel_hi:[1,0]\n"
            "s_cmp_eq_u32 %[rem], 1\n"
            "s_cbranch_scc1 Lf00_%=\n"
            "Lr00_%=:\n"
            "v_mfma_f32_16x16x32_bf16 v[140:143], v[192:195], v[124:127], 0\n"
            "ds_read_b32 v230, v211\n"
            "v_rcp_f32 v160, v160\n"
            "v_rcp_f32 v161, v161\n"
            "v_mfma_f32_4x4x4_16b_bf16 v[176:179], v[208:209], v[48:49], 0 cbsz:2 abid:1\n"
            "v_rcp_f32 v162, v162\n"
            "v_mfma_f32_4x4x4_16b_bf16 v[180:183], v[208:209], v[50:51], 0 cbsz:2 abid:1\n"
            "v_rcp_f32 v163, v163\n"
            "v_mfma_f32_4x4x4_16b_bf16 v[184:187], v[208:209], v[52:53], 0 cbsz:2 abid:1\n"
            "v_rcp_f32 v164, v164\n"
            "v_mfma_f32_4x4x4_16b_bf16 v[188:191], v[208:209], v[54:55], 0 cbsz:2 abid:1\n"
            "v_rcp_f32 v165, v165\n"
            "v_add_u32_dpp v220, v196, v214 quad_perm:[1,1,1,1] row_mask:0xf bank_mask:0xf\n"
            "v_mfma_f32_4x4x4_16b_bf16 v[204:207], v[200:201], v[198:199], v[204:207]\n"
            "v_rcp_f32 v166, v166\n"
            "buffer_load_dwordx4 v[48:51], v220, %[rsx], 0 offen\n"
            "buffer_load_dwordx4 v[52:55], v220, %[rsx], 0 offen offset:256\n"
            "v_rcp_f32 v167, v167\n"
            "v_pk_fma_f32 v[0:1], v[144:145], v[176:177], v[0:1] op_sel:[1,0,0]\n"
            "v_pk_fma_f32 v[2:3], v[144:145], v[178:179], v[2:3] op_sel:[1,0,0]\n"
            "v_pk_fma_f32 v[4:5], v[148:149], v[180:181], v[4:5] op_sel:[1,0,0]\n"
            "v_pk_fma_f32 v[6:7], v[148:149], v[182:183], v[6:7] op_sel:[1,0,0]\n"
            "v_pk_fma_f32 v[8:9], v[152:153], v[184:185], v[8:9] op_sel:[1,0,0]\n"
            "v_pk_fma_f32 v[10:11], v[152:153], v[186:187], v[10:11] op_sel:[1,0,0]\n"
            "v_pk_fma_f32 v[12:13], v[156:157], v[188:189], v[12:13] op_sel:[1,0,0]\n"
            "v_pk_fma_f32 v[14:15], v[156:157], v[190:191], v[14:15] op_sel:[1,0,0]\n"
            "v_pk_mul_f32 v[160:161], v[160:161], v[128:129]\n"
            "v_pk_mul_f32 v[162:163], v[162:163], v[130:131]\n"
            "v_pk_mul_f32 v[164:165], v[164:165], v[132:133]\n"
            "v_pk_mul_f32 v[166:167], v[166:167], v[134:135]\n"
            "s_cmp_eq_u32 %[rem], 2\n"
            "s_cbranch_scc1 Lf01_%=\n"
            "Lr01_%=:\n"
            "s_waitcnt vmcnt(12) lgkmcnt(0)\n"
            "v_mov_b64 v[192:193], v[232:233]\n"
            "v_mov_b64 v[194:195], v[234:235]\n"
            "v_add_u32 v230, v230, v229\n"
            "buffer_load_dwordx4 v[232:235], v230, %[rse], 0 offen\n"
            "v_mfma_f32_16x16x32_bf16 v[128:131], v[192:195], v[96:99], 0\n"
            "v_exp_f32 v168, v136\n"
            "v_exp_f32 v169, v137\n"
            "v_mfma_f32_4x4x4_16b_bf16 v[176:179], v[208:209], v[64:65], 0 cbsz:2 abid:2\n"
            "v_exp_f32 v170, v138\n"
            "v_mfma_f32_4x4x4_16b_bf16 v[180:183], v[208:209], v[66:67], 0 cbsz:2 abid:2\n"
            "v_exp_f32 v171, v139\n"
            "v_mfma_f32_4x4x4_16b_bf16 v[184:187], v[208:209], v[68:69], 0 cbsz:2 abid:2\n"
            "v_exp_f32 v172, v140\n"
            "v_mfma_f32_4x4x4_16b_bf16 v[188:191], v[208:209], v[70:71], 0 cbsz:2 abid:2\n"
            "v_exp_f32 v173, v141\n"
            "v_add_u32_dpp v221, v196, v214 quad_perm:[2,2,2,2] row_mask:0xf bank_mask:0xf\n"
            "v_exp_f32 v174, v142\n"
            "buffer_load_dwordx4 v[64:67], v221, %[rsx], 0 offen\n"
            "buffer_load_dwordx4 v[68:71], v221, %[rsx], 0 offen offset:256\n"
            "v_exp_f32 v175, v143\n"
            "v_pk_fma_f32 v[0:1], v[146:147], v[176:177], v[0:1] op_sel_hi:[0,1,1]\n"
            "v_pk_fma_f32 v[2:3], v[146:147], v[178:179], v[2:3] op_sel_hi:[0,1,1]\n"
            "v_pk_fma_f32 v[4:5], v[150:151], v[180:181], v[4:5] op_sel_hi:[0,1,1]\n"
            "v_pk_fma_f32 v[6:7], v[150:151], v[182:183], v[6:7] op_sel_hi:[0,1,1]\n"
            "v_pk_fma_f32 v[8:9], v[154:155], v[184:185], v[8:9] op_sel_hi:[0,1,1]\n"
            "v_pk_fma_f32 v[10:11], v[154:155], v[186:187], v[10:11] op_sel_hi:[0,1,1]\n"
            "v_pk_fma_f32 v[12:13], v[158:159], v[188:189], v[12:13] op_sel_hi:[0,1,1]\n"
            "v_pk_fma_f32 v[14:15], v[158:159], v[190:191], v[14:15] op_sel_hi:[0,1,1]\n"
            "v_pk_add_f32 v[168:169], v[168:169], 1.0 op_sel_hi:[1,0]\n"
            "v_pk_add_f32 v[170:171], v[170:171], 1.0 op_sel_hi:[1,0]\n"
            "v_pk_add_f32 v[172:173], v[172:173], 1.0 op_sel_hi:[1,0]\n"
            "v_pk_add_f32 v[174:175], v[174:175], 1.0 op_sel_hi:[1,0]\n"
            "v_cvt_pk_bf16_f32 v204, v204, v205\n"
            "v_cvt_pk_bf16_f32 v205, v206, v207\n"
            "ds_write_b64 v212, v[204:205]\n"
            "s_cmp_eq_u32 %[rem], 3\n"
            "s_cbranch_scc1 Lf02_%=\n"
            "Lr02_%=:\n"
            "v_mfma_f32_16x16x32_bf16 v[132:135], v[192:195], v[100:103], 0\n"
            "v_rcp_f32 v168, v168\n"
            "v_rcp_f32 v169, v169\n"
            "v_mfma_f32_4x4x4_16b_bf16 v[176:179], v[208:209], v[80:81], 0 cbsz:2 abid:3\n"
            "v_rcp_f32 v170, v170\n"
            "v_mfma_f32_4x4x4_16b_bf16 v[180:183], v[208:209], v[82:83], 0 cbsz:2 abid:3\n"
            "v_rcp_f32 v171, v171\n"
            "v_mfma_f32_4x4x4_16b_bf16 v[184:187], v[208:209], v[84:85], 0 cbsz:2 abid:3\n"
            "v_rcp_f32 v172, v172\n"
            "v_mfma_f32_4x4x4_16b_bf16 v[188:191], v[208:209], v[86:87], 0 cbsz:2 abid:3\n"
            "v_rcp_f32 v173, v173\n"
            "v_add_u32_dpp v222, v196, v214 quad_perm:[3,3,3,3] row_mask:0xf bank_mask:0xf\n"
            "v_rcp_f32 v174, v174\n"
            "buffer_load_dwordx4 v[80:83], v222, %[rsx], 0 offen\n"
            "buffer_load_dwordx4 v[84:87], v222, %[rsx], 0 offen offset:256\n"
            "v_rcp_f32 v175, v175\n"
            "v_pk_fma_f32 v[0:1], v[146:147], v[176:177], v[0:1] op_sel:[1,0,0]\n"
            "v_pk_fma_f32 v[2:3], v[146:147], v[178:179], v[2:3] op_sel:[1,0,0]\n"
            "v_pk_fma_f32 v[4:5], v[150:151], v[180:181], v[4:5] op_sel:[1,0,0]\n"
            "v_pk_fma_f32 v[6:7], v[150:151], v[182:183], v[6:7] op_sel:[1,0,0]\n"
            "v_pk_fma_f32 v[8:9], v[154:155], v[184:185], v[8:9] op_sel:[1,0,0]\n"
            "v_pk_fma_f32 v[10:11], v[154:155], v[186:187], v[10:11] op_sel:[1,0,0]\n"
            "v_pk_fma_f32 v[12:13], v[158:159], v[188:189], v[12:13] op_sel:[1,0,0]\n"
            "v_pk_fma_f32 v[14:15], v[158:159], v[190:191], v[14:15] op_sel:[1,0,0]\n"
            "v_pk_mul_f32 v[168:169], v[168:169], v[136:137]\n"
            "v_pk_mul_f32 v[170:171], v[170:171], v[138:139]\n"
            "v_pk_mul_f32 v[172:173], v[172:173], v[140:141]\n"
            "v_pk_mul_f32 v[174:175], v[174:175], v[142:143]\n"
            "s_cmp_eq_u32 %[rem], 4\n"
            "s_cbranch_scc1 Lf03_%=\n"
            "Lr03_%=:\n"
            "v_add_u32 v210, 0x100, v210\n"
            "ds_read_b128 v[196:199], v210\n"
            "v_mfma_f32_16x16x32_bf16 v[136:139], v[192:195], v[104:107], 0\n"
            "v_exp_f32 v144, v128\n"
            "v_exp_f32 v145, v129\n"
            "s_waitcnt vmcnt(13)\n"
            "v_mfma_f32_4x4x4_16b_bf16 v[176:179], v[208:209], v[40:41], 0 cbsz:2\n"
            "v_exp_f32 v146, v130\n"
            "v_mfma_f32_4x4x4_16b_bf16 v[180:183], v[208:209], v[42:43], 0 cbsz:2\n"
            "v_exp_f32 v147, v131\n"
            "v_mfma_f32_4x4x4_16b_bf16 v[184:187], v[208:209], v[44:45], 0 cbsz:2\n"
            "v_exp_f32 v148, v132\n"
            "v_mfma_f32_4x4x4_16b_bf16 v[188:191], v[208:209], v[46:47], 0 cbsz:2\n"
            "v_exp_f32 v149, v133\n"
            "v_exp_f32 v150, v134\n"
            "buffer_load_dwordx4 v[40:43], v219, %[rsx], 0 offen offset:512\n"
            "buffer_load_dwordx4 v[44:47], v219, %[rsx], 0 offen offset:768\n"
            "v_exp_f32 v151, v135\n"
            "v_pk_fma_f32 v[16:17], v[160:161], v[176:177], v[16:17] op_sel_hi:[0,1,1]\n"
            "v_pk_fma_f32 v[18:19], v[160:161], v[178:179], v[18:19] op_sel_hi:[0,1,1]\n"
            "v_pk_fma_f32 v[20:21], v[164:165], v[180:181], v[20:21] op_sel_hi:[0,1,1]\n"
            "v_pk_fma_f32 v[22:23], v[164:165], v[182:183], v[22:23] op_sel_hi:[0,1,1]\n"
            "v_pk_fma_f32 v[24:25], v[168:169], v[184:185], v[24:25] op_sel_hi:[0,1,1]\n"
            "v_pk_fma_f32 v[26:27], v[168:169], v[186:187], v[26:27] op_sel_hi:[0,1,1]\n"
            "v_pk_fma_f32 v[28:29], v[172:173], v[188:189], v[28:29] op_sel_hi:[0,1,1]\n"
            "v_pk_fma_f32 v[30:31], v[172:173], v[190:191], v[30:31] op_sel_hi:[0,1,1]\n"
            "v_pk_add_f32 v[144:145], v[144:145], 1.0 op_sel_hi:[1,0]\n"
            "v_pk_add_f32 v[146:147], v[146:147], 1.0 op_sel_hi:[1,0]\n"
            "v_pk_add_f32 v[148:149], v[148:149], 1.0 op_sel_hi:[1,0]\n"
            "v_pk_add_f32 v[150:151], v[150:151], 1.0 op_sel_hi:[1,0]\n"
            "s_cmp_eq_u32 %[rem], 1\n"
            "s_cbranch_scc1 Lf10_%=\n"
            "Lr10_%=:\n"
            "v_mfma_f32_16x16x32_bf16 v[140:143], v[192:195], v[108:111], 0\n"
            "v_rcp_f32 v144, v144\n"
            "v_rcp_f32 v145, v145\n"
            "v_mfma_f32_4x4x4_16b_bf16 v[176:179], v[208:209], v[56:57], 0 cbsz:2 abid:1\n"
            "v_rcp_f32 v146, v146\n"
            "v_mfma_f32_4x4x4_16b_bf16 v[180:183], v[208:209], v[58:59], 0 cbsz:2 abid:1\n"
            "v_rcp_f32 v147, v147\n"
            "v_mfma_f32_4x4x4_16b_bf16 v[184:187], v[208:209], v[60:61], 0 cbsz:2 abid:1\n"
            "v_rcp_f32 v148, v148\n"
            "v_mfma_f32_4x4x4_16b_bf16 v[188:191], v[208:209], v[62:63], 0 cbsz:2 abid:1\n"
            "v_rcp_f32 v149, v149\n"
            "v_rcp_f32 v150, v150\n"
            "buffer_load_dwordx4 v[56:59], v220, %[rsx], 0 offen offset:512\n"
            "buffer_load_dwordx4 v[60:63], v220, %[rsx], 0 offen offset:768\n"
            "v_rcp_f32 v151, v151\n"
            "v_pk_fma_f32 v[16:17], v[160:161], v[176:177], v[16:17] op_sel:[1,0,0]\n"
            "v_pk_fma_f32 v[18:19], v[160:161], v[178:179], v[18:19] op_sel:[1,0,0]\n"
            "v_pk_fma_f32 v[20:21], v[164:165], v[180:181], v[20:21] op_sel:[1,0,0]\n"
            "v_pk_fma_f32 v[22:23], v[164:165], v[182:183], v[22:23] op_sel:[1,0,0]\n"
            "v_pk_fma_f32 v[24:25], v[168:169], v[184:185], v[24:25] op_sel:[1,0,0]\n"
            "v_pk_fma_f32 v[26:27], v[168:169], v[186:187], v[26:27] op_sel:[1,0,0]\n"
            "v_pk_fma_f32 v[28:29], v[172:173], v[188:189], v[28:29] op_sel:[1,0,0]\n"
            "v_pk_fma_f32 v[30:31], v[172:173], v[190:191], v[30:31] op_sel:[1,0,0]\n"
            "v_pk_mul_f32 v[144:145], v[144:145], v[128:129]\n"
            "v_pk_mul_f32 v[146:147], v[146:147], v[130:131]\n"
            "v_pk_mul_f32 v[148:149], v[148:149], v[132:133]\n"
            "v_pk_mul_f32 v[150:151], v[150:151], v[134:135]\n"
            "s_cmp_eq_u32 %[rem], 2\n"
            "s_cbranch_scc1 Lf11_%=\n"
            "Lr11_%=:\n"
            "v_mfma_f32_16x16x32_bf16 v[128:131], v[192:195], v[112:115], 0\n"
            "v_exp_f32 v152, v136\n"
            "v_exp_f32 v153, v137\n"
            "s_waitcnt vmcnt(13)\n"
            "v_mfma_f32_4x4x4_16b_bf16 v[176:179], v[208:209], v[72:73], 0 cbsz:2 abid:2\n"
            "v_exp_f32 v154, v138\n"
            "v_mfma_f32_4x4x4_16b_bf16 v[180:183], v[208:209], v[74:75], 0 cbsz:2 abid:2\n"
            "v_exp_f32 v155, v139\n"
            "v_mfma_f32_4x4x4_16b_bf16 v[184:187], v[208:209], v[76:77], 0 cbsz:2 abid:2\n"
            "v_exp_f32 v156, v140\n"
            "v_mfma_f32_4x4x4_16b_bf16 v[188:191], v[208:209], v[78:79], 0 cbsz:2 abid:2\n"
            "v_exp_f32 v157, v141\n"
            "v_exp_f32 v158, v142\n"
            "buffer_load_dwordx4 v[72:75], v221, %[rsx], 0 offen offset:512\n"
            "buffer_load_dwordx4 v[76:79], v221, %[rsx], 0 offen offset:768\n"
            "v_exp_f32 v159, v143\n"
            "v_pk_fma_f32 v[16:17], v[162:163], v[176:177], v[16:17] op_sel_hi:[0,1,1]\n"
            "v_pk_fma_f32 v[18:19], v[162:163], v[178:179], v[18:19] op_sel_hi:[0,1,1]\n"
            "v_pk_fma_f32 v[20:21], v[166:167], v[180:181], v[20:21] op_sel_hi:[0,1,1]\n"
            "v_pk_fma_f32 v[22:23], v[166:167], v[182:183], v[22:23] op_sel_hi:[0,1,1]\n"
            "v_pk_fma_f32 v[24:25], v[170:171], v[184:185], v[24:25] op_sel_hi:[0,1,1]\n"
            "v_pk_fma_f32 v[26:27], v[170:171], v[186:187], v[26:27] op_sel_hi:[0,1,1]\n"
            "v_pk_fma_f32 v[28:29], v[174:175], v[188:189], v[28:29] op_sel_hi:[0,1,1]\n"
            "v_pk_fma_f32 v[30:31], v[174:175], v[190:191], v[30:31] op_sel_hi:[0,1,1]\n"
            "v_pk_add_f32 v[152:153], v[152:153], 1.0 op_sel_hi:[1,0]\n"
            "v_pk_add_f32 v[154:155], v[154:155], 1.0 op_sel_hi:[1,0]\n"
            "v_pk_add_f32 v[156:157], v[156:157], 1.0 op_sel_hi:[1,0]\n"
            "v_pk_add_f32 v[158:159], v[158:159], 1.0 op_sel_hi:[1,0]\n"
            "s_cmp_eq_u32 %[rem], 3\n"
            "s_cbranch_scc1 Lf12_%=\n"
            "Lr12_%=:\n"
            "v_mfma_f32_16x16x32_bf16 v[132:135], v[192:195], v[116:119], 0\n"
            "v_rcp_f32 v152, v152\n"
            "v_rcp_f32 v153, v153\n"
            "v_mfma_f32_4x4x4_16b_bf16 v[176:179], v[208:209], v[88:89], 0 cbsz:2 abid:3\n"
            "v_rcp_f32 v154, v154\n"
            "v_mfma_f32_4x4x4_16b_bf16 v[180:183], v[208:209], v[90:91], 0 cbsz:2 abid:3\n"
            "v_rcp_f32 v155, v155\n"
            "v_mfma_f32_4x4x4_16b_bf16 v[184:187], v[208:209], v[92:93], 0 cbsz:2 abid:3\n"
            "v_rcp_f32 v156, v156\n"
            "v_mfma_f32_4x4x4_16b_bf16 v[188:191], v[208:209], v[94:95], 0 cbsz:2 abid:3\n"
            "ds_read_b64 v[208:209], v213\n"
            "v_rcp_f32 v157, v157\n"
            "v_rcp_f32 v158, v158\n"
            "buffer_load_dwordx4 v[88:91], v222, %[rsx], 0 offen offset:512\n"
            "buffer_load_dwordx4 v[92:95], v222, %[rsx], 0 offen offset:768\n"
            "v_rcp_f32 v159, v159\n"
            "v_pk_fma_f32 v[16:17], v[162:163], v[176:177], v[16:17] op_sel:[1,0,0]\n"
            "v_pk_fma_f32 v[18:19], v[162:163], v[178:179], v[18:19] op_sel:[1,0,0]\n"
            "v_pk_fma_f32 v[20:21], v[166:167], v[180:181], v[20:21] op_sel:[1,0,0]\n"
            "v_pk_fma_f32 v[22:23], v[166:167], v[182:183], v[22:23] op_sel:[1,0,0]\n"
            "v_pk_fma_f32 v[24:25], v[170:171], v[184:185], v[24:25] op_sel:[1,0,0]\n"
            "v_pk_fma_f32 v[26:27], v[170:171], v[186:187], v[26:27] op_sel:[1,0,0]\n"
            "v_pk_fma_f32 v[28:29], v[174:175], v[188:189], v[28:29] op_sel:[1,0,0]\n"
            "v_pk_fma_f32 v[30:31], v[174:175], v[190:191], v[30:31] op_sel:[1,0,0]\n"
            "v_pk_mul_f32 v[152:153], v[152:153], v[136:137]\n"
            "v_pk_mul_f32 v[154:155], v[154:155], v[138:139]\n"
            "v_pk_mul_f32 v[156:157], v[156:157], v[140:141]\n"
            "v_pk_mul_f32 v[158:159], v[158:159], v[142:143]\n"
            "s_cmp_eq_u32 %[rem], 4\n"
            "s_cbranch_scc1 Lf13_%=\n"
            "Lr13_%=:\n"
            "s_and_b32 %[tmp], %[lens], 0xff\n"
            "s_add_u32 %[rem], %[rem], %[tmp]\n"
            "s_lshr_b32 %[lens], %[lens], 8\n"
            "v_mov_b32 v231, v236\n"
            "s_and_b32 %[tmp], %[fm], 14\n"
            "s_lshr_b32 %[fm], %[fm], 4\n"
            "s_cmp_lg_u32 %[tmp], 0\n"
            "s_cbranch_scc1 Lb1_%=\n"
            "v_mov_b32 v215, v216\n"
            "s_branch Lnext_%=\n"
            "Lb1_%=:\n"
            "s_cmp_lg_u32 %[tmp], 8\n"
            "s_cbranch_scc1 Lnext_%=\n"
            "s_waitcnt vmcnt(0)\n"
            "v_mov_b32 v176, %[cnt]\n"
            "v_mov_b32 v177, 1\n"
            "s_mov_b64 exec, 1\n"
            "ds_add_rtn_u32 v177, v176, v177\n"
            "s_waitcnt lgkmcnt(0)\n"
            "v_readfirstlane_b32 %[tmp], v177\n"
            "s_mov_b64 exec, 0xff\n"
            "v_mov_b32 v176, %[tag]\n"
            "v_mov_b32 v177, %[tag]\n"
            "v_mov_b32 v178, %[tag]\n"
            "v_mov_b32 v179, %[tag]\n"
            "s_cmp_lg_u32 %[tmp], 7\n"
            "s_cbranch_scc1 Lpub2_%=\n"
            "global_store_dwordx4 v214, v[176:179], %[flg] sc1\n"
            "Lpub2_%=:\n"
            "s_mov_b64 exec, -1\n"
            "s_branch Lnext_%=\n"
            "Lprio_%=:\n"
            "s_cmp_eq_u32 %[nst], 1\n"
            "s_cbranch_scc1 Llast_%=\n"
            "s_lshl_b32 %[tmp], %[nst], 1\n"
            "s_lshr_b32 %[tmp], %[ptab], %[tmp]\n"
            "s_bitcmp1_b32 %[tmp], 1\n"
            "s_cbranch_scc1 Lq2_%=\n"
            "s_bitcmp1_b32 %[tmp], 0\n"
            "s_setprio 0\n"
            "s_cbranch_scc0 Lq9_%=\n"
            "s_setprio 1\n"
            "s_branch Lq9_%=\n"
            "Lq2_%=:\n"
            "s_bitcmp1_b32 %[tmp], 0\n"
            "s_setprio 2\n"
            "s_cbranch_scc0 Lq9_%=\n"
            "s_setprio 3\n"
            "Lq9_%=:\n"
            "s_cmp_lg_u32 %[nst], 2\n"
            "s_cbranch_scc1 Ld2_%=\n"
            "v_mov_b32 v223, 0\n"
            "global_load_dword v223, v223, %[pfl] sc1\n"
            "s_branch Ld2_%=\n"
            "Lf00_%=:\n"
            "s_call_b64 %[ra], Lflush0_%=\n"
            "s_branch Lr00_%=\n"
            "Lf01_%=:\n"
            "s_call_b64 %[ra], Lflush0_%=\n"
            "s_branch Lr01_%=\n"
            "Lf02_%=:\n"
            "s_call_b64 %[ra], Lflush0_%=\n"
            "s_branch Lr02_%=\n"
            "Lf03_%=:\n"
            "s_call_b64 %[ra], Lflush0_%=\n"
            "s_branch Lr03_%=\n"
            "Lflush0_%=:\n"
            "s_bitcmp0_b32 %[fm], 0\n"
            "s_cbranch_scc1 Lfa0_%=\n"
            "ds_read_b128 v[176:179], v231 offset:0\n"
            "ds_read_b128 v[180:183], v231 offset:1024\n"
            "s_waitcnt lgkmcnt(0)\n"
            "v_mfma_f32_4x4x4_16b_bf16 v[0:3], v[224:225], v[176:177], v[0:3]\n"
            "v_mfma_f32_4x4x4_16b_bf16 v[4:7], v[224:225], v[178:179], v[4:7]\n"
            "v_mfma_f32_4x4x4_16b_bf16 v[8:11], v[224:225], v[180:181], v[8:11]\n"
            "v_mfma_f32_4x4x4_16b_bf16 v[12:15], v[224:225], v[182:183], v[12:15]\n"
            "s_nop 1\n"
            "Lfa0_%=:\n"
            "v_cvt_pk_bf16_f32 v176, v0, v1\n"
            "v_cvt_pk_bf16_f32 v177, v2, v3\n"
            "v_cvt_pk_bf16_f32 v178, v4, v5\n"
            "v_cvt_pk_bf16_f32 v179, v6, v7\n"
            "v_cvt_pk_bf16_f32 v180, v8, v9\n"
            "v_cvt_pk_bf16_f32 v181, v10, v11\n"
            "v_cvt_pk_bf16_f32 v182, v12, v13\n"
            "v_cvt_pk_bf16_f32 v183, v14, v15\n"
            "s_bitcmp1_b32 %[fm], 1\n"
            "s_cbranch_scc1 Lpark0_%=\n"
            "buffer_store_dwordx4 v[176:179], v215, %[rsw], 0 offen offset:0 sc1\n"
            "buffer_store_dwordx4 v[180:183], v215, %[rsw], 0 offen offset:256 sc1\n"
            "s_branch Lzero0_%=\n"
            "Lpark0_%=:\n"
            "ds_write_b128 v231, v[176:179] offset:0\n"
            "ds_write_b128 v231, v[180:183] offset:1024\n"
            "Lzero0_%=:\n"
            "v_mov_b64 v[0:1], 0\n"
            "v_mov_b64 v[2:3], 0\n"
            "v_mov_b64 v[4:5], 0\n"
            "v_mov_b64 v[6:7], 0\n"
            "v_mov_b64 v[8:9], 0\n"
            "v_mov_b64 v[10:11], 0\n"
            "v_mov_b64 v[12:13], 0\n"
            "v_mov_b64 v[14:15], 0\n"
            "s_setpc_b64 %[ra]\n"
            "Lf10_%=:\n"
            "s_call_b64 %[ra], Lflush1_%=\n"
            "s_branch Lr10_%=\n"
            "Lf11_%=:\n"
            "s_call_b64 %[ra], Lflush1_%=\n"
            "s_branch Lr11_%=\n"
            "Lf12_%=:\n"
            "s_call_b64 %[ra], Lflush1_%=\n"
            "s_branch Lr12_%=\n"
            "Lf13_%=:\n"
            "s_call_b64 %[ra], Lflush1_%=\n"
            "s_branch Lr13_%=\n"
            "Lflush1_%=:\n"
            "s_bitcmp0_b32 %[fm], 0\n"
            "s_cbranch_scc1 Lfa1_%=\n"
            "ds_read_b128 v[176:179], v231 offset:2048\n"
            "ds_read_b128 v[180:183], v231 offset:3072\n"
            "s_waitcnt lgkmcnt(0)\n"
            "v_mfma_f32_4x4x4_16b_bf16 v[16:19], v[224:225], v[176:177], v[16:19]\n"
            "v_mfma_f32_4x4x4_16b_bf16 v[20:23], v[224:225], v[178:179], v[20:23]\n"
            "v_mfma_f32_4x4x4_16b_bf16 v[24:27], v[224:225], v[180:181], v[24:27]\n"
            "v_mfma_f32_4x4x4_16b_bf16 v[28:31], v[224:225], v[182:183], v[28:31]\n"
            "s_nop 1\n"
            "Lfa1_%=:\n"
            "v_cvt_pk_bf16_f32 v176, v16, v17\n"
            "v_cvt_pk_bf16_f32 v177, v18, v19\n"
            "v_cvt_pk_bf16_f32 v178, v20, v21\n"
            "v_cvt_pk_bf16_f32 v179, v22, v23\n"
            "v_cvt_pk_bf16_f32 v180, v24, v25\n"
            "v_cvt_pk_bf16_f32 v181, v26, v27\n"
            "v_cvt_pk_bf16_f32 v182, v28, v29\n"
            "v_cvt_pk_bf16_f32 v183, v30, v31\n"
            "s_bitcmp1_b32 %[fm], 1\n"
            "s_cbranch_scc1 Lpark1_%=\n"
            "buffer_store_dwordx4 v[176:179], v215, %[rsw], 0 offen offset:512 sc1\n"
            "buffer_store_dwordx4 v[180:183], v215, %[rsw], 0 offen offset:768 sc1\n"
            "s_branch Lzero1_%=\n"
            "Lpark1_%=:\n"
            "ds_write_b128 v231, v[176:179] offset:2048\n"
            "ds_write_b128 v231, v[180:183] offset:3072\n"
            "Lzero1_%=:\n"
            "v_mov_b64 v[16:17], 0\n"
            "v_mov_b64 v[18:19], 0\n"
            "v_mov_b64 v[20:21], 0\n"
            "v_mov_b64 v[22:23], 0\n"
            "v_mov_b64 v[24:25], 0\n"
            "v_mov_b64 v[26:27], 0\n"
            "v_mov_b64 v[28:29], 0\n"
            "v_mov_b64 v[30:31], 0\n"
            "s_setpc_b64 %[ra]\n"
            "Lspin_%=:\n"
            "s_mov_b32 %[lens], %[spin]\n"
            "Lsp1_%=:\n"
            "v_readfirstlane_b32 %[nst], v223\n"
            "s_sub_u32 %[lens], %[lens], 1\n"
            "s_cmp_eq_u32 %[lens], 0\n"
            "s_cbranch_scc1 Lsp2_%=\n"
            "s_cmp_eq_u32 %[nst], %[tag]\n"
            "s_cbranch_scc1 Lsp2_%=\n"
            "s_sleep 1\n"
            "v_mov_b32 v223, 0\n"
            "global_load_dword v223, v223, %[pfl] sc1\n"
            "s_waitcnt vmcnt(0)\n"
            "s_branch Lsp1_%=\n"
            "Lsp2_%=:\n"
            "buffer_load_dwordx4 v[32:35], v217, %[rsw], %[oagg] offen offset:0 sc1\n"
            "buffer_load_dwordx4 v[36:39], v217, %[rsw], %[oagg] offen offset:256 sc1\n"
            "buffer_load_dwordx4 v[48:51], v217, %[rsw], %[oagg] offen offset:512 sc1\n"
            "buffer_load_dwordx4 v[52:55], v217, %[rsw], %[oagg] offen offset:768 sc1\n"
            "s_branch Lgo_%=\n"
            "Llast_%=:\n"
            "s_waitcnt vmcnt(16)\n"
            "v_readfirstlane_b32 %[nst], v223\n"
            "v_mov_b32 v222, v217\n"
            "s_cmp_eq_u32 %[nst], %[tag]\n"
            "s_cbranch_scc1 Lp4_%=\n"
            "v_bfrev_b32 v222, 1\n"
            "v_mov_b32 v223, 0\n"
            "global_load_dword v223, v223, %[pfl] sc1\n"
            "Lp4_%=:\n"
            "v_mfma_f32_16x16x32_bf16 v[136:139], v[192:195], v[120:123], 0\n"
            "v_exp_f32 v160, v128\n"
            "v_exp_f32 v161, v129\n"
            "s_waitcnt vmcnt(13) lgkmcnt(0)\n"
            "v_mfma_f32_4x4x4_16b_bf16 v[176:179], v[208:209], v[32:33], 0 cbsz:2\n"
            "v_exp_f32 v162, v130\n"
            "v_mfma_f32_4x4x4_16b_bf16 v[180:183], v[208:209], v[34:35], 0 cbsz:2\n"
            "v_exp_f32 v163, v131\n"
            "v_mfma_f32_4x4x4_16b_bf16 v[184:187], v[208:209], v[36:37], 0 cbsz:2\n"
            "v_exp_f32 v164, v132\n"
            "v_mfma_f32_4x4x4_16b_bf16 v[188:191], v[208:209], v[38:39], 0 cbsz:2\n"
            "v_exp_f32 v165, v133\n"
            "v_exp_f32 v166, v134\n"
            "buffer_load_dwordx4 v[32:35], v222, %[rsw], %[oagg] offen offset:0 sc1\n"
            "buffer_load_dwordx4 v[36:39], v222, %[rsw], %[oagg] offen offset:256 sc1\n"
            "ds_read_b128 v[236:239], v228 offset:0\n"
            "ds_read_b128 v[240:243], v228 offset:1024\n"
            "ds_read_b128 v[244:247], v228 offset:2048\n"
            "ds_read_b128 v[248:251], v228 offset:3072\n"
            "ds_read_b128 v[96:99], v228 offset:4096\n"
            "v_exp_f32 v167, v135\n"
            "v_pk_fma_f32 v[0:1], v[144:145], v[176:177], v[0:1] op_sel_hi:[0,1,1]\n"
            "v_pk_fma_f32 v[2:3], v[144:145], v[178:179], v[2:3] op_sel_hi:[0,1,1]\n"
            "v_pk_fma_f32 v[4:5], v[148:149], v[180:181], v[4:5] op_sel_hi:[0,1,1]\n"
            "v_pk_fma_f32 v[6:7], v[148:149], v[182:183], v[6:7] op_sel_hi:[0,1,1]\n"
            "v_pk_fma_f32 v[8:9], v[152:153], v[184:185], v[8:9] op_sel_hi:[0,1,1]\n"
            "v_pk_fma_f32 v[10:11], v[152:153], v[186:187], v[10:11] op_sel_hi:[0,1,1]\n"
            "v_pk_fma_f32 v[12:13], v[156:157], v[188:189], v[12:13] op_sel_hi:[0,1,1]\n"
            "v_pk_fma_f32 v[14:15], v[156:157], v[190:191], v[14:15] op_sel_hi:[0,1,1]\n"
            "v_pk_add_f32 v[160:161], v[160:161], 1.0 op_sel_hi:[1,0]\n"
            "v_pk_add_f32 v[162:163], v[162:163], 1.0 op_sel_hi:[1,0]\n"
            "v_pk_add_f32 v[164:165], v[164:165], 1.0 op_sel_hi:[1,0]\n"
            "v_pk_add_f32 v[166:167], v[166:167], 1.0 op_sel_hi:[1,0]\n"
            "v_mfma_f32_16x16x32_bf16 v[140:143], v[192:195], v[124:127], 0\n"
            "v_rcp_f32 v160, v160\n"
            "v_rcp_f32 v161, v161\n"
            "v_mfma_f32_4x4x4_16b_bf16 v[176:179], v[208:209], v[48:49], 0 cbsz:2 abid:1\n"
            "v_rcp_f32 v162, v162\n"
            "v_mfma_f32_4x4x4_16b_bf16 v[180:183], v[208:209], v[50:51], 0 cbsz:2 abid:1\n"
            "v_rcp_f32 v163, v163\n"
            "v_mfma_f32_4x4x4_16b_bf16 v[184:187], v[208:209], v[52:53], 0 cbsz:2 abid:1\n"
            "v_rcp_f32 v164, v164\n"
            "v_mfma_f32_4x4x4_16b_bf16 v[188:191], v[208:209], v[54:55], 0 cbsz:2 abid:1\n"
            "v_rcp_f32 v165, v165\n"
            "v_rcp_f32 v166, v166\n"
            "buffer_load_dwordx4 v[48:51], v222, %[rsw], %[oagg] offen offset:512 sc1\n"
            "buffer_load_dwordx4 v[52:55], v222, %[rsw], %[oagg] offen offset:768 sc1\n"
            "ds_read_b128 v[100:103], v228 offset:5120\n"
            "ds_read_b128 v[104:107], v228 offset:6144\n"
            "ds_read_b128 v[108:111], v228 offset:7168\n"
            "ds_read_b128 v[112:115], v228 offset:8192\n"
            "ds_read_b128 v[116:119], v228 offset:9216\n"
            "ds_read_b128 v[196:199], v228 offset:10240\n"
            "v_rcp_f32 v167, v167\n"
            "v_pk_fma_f32 v[0:1], v[144:145], v[176:177], v[0:1] op_sel:[1,0,0]\n"
            "v_pk_fma_f32 v[2:3], v[144:145], v[178:179], v[2:3] op_sel:[1,0,0]\n"
            "v_pk_fma_f32 v[4:5], v[148:149], v[180:181], v[4:5] op_sel:[1,0,0]\n"
            "v_pk_fma_f32 v[6:7], v[148:149], v[182:183], v[6:7] op_sel:[1,0,0]\n"
            "v_pk_fma_f32 v[8:9], v[152:153], v[184:185], v[8:9] op_sel:[1,0,0]\n"
            "v_pk_fma_f32 v[10:11], v[152:153], v[186:187], v[10:11] op_sel:[1,0,0]\n"
            "v_pk_fma_f32 v[12:13], v[156:157], v[188:189], v[12:13] op_sel:[1,0,0]\n"
            "v_pk_fma_f32 v[14:15], v[156:157], v[190:191], v[14:15] op_sel:[1,0,0]\n"
            "v_pk_mul_f32 v[160:161], v[160:161], v[128:129]\n"
            "v_pk_mul_f32 v[162:163], v[162:163], v[130:131]\n"
            "v_pk_mul_f32 v[164:165], v[164:165], v[132:133]\n"
            "v_pk_mul_f32 v[166:167], v[166:167], v[134:135]\n"
            "v_exp_f32 v168, v136\n"
            "v_exp_f32 v169, v137\n"
            "s_waitcnt vmcnt(12)\n"
            "v_mfma_f32_4x4x4_16b_bf16 v[176:179], v[208:209], v[64:65], 0 cbsz:2 abid:2\n"
            "v_exp_f32 v170, v138\n"
            "v_mfma_f32_4x4x4_16b_bf16 v[180:183], v[208:209], v[66:67], 0 cbsz:2 abid:2\n"
            "v_exp_f32 v171, v139\n"
            "v_mfma_f32_4x4x4_16b_bf16 v[184:187], v[208:209], v[68:69], 0 cbsz:2 abid:2\n"
            "v_exp_f32 v172, v140\n"
            "v_mfma_f32_4x4x4_16b_bf16 v[188:191], v[208:209], v[70:71], 0 cbsz:2 abid:2\n"
            "v_exp_f32 v173, v141\n"
            "v_exp_f32 v174, v142\n"
            "ds_read_b128 v[64:67], v231 offset:4096\n"
            "ds_read_b128 v[68:71], v231 offset:5120\n"
            "ds_read_b128 v[200:203], v228 offset:11264\n"
            "ds_read_b128 v[204:207], v228 offset:12288\n"
            "ds_read_b128 v[232:235], v228 offset:13312\n"
            "v_exp_f32 v175, v143\n"
            "v_pk_fma_f32 v[0:1], v[146:147], v[176:177], v[0:1] op_sel_hi:[0,1,1]\n"
            "v_pk_fma_f32 v[2:3], v[146:147], v[178:179], v[2:3] op_sel_hi:[0,1,1]\n"
            "v_pk_fma_f32 v[4:5], v[150:151], v[180:181], v[4:5] op_sel_hi:[0,1,1]\n"
            "v_pk_fma_f32 v[6:7], v[150:151], v[182:183], v[6:7] op_sel_hi:[0,1,1]\n"
            "v_pk_fma_f32 v[8:9], v[154:155], v[184:185], v[8:9] op_sel_hi:[0,1,1]\n"
            "v_pk_fma_f32 v[10:11], v[154:155], v[186:187], v[10:11] op_sel_hi:[0,1,1]\n"
            "v_pk_fma_f32 v[12:13], v[158:159], v[188:189], v[12:13] op_sel_hi:[0,1,1]\n"
            "v_pk_fma_f32 v[14:15], v[158:159], v[190:191], v[14:15] op_sel_hi:[0,1,1]\n"
            "v_pk_add_f32 v[168:169], v[168:169], 1.0 op_sel_hi:[1,0]\n"
            "v_pk_add_f32 v[170:171], v[170:171], 1.0 op_sel_hi:[1,0]\n"
            "v_pk_add_f32 v[172:173], v[172:173], 1.0 op_sel_hi:[1,0]\n"
            "v_pk_add_f32 v[174:175], v[174:175], 1.0 op_sel_hi:[1,0]\n"
            "v_rcp_f32 v168, v168\n"
            "v_rcp_f32 v169, v169\n"
            "v_mfma_f32_4x4x4_16b_bf16 v[176:179], v[208:209], v[80:81], 0 cbsz:2 abid:3\n"
            "v_rcp_f32 v170, v170\n"
            "v_mfma_f32_4x4x4_16b_bf16 v[180:183], v[208:209], v[82:83], 0 cbsz:2 abid:3\n"
            "v_rcp_f32 v171, v171\n"
            "v_mfma_f32_4x4x4_16b_bf16 v[184:187], v[208:209], v[84:85], 0 cbsz:2 abid:3\n"
            "v_rcp_f32 v172, v172\n"
            "v_mfma_f32_4x4x4_16b_bf16 v[188:191], v[208:209], v[86:87], 0 cbsz:2 abid:3\n"
            "v_rcp_f32 v173, v173\n"
            "v_rcp_f32 v174, v174\n"
            "ds_read_b128 v[80:83], v231 offset:6144\n"
            "ds_read_b128 v[84:87], v231 offset:7168\n"
            "ds_read_b128 v[120:123], v228 offset:14336\n"
            "ds_read_b128 v[124:127], v228 offset:15360\n"
            "ds_read_b128 v[192:195], v228 offset:16384\n"
            "ds_read_b128 v[128:131], v228 offset:17408\n"
            "ds_read_b128 v[132:135], v228 offset:18432\n"
            "v_rcp_f32 v175, v175\n"
            "v_pk_fma_f32 v[0:1], v[146:147], v[176:177], v[0:1] op_sel:[1,0,0]\n"
            "v_pk_fma_f32 v[2:3], v[146:147], v[178:179], v[2:3] op_sel:[1,0,0]\n"
            "v_pk_fma_f32 v[4:5], v[150:151], v[180:181], v[4:5] op_sel:[1,0,0]\n"
            "v_pk_fma_f32 v[6:7], v[150:151], v[182:183], v[6:7] op_sel:[1,0,0]\n"
            "v_pk_fma_f32 v[8:9], v[154:155], v[184:185], v[8:9] op_sel:[1,0,0]\n"
            "v_pk_fma_f32 v[10:11], v[154:155], v[186:187], v[10:11] op_sel:[1,0,0]\n"
            "v_pk_fma_f32 v[12:13], v[158:159], v[188:189], v[12:13] op_sel:[1,0,0]\n"
            "v_pk_fma_f32 v[14:15], v[158:159], v[190:191], v[14:15] op_sel:[1,0,0]\n"
            "v_pk_mul_f32 v[168:169], v[168:169], v[136:137]\n"
            "v_pk_mul_f32 v[170:171], v[170:171], v[138:139]\n"
            "v_pk_mul_f32 v[172:173], v[172:173], v[140:141]\n"
            "v_pk_mul_f32 v[174:175], v[174:175], v[142:143]\n"
            "s_waitcnt vmcnt(8)\n"
            "v_mfma_f32_4x4x4_16b_bf16 v[176:179], v[208:209], v[40:41], 0 cbsz:2\n"
            "v_mfma_f32_4x4x4_16b_bf16 v[180:183], v[208:209], v[42:43], 0 cbsz:2\n"
            "v_mfma_f32_4x4x4_16b_bf16 v[184:187], v[208:209], v[44:45], 0 cbsz:2\n"
            "v_mfma_f32_4x4x4_16b_bf16 v[188:191], v[208:209], v[46:47], 0 cbsz:2\n"
            "ds_read_b128 v[136:139], v228 offset:19456\n"
            "ds_read_b128 v[140:143], v228 offset:20480\n"
            "ds_read_b128 v[144:147], v228 offset:21504\n"
            "ds_read_b128 v[148:151], v228 offset:22528\n"
            "ds_read_b128 v[152:155], v228 offset:23552\n"
            "ds_read_b128 v[156:159], v228 offset:24576\n"
            "s_nop 1\n"
            "v_pk_fma_f32 v[16:17], v[160:161], v[176:177], v[16:17] op_sel_hi:[0,1,1]\n"
            "v_pk_fma_f32 v[18:19], v[160:161], v[178:179], v[18:19] op_sel_hi:[0,1,1]\n"
            "v_pk_fma_f32 v[20:21], v[164:165], v[180:181], v[20:21] op_sel_hi:[0,1,1]\n"
            "v_pk_fma_f32 v[22:23], v[164:165], v[182:183], v[22:23] op_sel_hi:[0,1,1]\n"
            "v_pk_fma_f32 v[24:25], v[168:169], v[184:185], v[24:25] op_sel_hi:[0,1,1]\n"
            "v_pk_fma_f32 v[26:27], v[168:169], v[186:187], v[26:27] op_sel_hi:[0,1,1]\n"
            "v_pk_fma_f32 v[28:29], v[172:173], v[188:189], v[28:29] op_sel_hi:[0,1,1]\n"
            "v_pk_fma_f32 v[30:31], v[172:173], v[190:191], v[30:31] op_sel_hi:[0,1,1]\n"
            "s_cmp_le_u32 %[njl], 1\n"
            "s_cbranch_scc1 Ltail_%=\n"
            "v_mfma_f32_4x4x4_16b_bf16 v[176:179], v[208:209], v[56:57], 0 cbsz:2 abid:1\n"
            "v_mfma_f32_4x4x4_16b_bf16 v[180:183], v[208:209], v[58:59], 0 cbsz:2 abid:1\n"
            "v_mfma_f32_4x4x4_16b_bf16 v[184:187], v[208:209], v[60:61], 0 cbsz:2 abid:1\n"
            "v_mfma_f32_4x4x4_16b_bf16 v[188:191], v[208:209], v[62:63], 0 cbsz:2 abid:1\n"
            "s_nop 1\n"
            "v_pk_fma_f32 v[16:17], v[160:161], v[176:177], v[16:17] op_sel:[1,0,0]\n"
            "v_pk_fma_f32 v[18:19], v[160:161], v[178:179], v[18:19] op_sel:[1,0,0]\n"
            "v_pk_fma_f32 v[20:21], v[164:165], v[180:181], v[20:21] op_sel:[1,0,0]\n"
            "v_pk_fma_f32 v[22:23], v[164:165], v[182:183], v[22:23] op_sel:[1,0,0]\n"
            "v_pk_fma_f32 v[24:25], v[168:169], v[184:185], v[24:25] op_sel:[1,0,0]\n"
            "v_pk_fma_f32 v[26:27], v[168:169], v[186:187], v[26:27] op_sel:[1,0,0]\n"
            "v_pk_fma_f32 v[28:29], v[172:173], v[188:189], v[28:29] op_sel:[1,0,0]\n"
            "v_pk_fma_f32 v[30:31], v[172:173], v[190:191], v[30:31] op_sel:[1,0,0]\n"
            "s_cmp_le_u32 %[njl], 2\n"
            "s_cbranch_scc1 Ltail_%=\n"
            "s_waitcnt vmcnt(4)\n"
            "v_mfma_f32_4x4x4_16b_bf16 v[176:179], v[208:209], v[72:73], 0 cbsz:2 abid:2\n"
            "v_mfma_f32_4x4x4_16b_bf16 v[180:183], v[208:209], v[74:75], 0 cbsz:2 abid:2\n"
            "v_mfma_f32_4x4x4_16b_bf16 v[184:187], v[208:209], v[76:77], 0 cbsz:2 abid:2\n"
            "v_mfma_f32_4x4x4_16b_bf16 v[188:191], v[208:209], v[78:79], 0 cbsz:2 abid:2\n"
            "s_nop 1\n"
            "v_pk_fma_f32 v[16:17], v[162:163], v[176:177], v[16:17] op_sel_hi:[0,1,1]\n"
            "v_pk_fma_f32 v[18:19], v[162:163], v[178:179], v[18:19] op_sel_hi:[0,1,1]\n"
            "v_pk_fma_f32 v[20:21], v[166:167], v[180:181], v[20:21] op_sel_hi:[0,1,1]\n"
            "v_pk_fma_f32 v[22:23], v[166:167], v[182:183], v[22:23] op_sel_hi:[0,1,1]\n"
            "v_pk_fma_f32 v[24:25], v[170:171], v[184:185], v[24:25] op_sel_hi:[0,1,1]\n"
            "v_pk_fma_f32 v[26:27], v[170:171], v[186:187], v[26:27] op_sel_hi:[0,1,1]\n"
            "v_pk_fma_f32 v[28:29], v[174:175], v[188:189], v[28:29] op_sel_hi:[0,1,1]\n"
            "v_pk_fma_f32 v[30:31], v[174:175], v[190:191], v[30:31] op_sel_hi:[0,1,1]\n"
            "s_cmp_le_u32 %[njl], 3\n"
            "s_cbranch_scc1 Ltail_%=\n"
            "v_mfma_f32_4x4x4_16b_bf16 v[176:179], v[208:209], v[88:89], 0 cbsz:2 abid:3\n"
            "v_mfma_f32_4x4x4_16b_bf16 v[180:183], v[208:209], v[90:91], 0 cbsz:2 abid:3\n"
            "v_mfma_f32_4x4x4_16b_bf16 v[184:187], v[208:209], v[92:93], 0 cbsz:2 abid:3\n"
            "v_mfma_f32_4x4x4_16b_bf16 v[188:191], v[208:209], v[94:95], 0 cbsz:2 abid:3\n"
            "s_nop 1\n"
            "v_pk_fma_f32 v[16:17], v[162:163], v[176:177], v[16:17] op_sel:[1,0,0]\n"
            "v_pk_fma_f32 v[18:19], v[162:163], v[178:179], v[18:19] op_sel:[1,0,0]\n"
            "v_pk_fma_f32 v[20:21], v[166:167], v[180:181], v[20:21] op_sel:[1,0,0]\n"
            "v_pk_fma_f32 v[22:23], v[166:167], v[182:183], v[22:23] op_sel:[1,0,0]\n"
            "v_pk_fma_f32 v[24:25], v[170:171], v[184:185], v[24:25] op_sel:[1,0,0]\n"
            "v_pk_fma_f32 v[26:27], v[170:171], v[186:187], v[26:27] op_sel:[1,0,0]\n"
            "v_pk_fma_f32 v[28:29], v[174:175], v[188:189], v[28:29] op_sel:[1,0,0]\n"
            "v_pk_fma_f32 v[30:31], v[174:175], v[190:191], v[30:31] op_sel:[1,0,0]\n"
            "Ltail_%=:\n"
            "s_setprio 3\n"
            "s_waitcnt vmcnt(4) lgkmcnt(11)\n"
            "ds_read_b128 v[72:75], v228 offset:25600\n"
            "ds_read_b128 v[76:79], v228 offset:26624\n"
            "ds_read_b128 v[88:91], v228 offset:27648\n"
            "ds_read_b128 v[92:95], v228 offset:28672\n"
            "ds_read_b128 v[160:163], v228 offset:29696\n"
            "ds_read_b128 v[164:167], v228 offset:30720\n"
            "ds_read_b128 v[168:171], v228 offset:31744\n"
            "v_mfma_f32_4x4x4_16b_bf16 v[0:3], v[224:225], v[64:65], v[0:3]\n"
            "v_mfma_f32_4x4x4_16b_bf16 v[4:7], v[224:225], v[66:67], v[4:7]\n"
            "v_mfma_f32_4x4x4_16b_bf16 v[8:11], v[224:225], v[68:69], v[8:11]\n"
            "v_mfma_f32_4x4x4_16b_bf16 v[12:15], v[224:225], v[70:71], v[12:15]\n"
            "v_mfma_f32_4x4x4_16b_bf16 v[16:19], v[224:225], v[80:81], v[16:19]\n"
            "v_mfma_f32_4x4x4_16b_bf16 v[20:23], v[224:225], v[82:83], v[20:23]\n"
            "v_mfma_f32_4x4x4_16b_bf16 v[24:27], v[224:225], v[84:85], v[24:27]\n"
            "v_mfma_f32_4x4x4_16b_bf16 v[28:31], v[224:225], v[86:87], v[28:31]\n"
            "s_cmp_lg_u32 %[nst], %[tag]\n"
            "s_cbranch_scc1 Lspin_%=\n"
            "Lgo_%=:\n"
            "buffer_load_dwordx4 v[40:43], v218, %[rsw], %[oagg] offen offset:0 sc1\n"
            "buffer_load_dwordx4 v[44:47], v218, %[rsw], %[oagg] offen offset:256 sc1\n"
            "buffer_load_dwordx4 v[56:59], v218, %[rsw], %[oagg] offen offset:512 sc1\n"
            "buffer_load_dwordx4 v[60:63], v218, %[rsw], %[oagg] offen offset:768 sc1\n"
            "s_waitcnt vmcnt(4)\n"
            "v_mfma_f32_4x4x4_16b_bf16 v[0:3], v[224:225], v[32:33], v[0:3]\n"
            "v_mfma_f32_4x4x4_16b_bf16 v[4:7], v[224:225], v[34:35], v[4:7]\n"
            "v_mfma_f32_4x4x4_16b_bf16 v[8:11], v[224:225], v[36:37], v[8:11]\n"
            "v_mfma_f32_4x4x4_16b_bf16 v[12:15], v[224:225], v[38:39], v[12:15]\n"
            "v_mfma_f32_4x4x4_16b_bf16 v[16:19], v[224:225], v[48:49], v[16:19]\n"
            "v_mfma_f32_4x4x4_16b_bf16 v[20:23], v[224:225], v[50:51], v[20:23]\n"
            "v_mfma_f32_4x4x4_16b_bf16 v[24:27], v[224:225], v[52:53], v[24:27]\n"
            "v_mfma_f32_4x4x4_16b_bf16 v[28:31], v[224:225], v[54:55], v[28:31]\n"
            "s_nop 1\n"
            "v_cvt_pk_bf16_f32 v176, v0, v4\n"
            "v_cvt_pk_bf16_f32 v177, v8, v12\n"
            "v_cvt_pk_bf16_f32 v178, v16, v20\n"
            "v_cvt_pk_bf16_f32 v179, v24, v28\n"
            "ds_write_b128 v226, v[176:179] offset:0\n"
            "v_cvt_pk_bf16_f32 v180, v1, v5\n"
            "v_cvt_pk_bf16_f32 v181, v9, v13\n"
            "v_cvt_pk_bf16_f32 v182, v17, v21\n"
            "v_cvt_pk_bf16_f32 v183, v25, v29\n"
            "ds_write_b128 v226, v[180:183] offset:272\n"
            "v_cvt_pk_bf16_f32 v184, v2, v6\n"
            "v_cvt_pk_bf16_f32 v185, v10, v14\n"
            "v_cvt_pk_bf16_f32 v186, v18, v22\n"
            "v_cvt_pk_bf16_f32 v187, v26, v30\n"
            "ds_write_b128 v226, v[184:187] offset:544\n"
            "v_cvt_pk_bf16_f32 v188, v3, v7\n"
            "v_cvt_pk_bf16_f32 v189, v11, v15\n"
            "v_cvt_pk_bf16_f32 v190, v19, v23\n"
            "v_cvt_pk_bf16_f32 v191, v27, v31\n"
            "ds_write_b128 v226, v[188:191] offset:816\n"
            "ds_read_b128 v[176:179], v227 offset:0\n"
            "ds_read_b128 v[180:183], v227 offset:64\n"
            "ds_read_b128 v[184:187], v227 offset:128\n"
            "ds_read_b128 v[188:191], v227 offset:192\n"
            "s_waitcnt lgkmcnt(3)\n"
            "v_mfma_f32_16x16x32_bf16 v[0:3], v[176:179], v[236:239], 0\n"
            "v_mfma_f32_16x16x32_bf16 v[4:7], v[176:179], v[96:99], 0\n"
            "s_waitcnt lgkmcnt(2)\n"
            "v_mfma_f32_16x16x32_bf16 v[0:3], v[180:183], v[240:243], v[0:3]\n"
            "v_mfma_f32_16x16x32_bf16 v[4:7], v[180:183], v[100:103], v[4:7]\n"
            "s_waitcnt lgkmcnt(1)\n"
            "v_mfma_f32_16x16x32_bf16 v[0:3], v[184:187], v[244:247], v[0:3]\n"
            "v_mfma_f32_16x16x32_bf16 v[4:7], v[184:187], v[104:107], v[4:7]\n"
            "s_waitcnt lgkmcnt(0)\n"
            "v_mfma_f32_16x16x32_bf16 v[0:3], v[188:191], v[248:251], v[0:3]\n"
            "v_mfma_f32_16x16x32_bf16 v[4:7], v[188:191], v[108:111], v[4:7]\n"
            "v_mfma_f32_16x16x32_bf16 v[8:11], v[176:179], v[112:115], 0\n"
            "v_mfma_f32_16x16x32_bf16 v[12:15], v[176:179], v[204:207], 0\n"
            "v_mfma_f32_16x16x32_bf16 v[8:11], v[180:183], v[116:119], v[8:11]\n"
            "v_mfma_f32_16x16x32_bf16 v[12:15], v[180:183], v[232:235], v[12:15]\n"
            "v_mfma_f32_16x16x32_bf16 v[8:11], v[184:187], v[196:199], v[8:11]\n"
            "v_mfma_f32_16x16x32_bf16 v[12:15], v[184:187], v[120:123], v[12:15]\n"
            "v_mfma_f32_16x16x32_bf16 v[8:11], v[188:191], v[200:203], v[8:11]\n"
            "v_mfma_f32_16x16x32_bf16 v[12:15], v[188:191], v[124:127], v[12:15]\n"
            "v_cvt_pk_bf16_f32 v0, v0, v1\n"
            "v_cvt_pk_bf16_f32 v1, v2, v3\n"
            "v_cvt_pk_bf16_f32 v2, v4, v5\n"
            "v_cvt_pk_bf16_f32 v3, v6, v7\n"
            "buffer_store_dwordx4 v[0:3], v217, %[rso], 0 offen offset:0 sc1\n"
            "v_mfma_f32_16x16x32_bf16 v[16:19], v[176:179], v[192:195], 0\n"
            "v_mfma_f32_16x16x32_bf16 v[20:23], v[176:179], v[140:143], 0\n"
            "v_mfma_f32_16x16x32_bf16 v[16:19], v[180:183], v[128:131], v[16:19]\n"
            "v_mfma_f32_16x16x32_bf16 v[20:23], v[180:183], v[144:147], v[20:23]\n"
            "v_mfma_f32_16x16x32_bf16 v[16:19], v[184:187], v[132:135], v[16:19]\n"
            "v_mfma_f32_16x16x32_bf16 v[20:23], v[184:187], v[148:151], v[20:23]\n"
            "v_mfma_f32_16x16x32_bf16 v[16:19], v[188:191], v[136:139], v[16:19]\n"
            "v_mfma_f32_16x16x32_bf16 v[20:23], v[188:191], v[152:155], v[20:23]\n"
            "v_cvt_pk_bf16_f32 v8, v8, v9\n"
            "v_cvt_pk_bf16_f32 v9, v10, v11\n"
            "v_cvt_pk_bf16_f32 v10, v12, v13\n"
            "v_cvt_pk_bf16_f32 v11, v14, v15\n"
            "buffer_store_dwordx4 v[8:11], v217, %[rso], 0 offen offset:256 sc1\n"
            "v_mfma_f32_16x16x32_bf16 v[24:27], v[176:179], v[156:159], 0\n"
            "v_mfma_f32_16x16x32_bf16 v[28:31], v[176:179], v[92:95], 0\n"
            "v_mfma_f32_16x16x32_bf16 v[24:27], v[180:183], v[72:75], v[24:27]\n"
            "v_mfma_f32_16x16x32_bf16 v[28:31], v[180:183], v[160:163], v[28:31]\n"
            "v_mfma_f32_16x16x32_bf16 v[24:27], v[184:187], v[76:79], v[24:27]\n"
            "v_mfma_f32_16x16x32_bf16 v[28:31], v[184:187], v[164:167], v[28:31]\n"
            "v_mfma_f32_16x16x32_bf16 v[24:27], v[188:191], v[88:91], v[24:27]\n"
            "v_mfma_f32_16x16x32_bf16 v[28:31], v[188:191], v[168:171], v[28:31]\n"
            "v_cvt_pk_bf16_f32 v16, v16, v17\n"
            "v_cvt_pk_bf16_f32 v17, v18, v19\n"
            "v_cvt_pk_bf16_f32 v18, v20, v21\n"
            "v_cvt_pk_bf16_f32 v19, v22, v23\n"
            "buffer_store_dwordx4 v[16:19], v217, %[rso], 0 offen offset:512 sc1\n"
            "s_nop 2\n"
            "v_cvt_pk_bf16_f32 v24, v24, v25\n"
            "v_cvt_pk_bf16_f32 v25, v26, v27\n"
            "v_cvt_pk_bf16_f32 v26, v28, v29\n"
            "v_cvt_pk_bf16_f32 v27, v30, v31\n"
            "buffer_store_dwordx4 v[24:27], v217, %[rso], 0 offen offset:768 sc1\n"
            "s_cmp_lt_u32 %[two], 6\n"
            "s_cbranch_scc1 Lend_%=\n"
            "ds_read_b128 v[64:67], v231 offset:0\n"
            "ds_read_b128 v[68:71], v231 offset:1024\n"
            "ds_read_b128 v[80:83], v231 offset:2048\n"
            "ds_read_b128 v[84:87], v231 offset:3072\n"
            "s_waitcnt vmcnt(4) lgkmcnt(0)\n"
            "v_mfma_f32_4x4x4_16b_bf16 v[0:3], v[224:225], v[64:65], 0\n"
            "v_mfma_f32_4x4x4_16b_bf16 v[4:7], v[224:225], v[66:67], 0\n"
            "v_mfma_f32_4x4x4_16b_bf16 v[8:11], v[224:225], v[68:69], 0\n"
            "v_mfma_f32_4x4x4_16b_bf16 v[12:15], v[224:225], v[70:71], 0\n"
            "v_mfma_f32_4x4x4_16b_bf16 v[16:19], v[224:225], v[80:81], 0\n"
            "v_mfma_f32_4x4x4_16b_bf16 v[20:23], v[224:225], v[82:83], 0\n"
            "v_mfma_f32_4x4x4_16b_bf16 v[24:27], v[224:225], v[84:85], 0\n"
            "v_mfma_f32_4x4x4_16b_bf16 v[28:31], v[224:225], v[86:87], 0\n"
            "v_mfma_f32_4x4x4_16b_bf16 v[0:3], v[224:225], v[40:41], v[0:3]\n"
            "v_mfma_f32_4x4x4_16b_bf16 v[4:7], v[224:225], v[42:43], v[4:7]\n"
            "v_mfma_f32_4x4x4_16b_bf16 v[8:11], v[224:225], v[44:45], v[8:11]\n"
            "v_mfma_f32_4x4x4_16b_bf16 v[12:15], v[224:225], v[46:47], v[12:15]\n"
            "v_mfma_f32_4x4x4_16b_bf16 v[16:19], v[224:225], v[56:57], v[16:19]\n"
            "v_mfma_f32_4x4x4_16b_bf16 v[20:23], v[224:225], v[58:59], v[20:23]\n"
            "v_mfma_f32_4x4x4_16b_bf16 v[24:27], v[224:225], v[60:61], v[24:27]\n"
            "v_mfma_f32_4x4x4_16b_bf16 v[28:31], v[224:225], v[62:63], v[28:31]\n"
            "s_nop 1\n"
            "v_cvt_pk_bf16_f32 v176, v0, v4\n"
            "v_cvt_pk_bf16_f32 v177, v8, v12\n"
            "v_cvt_pk_bf16_f32 v178, v16, v20\n"
            "v_cvt_pk_bf16_f32 v179, v24, v28\n"
            "ds_write_b128 v226, v[176:179] offset:0\n"
            "v_cvt_pk_bf16_f32 v180, v1, v5\n"
            "v_cvt_pk_bf16_f32 v181, v9, v13\n"
            "v_cvt_pk_bf16_f32 v182, v17, v21\n"
            "v_cvt_pk_bf16_f32 v183, v25, v29\n"
            "ds_write_b128 v226, v[180:183] offset:272\n"
            "v_cvt_pk_bf16_f32 v184, v2, v6\n"
            "v_cvt_pk_bf16_f32 v185, v10, v14\n"
            "v_cvt_pk_bf16_f32 v186, v18, v22\n"
            "v_cvt_pk_bf16_f32 v187, v26, v30\n"
            "ds_write_b128 v226, v[184:187] offset:544\n"
            "v_cvt_pk_bf16_f32 v188, v3, v7\n"
            "v_cvt_pk_bf16_f32 v189, v11, v15\n"
            "v_cvt_pk_bf16_f32 v190, v19, v23\n"
            "v_cvt_pk_bf16_f32 v191, v27, v31\n"
            "ds_write_b128 v226, v[188:191] offset:816\n"
            "ds_read_b128 v[176:179], v227 offset:0\n"
            "ds_read_b128 v[180:183], v227 offset:64\n"
            "ds_read_b128 v[184:187], v227 offset:128\n"
            "ds_read_b128 v[188:191], v227 offset:192\n"
            "s_waitcnt lgkmcnt(3)\n"
            "v_mfma_f32_16x16x32_bf16 v[0:3], v[176:179], v[236:239], 0\n"
            "v_mfma_f32_16x16x32_bf16 v[4:7], v[176:179], v[96:99], 0\n"
            "s_waitcnt lgkmcnt(2)\n"
            "v_mfma_f32_16x16x32_bf16 v[0:3], v[180:183], v[240:243], v[0:3]\n"
            "v_mfma_f32_16x16x32_bf16 v[4:7], v[180:183], v[100:103], v[4:7]\n"
            "s_waitcnt lgkmcnt(1)\n"
            "v_mfma_f32_16x16x32_bf16 v[0:3], v[184:187], v[244:247], v[0:3]\n"
            "v_mfma_f32_16x16x32_bf16 v[4:7], v[184:187], v[104:107], v[4:7]\n"
            "s_waitcnt lgkmcnt(0)\n"
            "v_mfma_f32_16x16x32_bf16 v[0:3], v[188:191], v[248:251], v[0:3]\n"
            "v_mfma_f32_16x16x32_bf16 v[4:7], v[188:191], v[108:111], v[4:7]\n"
            "v_mfma_f32_16x16x32_bf16 v[8:11], v[176:179], v[112:115], 0\n"
            "v_mfma_f32_16x16x32_bf16 v[12:15], v[176:179], v[204:207], 0\n"
            "v_mfma_f32_16x16x32_bf16 v[8:11], v[180:183], v[116:119], v[8:11]\n"
            "v_mfma_f32_16x16x32_bf16 v[12:15], v[180:183], v[232:235], v[12:15]\n"
            "v_mfma_f32_16x16x32_bf16 v[8:11], v[184:187], v[196:199], v[8:11]\n"
            "v_mfma_f32_16x16x32_bf16 v[12:15], v[184:187], v[120:123], v[12:15]\n"
            "v_mfma_f32_16x16x32_bf16 v[8:11], v[188:191], v[200:203], v[8:11]\n"
            "v_mfma_f32_16x16x32_bf16 v[12:15], v[188:191], v[124:127], v[12:15]\n"
            "v_cvt_pk_bf16_f32 v0, v0, v1\n"
            "v_cvt_pk_bf16_f32 v1, v2, v3\n"
            "v_cvt_pk_bf16_f32 v2, v4, v5\n"
            "v_cvt_pk_bf16_f32 v3, v6, v7\n"
            "buffer_store_dwordx4 v[0:3], v218, %[rso], 0 offen offset:0 sc1\n"
            "v_mfma_f32_16x16x32_bf16 v[16:19], v[176:179], v[192:195], 0\n"
            "v_mfma_f32_16x16x32_bf16 v[20:23], v[176:179], v[140:143], 0\n"
            "v_mfma_f32_16x16x32_bf16 v[16:19], v[180:183], v[128:131], v[16:19]\n"
            "v_mfma_f32_16x16x32_bf16 v[20:23], v[180:183], v[144:147], v[20:23]\n"
            "v_mfma_f32_16x16x32_bf16 v[16:19], v[184:187], v[132:135], v[16:19]\n"
            "v_mfma_f32_16x16x32_bf16 v[20:23], v[184:187], v[148:151], v[20:23]\n"
            "v_mfma_f32_16x16x32_bf16 v[16:19], v[188:191], v[136:139], v[16:19]\n"
            "v_mfma_f32_16x16x32_bf16 v[20:23], v[188:191], v[152:155], v[20:23]\n"
            "v_cvt_pk_bf16_f32 v8, v8, v9\n"
            "v_cvt_pk_bf16_f32 v9, v10, v11\n"
            "v_cvt_pk_bf16_f32 v10, v12, v13\n"
            "v_cvt_pk_bf16_f32 v11, v14, v15\n"
            "buffer_store_dwordx4 v[8:11], v218, %[rso], 0 offen offset:256 sc1\n"
            "v_mfma_f32_16x16x32_bf16 v[24:27], v[176:179], v[156:159], 0\n"
            "v_mfma_f32_16x16x32_bf16 v[28:31], v[176:179], v[92:95], 0\n"
            "v_mfma_f32_16x16x32_bf16 v[24:27], v[180:183], v[72:75], v[24:27]\n"
            "v_mfma_f32_16x16x32_bf16 v[28:31], v[180:183], v[160:163], v[28:31]\n"
            "v_mfma_f32_16x16x32_bf16 v[24:27], v[184:187], v[76:79], v[24:27]\n"
            "v_mfma_f32_16x16x32_bf16 v[28:31], v[184:187], v[164:167], v[28:31]\n"
            "v_mfma_f32_16x16x32_bf16 v[24:27], v[188:191], v[88:91], v[24:27]\n"
            "v_mfma_f32_16x16x32_bf16 v[28:31], v[188:191], v[168:171], v[28:31]\n"
            "v_cvt_pk_bf16_f32 v16, v16, v17\n"
            "v_cvt_pk_bf16_f32 v17, v18, v19\n"
            "v_cvt_pk_bf16_f32 v18, v20, v21\n"
            "v_cvt_pk_bf16_f32 v19, v22, v23\n"
            "buffer_store_dwordx4 v[16:19], v218, %[rso], 0 offen offset:512 sc1\n"
            "s_nop 2\n"
            "v_cvt_pk_bf16_f32 v24, v24, v25\n"
            "v_cvt_pk_bf16_f32 v25, v26, v27\n"
            "v_cvt_pk_bf16_f32 v26, v28, v29\n"
            "v_cvt_pk_bf16_f32 v27, v30, v31\n"
            "buffer_store_dwordx4 v[24:27], v218, %[rso], 0 offen offset:768 sc1\n"
            "Lend_%=:\n"
            "s_waitcnt vmcnt(4)\n"
            : "+{v[32:63]}"(oxa), "+{v[64:95]}"(oxb), "+{v[96:127]}"(owr), "+{v[192:195]}"(efA), "+{v[196:199]}"(S), "+{v[200:203]}"(cgA), "+{v[208:223]}"(omi), "+{v[224:231]}"(ot),
              "+{v[232:235]}"(efN), "+{v236}"(ap1),
              [rem] "+s"(rem), [lens] "+s"(lens), [fm] "+s"(fm), [nst] "+s"(nst), [ra] "=&s"(ra), [tmp] "=&s"(tmp)
            : [rse] "s"(de), [rsx] "s"(dx), [rso] "s"(mkdesc(out, (unsigned)N * 1024u)), [rsw] "s"(mkdesc(ws, W_END)),
              [cnt] "s"(lds0 + L_MISC + 4), [tag] "s"(tag), [flg] "s"(ws + W_FLG + bx * 128), [pfl] "s"(ws + W_FLG + (bx ^ 4) * 128),
              [njl] "s"(((e2 - 1) & 3) + 1), [two] "s"(w), [evt] "s"((unsigned)__builtin_amdgcn_readfirstlane(w < 4 ? evt_of(PTO) : (w < 6 ? evt_of(PTY) : evt_of(PTZ)))), [ptab] "s"((unsigned)__builtin_amdgcn_readfirstlane(w < 4 ? PTO : (w < 6 ? PTY : PTZ))), [oagg] "s"(W_AGG), [spin] "s"(SPIN_MAX)
            : "memory", "vcc", "scc", "v0", "v1", "v2", "v3", "v4", "v5", "v6", "v7", "v8", "v9", "v10", "v11", "v12", "v13", "v14", "v15", "v16", "v17", "v18", "v19", "v20", "v21", "v22", "v23", "v24", "v25", "v26", "v27", "v28", "v29", "v30", "v31", "v128", "v129", "v130", "v131", "v132", "v133", "v134", "v135", "v136", "v137", "v138", "v139", "v140", "v141", "v142", "v143", "v144", "v145", "v146", "v147", "v148", "v149", "v150", "v151", "v152", "v153", "v154", "v155", "v156", "v157", "v158", "v159", "v160", "v161", "v162", "v163", "v164", "v165", "v166", "v167", "v168", "v169", "v170", "v171", "v172", "v173", "v174", "v175", "v176", "v177", "v178", "v179", "v180", "v181", "v182", "v183", "v184", "v185", "v186", "v187", "v188", "v189", "v190", "v191", "v204", "v205", "v206", "v207", "v237", "v238", "v239", "v240", "v241", "v242", "v243", "v244", "v245", "v246", "v247", "v248", "v249", "v250", "v251");
        if (bx == 8 && w == 0) { for (;;) { u64 t_; asm volatile("s_memrealtime %0\n\ts_waitcnt lgkmcnt(0)" : "=s"(t_) :: "memory"); if (t_ - tprobe >= 6000ull) break; } }
    } else {
        if (bx == 0 && w == 4) flag_store(ws + W_CTL, tag);
        const __amdgpu_buffer_rsrc_t rso = __builtin_amdgcn_make_buffer_rsrc((void*)out, 0, N * 1024, 0x00020000);
#pragma unroll 1
        for (int it = 0; it < 10; ++it) {
            const int nl = (it < 5 ? sS + 8 * it : sK + 8 * (it - 5)) + w, node = node0 + nl;
            const bool nv = nl < npb;
            const unsigned ro = nv ? (unsigned)node * 1024u + (unsigned)lane * 16u : OOB_OFF;
            float a8[8] = {0.f, 0.f, 0.f, 0.f, 0.f, 0.f, 0.f, 0.f};
#pragma unroll 1
            for (int e0 = 0; e0 < (nv ? E : 0); e0 += 64) {
                const int ec = min(e0 + lane, E - 1);
                u64 mm = __ballot(e0 + lane < E && dst[ec] == node && (src[ec] >= nh) == (half != 0));
#pragma unroll 1
                while (mm) {
                    const int e = e0 + __builtin_ctzll(mm);
                    mm &= mm - 1;
                    const unsigned short* fe = (const unsigned short*)ef + (size_t)e * DE_DIM;
                    const unsigned short* ye = (const unsigned short*)Y + (size_t)e * M_DIM;
                    const unsigned short* xp = x + (size_t)src[e] * (C_DIM * M_DIM) + lane * 8;
                    float z0 = 0.f, z1 = 0.f;
#pragma unroll 1
                    for (int f = 0; f < DE_DIM; ++f) { z0 += bf2f(fe[f]) * bf2f(w_rad[f * C_DIM + 2 * lane]); z1 += bf2f(fe[f]) * bf2f(w_rad[f * C_DIM + 2 * lane + 1]); }
                    const float r0 = z0 / (1.f + __expf(-z0)), r1 = z1 / (1.f + __expf(-z1));
#pragma unroll 1
                    for (int i = 0; i < 4; ++i) {
                        const float x0 = bf2f(xp[i]) * r0, x1 = bf2f(xp[4 + i]) * r1;
#pragma unroll
                        for (int o = 0; o < 4; ++o) {
                            float wv4 = 0.f;
#pragma unroll
                            for (int f = 0; f < 4; ++f) wv4 += bf2f(cg[(i * 4 + f) * 4 + o]) * bf2f(ye[f]);
                            a8[o] += x0 * wv4; a8[4 + o] += x1 * wv4;
                        }
                    }
                }
            }
            if (it >= 5) {
                const u32x4 pv = __builtin_amdgcn_raw_buffer_load_b128(rsw, ro, W_AGG, SC1);
#pragma unroll
                for (int i = 0; i < 4; ++i) { a8[2 * i] += __uint_as_float(pv[i] << 16); a8[2 * i + 1] += __uint_as_float(pv[i] & 0xffff0000u); }
            }
            const u32x4 rv = (u32x4){pack2bf(a8[0], a8[1]), pack2bf(a8[2], a8[3]), pack2bf(a8[4], a8[5]), pack2bf(a8[6], a8[7])};
            if (it < 5) {
                __builtin_amdgcn_raw_buffer_store_b128(rv, rsw, ro, W_AGG, SC1);
                if (it == 4) {
                    asm volatile("s_waitcnt vmcnt(0)" ::: "memory");
                    if (lds_arrive_rtn(lds0 + L_MISC + 4) == 7u) flag_store(ws + W_FLG + bx * 128, tag);
                    poll_tag(ws + W_FLG + (bx ^ 4) * 128, tag);
                }
            } else {
                *(u32x4*)(smem + L_PRV + w * 1024 + lane * 16) = rv;
#pragma unroll
                for (int i = 0; i < 8; ++i) a8[i] = 0.f;
#pragma unroll 1
                for (int c = 0; c < C_DIM; ++c) {
                    const unsigned short* ap = (const unsigned short*)(smem + L_PRV + w * 1024 + c * 8);
                    const float w0 = bf2f(w_mix[c * C_DIM + 2 * lane]), w1 = bf2f(w_mix[c * C_DIM + 2 * lane + 1]);
#pragma unroll
                    for (int m = 0; m < 4; ++m) { a8[m] += bf2f(ap[m]) * w0; a8[4 + m] += bf2f(ap[m]) * w1; }
                }
                __builtin_amdgcn_raw_buffer_store_b128((u32x4){pack2bf(a8[0], a8[1]), pack2bf(a8[2], a8[3]), pack2bf(a8[4], a8[5]), pack2bf(a8[6], a8[7])}, rso, ro, 0, 0);
            }
        }
    }
}

__global__ void kf_zero(float* __restrict__ agg, size_t n) {
    for (size_t i = (size_t)blockIdx.x * blockDim.x + threadIdx.x; i < n; i += (size_t)gridDim.x * blockDim.x) agg[i] = 0.f;
}
__global__ void kf_edges(const unsigned short* __restrict__ x, const int* __restrict__ src, const int* __restrict__ dst,
                         const unsigned short* __restrict__ Y, const unsigned short* __restrict__ ef,
                         const unsigned short* __restrict__ w_rad, const unsigned short* __restrict__ cg,
                         float* __restrict__ agg, int E) {
    const int ch = threadIdx.x;
    for (int e = blockIdx.x; e < E; e += gridDim.x) {
        float z = 0.f;
        for (int f = 0; f < DE_DIM; ++f) z += bf2f(ef[(size_t)e * DE_DIM + f]) * bf2f(w_rad[f * C_DIM + ch]);
        const float r = z / (1.f + __expf(-z));
        float a4[4] = {0.f, 0.f, 0.f, 0.f};
        for (int ii = 0; ii < 4; ++ii) {
            const float xv = bf2f(x[((size_t)src[e] * C_DIM + ch) * M_DIM + ii]) * r;
            for (int o = 0; o < 4; ++o) {
                float wv = 0.f;
                for (int f = 0; f < 4; ++f) wv += bf2f(cg[(ii * 4 + f) * 4 + o]) * bf2f(Y[(size_t)e * 4 + f]);
                a4[o] += xv * wv;
            }
        }
        for (int o = 0; o < 4; ++o) atomicAdd(&agg[((size_t)dst[e] * C_DIM + ch) * M_DIM + o], a4[o]);
    }
}
__global__ void kf_mix(const float* __restrict__ agg, const unsigned short* __restrict__ w_mix, unsigned short* __restrict__ out, int N) {
    const int d = threadIdx.x;
    for (int n = blockIdx.x; n < N; n += gridDim.x) {
        float a4[4] = {0.f, 0.f, 0.f, 0.f};
        for (int c = 0; c < C_DIM; ++c) {
            const float wv = bf2f(w_mix[c * C_DIM + d]);
            for (int m = 0; m < 4; ++m) a4[m] += bf2f(f2bf(agg[((size_t)n * C_DIM + c) * M_DIM + m])) * wv;
        }
        for (int m = 0; m < 4; ++m) out[((size_t)n * C_DIM + d) * M_DIM + m] = f2bf(a4[m]);
    }
}

extern "C" void kernel_launch(void* const* d_in, const int* in_sizes, int n_in,
                              void* d_out, int out_size, void* d_ws, size_t ws_size,
                              hipStream_t stream) {
    const unsigned short* x = (const unsigned short*)d_in[0];
    const int* src = (const int*)d_in[1];
    const int* dst = (const int*)d_in[2];
    const unsigned short* Y = (const unsigned short*)d_in[3];
    const unsigned short* ef = (const unsigned short*)d_in[4];
    const unsigned short* w_rad = (const unsigned short*)d_in[5];
    const unsigned short* cg = (const unsigned short*)d_in[6];
    const unsigned short* w_mix = (const unsigned short*)d_in[7];
    unsigned short* out = (unsigned short*)d_out;
    const int N = in_sizes[0] / (C_DIM * M_DIM);
    const int E = in_sizes[1];
    const int epb = (E + 255) / 256;
    const int nh = (N + 1) / 2;
    const bool fast = N >= 256 && N <= NB_MAX * NPB_MAX && E > 0 && epb <= EPB_MAX && ws_size >= (size_t)W_END;
    if (!fast) {
        float* agg = (float*)d_ws;
        if (N <= 0) return;
        kf_zero<<<1024, 256, 0, stream>>>(agg, (size_t)N * C_DIM * M_DIM);
        if (E > 0) kf_edges<<<4096, C_DIM, 0, stream>>>(x, src, dst, Y, ef, w_rad, cg, agg, E);
        kf_mix<<<2048, C_DIM, 0, stream>>>(agg, w_mix, out, N);
        return;
    }
    const unsigned nb_magic = (unsigned)(((1ull << 39) + N - 1) / N);
    k_fused<<<256, 512, 0, stream>>>(x, src, dst, (const u32x2*)Y, (const u32x4*)ef, w_rad, cg, w_mix, out, (unsigned char*)d_ws,
                                     N, E, epb, nb_magic, nh, (unsigned)(4 * 65536 / nh));
}
```

```cpp
#include <hip/hip_runtime.h>
#include <hip/hip_bf16.h>
#include <stdint.h>

typedef __attribute__((ext_vector_type(8))) short bf16x8;
typedef __attribute__((ext_vector_type(4))) short bf16x4;
typedef __attribute__((ext_vector_type(4))) float f32x4;
typedef __attribute__((ext_vector_type(2))) unsigned u32x2;
typedef __attribute__((ext_vector_type(3))) unsigned u32x3;
typedef __attribute__((ext_vector_type(4))) unsigned u32x4;
typedef __attribute__((ext_vector_type(8))) unsigned u32x8;
typedef __attribute__((ext_vector_type(16))) unsigned u32x16;
typedef __attribute__((ext_vector_type(32))) unsigned u32x32;
typedef unsigned long long u64;

#define C_DIM 128
#define M_DIM 4
#define DE_DIM 16
#define NPB_MAX 80
#define NB_MAX 128
#define EPB_MAX 1280
#define OOB_OFF 0x80000000u
#define SC1 16
#define SPIN_MAX (1u << 22)
#define W_FLG 4096
#define W_AGG 5804032
#define W_END 16289792
#define L_LST 4096
#define L_TOTAL 154624
#define SY_C3 56

#define SB() __builtin_amdgcn_sched_barrier(0)
static __device__ __forceinline__ int lane_opaque() {
    int l;
    asm volatile("v_mbcnt_lo_u32_b32 %0, -1, 0\n\tv_mbcnt_hi_u32_b32 %0, -1, %0" : "=v"(l));
    return l;
}

static __device__ __forceinline__ float bf2f(unsigned short v) { return __uint_as_float(((unsigned)v) << 16); }
typedef __bf16 bf16v2 __attribute__((ext_vector_type(2)));
static __device__ __forceinline__ unsigned pack2bf(float lo, float hi) {
    bf16v2 r = (bf16v2){(__bf16)lo, (__bf16)hi};
    return __builtin_bit_cast(unsigned, r);
}
static __device__ __forceinline__ unsigned short f2bf(float v) { return (unsigned short)(pack2bf(v, 0.f) & 0xffffu); }
static __device__ __forceinline__ void flag_store(const void* p, unsigned v) {
    unsigned t0, t1; u64 sv;
    asm volatile("v_mov_b32 %0, 0\n\tv_mov_b32 %1, %4\n\ts_mov_b64 %2, exec\n\ts_mov_b64 exec, 1\n\t"
                 "global_store_dword %0, %1, %3 sc1\n\ts_mov_b64 exec, %2" : "=&v"(t0), "=&v"(t1), "=&s"(sv) : "s"(p), "s"(v) : "memory");
}
static __device__ __forceinline__ unsigned lds_arrive_rtn(unsigned a) {
    unsigned t0, t1; u64 sv;
    asm volatile("v_mov_b32 %0, %3\n\tv_mov_b32 %1, 1\n\ts_mov_b64 %2, exec\n\ts_mov_b64 exec, 1\n\t"
                 "ds_add_rtn_u32 %1, %0, %1\n\ts_mov_b64 exec, %2\n\ts_waitcnt lgkmcnt(0)" : "=&v"(t0), "=&v"(t1), "=&s"(sv) : "s"(a) : "memory");
    return (unsigned)__builtin_amdgcn_readfirstlane((int)t1);
}
template <int SLEEP = 0> static __device__ __forceinline__ void lds_wait(unsigned a, unsigned n) {
    for (;;) {
        unsigned v;
        asm volatile("ds_read_b32 %0, %1\n\ts_waitcnt lgkmcnt(0)" : "=v"(v) : "v"(a) : "memory");
        if ((unsigned)__builtin_amdgcn_readfirstlane((int)v) >= n) break;
        if (SLEEP) __builtin_amdgcn_s_sleep(SLEEP);
    }
}
static __device__ __forceinline__ unsigned ld_agent(const void* p) { return __hip_atomic_load((const unsigned*)p, __ATOMIC_RELAXED, __HIP_MEMORY_SCOPE_AGENT); }
static __device__ __forceinline__ void poll_tag(const void* p, unsigned tag) {
#pragma unroll 1
    for (unsigned spin = 0; spin < SPIN_MAX; ++spin) {
        if (__all(ld_agent(p) == tag)) break;
        __builtin_amdgcn_s_sleep(2);
    }
    __builtin_amdgcn_fence(__ATOMIC_ACQUIRE, "wavefront");
}

#define BUF(t, n) t n, unsigned n##_bytes, unsigned n##_fmt
__global__ __launch_bounds__(512, 2) void k_fused(
    BUF(const void*, dst_), BUF(const void*, src_), BUF(const void*, Y_), BUF(void*, ws_), BUF(const void*, ef_), BUF(const void*, x_), BUF(void*, out_), BUF(const void*, w_rad_),
    BUF(const void*, cg_), BUF(const void*, w_mix_), unsigned N_, unsigned E_, unsigned epb_, unsigned magic_, unsigned shift_, unsigned nhb_, unsigned nh_, unsigned zero_)
{
    __shared__ __attribute__((aligned(256))) unsigned char smem[L_TOTAL];
    const __attribute__((address_space(4))) u64* ka = (const __attribute__((address_space(4))) u64*)__builtin_amdgcn_kernarg_segment_ptr();
    const int bx = blockIdx.x;
    unsigned slow, tag, wq, tid = threadIdx.x;
    asm volatile(
            "s_load_dwordx16 s[8:23], s[0:1], 0\n"
            "s_load_dwordx16 s[40:55], s[0:1], 128\n"
            "s_load_dwordx16 s[24:39], s[0:1], 64\n"
            "s_getpc_b64 s[4:5]\n"
            "v_readfirstlane_b32 s3, v0\n"
            "v_and_b32 v243, 63, v0\n"
            "s_setprio 3\n"
            "s_lshr_b32 s3, s3, 6\n"
            "v_mul_u32_u24 v54, 3, v243\n"
            "s_cmp_lg_u32 s3, 4\n"
            "s_cbranch_scc1 Lncp_%=\n"
            "s_and_b32 s4, s4, 0xffffff80\n"
            "s_movk_i32 s6, 16384\n"
            "s_mov_b32 s7, 0x20000\n"
            "s_lshr_b32 s68, s2, 3\n"
            "s_lshl_b32 s68, s68, 7\n"
            "v_lshlrev_b32 v81, 12, v243\n"
            "buffer_load_dword v53, v81, s[4:7], s68 offen\n"
            "Lncp_%=:\n"
            "s_mul_i32 s68, s3, 162\n"
            "v_cmp_gt_u32 vcc, 54, v243\n"
            "v_add_u32 v54, s68, v54\n"
            "v_mov_b32 v82, 0x1000000\n"
            "v_cndmask_b32 v54, v82, v54, vcc\n"
            "v_mov_b32 v219, 0\n"
            "s_waitcnt lgkmcnt(0)\n"
            "s_mul_i32 s95, s2, s50\n"
            "v_add_lshl_u32 v81, v54, s95, 2\n"
            "buffer_load_dwordx3 v[32:34], v81, s[8:11], 0 offen nt\n"
            "buffer_load_dwordx3 v[36:38], v81, s[12:15], 0 offen nt\n"
            "v_lshlrev_b32 v82, 1, v81\n"
            "buffer_load_dword v40, off, s[20:23], 0\n"
            "buffer_load_dwordx4 v[42:45], v82, s[16:19], 0 offen nt\n"
            "buffer_load_dwordx2 v[46:47], v82, s[16:19], 0 offen offset:16 nt\n"
            "v_lshlrev_b32 v193, 7, v243\n"
            "s_lshl_b32 s54, s95, 5\n"
            "s_bfe_u32 s8, s2, 0x10002\n"
            "s_and_b32 s9, s2, 3\n"
            "s_lshl_b32 s9, s9, 5\n"
            "s_lshr_b32 s68, s2, 3\n"
            "s_add_u32 s9, s9, s68\n"
            "s_mul_i32 s55, s8, s53\n"
            "s_lshl_b32 s69, s9, 13\n"
            "s_add_u32 s69, s69, s55\n"
            "s_cmp_lt_u32 s3, 4\n"
            "s_cbranch_scc1 Lx0_%=\n"
            "v_lshlrev_b32 v81, 4, v0\n"
            "v_and_b32 v81, 0xe00, v81\n"
            "v_and_b32 v82, 31, v0\n"
            "v_lshl_or_b32 v81, v82, 3, v81\n"
            "buffer_load_dwordx2 v[48:49], v81, s[36:39], 0 offen\n"
            "buffer_load_dwordx2 v[50:51], v81, s[36:39], 0 offen offset:256\n"
            "s_cmp_eq_u32 s3, 4\n"
            "s_cbranch_scc1 Lx4_%=\n"
            "s_sub_u32 s68, 7, s3\n"
            "s_cmp_lt_u32 s68, 3\n"
            "s_cselect_b32 s68, s68, 0x20000\n"
            "s_lshl_b32 s68, s68, 13\n"
            "s_add_u32 s68, s68, s54\n"
            "buffer_load_dword v52, v193, s[24:27], s68 offen\n"
            "s_branch Lx1_%=\n"
            "Lx4_%=:\n"
            "v_lshlrev_b32 v81, 2, v243\n"
            "buffer_load_dword v52, v81, s[40:43], 0 offen\n"
            "s_branch Lx1_%=\n"
            "Lx0_%=:\n"
            "s_add_u32 s68, s3, 3\n"
            "s_cmp_lt_u32 s68, 3\n"
            "s_cselect_b32 s68, s68, 0x20000\n"
            "s_lshl_b32 s68, s68, 13\n"
            "s_add_u32 s68, s68, s54\n"
            "buffer_load_dword v52, v193, s[24:27], s68 offen\n"
            "s_and_b32 s68, s9, 31\n"
            "s_lshl_b32 s68, s68, 7\n"
            "s_cmp_eq_u32 s3, 3\n"
            "s_cselect_b32 s68, s68, 0x40000000\n"
            "v_lshlrev_b32 v81, 5, v193\n"
            "buffer_load_dword v48, v81, s[44:47], s68 offen\n"
            "buffer_load_dword v49, off, s[20:23], 0\n"
            "Lx1_%=:\n"
            "v_mov_b64 v[82:83], 0\n"
            "v_mov_b64 v[84:85], 0\n"
            "v_lshlrev_b32 v81, 4, v0\n"
            "s_cmp_lt_u32 s3, 4\n"
            "s_cselect_b32 s68, 0, 70656\n"
            "v_add_u32 v86, s68, v81\n"
            "ds_write_b128 v86, v[82:85]\n"
            "s_cmov_b32 s68, 78848\n"
            "v_add_u32 v86, s68, v81\n"
            "ds_write_b128 v86, v[82:85]\n"
            "v_and_b32 v196, 15, v0\n"
            "v_mov_b32 v209, 1\n"
            "s_cmp_lg_u32 s3, 0\n"
            "s_cbranch_scc1 Lnk_%=\n"
            "v_lshlrev_b32 v81, 2, v243\n"
            "v_sub_u32 v82, 127, v243\n"
            "ds_write_b32 v81, v82 offset:96\n"
            "v_lshlrev_b32 v81, 2, v196\n"
            "v_sub_u32 v82, 63, v196\n"
            "ds_write_b32 v81, v82 offset:352\n"
            "v_bfrev_b32 v82, 1\n"
            "ds_write2_b32 v219, v82, v82 offset0:20 offset1:21\n"
            "Lnk_%=:\n"
            "s_sub_i32 s96, s49, s95\n"
            "s_min_i32 s96, s96, s50\n"
            "s_max_i32 s96, s96, 0\n"
            "s_and_b32 s12, s3, 3\n"
            "s_lshl_b32 s12, s12, 3\n"
            "s_lshl_b32 s68, 1, s12\n"
            "v_mov_b32 v80, s68\n"
            "s_sub_u32 s68, s68, 1\n"
            "s_and_b32 s68, s68, 0x10101010\n"
            "s_cmp_lt_u32 s3, 4\n"
            "s_cselect_b32 s13, s68, 0x10101010\n"
            "s_cselect_b32 s14, 0, s68\n"
            "s_mov_b32 s39, 0x10101010\n"
            "v_cmp_gt_u32 s[72:73], s96, v54\n"
            "v_lshlrev_b32 v77, 9, v54\n"
            "v_add_u32 v81, 1, v54\n"
            "v_cmp_gt_u32 s[74:75], s96, v81\n"
            "v_lshlrev_b32 v78, 9, v81\n"
            "v_add_u32 v81, 2, v54\n"
            "v_cmp_gt_u32 s[76:77], s96, v81\n"
            "v_lshlrev_b32 v79, 9, v81\n"
            "s_mul_i32 s10, s9, s48\n"
            "s_add_u32 s10, s10, 127\n"
            "s_add_u32 s11, s10, s48\n"
            "s_lshr_b32 s10, s10, 7\n"
            "s_lshr_b32 s11, s11, 7\n"
            "s_sub_u32 s11, s11, s10\n"
            "v_lshlrev_b32 v192, 4, v243\n"
            "v_lshlrev_b32 v194, 3, v243\n"
            "v_lshrrev_b32 v195, 4, v243\n"
            "v_lshlrev_b32 v214, 4, v196\n"
            "v_and_b32 v249, 3, v0\n"
            "v_lshlrev_b32 v250, 4, v249\n"
            "v_mov_b32 v251, 80\n"
            "v_lshrrev_b32 v83, 2, v196\n"
            "s_lshl_b32 s68, s3, 9\n"
            "s_add_u32 s68, s68, 61440\n"
            "v_add_u32 v212, s68, v194\n"
            "v_lshl_add_u32 v213, v195, 7, s68\n"
            "v_lshl_add_u32 v213, v249, 5, v213\n"
            "v_lshl_add_u32 v213, v83, 3, v213\n"
            "v_mov_b32 v224, 0x3f80\n"
            "v_mov_b32 v225, 0\n"
            "v_lshlrev_b64 v[224:225], v250, v[224:225]\n"
            "s_mul_i32 s68, s3, 4352\n"
            "s_sub_u32 s38, s3, 3\n"
            "s_max_i32 s38, s38, 0\n"
            "s_lshl_b32 s38, s38, 12\n"
            "s_add_u32 s68, s68, s38\n"
            "s_add_u32 s68, s68, 70656\n"
            "v_mov_b32 v81, s68\n"
            "v_add_u32 v231, s68, v192\n"
            "v_add_u32 v239, 0xfffff000, v231\n"
            "v_mov_b32 v236, v231\n"
            "s_add_u32 s38, s3, 3\n"
            "s_and_b32 s38, s38, 3\n"
            "s_lshl2_add_u32 s41, s38, 16\n"
            "s_mul_i32 s38, s38, 8448\n"
            "s_add_u32 s38, s38, 88064\n"
            "v_add_u32 v252, s38, v192\n"
            "s_movk_i32 s38, 272\n"
            "v_mad_u32_u24 v227, v196, s38, v81\n"
            "v_lshl_add_u32 v227, v195, 4, v227\n"
            "s_movk_i32 s38, 1088\n"
            "v_mad_u32_u24 v226, v195, s38, v81\n"
            "v_add_u32 v226, v226, v214\n"
            "v_add_u32 v228, 121856, v192\n"
            "v_and_b32 v229, 16, v0\n"
            "s_mul_i32 s68, s3, 640\n"
            "s_add_u32 s68, s68, 65536\n"
            "v_add_u32 v247, s68, v194\n"
            "v_lshl_add_u32 v81, v249, 3, s68\n"
            "v_lshl_add_u32 v237, v195, 1, v81\n"
            "v_add_u32 v237, 32, v237\n"
            "v_lshl_add_u32 v238, v83, 1, v81\n"
            "v_add_u32 v238, 64, v238\n"
            "v_and_b32 v81, 7, v0\n"
            "v_lshlrev_b32 v82, 2, v81\n"
            "s_mov_b32 s68, 1969373488\n"
            "v_bfe_u32 v240, s68, v82, 4\n"
            "v_lshlrev_b32 v240, 4, v240\n"
            "v_add_u32 v240, 2304, v240\n"
            "s_mov_b32 s68, 1969816083\n"
            "v_bfe_u32 v241, s68, v82, 4\n"
            "v_lshlrev_b32 v241, 4, v241\n"
            "v_add_u32 v241, 2048, v241\n"
            "s_mov_b32 s68, 2554920959\n"
            "v_bfe_u32 v242, s68, v82, 4\n"
            "v_lshlrev_b32 v242, 4, v242\n"
            "v_add_u32 v242, 2048, v242\n"
            "v_cmp_lt_u32 vcc, 5, v81\n"
            "v_add_u32 v82, 256, v242\n"
            "s_nop 0\n"
            "v_cndmask_b32 v242, v242, v82, vcc\n"
            "v_cmp_lt_u32 vcc, 3, v81\n"
            "s_nop 1\n"
            "v_cndmask_b32 v242, 8, v242, vcc\n"
            "s_mov_b32 s68, 1998716928\n"
            "v_lshlrev_b32 v82, 2, v81\n"
            "v_bfe_u32 v208, s68, v82, 4\n"
            "s_add_u32 s38, s3, 1\n"
            "s_and_b32 s38, s38, 3\n"
            "s_add_u32 s38, s38, 4\n"
            "v_readlane_b32 s64, v240, s3\n"
            "v_readlane_b32 s65, v241, s3\n"
            "v_readlane_b32 s66, v242, s3\n"
            "v_readlane_b32 s40, v242, s38\n"
            "v_lshl_add_u32 v244, v195, 2, s64\n"
            "v_lshl_add_u32 v245, v195, 2, s65\n"
            "v_lshl_add_u32 v246, v195, 2, s66\n"
            "v_lshl_add_u32 v248, v83, 2, s64\n"
            "s_add_u32 s64, s64, 512\n"
            "s_add_u32 s65, s65, 512\n"
            "s_add_u32 s66, s66, 512\n"
            "s_add_u32 s40, s40, 512\n"
            "s_lshl_b32 s68, s2, 7\n"
            "s_add_u32 s38, s68, 4096\n"
            "s_add_u32 s56, s20, s38\n"
            "s_addc_u32 s57, s21, 0\n"
            "s_xor_b32 s38, s38, 512\n"
            "s_add_u32 s58, s20, s38\n"
            "s_addc_u32 s59, s21, 0\n"
            "s_cmp_lt_u32 s3, 6\n"
            "s_movk_i32 s60, 78\n"
            "s_cmovk_i32 s60, 150\n"
            "s_mov_b32 s61, 4294961748\n"
            "s_cmov_b32 s61, 4294945104\n"
            "s_movk_i32 s62, 0x3862\n"
            "s_cmov_b32 s62, 0x9062\n"
            "s_cmp_lt_u32 s3, 4\n"
            "s_cmovk_i32 s60, 590\n"
            "s_cmov_b32 s61, 4294612224\n"
            "s_cmovk_i32 s62, 0x82\n"
            "s_mov_b32 s63, 5804032\n"
            "s_lshl3_add_u32 s68, s9, s3\n"
            "s_mul_i32 s68, s68, 512\n"
            "s_add_u32 s67, s68, s55\n"
            "s_add_u32 s67, s67, 3145728\n"
            "s_lshr_b32 s38, s3, 2\n"
            "s_lshl_b32 s38, s38, 10\n"
            "s_lshl_b32 s15, s3, 9\n"
            "s_add_u32 s15, s15, 24576\n"
            "v_add_u32 v94, s15, v194\n"
            "s_lshr_b32 s68, s38, 1\n"
            "s_sub_u32 s15, s15, s68\n"
            "s_and_b32 s68, s3, 1\n"
            "s_lshl1_add_u32 s68, s2, s68\n"
            "s_lshl_b32 s68, s68, 10\n"
            "s_add_u32 s36, s68, 36864\n"
            "v_mov_b32 v93, 268439552\n"
            "v_lshlrev_b32 v95, 4, v0\n"
            "s_mul_i32 s68, s2, 20480\n"
            "s_add_u32 s68, s68, 557056\n"
            "s_add_u32 s4, s20, s68\n"
            "s_addc_u32 s5, s21, 0\n"
            "s_lshl_b32 s6, s96, 4\n"
            "s_add_u32 s6, s6, 4223\n"
            "s_and_b32 s6, s6, 0xffffff80\n"
            "s_mov_b32 s7, 0x20000\n"
            "v_mov_b64 v[0:1], 0\n"
            "v_mov_b64 v[2:3], 0\n"
            "v_mov_b64 v[4:5], 0\n"
            "v_mov_b64 v[6:7], 0\n"
            "v_mov_b64 v[8:9], 0\n"
            "v_mov_b64 v[10:11], 0\n"
            "v_mov_b64 v[12:13], 0\n"
            "v_mov_b64 v[14:15], 0\n"
            "v_mov_b64 v[16:17], 0\n"
            "v_mov_b64 v[18:19], 0\n"
            "v_mov_b64 v[20:21], 0\n"
            "v_mov_b64 v[22:23], 0\n"
            "v_mov_b64 v[24:25], 0\n"
            "v_mov_b64 v[26:27], 0\n"
            "v_mov_b64 v[28:29], 0\n"
            "v_mov_b64 v[30:31], 0\n"
            "s_waitcnt lgkmcnt(0)\n"
            "s_barrier\n"
            "s_waitcnt vmcnt(6)\n"
            "v_lshlrev_b32 v81, s52, v32\n"
            "v_lshlrev_b32 v84, s52, v36\n"
            "v_lshlrev_b32 v82, s52, v33\n"
            "v_lshlrev_b32 v85, s52, v37\n"
            "v_lshlrev_b32 v83, s52, v34\n"
            "v_lshlrev_b32 v86, s52, v38\n"
            "v_mul_hi_u32_u24 v81, s51, v81\n"
            "v_mul_hi_u32_u24 v84, s51, v84\n"
            "v_lshl_add_u32 v68, v81, 2, s38\n"
            "v_and_b32 v81, 64, v84\n"
            "v_lshl_add_u32 v68, v81, 3, v68\n"
            "s_mov_b64 exec, s[72:73]\n"
            "ds_add_rtn_u32 v71, v68, v80 offset:2048\n"
            "s_mov_b64 exec, -1\n"
            "v_mul_hi_u32_u24 v82, s51, v82\n"
            "v_mul_hi_u32_u24 v85, s51, v85\n"
            "v_lshl_add_u32 v69, v82, 2, s38\n"
            "v_and_b32 v82, 64, v85\n"
            "v_lshl_add_u32 v69, v82, 3, v69\n"
            "s_mov_b64 exec, s[74:75]\n"
            "ds_add_rtn_u32 v72, v69, v80 offset:2048\n"
            "s_mov_b64 exec, -1\n"
            "v_mul_hi_u32_u24 v83, s51, v83\n"
            "v_mul_hi_u32_u24 v86, s51, v86\n"
            "v_lshl_add_u32 v70, v83, 2, s38\n"
            "v_and_b32 v83, 64, v86\n"
            "v_lshl_add_u32 v70, v83, 3, v70\n"
            "s_mov_b64 exec, s[76:77]\n"
            "ds_add_rtn_u32 v73, v70, v80 offset:2048\n"
            "s_mov_b64 exec, -1\n"
            "v_bfe_u32 v84, v84, 4, 2\n"
            "v_and_b32 v81, 127, v32\n"
            "v_lshl_or_b32 v81, v81, 2, v84\n"
            "v_bfe_u32 v85, v85, 4, 2\n"
            "v_and_b32 v82, 127, v33\n"
            "v_lshl_or_b32 v82, v82, 2, v85\n"
            "v_bfe_u32 v86, v86, 4, 2\n"
            "v_and_b32 v83, 127, v34\n"
            "v_lshl_or_b32 v83, v83, 2, v86\n"
            "s_waitcnt vmcnt(5)\n"
            "v_readfirstlane_b32 s90, v40\n"
            "s_mul_i32 s90, s90, 1664525\n"
            "s_add_u32 s90, s90, 1013904223\n"
            "s_lshl_b32 s93, s90, 14\n"
            "s_and_b32 s94, s90, 0xfff00000\n"
            "s_waitcnt lgkmcnt(0)\n"
            "s_barrier\n"
            "ds_read_b128 v[128:131], v192 offset:2048\n"
            "ds_read_b128 v[132:135], v192 offset:3072\n"
            "v_or3_b32 v59, v81, v77, s94\n"
            "v_or_b32 v57, s93, v36\n"
            "v_lshrrev_b32 v74, 1, v68\n"
            "v_add_u32 v74, s15, v74\n"
            "v_bfe_u32 v71, v71, s12, 8\n"
            "v_or3_b32 v63, v82, v78, s94\n"
            "v_or_b32 v61, s93, v37\n"
            "v_lshrrev_b32 v75, 1, v69\n"
            "v_add_u32 v75, s15, v75\n"
            "v_bfe_u32 v72, v72, s12, 8\n"
            "v_or3_b32 v67, v83, v79, s94\n"
            "v_or_b32 v65, s93, v38\n"
            "v_lshrrev_b32 v76, 1, v70\n"
            "v_add_u32 v76, s15, v76\n"
            "v_bfe_u32 v73, v73, s12, 8\n"
            "s_waitcnt lgkmcnt(1)\n"
            "v_dot4_u32_u8 v136, v128, s39, 0\n"
            "v_dot4_u32_u8 v137, v129, s39, 0\n"
            "v_dot4_u32_u8 v138, v130, s39, 0\n"
            "v_dot4_u32_u8 v139, v131, s39, 0\n"
            "s_waitcnt lgkmcnt(0)\n"
            "v_dot4_u32_u8 v136, v132, s39, v136\n"
            "v_dot4_u32_u8 v137, v133, s39, v137\n"
            "v_dot4_u32_u8 v138, v134, s39, v138\n"
            "v_dot4_u32_u8 v139, v135, s39, v139\n"
            "v_dot4_u32_u8 v140, v128, s13, 0\n"
            "v_dot4_u32_u8 v141, v129, s13, v136\n"
            "v_dot4_u32_u8 v142, v130, s13, 0\n"
            "v_dot4_u32_u8 v143, v131, s13, v138\n"
            "v_add_u32 v87, v136, v137\n"
            "v_add3_u32 v88, v87, v138, v139\n"
            "v_dot4_u32_u8 v140, v132, s14, v140\n"
            "v_dot4_u32_u8 v141, v133, s14, v141\n"
            "s_mov_b32 s68, 0x10001\n"
            "v_add_u32_dpp v89, v88, v88 row_shr:1 row_mask:0xf bank_mask:0xf bound_ctrl:1\n"
            "v_dot4_u32_u8 v142, v134, s14, v142\n"
            "v_dot4_u32_u8 v143, v135, s14, v143\n"
            "v_add_u32_dpp v89, v89, v89 row_shr:2 row_mask:0xf bank_mask:0xf bound_ctrl:1\n"
            "v_lshl_add_u32 v140, v141, 16, v140\n"
            "v_mad_u32_u24 v90, v87, s68, v93\n"
            "v_add_u32_dpp v89, v89, v89 row_shr:4 row_mask:0xf bank_mask:0xf bound_ctrl:1\n"
            "v_add_u32 v140, v140, v93\n"
            "v_lshl_add_u32 v141, v143, 16, v142\n"
            "v_add_u32_dpp v89, v89, v89 row_shr:8 row_mask:0xf bank_mask:0xf bound_ctrl:1\n"
            "v_add_u32 v141, v141, v90\n"
            "s_and_b32 s38, s3, 6\n"
            "v_add_u32_dpp v89, v89, v89 row_bcast:15 row_mask:0xa bank_mask:0xf\n"
            "s_cmp_lg_u32 s38, 4\n"
            "s_nop 0\n"
            "v_add_u32_dpp v89, v89, v89 row_bcast:31 row_mask:0xc bank_mask:0xf\n"
            "v_sub_u32 v89, v89, v88\n"
            "v_mad_u32_u24 v140, v89, s68, v140\n"
            "v_mad_u32_u24 v141, v89, s68, v141\n"
            "ds_write_b64 v94, v[140:141]\n"
            "ds_read_u16 v74, v74\n"
            "ds_read_u16 v75, v75\n"
            "ds_read_u16 v76, v76\n"
            "s_cbranch_scc1 Lnt_%=\n"
            "s_bitcmp1_b32 s3, 0\n"
            "s_cbranch_scc0 Lt0_%=\n"
            "v_add_u32 v89, v89, v87\n"
            "v_mov_b64 v[136:137], v[138:139]\n"
            "Lt0_%=:\n"
            "v_lshl_add_u32 v128, v136, 16, v89\n"
            "v_add_u32 v89, v89, v136\n"
            "v_lshl_add_u32 v130, v137, 16, v89\n"
            "v_mov_b32 v129, s90\n"
            "v_mov_b32 v131, s90\n"
            "buffer_store_dwordx4 v[128:131], v192, s[20:23], s36 offen sc1\n"
            "s_waitcnt vmcnt(4)\n"
            "s_branch Lt1_%=\n"
            "Lnt_%=:\n"
            "s_waitcnt vmcnt(3)\n"
            "Lt1_%=:\n"
            "v_mov_b32 v56, v42\n"
            "v_mov_b32 v58, v43\n"
            "v_mov_b32 v60, v44\n"
            "v_mov_b32 v62, v45\n"
            "v_mov_b32 v64, v46\n"
            "v_mov_b32 v66, v47\n"
            "s_waitcnt lgkmcnt(0)\n"
            "v_lshl_add_u32 v74, v71, 4, v74\n"
            "v_lshl_add_u32 v75, v72, 4, v75\n"
            "v_lshl_add_u32 v76, v73, 4, v76\n"
            "s_mov_b64 exec, s[72:73]\n"
            "ds_write_b128 v74, v[56:59]\n"
            "s_mov_b64 exec, s[74:75]\n"
            "ds_write_b128 v75, v[60:63]\n"
            "s_mov_b64 exec, s[76:77]\n"
            "ds_write_b128 v76, v[64:67]\n"
            "s_mov_b64 exec, -1\n"
            "s_cmp_lt_u32 s3, 4\n"
            "s_waitcnt lgkmcnt(0)\n"
            "s_barrier\n"
            "s_cbranch_scc1 Lcol_%=\n"
            "ds_read_b128 v[56:59], v95 offset:0\n"
            "ds_read_b128 v[60:63], v95 offset:4096\n"
            "ds_read_b128 v[64:67], v95 offset:8192\n"
            "ds_read_b128 v[68:71], v95 offset:12288\n"
            "ds_read_b128 v[72:75], v95 offset:16384\n"
            "s_movk_i32 s72, 4096\n"
            "s_movk_i32 s73, 8192\n"
            "s_movk_i32 s74, 12288\n"
            "s_movk_i32 s75, 16384\n"
            "s_waitcnt lgkmcnt(4)\n"
            "buffer_store_dwordx4 v[56:59], v95, s[4:7], 0 offen sc1\n"
            "s_waitcnt lgkmcnt(3)\n"
            "buffer_store_dwordx4 v[60:63], v95, s[4:7], s72 offen sc1\n"
            "s_waitcnt lgkmcnt(2)\n"
            "buffer_store_dwordx4 v[64:67], v95, s[4:7], s73 offen sc1\n"
            "s_waitcnt lgkmcnt(1)\n"
            "buffer_store_dwordx4 v[68:71], v95, s[4:7], s74 offen sc1\n"
            "s_waitcnt lgkmcnt(0)\n"
            "buffer_store_dwordx4 v[72:75], v95, s[4:7], s75 offen sc1\n"
            "s_setprio 2\n"
            "s_lshl_b32 s68, s3, 11\n"
            "s_sub_u32 s68, s68, 8192\n"
            "v_lshl_add_u32 v81, v195, 9, s68\n"
            "v_add_u32 v81, v81, v214\n"
            "s_movk_i32 s76, 0x6000\n"
            "buffer_load_dwordx4 v[128:131], v81, s[44:47], 0 offen\n"
            "buffer_load_dwordx4 v[132:135], v81, s[44:47], 0 offen offset:256\n"
            "buffer_load_dwordx4 v[136:139], v81, s[44:47], s73 offen\n"
            "buffer_load_dwordx4 v[140:143], v81, s[44:47], s73 offen offset:256\n"
            "buffer_load_dwordx4 v[144:147], v81, s[44:47], s75 offen\n"
            "buffer_load_dwordx4 v[148:151], v81, s[44:47], s75 offen offset:256\n"
            "buffer_load_dwordx4 v[152:155], v81, s[44:47], s76 offen\n"
            "buffer_load_dwordx4 v[156:159], v81, s[44:47], s76 offen offset:256\n"
            "v_lshlrev_b32 v81, 4, v95\n"
            "v_and_b32 v81, 0x1800, v81\n"
            "v_lshrrev_b32 v82, 3, v95\n"
            "v_and_b32 v82, 0x100, v82\n"
            "v_lshlrev_b32 v83, 1, v95\n"
            "v_and_b32 v83, 0xe0, v83\n"
            "v_lshrrev_b32 v84, 7, v95\n"
            "v_and_b32 v84, 12, v84\n"
            "v_or3_b32 v81, v81, v82, v83\n"
            "v_add_u32 v84, 113408, v84\n"
            "v_add_u32 v81, v81, v84\n"
            "s_mov_b32 s78, 0xbfb8aa3b\n"
            "s_waitcnt vmcnt(14)\n"
            "v_lshlrev_b32 v160, 16, v48\n"
            "v_lshlrev_b32 v161, 16, v50\n"
            "v_mul_f32 v160, s78, v160\n"
            "v_mul_f32 v161, s78, v161\n"
            "v_cvt_pk_bf16_f32 v164, v160, v161\n"
            "v_lshlrev_b32 v162, 16, v164\n"
            "v_and_b32 v163, 0xffff0000, v164\n"
            "v_sub_f32 v160, v160, v162\n"
            "v_sub_f32 v161, v161, v163\n"
            "v_cvt_pk_bf16_f32 v165, v160, v161\n"
            "ds_write_b32 v81, v164 offset:0\n"
            "ds_write_b32 v81, v165 offset:512\n"
            "v_and_b32 v160, 0xffff0000, v48\n"
            "v_and_b32 v161, 0xffff0000, v50\n"
            "v_mul_f32 v160, s78, v160\n"
            "v_mul_f32 v161, s78, v161\n"
            "v_cvt_pk_bf16_f32 v166, v160, v161\n"
            "v_lshlrev_b32 v162, 16, v166\n"
            "v_and_b32 v163, 0xffff0000, v166\n"
            "v_sub_f32 v160, v160, v162\n"
            "v_sub_f32 v161, v161, v163\n"
            "v_cvt_pk_bf16_f32 v167, v160, v161\n"
            "ds_write_b32 v81, v166 offset:1024\n"
            "ds_write_b32 v81, v167 offset:1536\n"
            "v_lshlrev_b32 v160, 16, v49\n"
            "v_lshlrev_b32 v161, 16, v51\n"
            "v_mul_f32 v160, s78, v160\n"
            "v_mul_f32 v161, s78, v161\n"
            "v_cvt_pk_bf16_f32 v168, v160, v161\n"
            "v_lshlrev_b32 v162, 16, v168\n"
            "v_and_b32 v163, 0xffff0000, v168\n"
            "v_sub_f32 v160, v160, v162\n"
            "v_sub_f32 v161, v161, v163\n"
            "v_cvt_pk_bf16_f32 v169, v160, v161\n"
            "ds_write_b32 v81, v168 offset:16\n"
            "ds_write_b32 v81, v169 offset:528\n"
            "v_and_b32 v160, 0xffff0000, v49\n"
            "v_and_b32 v161, 0xffff0000, v51\n"
            "v_mul_f32 v160, s78, v160\n"
            "v_mul_f32 v161, s78, v161\n"
            "v_cvt_pk_bf16_f32 v170, v160, v161\n"
            "v_lshlrev_b32 v162, 16, v170\n"
            "v_and_b32 v163, 0xffff0000, v170\n"
            "v_sub_f32 v160, v160, v162\n"
            "v_sub_f32 v161, v161, v163\n"
            "v_cvt_pk_bf16_f32 v171, v160, v161\n"
            "ds_write_b32 v81, v170 offset:1040\n"
            "ds_write_b32 v81, v171 offset:1552\n"
            "s_cmp_lg_u32 s3, 4\n"
            "s_cbranch_scc1 Lncg_%=\n"
            "v_lshlrev_b32 v81, 2, v243\n"
            "s_waitcnt vmcnt(14)\n"
            "ds_write_b32 v81, v52 offset:61440\n"
            "v_lshrrev_b32 v81, 1, v243\n"
            "v_and_b32 v81, 6, v81\n"
            "v_lshl_add_u32 v81, v249, 5, v81\n"
            "ds_read_u16 v160, v81 offset:61440\n"
            "ds_read_u16 v161, v81 offset:61448\n"
            "ds_read_u16 v162, v81 offset:61456\n"
            "ds_read_u16 v163, v81 offset:61464\n"
            "s_mov_b32 s78, 0xbf317218\n"
            "s_waitcnt lgkmcnt(0)\n"
            "v_lshlrev_b32 v160, 16, v160\n"
            "v_lshlrev_b32 v161, 16, v161\n"
            "v_lshlrev_b32 v162, 16, v162\n"
            "v_lshlrev_b32 v163, 16, v163\n"
            "v_mul_f32 v160, s78, v160\n"
            "v_mul_f32 v161, s78, v161\n"
            "v_mul_f32 v162, s78, v162\n"
            "v_mul_f32 v163, s78, v163\n"
            "v_cvt_pk_bf16_f32 v168, v160, v161\n"
            "v_cvt_pk_bf16_f32 v169, v162, v163\n"
            "v_lshlrev_b32 v164, 16, v168\n"
            "v_and_b32 v165, 0xffff0000, v168\n"
            "v_lshlrev_b32 v166, 16, v169\n"
            "v_and_b32 v167, 0xffff0000, v169\n"
            "v_sub_f32 v160, v160, v164\n"
            "v_sub_f32 v161, v161, v165\n"
            "v_sub_f32 v162, v162, v166\n"
            "v_sub_f32 v163, v163, v167\n"
            "v_cvt_pk_bf16_f32 v170, v160, v161\n"
            "v_cvt_pk_bf16_f32 v171, v162, v163\n"
            "ds_write_b128 v192, v[168:171] offset:60416\n"
            "Lncg_%=:\n"
            "s_mov_b64 exec, 1\n"
            "ds_add_u32 v219, v209 offset:64\n"
            "s_mov_b64 exec, -1\n"
            "s_mul_i32 s68, s3, 27\n"
            "s_sub_u32 s68, s68, 108\n"
            "v_lshrrev_b32 v81, 1, v243\n"
            "v_add_u32 v81, s68, v81\n"
            "v_min_u32 v81, 79, v81\n"
            "v_lshlrev_b32 v180, 2, v81\n"
            "v_cmp_lt_u32 vcc, 39, v81\n"
            "v_and_b32 v82, 1, v243\n"
            "v_mul_u32_u24 v181, 80, v82\n"
            "v_add_u32 v82, 160, v181\n"
            "v_cndmask_b32 v181, v181, v82, vcc\n"
            "v_cndmask_b32 v82, 1, 0, vcc\n"
            "v_xor_b32 v82, s8, v82\n"
            "v_lshlrev_b32 v182, 8, v82\n"
            "v_mul_u32_u24 v183, 704, v81\n"
            "v_add_u32 v183, 4096, v183\n"
            "v_cmp_gt_u32 vcc, s11, v81\n"
            "v_add_lshl_u32 v184, v81, s10, 10\n"
            "v_bfrev_b32 v82, 1\n"
            "v_cndmask_b32 v184, v82, v184, vcc\n"
            "s_branch Lhpa_%=\n"
            "Lhp_%=:\n"
            "s_sleep 1\n"
            "Lhpa_%=:\n"
            "ds_read_b32 v220, v219 offset:64\n"
            "s_waitcnt lgkmcnt(0)\n"
            "v_readfirstlane_b32 s68, v220\n"
            "s_cmp_lt_u32 s68, 4\n"
            "s_cbranch_scc1 Lhp_%=\n"
            "v_add_u32 v81, 113408, v192\n"
            "ds_read_b128 v[96:99], v81 offset:0\n"
            "ds_read_b128 v[100:103], v81 offset:1024\n"
            "ds_read_b128 v[104:107], v81 offset:2048\n"
            "ds_read_b128 v[108:111], v81 offset:3072\n"
            "ds_read_b128 v[112:115], v81 offset:4096\n"
            "ds_read_b128 v[116:119], v81 offset:5120\n"
            "ds_read_b128 v[120:123], v81 offset:6144\n"
            "ds_read_b128 v[124:127], v81 offset:7168\n"
            "ds_read_b128 v[200:203], v192 offset:60416\n"
            "v_lshrrev_b32 v82, 2, v196\n"
            "s_lshl_b32 s68, s3, 10\n"
            "s_add_u32 s68, s68, 117760\n"
            "v_lshl_add_u32 v82, v82, 13, s68\n"
            "v_lshl_add_u32 v82, v195, 8, v82\n"
            "v_lshl_add_u32 v82, v249, 6, v82\n"
            "s_mov_b32 s78, 0x05040100\n"
            "s_mov_b32 s79, 0x07060302\n"
            "s_waitcnt vmcnt(0)\n"
            "v_perm_b32 v160, v132, v128, s78\n"
            "v_perm_b32 v161, v140, v136, s78\n"
            "v_perm_b32 v162, v148, v144, s78\n"
            "v_perm_b32 v163, v156, v152, s78\n"
            "ds_write_b128 v82, v[160:163] offset:0\n"
            "v_perm_b32 v164, v132, v128, s79\n"
            "v_perm_b32 v165, v140, v136, s79\n"
            "v_perm_b32 v166, v148, v144, s79\n"
            "v_perm_b32 v167, v156, v152, s79\n"
            "ds_write_b128 v82, v[164:167] offset:4096\n"
            "v_perm_b32 v168, v133, v129, s78\n"
            "v_perm_b32 v169, v141, v137, s78\n"
            "v_perm_b32 v170, v149, v145, s78\n"
            "v_perm_b32 v171, v157, v153, s78\n"
            "ds_write_b128 v82, v[168:171] offset:16\n"
            "v_perm_b32 v172, v133, v129, s79\n"
            "v_perm_b32 v173, v141, v137, s79\n"
            "v_perm_b32 v174, v149, v145, s79\n"
            "v_perm_b32 v175, v157, v153, s79\n"
            "ds_write_b128 v82, v[172:175] offset:4112\n"
            "v_perm_b32 v160, v134, v130, s78\n"
            "v_perm_b32 v161, v142, v138, s78\n"
            "v_perm_b32 v162, v150, v146, s78\n"
            "v_perm_b32 v163, v158, v154, s78\n"
            "ds_write_b128 v82, v[160:163] offset:32\n"
            "v_perm_b32 v164, v134, v130, s79\n"
            "v_perm_b32 v165, v142, v138, s79\n"
            "v_perm_b32 v166, v150, v146, s79\n"
            "v_perm_b32 v167, v158, v154, s79\n"
            "ds_write_b128 v82, v[164:167] offset:4128\n"
            "v_perm_b32 v168, v135, v131, s78\n"
            "v_perm_b32 v169, v143, v139, s78\n"
            "v_perm_b32 v170, v151, v147, s78\n"
            "v_perm_b32 v171, v159, v155, s78\n"
            "ds_write_b128 v82, v[168:171] offset:48\n"
            "v_perm_b32 v172, v135, v131, s79\n"
            "v_perm_b32 v173, v143, v139, s79\n"
            "v_perm_b32 v174, v151, v147, s79\n"
            "v_perm_b32 v175, v159, v155, s79\n"
            "ds_write_b128 v82, v[172:175] offset:4144\n"
            "s_cmp_lg_u32 s3, 7\n"
            "s_cbranch_scc1 Lsort_%=\n"
            "s_mov_b64 exec, 1\n"
            "ds_add_u32 v219, v209 offset:48\n"
            "s_mov_b64 exec, -1\n"
            "s_branch Lria_%=\n"
            "Lri_%=:\n"
            "s_sleep 1\n"
            "Lria_%=:\n"
            "ds_read_b32 v220, v219 offset:60\n"
            "s_waitcnt lgkmcnt(0)\n"
            "v_readfirstlane_b32 s68, v220\n"
            "s_cmp_lt_u32 s68, 4\n"
            "s_cbranch_scc1 Lri_%=\n"
            "s_add_u32 s68, s54, 24576\n"
            "s_nop 1\n"
            "buffer_load_dword v230, v193, s[24:27], s68 offen\n"
            "s_add_u32 s68, s54, 32768\n"
            "buffer_load_dword v230, v193, s[24:27], s68 offen\n"
            "s_add_u32 s68, s69, 0\n"
            "buffer_load_dword v230, v193, s[28:31], s68 offen\n"
            "s_add_u32 s68, s69, 1048576\n"
            "buffer_load_dword v230, v193, s[28:31], s68 offen\n"
            "s_add_u32 s68, s69, 2097152\n"
            "buffer_load_dword v230, v193, s[28:31], s68 offen\n"
            "s_branch Lw7a_%=\n"
            "Lw7_%=:\n"
            "s_sleep 1\n"
            "Lw7a_%=:\n"
            "ds_read_b32 v220, v219 offset:44\n"
            "s_waitcnt lgkmcnt(0)\n"
            "v_readfirstlane_b32 s68, v220\n"
            "s_cmp_lt_u32 s68, 4\n"
            "s_cbranch_scc1 Lw7_%=\n"
            "s_setprio 3\n"
            "s_branch Lmain_%=\n"
            "Lsort_%=:\n"
            "s_branch Lc3a_%=\n"
            "Lc3_%=:\n"
            "s_sleep 1\n"
            "Lc3a_%=:\n"
            "ds_read_b32 v220, v219 offset:44\n"
            "s_waitcnt lgkmcnt(0)\n"
            "v_readfirstlane_b32 s68, v220\n"
            "s_cmp_lt_u32 s68, 3\n"
            "s_cbranch_scc1 Lc3_%=\n"
            "s_setprio 3\n"
            "Lc4_%=:\n"
            "ds_read_b32 v220, v219 offset:44\n"
            "s_waitcnt lgkmcnt(0)\n"
            "v_readfirstlane_b32 s68, v220\n"
            "s_cmp_lt_u32 s68, 4\n"
            "s_cbranch_scc1 Lc4_%=\n"
            "ds_read_b32 v160, v180 offset:96\n"
            "ds_read_b128 v[128:131], v181 offset:96\n"
            "ds_read_b128 v[132:135], v181 offset:112\n"
            "ds_read_b128 v[136:139], v181 offset:128\n"
            "ds_read_b128 v[140:143], v181 offset:144\n"
            "ds_read_b128 v[144:147], v181 offset:160\n"
            "s_waitcnt lgkmcnt(4)\n"
            "v_sub_u32 v128, v160, v128\n"
            "v_sub_u32 v129, v160, v129\n"
            "v_sub_u32 v130, v160, v130\n"
            "v_sub_u32 v131, v160, v131\n"
            "v_lshrrev_b32 v161, 31, v128\n"
            "v_lshrrev_b32 v162, 31, v129\n"
            "v_alignbit_b32 v161, v161, v130, 31\n"
            "v_alignbit_b32 v162, v162, v131, 31\n"
            "v_lshrrev_b32 v163, 7, v160\n"
            "s_waitcnt lgkmcnt(3)\n"
            "v_sub_u32 v132, v160, v132\n"
            "v_sub_u32 v133, v160, v133\n"
            "v_sub_u32 v134, v160, v134\n"
            "v_sub_u32 v135, v160, v135\n"
            "v_alignbit_b32 v161, v161, v132, 31\n"
            "v_alignbit_b32 v162, v162, v133, 31\n"
            "v_alignbit_b32 v161, v161, v134, 31\n"
            "v_alignbit_b32 v162, v162, v135, 31\n"
            "v_cmp_lt_u32 vcc, 44, v163\n"
            "v_max_u32 v164, 8, v163\n"
            "s_waitcnt lgkmcnt(2)\n"
            "v_sub_u32 v136, v160, v136\n"
            "v_sub_u32 v137, v160, v137\n"
            "v_sub_u32 v138, v160, v138\n"
            "v_sub_u32 v139, v160, v139\n"
            "v_alignbit_b32 v161, v161, v136, 31\n"
            "v_alignbit_b32 v162, v162, v137, 31\n"
            "v_alignbit_b32 v161, v161, v138, 31\n"
            "v_alignbit_b32 v162, v162, v139, 31\n"
            "v_mov_b32 v165, 255\n"
            "v_lshl_or_b32 v183, v163, 16, v183\n"
            "s_waitcnt lgkmcnt(1)\n"
            "v_sub_u32 v140, v160, v140\n"
            "v_sub_u32 v141, v160, v141\n"
            "v_sub_u32 v142, v160, v142\n"
            "v_sub_u32 v143, v160, v143\n"
            "v_alignbit_b32 v161, v161, v140, 31\n"
            "v_alignbit_b32 v162, v162, v141, 31\n"
            "v_alignbit_b32 v161, v161, v142, 31\n"
            "v_alignbit_b32 v162, v162, v143, 31\n"
            "v_cndmask_b32 v164, v164, v165, vcc\n"
            "s_waitcnt lgkmcnt(0)\n"
            "v_sub_u32 v144, v160, v144\n"
            "v_sub_u32 v145, v160, v145\n"
            "v_sub_u32 v146, v160, v146\n"
            "v_sub_u32 v147, v160, v147\n"
            "v_alignbit_b32 v161, v161, v144, 31\n"
            "v_alignbit_b32 v162, v162, v145, 31\n"
            "v_alignbit_b32 v161, v161, v146, 31\n"
            "v_alignbit_b32 v162, v162, v147, 31\n"
            "v_bcnt_u32_b32 v161, v161, 0\n"
            "v_bcnt_u32_b32 v161, v162, v161\n"
            "s_nop 1\n"
            "v_add_u32_dpp v161, v161, v161 quad_perm:[1,0,3,2] row_mask:0xf bank_mask:0xf\n"
            "v_lshl_add_u32 v182, v161, 2, v182\n"
            "ds_write2st64_b32 v182, v183, v164 offset0:10 offset1:8\n"
            "ds_write_b32 v182, v184 offset:3072\n"
            "s_mov_b64 exec, 1\n"
            "ds_add_u32 v219, v209 offset:48\n"
            "s_mov_b64 exec, -1\n"
            "s_lshl3_add_u32 s68, s2, s3\n"
            "s_cmp_lg_u32 s68, 4\n"
            "s_cbranch_scc1 Lmain_%=\n"
            "v_mov_b32 v160, s90\n"
            "s_mov_b64 exec, 1\n"
            "buffer_store_dword v160, off, s[20:23], 0 sc1\n"
            "s_mov_b64 exec, -1\n"
            "s_branch Lmain_%=\n"
            "Lcol_%=:\n"
            "s_lshl_b32 s38, s8, 7\n"
            "s_add_u32 s38, s38, s9\n"
            "s_bfe_u32 s68, s38, 0x10001\n"
            "s_lshl_b32 s68, s68, 10\n"
            "s_lshr_b32 s72, s38, 2\n"
            "s_lshl4_add_u32 s68, s72, s68\n"
            "s_and_b32 s72, s38, 1\n"
            "s_lshl3_add_u32 s68, s72, s68\n"
            "s_add_u32 s68, s68, 36864\n"
            "v_lshl_add_u32 v180, v95, 7, s68\n"
            "s_sleep 8\n"
            "buffer_load_dwordx2 v[142:143], v180, s[20:23], 0 offen sc1\n"
            "s_mul_i32 s17, s3, 1792\n"
            "s_add_u32 s17, s17, 74752\n"
            "v_lshl_add_u32 v181, v243, 2, s17\n"
            "s_mul_i32 s15, s3, 1310720\n"
            "s_add_u32 s15, s15, 561152\n"
            "s_add_u32 s16, s15, 4096\n"
            "s_mul_i32 s18, s3, 640\n"
            "s_add_u32 s18, s18, 71680\n"
            "s_lshl_b32 s19, s10, 2\n"
            "s_lshl_b32 s97, s50, 5\n"
            "s_mul_i32 s98, s97, s3\n"
            "s_lshl_b32 s98, s98, 6\n"
            "v_mul_u32_u24 v182, 704, v243\n"
            "v_add_u32 v182, 4096, v182\n"
            "v_add_u32 v184, 704, v182\n"
            "v_mul_u32_u24 v183, 704, v196\n"
            "v_add_u32 v183, 49152, v183\n"
            "v_add_u32 v185, 704, v183\n"
            "v_add_u32 v187, s18, v194\n"
            "v_lshl_add_u32 v188, v196, 3, s18\n"
            "s_mov_b32 s68, 16797696\n"
            "v_mul_lo_u32 v189, v243, s68\n"
            "v_mov_b32 v190, 128\n"
            "v_mov_b32 v191, 2.0\n"
            "v_mov_b32 v186, s98\n"
            "v_mov_b32 v197, v80\n"
            "s_branch Lcpa_%=\n"
            "Lcp_%=:\n"
            "s_sleep 1\n"
            "Lcpa_%=:\n"
            "ds_read_b32 v220, v219 offset:64\n"
            "s_waitcnt lgkmcnt(0)\n"
            "v_readfirstlane_b32 s68, v220\n"
            "s_cmp_lt_u32 s68, 4\n"
            "s_cbranch_scc1 Lcp_%=\n"
            "v_add_u32 v81, 113408, v192\n"
            "ds_read_b128 v[96:99], v81 offset:0\n"
            "ds_read_b128 v[100:103], v81 offset:1024\n"
            "ds_read_b128 v[104:107], v81 offset:2048\n"
            "ds_read_b128 v[108:111], v81 offset:3072\n"
            "ds_read_b128 v[112:115], v81 offset:4096\n"
            "ds_read_b128 v[116:119], v81 offset:5120\n"
            "ds_read_b128 v[120:123], v81 offset:6144\n"
            "ds_read_b128 v[124:127], v81 offset:7168\n"
            "ds_read_b128 v[200:203], v192 offset:60416\n"
            "s_mov_b32 s99, 0x400000\n"
            "Lpoll_%=:\n"
            "s_waitcnt vmcnt(0)\n"
            "v_cmp_ne_u32 vcc, s90, v143\n"
            "s_cbranch_vccnz Lpoll2_%=\n"
            "Lpoll3_%=:\n"
            "v_lshrrev_b32 v144, 18, v142\n"
            "v_and_b32 v146, 0xffff, v142\n"
            "v_cmp_ne_u32 s[86:87], 0, v144\n"
            "v_add_u32_dpp v145, v144, v144 row_shr:1 row_mask:0xf bank_mask:0xf bound_ctrl:1\n"
            "v_add_u32 v146, v146, v189\n"
            "v_cmp_lt_u32 s[88:89], 64, v144\n"
            "v_add_u32_dpp v145, v145, v145 row_shr:2 row_mask:0xf bank_mask:0xf bound_ctrl:1\n"
            "v_lshl_add_u32 v146, v144, 2, v146\n"
            "v_sub_u32 v147, s17, v144\n"
            "v_add_u32_dpp v145, v145, v145 row_shr:4 row_mask:0xf bank_mask:0xf bound_ctrl:1\n"
            "s_add_u32 s38, s17, -4\n"
            "s_nop 0\n"
            "v_add_u32_dpp v145, v145, v145 row_shr:8 row_mask:0xf bank_mask:0xf bound_ctrl:1\n"
            "s_nop 1\n"
            "v_add_u32_dpp v145, v145, v145 row_bcast:15 row_mask:0xa bank_mask:0xf\n"
            "s_nop 1\n"
            "v_add_u32_dpp v145, v145, v145 row_bcast:31 row_mask:0xc bank_mask:0xf\n"
            "v_cmp_ge_u32 vcc, 1792, v145\n"
            "v_readlane_b32 s37, v145, 63\n"
            "v_add_u32 v147, v147, v145\n"
            "v_add_u32 v148, s38, v145\n"
            "v_lshlrev_b32 v145, 2, v145\n"
            "v_and_b32 v148, -64, v148\n"
            "v_sub_u32 v146, v146, v145\n"
            "v_max_u32 v148, v148, v147\n"
            "s_and_b64 exec, vcc, s[86:87]\n"
            "ds_write_b32 v147, v146\n"
            "ds_write_b32 v148, v146\n"
            "s_mov_b64 exec, -1\n"
            "ds_read2st64_b32 v[74:75], v181 offset0:0 offset1:1\n"
            "ds_read2st64_b32 v[76:77], v181 offset0:2 offset1:3\n"
            "ds_read2st64_b32 v[78:79], v181 offset0:4 offset1:5\n"
            "ds_read_b32 v80, v181 offset:1536\n"
            "s_cmp_gt_u32 s37, 1792\n"
            "s_cbranch_scc1 Lover_%=\n"
            "Lover2_%=:\n"
            "s_lshr_b32 s37, s37, 2\n"
            "v_cmp_gt_i32 s[72:73], s37, v243\n"
            "s_sub_i32 s38, s37, 64\n"
            "v_cmp_gt_i32 s[74:75], s38, v243\n"
            "s_sub_i32 s38, s37, 128\n"
            "v_cmp_gt_i32 s[76:77], s38, v243\n"
            "s_sub_i32 s38, s37, 192\n"
            "v_cmp_gt_i32 s[78:79], s38, v243\n"
            "s_sub_i32 s38, s37, 256\n"
            "v_cmp_gt_i32 s[80:81], s38, v243\n"
            "s_sub_i32 s38, s37, 320\n"
            "v_cmp_gt_i32 s[82:83], s38, v243\n"
            "s_sub_i32 s38, s37, 384\n"
            "v_cmp_gt_i32 s[84:85], s38, v243\n"
            "v_cndmask_b32 v81, v191, v192, s[72:73]\n"
            "v_cndmask_b32 v82, v191, v192, s[74:75]\n"
            "v_cndmask_b32 v83, v191, v192, s[76:77]\n"
            "v_cndmask_b32 v84, v191, v192, s[78:79]\n"
            "v_cndmask_b32 v85, v191, v192, s[80:81]\n"
            "v_cndmask_b32 v86, v191, v192, s[82:83]\n"
            "v_cndmask_b32 v87, v191, v192, s[84:85]\n"
            "s_cmp_lg_u64 s[88:89], 0\n"
            "s_waitcnt lgkmcnt(0)\n"
            "v_max_u32_dpp v74, v74, v74 row_shr:1 row_mask:0xf bank_mask:0xf bound_ctrl:1\n"
            "v_max_u32_dpp v75, v75, v75 row_shr:1 row_mask:0xf bank_mask:0xf bound_ctrl:1\n"
            "v_max_u32_dpp v76, v76, v76 row_shr:1 row_mask:0xf bank_mask:0xf bound_ctrl:1\n"
            "v_max_u32_dpp v77, v77, v77 row_shr:1 row_mask:0xf bank_mask:0xf bound_ctrl:1\n"
            "v_max_u32_dpp v78, v78, v78 row_shr:1 row_mask:0xf bank_mask:0xf bound_ctrl:1\n"
            "v_max_u32_dpp v79, v79, v79 row_shr:1 row_mask:0xf bank_mask:0xf bound_ctrl:1\n"
            "v_max_u32_dpp v80, v80, v80 row_shr:1 row_mask:0xf bank_mask:0xf bound_ctrl:1\n"
            "v_max_u32_dpp v74, v74, v74 row_shr:2 row_mask:0xf bank_mask:0xf bound_ctrl:1\n"
            "v_max_u32_dpp v75, v75, v75 row_shr:2 row_mask:0xf bank_mask:0xf bound_ctrl:1\n"
            "v_max_u32_dpp v76, v76, v76 row_shr:2 row_mask:0xf bank_mask:0xf bound_ctrl:1\n"
            "v_max_u32_dpp v77, v77, v77 row_shr:2 row_mask:0xf bank_mask:0xf bound_ctrl:1\n"
            "v_max_u32_dpp v78, v78, v78 row_shr:2 row_mask:0xf bank_mask:0xf bound_ctrl:1\n"
            "v_max_u32_dpp v79, v79, v79 row_shr:2 row_mask:0xf bank_mask:0xf bound_ctrl:1\n"
            "v_max_u32_dpp v80, v80, v80 row_shr:2 row_mask:0xf bank_mask:0xf bound_ctrl:1\n"
            "v_max_u32_dpp v74, v74, v74 row_shr:4 row_mask:0xf bank_mask:0xf bound_ctrl:1\n"
            "v_max_u32_dpp v75, v75, v75 row_shr:4 row_mask:0xf bank_mask:0xf bound_ctrl:1\n"
            "v_max_u32_dpp v76, v76, v76 row_shr:4 row_mask:0xf bank_mask:0xf bound_ctrl:1\n"
            "v_max_u32_dpp v77, v77, v77 row_shr:4 row_mask:0xf bank_mask:0xf bound_ctrl:1\n"
            "v_max_u32_dpp v78, v78, v78 row_shr:4 row_mask:0xf bank_mask:0xf bound_ctrl:1\n"
            "v_max_u32_dpp v79, v79, v79 row_shr:4 row_mask:0xf bank_mask:0xf bound_ctrl:1\n"
            "v_max_u32_dpp v80, v80, v80 row_shr:4 row_mask:0xf bank_mask:0xf bound_ctrl:1\n"
            "v_max_u32_dpp v74, v74, v74 row_shr:8 row_mask:0xf bank_mask:0xf bound_ctrl:1\n"
            "v_max_u32_dpp v75, v75, v75 row_shr:8 row_mask:0xf bank_mask:0xf bound_ctrl:1\n"
            "v_max_u32_dpp v76, v76, v76 row_shr:8 row_mask:0xf bank_mask:0xf bound_ctrl:1\n"
            "v_max_u32_dpp v77, v77, v77 row_shr:8 row_mask:0xf bank_mask:0xf bound_ctrl:1\n"
            "v_max_u32_dpp v78, v78, v78 row_shr:8 row_mask:0xf bank_mask:0xf bound_ctrl:1\n"
            "v_max_u32_dpp v79, v79, v79 row_shr:8 row_mask:0xf bank_mask:0xf bound_ctrl:1\n"
            "v_max_u32_dpp v80, v80, v80 row_shr:8 row_mask:0xf bank_mask:0xf bound_ctrl:1\n"
            "s_cbranch_scc1 Llng_%=\n"
            "Llng2_%=:\n"
            "v_mad_u32_u24 v81, v74, 1, v81\n"
            "buffer_load_dwordx4 v[34:37], v81, s[20:23], s15 offen offset:0 sc1\n"
            "v_mad_u32_u24 v82, v75, 1, v82\n"
            "buffer_load_dwordx4 v[40:43], v82, s[20:23], s15 offen offset:1024 sc1\n"
            "v_mad_u32_u24 v83, v76, 1, v83\n"
            "buffer_load_dwordx4 v[46:49], v83, s[20:23], s15 offen offset:2048 sc1\n"
            "v_mad_u32_u24 v84, v77, 1, v84\n"
            "buffer_load_dwordx4 v[52:55], v84, s[20:23], s15 offen offset:3072 sc1\n"
            "v_mad_u32_u24 v85, v78, 1, v85\n"
            "buffer_load_dwordx4 v[58:61], v85, s[20:23], s16 offen offset:0 sc1\n"
            "v_mad_u32_u24 v86, v79, 1, v86\n"
            "buffer_load_dwordx4 v[64:67], v86, s[20:23], s16 offen offset:1024 sc1\n"
            "v_mad_u32_u24 v87, v80, 1, v87\n"
            "buffer_load_dwordx4 v[70:73], v87, s[20:23], s16 offen offset:2048 sc1\n"
            "s_mov_b64 exec, 1\n"
            "ds_add_u32 v219, v209 offset:60\n"
            "s_mov_b64 exec, -1\n"
            "v_lshrrev_b32 v88, 24, v74\n"
            "v_mad_u32_u24 v88, v88, s97, v186\n"
            "v_lshrrev_b32 v89, 24, v75\n"
            "v_mad_u32_u24 v89, v89, s97, v186\n"
            "v_lshrrev_b32 v90, 24, v76\n"
            "v_mad_u32_u24 v90, v90, s97, v186\n"
            "v_lshrrev_b32 v91, 24, v77\n"
            "v_mad_u32_u24 v91, v91, s97, v186\n"
            "v_lshrrev_b32 v92, 24, v78\n"
            "v_mad_u32_u24 v92, v92, s97, v186\n"
            "v_lshrrev_b32 v93, 24, v79\n"
            "v_mad_u32_u24 v93, v93, s97, v186\n"
            "v_lshrrev_b32 v94, 24, v80\n"
            "v_mad_u32_u24 v94, v94, s97, v186\n"
            "s_movk_i32 s38, 0x3fff\n"
            "s_mov_b32 s36, 0xfffff\n"
            "s_waitcnt vmcnt(6)\n"
            "Lchk0_%=:\n"
            "v_xor_b32 v35, s93, v35\n"
            "v_xor_b32 v37, s94, v37\n"
            "v_cmp_lt_u32 vcc, s38, v35\n"
            "v_cmp_lt_u32 s[86:87], s36, v37\n"
            "v_subrev_u32 v135, s19, v37\n"
            "v_and_b32 v135, 0x1ff, v135\n"
            "s_or_b64 vcc, vcc, s[86:87]\n"
            "s_and_b64 vcc, vcc, s[72:73]\n"
            "s_cbranch_scc1 Lre0_%=\n"
            "Lok0_%=:\n"
            "v_lshlrev_b32 v144, 2, v135\n"
            "v_and_b32 v145, 0x1fc, v135\n"
            "s_mov_b64 exec, s[72:73]\n"
            "ds_add_rtn_u32 v128, v144, v197 offset:512\n"
            "ds_add_u32 v145, v190 offset:96\n"
            "s_mov_b64 exec, -1\n"
            "s_waitcnt vmcnt(5)\n"
            "Lchk1_%=:\n"
            "v_xor_b32 v41, s93, v41\n"
            "v_xor_b32 v43, s94, v43\n"
            "v_cmp_lt_u32 vcc, s38, v41\n"
            "v_cmp_lt_u32 s[86:87], s36, v43\n"
            "v_subrev_u32 v136, s19, v43\n"
            "v_and_b32 v136, 0x1ff, v136\n"
            "s_or_b64 vcc, vcc, s[86:87]\n"
            "s_and_b64 vcc, vcc, s[74:75]\n"
            "s_cbranch_scc1 Lre1_%=\n"
            "Lok1_%=:\n"
            "v_lshlrev_b32 v144, 2, v136\n"
            "v_and_b32 v145, 0x1fc, v136\n"
            "s_mov_b64 exec, s[74:75]\n"
            "ds_add_rtn_u32 v129, v144, v197 offset:512\n"
            "ds_add_u32 v145, v190 offset:96\n"
            "s_mov_b64 exec, -1\n"
            "s_waitcnt vmcnt(4)\n"
            "Lchk2_%=:\n"
            "v_xor_b32 v47, s93, v47\n"
            "v_xor_b32 v49, s94, v49\n"
            "v_cmp_lt_u32 vcc, s38, v47\n"
            "v_cmp_lt_u32 s[86:87], s36, v49\n"
            "v_subrev_u32 v137, s19, v49\n"
            "v_and_b32 v137, 0x1ff, v137\n"
            "s_or_b64 vcc, vcc, s[86:87]\n"
            "s_and_b64 vcc, vcc, s[76:77]\n"
            "s_cbranch_scc1 Lre2_%=\n"
            "Lok2_%=:\n"
            "v_lshlrev_b32 v144, 2, v137\n"
            "v_and_b32 v145, 0x1fc, v137\n"
            "s_mov_b64 exec, s[76:77]\n"
            "ds_add_rtn_u32 v130, v144, v197 offset:512\n"
            "ds_add_u32 v145, v190 offset:96\n"
            "s_mov_b64 exec, -1\n"
            "s_waitcnt vmcnt(3)\n"
            "Lchk3_%=:\n"
            "v_xor_b32 v53, s93, v53\n"
            "v_xor_b32 v55, s94, v55\n"
            "v_cmp_lt_u32 vcc, s38, v53\n"
            "v_cmp_lt_u32 s[86:87], s36, v55\n"
            "v_subrev_u32 v138, s19, v55\n"
            "v_and_b32 v138, 0x1ff, v138\n"
            "s_or_b64 vcc, vcc, s[86:87]\n"
            "s_and_b64 vcc, vcc, s[78:79]\n"
            "s_cbranch_scc1 Lre3_%=\n"
            "Lok3_%=:\n"
            "v_lshlrev_b32 v144, 2, v138\n"
            "v_and_b32 v145, 0x1fc, v138\n"
            "s_mov_b64 exec, s[78:79]\n"
            "ds_add_rtn_u32 v131, v144, v197 offset:512\n"
            "ds_add_u32 v145, v190 offset:96\n"
            "s_mov_b64 exec, -1\n"
            "s_waitcnt vmcnt(2)\n"
            "Lchk4_%=:\n"
            "v_xor_b32 v59, s93, v59\n"
            "v_xor_b32 v61, s94, v61\n"
            "v_cmp_lt_u32 vcc, s38, v59\n"
            "v_cmp_lt_u32 s[86:87], s36, v61\n"
            "v_subrev_u32 v139, s19, v61\n"
            "v_and_b32 v139, 0x1ff, v139\n"
            "s_or_b64 vcc, vcc, s[86:87]\n"
            "s_and_b64 vcc, vcc, s[80:81]\n"
            "s_cbranch_scc1 Lre4_%=\n"
            "Lok4_%=:\n"
            "v_lshlrev_b32 v144, 2, v139\n"
            "v_and_b32 v145, 0x1fc, v139\n"
            "s_mov_b64 exec, s[80:81]\n"
            "ds_add_rtn_u32 v132, v144, v197 offset:512\n"
            "ds_add_u32 v145, v190 offset:96\n"
            "s_mov_b64 exec, -1\n"
            "s_waitcnt vmcnt(1)\n"
            "Lchk5_%=:\n"
            "v_xor_b32 v65, s93, v65\n"
            "v_xor_b32 v67, s94, v67\n"
            "v_cmp_lt_u32 vcc, s38, v65\n"
            "v_cmp_lt_u32 s[86:87], s36, v67\n"
            "v_subrev_u32 v140, s19, v67\n"
            "v_and_b32 v140, 0x1ff, v140\n"
            "s_or_b64 vcc, vcc, s[86:87]\n"
            "s_and_b64 vcc, vcc, s[82:83]\n"
            "s_cbranch_scc1 Lre5_%=\n"
            "Lok5_%=:\n"
            "v_lshlrev_b32 v144, 2, v140\n"
            "v_and_b32 v145, 0x1fc, v140\n"
            "s_mov_b64 exec, s[82:83]\n"
            "ds_add_rtn_u32 v133, v144, v197 offset:512\n"
            "ds_add_u32 v145, v190 offset:96\n"
            "s_mov_b64 exec, -1\n"
            "s_cmp_eq_u64 s[84:85], 0\n"
            "s_cbranch_scc1 Lr7_%=\n"
            "s_waitcnt vmcnt(0)\n"
            "Lchk6_%=:\n"
            "v_xor_b32 v71, s93, v71\n"
            "v_xor_b32 v73, s94, v73\n"
            "v_cmp_lt_u32 vcc, s38, v71\n"
            "v_cmp_lt_u32 s[86:87], s36, v73\n"
            "v_subrev_u32 v141, s19, v73\n"
            "v_and_b32 v141, 0x1ff, v141\n"
            "s_or_b64 vcc, vcc, s[86:87]\n"
            "s_and_b64 vcc, vcc, s[84:85]\n"
            "s_cbranch_scc1 Lre6_%=\n"
            "Lok6_%=:\n"
            "v_lshlrev_b32 v144, 2, v141\n"
            "v_and_b32 v145, 0x1fc, v141\n"
            "s_mov_b64 exec, s[84:85]\n"
            "ds_add_rtn_u32 v134, v144, v197 offset:512\n"
            "ds_add_u32 v145, v190 offset:96\n"
            "s_mov_b64 exec, -1\n"
            "Lr7_%=:\n"
            "s_mov_b64 exec, 1\n"
            "ds_add_u32 v219, v209 offset:44\n"
            "s_mov_b64 exec, -1\n"
            "v_lshlrev_b32 v32, 10, v35\n"
            "v_lshrrev_b32 v33, 9, v37\n"
            "v_mov_b32 v35, v36\n"
            "v_lshl_add_u32 v33, v33, 5, v88\n"
            "v_lshl_add_u32 v135, v135, 1, s18\n"
            "v_lshlrev_b32 v38, 10, v41\n"
            "v_lshrrev_b32 v39, 9, v43\n"
            "v_mov_b32 v41, v42\n"
            "v_lshl_add_u32 v39, v39, 5, v89\n"
            "v_lshl_add_u32 v136, v136, 1, s18\n"
            "v_lshlrev_b32 v44, 10, v47\n"
            "v_lshrrev_b32 v45, 9, v49\n"
            "v_mov_b32 v47, v48\n"
            "v_lshl_add_u32 v45, v45, 5, v90\n"
            "v_lshl_add_u32 v137, v137, 1, s18\n"
            "v_lshlrev_b32 v50, 10, v53\n"
            "v_lshrrev_b32 v51, 9, v55\n"
            "v_mov_b32 v53, v54\n"
            "v_lshl_add_u32 v51, v51, 5, v91\n"
            "v_lshl_add_u32 v138, v138, 1, s18\n"
            "v_lshlrev_b32 v56, 10, v59\n"
            "v_lshrrev_b32 v57, 9, v61\n"
            "v_mov_b32 v59, v60\n"
            "v_lshl_add_u32 v57, v57, 5, v92\n"
            "v_lshl_add_u32 v139, v139, 1, s18\n"
            "v_lshlrev_b32 v62, 10, v65\n"
            "v_lshrrev_b32 v63, 9, v67\n"
            "v_mov_b32 v65, v66\n"
            "v_lshl_add_u32 v63, v63, 5, v93\n"
            "v_lshl_add_u32 v140, v140, 1, s18\n"
            "v_lshlrev_b32 v68, 10, v71\n"
            "v_lshrrev_b32 v69, 9, v73\n"
            "v_mov_b32 v71, v72\n"
            "v_lshl_add_u32 v69, v69, 5, v94\n"
            "v_lshl_add_u32 v141, v141, 1, s18\n"
            "Lcc_%=:\n"
            "ds_read_b32 v220, v219 offset:44\n"
            "s_waitcnt lgkmcnt(0)\n"
            "v_readfirstlane_b32 s68, v220\n"
            "s_cmp_lt_u32 s68, 4\n"
            "s_cbranch_scc1 Lcc_%=\n"
            "ds_read_b128 v[144:147], v192 offset:512\n"
            "ds_read_b128 v[148:151], v214 offset:1536\n"
            "v_bfe_u32 v128, v128, s12, 8\n"
            "v_bfe_u32 v129, v129, s12, 8\n"
            "v_bfe_u32 v130, v130, s12, 8\n"
            "v_bfe_u32 v131, v131, s12, 8\n"
            "v_bfe_u32 v132, v132, s12, 8\n"
            "v_bfe_u32 v133, v133, s12, 8\n"
            "v_bfe_u32 v134, v134, s12, 8\n"
            "s_waitcnt lgkmcnt(1)\n"
            "v_dot4_u32_u8 v152, v144, s13, v182\n"
            "v_dot4_u32_u8 v182, v144, s39, v182\n"
            "v_dot4_u32_u8 v153, v145, s13, v182\n"
            "v_dot4_u32_u8 v182, v145, s39, v182\n"
            "v_dot4_u32_u8 v154, v146, s13, v182\n"
            "v_dot4_u32_u8 v182, v146, s39, v182\n"
            "v_dot4_u32_u8 v155, v147, s13, v182\n"
            "v_dot4_u32_u8 v182, v147, s39, v182\n"
            "s_waitcnt lgkmcnt(0)\n"
            "v_dot4_u32_u8 v156, v148, s13, v183\n"
            "v_dot4_u32_u8 v183, v148, s39, v183\n"
            "v_dot4_u32_u8 v157, v149, s13, v183\n"
            "v_dot4_u32_u8 v183, v149, s39, v183\n"
            "v_lshl_or_b32 v152, v153, 16, v152\n"
            "v_lshl_or_b32 v153, v155, 16, v154\n"
            "ds_write_b64 v187, v[152:153]\n"
            "v_dot4_u32_u8 v158, v150, s13, v183\n"
            "v_dot4_u32_u8 v183, v150, s39, v183\n"
            "v_dot4_u32_u8 v159, v151, s13, v183\n"
            "v_dot4_u32_u8 v183, v151, s39, v183\n"
            "v_lshl_or_b32 v156, v157, 16, v156\n"
            "v_cmp_gt_u32 vcc, v182, v184\n"
            "v_lshl_or_b32 v157, v159, 16, v158\n"
            "ds_write_b64 v188, v[156:157] offset:512\n"
            "ds_read_u16 v135, v135\n"
            "ds_read_u16 v136, v136\n"
            "ds_read_u16 v137, v137\n"
            "ds_read_u16 v138, v138\n"
            "ds_read_u16 v139, v139\n"
            "ds_read_u16 v140, v140\n"
            "ds_read_u16 v141, v141\n"
            "v_cmp_gt_u32 s[86:87], v183, v185\n"
            "s_or_b64 vcc, vcc, s[86:87]\n"
            "s_cbranch_scc1 Lbig_%=\n"
            "s_waitcnt lgkmcnt(6)\n"
            "v_lshl_add_u32 v135, v128, 4, v135\n"
            "s_mov_b64 exec, s[72:73]\n"
            "ds_write_b128 v135, v[32:35]\n"
            "s_waitcnt lgkmcnt(6)\n"
            "v_lshl_add_u32 v136, v129, 4, v136\n"
            "s_mov_b64 exec, s[74:75]\n"
            "ds_write_b128 v136, v[38:41]\n"
            "s_waitcnt lgkmcnt(6)\n"
            "v_lshl_add_u32 v137, v130, 4, v137\n"
            "s_mov_b64 exec, s[76:77]\n"
            "ds_write_b128 v137, v[44:47]\n"
            "s_waitcnt lgkmcnt(6)\n"
            "v_lshl_add_u32 v138, v131, 4, v138\n"
            "s_mov_b64 exec, s[78:79]\n"
            "ds_write_b128 v138, v[50:53]\n"
            "s_waitcnt lgkmcnt(6)\n"
            "v_lshl_add_u32 v139, v132, 4, v139\n"
            "s_mov_b64 exec, s[80:81]\n"
            "ds_write_b128 v139, v[56:59]\n"
            "s_waitcnt lgkmcnt(6)\n"
            "v_lshl_add_u32 v140, v133, 4, v140\n"
            "s_mov_b64 exec, s[82:83]\n"
            "ds_write_b128 v140, v[62:65]\n"
            "s_waitcnt lgkmcnt(6)\n"
            "v_lshl_add_u32 v141, v134, 4, v141\n"
            "s_mov_b64 exec, s[84:85]\n"
            "ds_write_b128 v141, v[68:71]\n"
            "Lbig_%=:\n"
            "s_mov_b64 exec, 1\n"
            "ds_add_u32 v219, v209 offset:56\n"
            "s_mov_b64 exec, -1\n"
            "Lmain_%=:\n"
            "Ls_%=:\n"
            "ds_read_b32 v220, v219 offset:48\n"
            "s_waitcnt lgkmcnt(0)\n"
            "v_readfirstlane_b32 s68, v220\n"
            "s_cmp_lt_u32 s68, 4\n"
            "s_cbranch_scc1 Ls_%=\n"
            "ds_read_u16 v132, v248 offset:512\n"
            "ds_read_u16 v133, v248 offset:514\n"
            "ds_read_u16 v134, v244 offset:512\n"
            "ds_read_u16 v135, v244 offset:514\n"
            "ds_read_b32 v128, v240\n"
            "ds_read_b32 v129, v241\n"
            "ds_read_b32 v130, v242\n"
            "ds_read_b32 v131, v219\n"
            "ds_read_b32 v217, v244 offset:1024\n"
            "ds_read_b32 v215, v245 offset:1024\n"
            "ds_read_b32 v218, v246 offset:1024\n"
            "v_add_u32 v136, 4, v249\n"
            "s_waitcnt lgkmcnt(7)\n"
            "v_cmp_gt_u32 vcc, v133, v249\n"
            "v_cmp_gt_u32 s[70:71], v135, v249\n"
            "v_cmp_gt_u32 s[86:87], v133, v136\n"
            "v_cmp_gt_u32 s[88:89], v135, v136\n"
            "v_add_u32 v132, v132, v250\n"
            "v_add_u32 v134, v134, v250\n"
            "v_cndmask_b32 v232, v251, v132, vcc\n"
            "v_cndmask_b32 v221, v251, v134, s[70:71]\n"
            "v_add_u32 v132, 64, v132\n"
            "v_add_u32 v134, 64, v134\n"
            "v_cndmask_b32 v233, v251, v132, s[86:87]\n"
            "v_cndmask_b32 v210, v251, v134, s[88:89]\n"
            "Lw_%=:\n"
            "ds_read_b32 v220, v219 offset:56\n"
            "s_waitcnt lgkmcnt(0)\n"
            "v_readfirstlane_b32 s68, v220\n"
            "s_cmp_lt_u32 s68, 4\n"
            "s_cbranch_scc1 Lw_%=\n"
            "ds_read_b32 v232, v232 offset:4\n"
            "ds_read_b128 v[196:199], v221\n"
            "ds_read_b32 v233, v233 offset:4\n"
            "s_waitcnt lgkmcnt(2)\n"
            "v_add_u32 v232, v232, v229\n"
            "buffer_load_dwordx4 v[192:195], v232, s[24:27], 0 offen\n"
            "s_waitcnt lgkmcnt(0)\n"
            "v_add_u32 v233, v233, v229\n"
            "v_add_u32_dpp v220, v196, v214 quad_perm:[0,0,0,0] row_mask:0xf bank_mask:0xf\n"
            "v_add_u32_dpp v221, v196, v214 quad_perm:[1,1,1,1] row_mask:0xf bank_mask:0xf\n"
            "v_add_u32_dpp v222, v196, v214 quad_perm:[2,2,2,2] row_mask:0xf bank_mask:0xf\n"
            "v_add_u32_dpp v223, v196, v214 quad_perm:[3,3,3,3] row_mask:0xf bank_mask:0xf\n"
            "buffer_load_dwordx4 v[32:35], v220, s[28:31], 0 offen\n"
            "buffer_load_dwordx4 v[36:39], v220, s[28:31], 0 offen offset:256\n"
            "v_mfma_f32_4x4x4_16b_bf16 v[204:207], v[202:203], v[198:199], 0\n"
            "buffer_load_dwordx4 v[48:51], v221, s[28:31], 0 offen\n"
            "buffer_load_dwordx4 v[52:55], v221, s[28:31], 0 offen offset:256\n"
            "buffer_load_dwordx4 v[232:235], v233, s[24:27], 0 offen\n"
            "buffer_load_dwordx4 v[64:67], v222, s[28:31], 0 offen\n"
            "buffer_load_dwordx4 v[68:71], v222, s[28:31], 0 offen offset:256\n"
            "v_mfma_f32_4x4x4_16b_bf16 v[204:207], v[200:201], v[198:199], v[204:207]\n"
            "buffer_load_dwordx4 v[80:83], v223, s[28:31], 0 offen\n"
            "buffer_load_dwordx4 v[84:87], v223, s[28:31], 0 offen offset:256\n"
            "buffer_load_dwordx4 v[40:43], v220, s[28:31], 0 offen offset:512\n"
            "buffer_load_dwordx4 v[44:47], v220, s[28:31], 0 offen offset:768\n"
            "buffer_load_dwordx4 v[56:59], v221, s[28:31], 0 offen offset:512\n"
            "buffer_load_dwordx4 v[60:63], v221, s[28:31], 0 offen offset:768\n"
            "buffer_load_dwordx4 v[72:75], v222, s[28:31], 0 offen offset:512\n"
            "buffer_load_dwordx4 v[76:79], v222, s[28:31], 0 offen offset:768\n"
            "buffer_load_dwordx4 v[88:91], v223, s[28:31], 0 offen offset:512\n"
            "buffer_load_dwordx4 v[92:95], v223, s[28:31], 0 offen offset:768\n"
            "v_max_u32 v128, 12, v128\n"
            "v_mov_b32_dpp v134, v130 quad_perm:[1,2,3,0] row_mask:0xf bank_mask:0xf\n"
            "v_add_u32 v132, v128, v129\n"
            "v_sub_u32 v133, 0, v132\n"
            "v_lshl_add_u32 v135, v132, 1, v208\n"
            "v_and_b32 v133, 3, v133\n"
            "v_lshl_add_u32 v133, v132, 1, v133\n"
            "v_add3_u32 v135, v135, v134, v130\n"
            "v_readlane_b32 s73, v128, s3\n"
            "s_nop 0\n"
            "v_add_u32_dpp v135, v133, v135 row_shr:4 row_mask:0xf bank_mask:0xf bound_ctrl:1\n"
            "v_readlane_b32 s75, v129, s3\n"
            "v_readlane_b32 s80, v130, s3\n"
            "v_sub_u32_dpp v133, v135, v135 quad_perm:[1,2,3,0] row_mask:0xf bank_mask:0xf\n"
            "v_mov_b32_dpp v136, v135 quad_perm:[3,0,1,2] row_mask:0xf bank_mask:0xf\n"
            "v_lshl_add_u32 v137, v134, 3, 8\n"
            "v_mad_i32_i24 v133, v133, 3, v137\n"
            "v_sub_u32_dpp v136, v135, v136 quad_perm:[2,3,0,1] row_mask:0xf bank_mask:0xf\n"
            "v_add_u32 v134, -4, v134\n"
            "v_add_u32 v133, v133, v136\n"
            "v_ashrrev_i32 v133, 4, v133\n"
            "v_max_i32 v133, 4, v133\n"
            "v_min_i32 v133, v133, v134\n"
            "v_max_i32 v133, 0, v133\n"
            "s_lshr_b32 s69, s73, 1\n"
            "s_max_i32 s69, s69, 8\n"
            "v_mov_b32_dpp v134, v133 quad_perm:[3,0,1,2] row_mask:0xf bank_mask:0xf\n"
            "v_readlane_b32 s77, v133, s3\n"
            "v_sub_u32 v134, v130, v134\n"
            "v_add3_u32 v132, v132, v133, v131\n"
            "v_readlane_b32 s76, v134, s3\n"
            "v_add_u32 v132, v132, v134\n"
            "s_add_u32 s78, s69, s77\n"
            "v_cmp_lt_u32 vcc, 72, v132\n"
            "s_add_u32 s79, s78, s75\n"
            "s_sub_u32 s83, s79, s80\n"
            "s_add_u32 s83, s83, s76\n"
            "s_cbranch_vccnz Lslow_%=\n"
            "s_add_u32 s80, s79, s76\n"
            "s_sub_u32 s84, s80, s69\n"
            "s_add_u32 s73, s73, s84\n"
            "s_pack_ll_b32_b16 s81, s40, s69\n"
            "s_pack_ll_b32_b16 s82, s65, s78\n"
            "s_pack_ll_b32_b16 s83, s66, s83\n"
            "s_pack_ll_b32_b16 s84, s64, s84\n"
            "v_mov_b32 v138, 0xffff\n"
            "v_mov_b32 v136, s64\n"
            "v_cmpx_le_u32 vcc, s69, v243\n"
            "v_mov_b32 v136, s81\n"
            "v_cmpx_le_u32 vcc, s78, v243\n"
            "v_mov_b32 v136, s82\n"
            "v_cmpx_le_u32 vcc, s79, v243\n"
            "v_mov_b32 v136, s83\n"
            "v_cmpx_le_u32 vcc, s80, v243\n"
            "v_mov_b32 v136, s84\n"
            "s_mov_b64 exec, -1\n"
            "v_and_b32 v137, 0xffff, v136\n"
            "ds_read_b128 v[140:143], v137\n"
            "v_ashrrev_i32 v136, 16, v136\n"
            "v_sub_u32 v136, v243, v136\n"
            "v_lshl_or_b32 v137, v136, 16, v138\n"
            "v_lshlrev_b32 v136, 4, v136\n"
            "v_cvt_pk_bf16_f32 v204, v204, v205\n"
            "v_cvt_pk_bf16_f32 v205, v206, v207\n"
            "ds_write_b64 v212, v[204:205]\n"
            "ds_read_b128 v[196:199], v210\n"
            "ds_read_b64 v[208:209], v213\n"
            "s_add_u32 s73, s73, 3\n"
            "s_and_b32 s74, s73, 3\n"
            "s_add_u32 s74, s74, 1\n"
            "s_lshr_b32 s73, s73, 2\n"
            "s_mov_b32 s68, 0x05040100\n"
            "v_add_u32 v217, v217, v214\n"
            "v_add3_u32 v215, v215, v214, s63\n"
            "v_add_u32 v218, v218, v214\n"
            "v_not_b32 v223, s90\n"
            "s_waitcnt lgkmcnt(3)\n"
            "v_cmp_gt_u32 vcc, v140, v137\n"
            "v_cmp_gt_u32 s[70:71], v141, v137\n"
            "v_cmp_gt_u32 s[86:87], v142, v137\n"
            "v_cmp_gt_u32 s[88:89], v143, v137\n"
            "v_add_u32 v140, v140, v136\n"
            "v_add_u32 v141, v141, v136\n"
            "v_add_u32 v142, v142, v136\n"
            "v_add_u32 v143, v143, v136\n"
            "v_cndmask_b32 v140, v251, v140, vcc\n"
            "v_cndmask_b32 v141, v251, v141, s[70:71]\n"
            "v_cndmask_b32 v142, v251, v142, s[86:87]\n"
            "v_cndmask_b32 v143, v251, v143, s[88:89]\n"
            "v_perm_b32 v140, v141, v140, s68\n"
            "v_perm_b32 v141, v143, v142, s68\n"
            "ds_write_b64 v247, v[140:141]\n"
            "ds_read_u16 v211, v238\n"
            "s_cmp_gt_u32 s73, 14\n"
            "s_cbranch_scc1 Llong_%=\n"
            "Llong2_%=:\n"
            "v_add_u32 v216, s63, v218\n"
            "s_lshl_b32 s72, s75, 8\n"
            "s_lshl_b32 s76, s76, 16\n"
            "s_add_u32 s72, s72, s76\n"
            "s_add_u32 s72, s72, s77\n"
            "s_add_u32 s72, s72, 0xfefbfbfc\n"
            "s_add_u32 s75, s75, 0xfefc\n"
            "s_cmp_lt_u32 s3, 4\n"
            "s_cselect_b32 s72, s75, s72\n"
            "s_cmp_lt_u32 s3, 6\n"
            "s_cselect_b32 s75, 1, 0\n"
            "v_lshl_or_b32 v218, s75, 31, v218\n"
            "s_mov_b32 s85, 30\n"
            "s_waitcnt vmcnt(17)\n"
            "v_mfma_f32_16x16x32_bf16 v[176:179], v[192:195], v[96:99], 0\n"
            "v_mfma_f32_16x16x32_bf16 v[180:183], v[192:195], v[100:103], 0\n"
            "v_mfma_f32_16x16x32_bf16 v[184:187], v[192:195], v[104:107], 0\n"
            "v_mfma_f32_16x16x32_bf16 v[188:191], v[192:195], v[108:111], 0\n"
            "v_mfma_f32_16x16x32_bf16 v[128:131], v[192:195], v[112:115], 0\n"
            "v_mfma_f32_16x16x32_bf16 v[132:135], v[192:195], v[116:119], 0\n"
            "s_nop 2\n"
            "v_exp_f32 v144, v176\n"
            "v_exp_f32 v145, v177\n"
            "v_exp_f32 v146, v178\n"
            "v_exp_f32 v147, v179\n"
            "v_exp_f32 v148, v180\n"
            "v_exp_f32 v149, v181\n"
            "v_exp_f32 v150, v182\n"
            "v_exp_f32 v151, v183\n"
            "v_exp_f32 v152, v184\n"
            "v_exp_f32 v153, v185\n"
            "v_exp_f32 v154, v186\n"
            "v_exp_f32 v155, v187\n"
            "v_exp_f32 v156, v188\n"
            "v_exp_f32 v157, v189\n"
            "v_exp_f32 v158, v190\n"
            "v_exp_f32 v159, v191\n"
            "v_pk_add_f32 v[144:145], v[144:145], 1.0 op_sel_hi:[1,0]\n"
            "v_pk_add_f32 v[146:147], v[146:147], 1.0 op_sel_hi:[1,0]\n"
            "v_pk_add_f32 v[148:149], v[148:149], 1.0 op_sel_hi:[1,0]\n"
            "v_pk_add_f32 v[150:151], v[150:151], 1.0 op_sel_hi:[1,0]\n"
            "v_pk_add_f32 v[152:153], v[152:153], 1.0 op_sel_hi:[1,0]\n"
            "v_pk_add_f32 v[154:155], v[154:155], 1.0 op_sel_hi:[1,0]\n"
            "v_pk_add_f32 v[156:157], v[156:157], 1.0 op_sel_hi:[1,0]\n"
            "v_pk_add_f32 v[158:159], v[158:159], 1.0 op_sel_hi:[1,0]\n"
            "v_rcp_f32 v144, v144\n"
            "v_rcp_f32 v145, v145\n"
            "v_rcp_f32 v146, v146\n"
            "v_rcp_f32 v147, v147\n"
            "v_rcp_f32 v148, v148\n"
            "v_rcp_f32 v149, v149\n"
            "v_rcp_f32 v150, v150\n"
            "v_rcp_f32 v151, v151\n"
            "v_rcp_f32 v152, v152\n"
            "v_rcp_f32 v153, v153\n"
            "v_rcp_f32 v154, v154\n"
            "v_rcp_f32 v155, v155\n"
            "v_rcp_f32 v156, v156\n"
            "v_rcp_f32 v157, v157\n"
            "v_rcp_f32 v158, v158\n"
            "v_rcp_f32 v159, v159\n"
            "v_pk_mul_f32 v[144:145], v[144:145], v[176:177]\n"
            "v_pk_mul_f32 v[146:147], v[146:147], v[178:179]\n"
            "v_pk_mul_f32 v[148:149], v[148:149], v[180:181]\n"
            "v_pk_mul_f32 v[150:151], v[150:151], v[182:183]\n"
            "v_pk_mul_f32 v[152:153], v[152:153], v[184:185]\n"
            "v_pk_mul_f32 v[154:155], v[154:155], v[186:187]\n"
            "v_pk_mul_f32 v[156:157], v[156:157], v[188:189]\n"
            "v_pk_mul_f32 v[158:159], v[158:159], v[190:191]\n"
            "s_branch Ldisp_%=\n"
            "Lfast_%=:\n"
            "v_mfma_f32_16x16x32_bf16 v[136:139], v[192:195], v[120:123], 0\n"
            "v_exp_f32 v160, v128\n"
            "v_add_u32 v237, 32, v237\n"
            "v_exp_f32 v161, v129\n"
            "s_waitcnt vmcnt(13) lgkmcnt(0)\n"
            "v_mfma_f32_4x4x4_16b_bf16 v[176:179], v[208:209], v[32:33], 0 cbsz:2\n"
            "v_exp_f32 v162, v130\n"
            "v_mfma_f32_4x4x4_16b_bf16 v[180:183], v[208:209], v[34:35], 0 cbsz:2\n"
            "v_exp_f32 v163, v131\n"
            "v_mfma_f32_4x4x4_16b_bf16 v[184:187], v[208:209], v[36:37], 0 cbsz:2\n"
            "v_exp_f32 v164, v132\n"
            "v_mfma_f32_4x4x4_16b_bf16 v[188:191], v[208:209], v[38:39], 0 cbsz:2\n"
            "v_exp_f32 v165, v133\n"
            "v_add_u32_dpp v219, v196, v214 quad_perm:[0,0,0,0] row_mask:0xf bank_mask:0xf\n"
            "v_mfma_f32_4x4x4_16b_bf16 v[204:207], v[202:203], v[198:199], 0\n"
            "v_exp_f32 v166, v134\n"
            "buffer_load_dwordx4 v[32:35], v219, s[28:31], 0 offen\n"
            "buffer_load_dwordx4 v[36:39], v219, s[28:31], 0 offen offset:256\n"
            "v_exp_f32 v167, v135\n"
            "v_pk_fma_f32 v[0:1], v[144:145], v[176:177], v[0:1] op_sel_hi:[0,1,1]\n"
            "v_pk_fma_f32 v[2:3], v[144:145], v[178:179], v[2:3] op_sel_hi:[0,1,1]\n"
            "v_pk_fma_f32 v[4:5], v[148:149], v[180:181], v[4:5] op_sel_hi:[0,1,1]\n"
            "v_pk_fma_f32 v[6:7], v[148:149], v[182:183], v[6:7] op_sel_hi:[0,1,1]\n"
            "v_pk_fma_f32 v[8:9], v[152:153], v[184:185], v[8:9] op_sel_hi:[0,1,1]\n"
            "v_pk_fma_f32 v[10:11], v[152:153], v[186:187], v[10:11] op_sel_hi:[0,1,1]\n"
            "v_pk_fma_f32 v[12:13], v[156:157], v[188:189], v[12:13] op_sel_hi:[0,1,1]\n"
            "v_pk_fma_f32 v[14:15], v[156:157], v[190:191], v[14:15] op_sel_hi:[0,1,1]\n"
            "v_pk_add_f32 v[160:161], v[160:161], 1.0 op_sel_hi:[1,0]\n"
            "v_pk_add_f32 v[162:163], v[162:163], 1.0 op_sel_hi:[1,0]\n"
            "v_pk_add_f32 v[164:165], v[164:165], 1.0 op_sel_hi:[1,0]\n"
            "v_pk_add_f32 v[166:167], v[166:167], 1.0 op_sel_hi:[1,0]\n"
            "v_mfma_f32_16x16x32_bf16 v[140:143], v[192:195], v[124:127], 0\n"
            "ds_read_b32 v230, v211 offset:4\n"
            "ds_read_u16 v210, v237\n"
            "v_rcp_f32 v160, v160\n"
            "v_rcp_f32 v161, v161\n"
            "v_mfma_f32_4x4x4_16b_bf16 v[176:179], v[208:209], v[48:49], 0 cbsz:2 abid:1\n"
            "v_rcp_f32 v162, v162\n"
            "v_mfma_f32_4x4x4_16b_bf16 v[180:183], v[208:209], v[50:51], 0 cbsz:2 abid:1\n"
            "v_rcp_f32 v163, v163\n"
            "v_mfma_f32_4x4x4_16b_bf16 v[184:187], v[208:209], v[52:53], 0 cbsz:2 abid:1\n"
            "v_rcp_f32 v164, v164\n"
            "v_mfma_f32_4x4x4_16b_bf16 v[188:191], v[208:209], v[54:55], 0 cbsz:2 abid:1\n"
            "v_rcp_f32 v165, v165\n"
            "v_add_u32_dpp v220, v196, v214 quad_perm:[1,1,1,1] row_mask:0xf bank_mask:0xf\n"
            "v_mfma_f32_4x4x4_16b_bf16 v[204:207], v[200:201], v[198:199], v[204:207]\n"
            "v_rcp_f32 v166, v166\n"
            "buffer_load_dwordx4 v[48:51], v220, s[28:31], 0 offen\n"
            "buffer_load_dwordx4 v[52:55], v220, s[28:31], 0 offen offset:256\n"
            "v_rcp_f32 v167, v167\n"
            "v_pk_fma_f32 v[0:1], v[144:145], v[176:177], v[0:1] op_sel:[1,0,0]\n"
            "v_pk_fma_f32 v[2:3], v[144:145], v[178:179], v[2:3] op_sel:[1,0,0]\n"
            "v_pk_fma_f32 v[4:5], v[148:149], v[180:181], v[4:5] op_sel:[1,0,0]\n"
            "v_pk_fma_f32 v[6:7], v[148:149], v[182:183], v[6:7] op_sel:[1,0,0]\n"
            "v_pk_fma_f32 v[8:9], v[152:153], v[184:185], v[8:9] op_sel:[1,0,0]\n"
            "v_pk_fma_f32 v[10:11], v[152:153], v[186:187], v[10:11] op_sel:[1,0,0]\n"
            "v_pk_fma_f32 v[12:13], v[156:157], v[188:189], v[12:13] op_sel:[1,0,0]\n"
            "v_pk_fma_f32 v[14:15], v[156:157], v[190:191], v[14:15] op_sel:[1,0,0]\n"
            "v_pk_mul_f32 v[160:161], v[160:161], v[128:129]\n"
            "v_pk_mul_f32 v[162:163], v[162:163], v[130:131]\n"
            "v_pk_mul_f32 v[164:165], v[164:165], v[132:133]\n"
            "v_pk_mul_f32 v[166:167], v[166:167], v[134:135]\n"
            "s_waitcnt vmcnt(12) lgkmcnt(0)\n"
            "v_mov_b64 v[192:193], v[232:233]\n"
            "v_mov_b64 v[194:195], v[234:235]\n"
            "v_add_u32 v230, v230, v229\n"
            "buffer_load_dwordx4 v[232:235], v230, s[24:27], 0 offen\n"
            "s_bitcmp0_b32 s85, 0\n"
            "s_cbranch_scc1 Lnpf_%=\n"
            "s_buffer_load_dwordx2 s[86:87], s[28:31], s67 offset:60\n"
            "s_buffer_load_dwordx2 s[86:87], s[28:31], s67 offset:188\n"
            "s_buffer_load_dwordx2 s[86:87], s[28:31], s67 offset:316\n"
            "s_buffer_load_dwordx2 s[86:87], s[28:31], s67 offset:444\n"
            "s_add_u32 s67, s67, 524288\n"
            "Lnpf_%=:\n"
            "s_lshr_b32 s85, s85, 1\n"
            "v_mfma_f32_16x16x32_bf16 v[128:131], v[192:195], v[96:99], 0\n"
            "v_exp_f32 v168, v136\n"
            "v_exp_f32 v169, v137\n"
            "v_mfma_f32_4x4x4_16b_bf16 v[176:179], v[208:209], v[64:65], 0 cbsz:2 abid:2\n"
            "v_exp_f32 v170, v138\n"
            "v_mfma_f32_4x4x4_16b_bf16 v[180:183], v[208:209], v[66:67], 0 cbsz:2 abid:2\n"
            "v_exp_f32 v171, v139\n"
            "v_mfma_f32_4x4x4_16b_bf16 v[184:187], v[208:209], v[68:69], 0 cbsz:2 abid:2\n"
            "v_exp_f32 v172, v140\n"
            "v_mfma_f32_4x4x4_16b_bf16 v[188:191], v[208:209], v[70:71], 0 cbsz:2 abid:2\n"
            "v_exp_f32 v173, v141\n"
            "v_add_u32_dpp v221, v196, v214 quad_perm:[2,2,2,2] row_mask:0xf bank_mask:0xf\n"
            "v_exp_f32 v174, v142\n"
            "buffer_load_dwordx4 v[64:67], v221, s[28:31], 0 offen\n"
            "buffer_load_dwordx4 v[68:71], v221, s[28:31], 0 offen offset:256\n"
            "v_exp_f32 v175, v143\n"
            "v_pk_fma_f32 v[0:1], v[146:147], v[176:177], v[0:1] op_sel_hi:[0,1,1]\n"
            "v_pk_fma_f32 v[2:3], v[146:147], v[178:179], v[2:3] op_sel_hi:[0,1,1]\n"
            "v_pk_fma_f32 v[4:5], v[150:151], v[180:181], v[4:5] op_sel_hi:[0,1,1]\n"
            "v_pk_fma_f32 v[6:7], v[150:151], v[182:183], v[6:7] op_sel_hi:[0,1,1]\n"
            "v_pk_fma_f32 v[8:9], v[154:155], v[184:185], v[8:9] op_sel_hi:[0,1,1]\n"
            "v_pk_fma_f32 v[10:11], v[154:155], v[186:187], v[10:11] op_sel_hi:[0,1,1]\n"
            "v_pk_fma_f32 v[12:13], v[158:159], v[188:189], v[12:13] op_sel_hi:[0,1,1]\n"
            "v_pk_fma_f32 v[14:15], v[158:159], v[190:191], v[14:15] op_sel_hi:[0,1,1]\n"
            "v_pk_add_f32 v[168:169], v[168:169], 1.0 op_sel_hi:[1,0]\n"
            "v_pk_add_f32 v[170:171], v[170:171], 1.0 op_sel_hi:[1,0]\n"
            "v_pk_add_f32 v[172:173], v[172:173], 1.0 op_sel_hi:[1,0]\n"
            "v_pk_add_f32 v[174:175], v[174:175], 1.0 op_sel_hi:[1,0]\n"
            "v_cvt_pk_bf16_f32 v204, v204, v205\n"
            "v_cvt_pk_bf16_f32 v205, v206, v207\n"
            "ds_write_b64 v212, v[204:205]\n"
            "v_mfma_f32_16x16x32_bf16 v[132:135], v[192:195], v[100:103], 0\n"
            "v_rcp_f32 v168, v168\n"
            "v_rcp_f32 v169, v169\n"
            "v_mfma_f32_4x4x4_16b_bf16 v[176:179], v[208:209], v[80:81], 0 cbsz:2 abid:3\n"
            "v_rcp_f32 v170, v170\n"
            "v_mfma_f32_4x4x4_16b_bf16 v[180:183], v[208:209], v[82:83], 0 cbsz:2 abid:3\n"
            "v_rcp_f32 v171, v171\n"
            "v_mfma_f32_4x4x4_16b_bf16 v[184:187], v[208:209], v[84:85], 0 cbsz:2 abid:3\n"
            "v_rcp_f32 v172, v172\n"
            "v_mfma_f32_4x4x4_16b_bf16 v[188:191], v[208:209], v[86:87], 0 cbsz:2 abid:3\n"
            "v_rcp_f32 v173, v173\n"
            "v_add_u32_dpp v222, v196, v214 quad_perm:[3,3,3,3] row_mask:0xf bank_mask:0xf\n"
            "v_rcp_f32 v174, v174\n"
            "buffer_load_dwordx4 v[80:83], v222, s[28:31], 0 offen\n"
            "buffer_load_dwordx4 v[84:87], v222, s[28:31], 0 offen offset:256\n"
            "v_rcp_f32 v175, v175\n"
            "v_pk_fma_f32 v[0:1], v[146:147], v[176:177], v[0:1] op_sel:[1,0,0]\n"
            "v_pk_fma_f32 v[2:3], v[146:147], v[178:179], v[2:3] op_sel:[1,0,0]\n"
            "v_pk_fma_f32 v[4:5], v[150:151], v[180:181], v[4:5] op_sel:[1,0,0]\n"
            "v_pk_fma_f32 v[6:7], v[150:151], v[182:183], v[6:7] op_sel:[1,0,0]\n"
            "v_pk_fma_f32 v[8:9], v[154:155], v[184:185], v[8:9] op_sel:[1,0,0]\n"
            "v_pk_fma_f32 v[10:11], v[154:155], v[186:187], v[10:11] op_sel:[1,0,0]\n"
            "v_pk_fma_f32 v[12:13], v[158:159], v[188:189], v[12:13] op_sel:[1,0,0]\n"
            "v_pk_fma_f32 v[14:15], v[158:159], v[190:191], v[14:15] op_sel:[1,0,0]\n"
            "v_pk_mul_f32 v[168:169], v[168:169], v[136:137]\n"
            "v_pk_mul_f32 v[170:171], v[170:171], v[138:139]\n"
            "v_pk_mul_f32 v[172:173], v[172:173], v[140:141]\n"
            "v_pk_mul_f32 v[174:175], v[174:175], v[142:143]\n"
            "v_add_u32 v238, 32, v238\n"
            "ds_read_b128 v[196:199], v210\n"
            "ds_read_u16 v211, v238\n"
            "v_mfma_f32_16x16x32_bf16 v[136:139], v[192:195], v[104:107], 0\n"
            "v_exp_f32 v144, v128\n"
            "v_exp_f32 v145, v129\n"
            "s_waitcnt vmcnt(13)\n"
            "v_mfma_f32_4x4x4_16b_bf16 v[176:179], v[208:209], v[40:41], 0 cbsz:2\n"
            "v_exp_f32 v146, v130\n"
            "v_mfma_f32_4x4x4_16b_bf16 v[180:183], v[208:209], v[42:43], 0 cbsz:2\n"
            "v_exp_f32 v147, v131\n"
            "v_mfma_f32_4x4x4_16b_bf16 v[184:187], v[208:209], v[44:45], 0 cbsz:2\n"
            "v_exp_f32 v148, v132\n"
            "v_mfma_f32_4x4x4_16b_bf16 v[188:191], v[208:209], v[46:47], 0 cbsz:2\n"
            "v_exp_f32 v149, v133\n"
            "v_exp_f32 v150, v134\n"
            "buffer_load_dwordx4 v[40:43], v219, s[28:31], 0 offen offset:512\n"
            "buffer_load_dwordx4 v[44:47], v219, s[28:31], 0 offen offset:768\n"
            "v_exp_f32 v151, v135\n"
            "v_pk_fma_f32 v[16:17], v[160:161], v[176:177], v[16:17] op_sel_hi:[0,1,1]\n"
            "v_pk_fma_f32 v[18:19], v[160:161], v[178:179], v[18:19] op_sel_hi:[0,1,1]\n"
            "v_pk_fma_f32 v[20:21], v[164:165], v[180:181], v[20:21] op_sel_hi:[0,1,1]\n"
            "v_pk_fma_f32 v[22:23], v[164:165], v[182:183], v[22:23] op_sel_hi:[0,1,1]\n"
            "v_pk_fma_f32 v[24:25], v[168:169], v[184:185], v[24:25] op_sel_hi:[0,1,1]\n"
            "v_pk_fma_f32 v[26:27], v[168:169], v[186:187], v[26:27] op_sel_hi:[0,1,1]\n"
            "v_pk_fma_f32 v[28:29], v[172:173], v[188:189], v[28:29] op_sel_hi:[0,1,1]\n"
            "v_pk_fma_f32 v[30:31], v[172:173], v[190:191], v[30:31] op_sel_hi:[0,1,1]\n"
            "v_pk_add_f32 v[144:145], v[144:145], 1.0 op_sel_hi:[1,0]\n"
            "v_pk_add_f32 v[146:147], v[146:147], 1.0 op_sel_hi:[1,0]\n"
            "v_pk_add_f32 v[148:149], v[148:149], 1.0 op_sel_hi:[1,0]\n"
            "v_pk_add_f32 v[150:151], v[150:151], 1.0 op_sel_hi:[1,0]\n"
            "v_mfma_f32_16x16x32_bf16 v[140:143], v[192:195], v[108:111], 0\n"
            "v_rcp_f32 v144, v144\n"
            "v_rcp_f32 v145, v145\n"
            "v_mfma_f32_4x4x4_16b_bf16 v[176:179], v[208:209], v[56:57], 0 cbsz:2 abid:1\n"
            "v_rcp_f32 v146, v146\n"
            "v_mfma_f32_4x4x4_16b_bf16 v[180:183], v[208:209], v[58:59], 0 cbsz:2 abid:1\n"
            "v_rcp_f32 v147, v147\n"
            "v_mfma_f32_4x4x4_16b_bf16 v[184:187], v[208:209], v[60:61], 0 cbsz:2 abid:1\n"
            "v_rcp_f32 v148, v148\n"
            "v_mfma_f32_4x4x4_16b_bf16 v[188:191], v[208:209], v[62:63], 0 cbsz:2 abid:1\n"
            "v_rcp_f32 v149, v149\n"
            "v_rcp_f32 v150, v150\n"
            "buffer_load_dwordx4 v[56:59], v220, s[28:31], 0 offen offset:512\n"
            "buffer_load_dwordx4 v[60:63], v220, s[28:31], 0 offen offset:768\n"
            "v_rcp_f32 v151, v151\n"
            "v_pk_fma_f32 v[16:17], v[160:161], v[176:177], v[16:17] op_sel:[1,0,0]\n"
            "v_pk_fma_f32 v[18:19], v[160:161], v[178:179], v[18:19] op_sel:[1,0,0]\n"
            "v_pk_fma_f32 v[20:21], v[164:165], v[180:181], v[20:21] op_sel:[1,0,0]\n"
            "v_pk_fma_f32 v[22:23], v[164:165], v[182:183], v[22:23] op_sel:[1,0,0]\n"
            "v_pk_fma_f32 v[24:25], v[168:169], v[184:185], v[24:25] op_sel:[1,0,0]\n"
            "v_pk_fma_f32 v[26:27], v[168:169], v[186:187], v[26:27] op_sel:[1,0,0]\n"
            "v_pk_fma_f32 v[28:29], v[172:173], v[188:189], v[28:29] op_sel:[1,0,0]\n"
            "v_pk_fma_f32 v[30:31], v[172:173], v[190:191], v[30:31] op_sel:[1,0,0]\n"
            "v_pk_mul_f32 v[144:145], v[144:145], v[128:129]\n"
            "v_pk_mul_f32 v[146:147], v[146:147], v[130:131]\n"
            "v_pk_mul_f32 v[148:149], v[148:149], v[132:133]\n"
            "v_pk_mul_f32 v[150:151], v[150:151], v[134:135]\n"
            "v_mfma_f32_16x16x32_bf16 v[128:131], v[192:195], v[112:115], 0\n"
            "v_exp_f32 v152, v136\n"
            "v_exp_f32 v153, v137\n"
            "s_waitcnt vmcnt(13)\n"
            "v_mfma_f32_4x4x4_16b_bf16 v[176:179], v[208:209], v[72:73], 0 cbsz:2 abid:2\n"
            "v_exp_f32 v154, v138\n"
            "v_mfma_f32_4x4x4_16b_bf16 v[180:183], v[208:209], v[74:75], 0 cbsz:2 abid:2\n"
            "v_exp_f32 v155, v139\n"
            "v_mfma_f32_4x4x4_16b_bf16 v[184:187], v[208:209], v[76:77], 0 cbsz:2 abid:2\n"
            "v_exp_f32 v156, v140\n"
            "v_mfma_f32_4x4x4_16b_bf16 v[188:191], v[208:209], v[78:79], 0 cbsz:2 abid:2\n"
            "v_exp_f32 v157, v141\n"
            "v_exp_f32 v158, v142\n"
            "buffer_load_dwordx4 v[72:75], v221, s[28:31], 0 offen offset:512\n"
            "buffer_load_dwordx4 v[76:79], v221, s[28:31], 0 offen offset:768\n"
            "v_exp_f32 v159, v143\n"
            "v_pk_fma_f32 v[16:17], v[162:163], v[176:177], v[16:17] op_sel_hi:[0,1,1]\n"
            "v_pk_fma_f32 v[18:19], v[162:163], v[178:179], v[18:19] op_sel_hi:[0,1,1]\n"
            "v_pk_fma_f32 v[20:21], v[166:167], v[180:181], v[20:21] op_sel_hi:[0,1,1]\n"
            "v_pk_fma_f32 v[22:23], v[166:167], v[182:183], v[22:23] op_sel_hi:[0,1,1]\n"
            "v_pk_fma_f32 v[24:25], v[170:171], v[184:185], v[24:25] op_sel_hi:[0,1,1]\n"
            "v_pk_fma_f32 v[26:27], v[170:171], v[186:187], v[26:27] op_sel_hi:[0,1,1]\n"
            "v_pk_fma_f32 v[28:29], v[174:175], v[188:189], v[28:29] op_sel_hi:[0,1,1]\n"
            "v_pk_fma_f32 v[30:31], v[174:175], v[190:191], v[30:31] op_sel_hi:[0,1,1]\n"
            "v_pk_add_f32 v[152:153], v[152:153], 1.0 op_sel_hi:[1,0]\n"
            "v_pk_add_f32 v[154:155], v[154:155], 1.0 op_sel_hi:[1,0]\n"
            "v_pk_add_f32 v[156:157], v[156:157], 1.0 op_sel_hi:[1,0]\n"
            "v_pk_add_f32 v[158:159], v[158:159], 1.0 op_sel_hi:[1,0]\n"
            "v_mfma_f32_16x16x32_bf16 v[132:135], v[192:195], v[116:119], 0\n"
            "v_rcp_f32 v152, v152\n"
            "v_rcp_f32 v153, v153\n"
            "v_mfma_f32_4x4x4_16b_bf16 v[176:179], v[208:209], v[88:89], 0 cbsz:2 abid:3\n"
            "v_rcp_f32 v154, v154\n"
            "v_mfma_f32_4x4x4_16b_bf16 v[180:183], v[208:209], v[90:91], 0 cbsz:2 abid:3\n"
            "v_rcp_f32 v155, v155\n"
            "v_mfma_f32_4x4x4_16b_bf16 v[184:187], v[208:209], v[92:93], 0 cbsz:2 abid:3\n"
            "v_rcp_f32 v156, v156\n"
            "v_mfma_f32_4x4x4_16b_bf16 v[188:191], v[208:209], v[94:95], 0 cbsz:2 abid:3\n"
            "ds_read_b64 v[208:209], v213\n"
            "v_rcp_f32 v157, v157\n"
            "v_rcp_f32 v158, v158\n"
            "buffer_load_dwordx4 v[88:91], v222, s[28:31], 0 offen offset:512\n"
            "buffer_load_dwordx4 v[92:95], v222, s[28:31], 0 offen offset:768\n"
            "v_rcp_f32 v159, v159\n"
            "v_pk_fma_f32 v[16:17], v[162:163], v[176:177], v[16:17] op_sel:[1,0,0]\n"
            "v_pk_fma_f32 v[18:19], v[162:163], v[178:179], v[18:19] op_sel:[1,0,0]\n"
            "v_pk_fma_f32 v[20:21], v[166:167], v[180:181], v[20:21] op_sel:[1,0,0]\n"
            "v_pk_fma_f32 v[22:23], v[166:167], v[182:183], v[22:23] op_sel:[1,0,0]\n"
            "v_pk_fma_f32 v[24:25], v[170:171], v[184:185], v[24:25] op_sel:[1,0,0]\n"
            "v_pk_fma_f32 v[26:27], v[170:171], v[186:187], v[26:27] op_sel:[1,0,0]\n"
            "v_pk_fma_f32 v[28:29], v[174:175], v[188:189], v[28:29] op_sel:[1,0,0]\n"
            "v_pk_fma_f32 v[30:31], v[174:175], v[190:191], v[30:31] op_sel:[1,0,0]\n"
            "v_pk_mul_f32 v[152:153], v[152:153], v[136:137]\n"
            "v_pk_mul_f32 v[154:155], v[154:155], v[138:139]\n"
            "v_pk_mul_f32 v[156:157], v[156:157], v[140:141]\n"
            "v_pk_mul_f32 v[158:159], v[158:159], v[142:143]\n"
            "s_sub_u32 s69, s69, 4\n"
            "Lnext_%=:\n"
            "s_sub_u32 s73, s73, 1\n"
            "Ldisp_%=:\n"
            "s_bitcmp1_b32 s60, s73\n"
            "s_cbranch_scc1 Lprio_%=\n"
            "Ld2_%=:\n"
            "s_cmp_gt_u32 s69, 4\n"
            "s_cbranch_scc1 Lfast_%=\n"
            "v_mfma_f32_16x16x32_bf16 v[136:139], v[192:195], v[120:123], 0\n"
            "v_exp_f32 v160, v128\n"
            "v_add_u32 v237, 32, v237\n"
            "v_exp_f32 v161, v129\n"
            "s_waitcnt vmcnt(13) lgkmcnt(0)\n"
            "v_mfma_f32_4x4x4_16b_bf16 v[176:179], v[208:209], v[32:33], 0 cbsz:2\n"
            "v_exp_f32 v162, v130\n"
            "v_mfma_f32_4x4x4_16b_bf16 v[180:183], v[208:209], v[34:35], 0 cbsz:2\n"
            "v_exp_f32 v163, v131\n"
            "v_mfma_f32_4x4x4_16b_bf16 v[184:187], v[208:209], v[36:37], 0 cbsz:2\n"
            "v_exp_f32 v164, v132\n"
            "v_mfma_f32_4x4x4_16b_bf16 v[188:191], v[208:209], v[38:39], 0 cbsz:2\n"
            "v_exp_f32 v165, v133\n"
            "v_add_u32_dpp v219, v196, v214 quad_perm:[0,0,0,0] row_mask:0xf bank_mask:0xf\n"
            "v_mfma_f32_4x4x4_16b_bf16 v[204:207], v[202:203], v[198:199], 0\n"
            "v_exp_f32 v166, v134\n"
            "buffer_load_dwordx4 v[32:35], v219, s[28:31], 0 offen\n"
            "buffer_load_dwordx4 v[36:39], v219, s[28:31], 0 offen offset:256\n"
            "v_exp_f32 v167, v135\n"
            "v_pk_fma_f32 v[0:1], v[144:145], v[176:177], v[0:1] op_sel_hi:[0,1,1]\n"
            "v_pk_fma_f32 v[2:3], v[144:145], v[178:179], v[2:3] op_sel_hi:[0,1,1]\n"
            "v_pk_fma_f32 v[4:5], v[148:149], v[180:181], v[4:5] op_sel_hi:[0,1,1]\n"
            "v_pk_fma_f32 v[6:7], v[148:149], v[182:183], v[6:7] op_sel_hi:[0,1,1]\n"
            "v_pk_fma_f32 v[8:9], v[152:153], v[184:185], v[8:9] op_sel_hi:[0,1,1]\n"
            "v_pk_fma_f32 v[10:11], v[152:153], v[186:187], v[10:11] op_sel_hi:[0,1,1]\n"
            "v_pk_fma_f32 v[12:13], v[156:157], v[188:189], v[12:13] op_sel_hi:[0,1,1]\n"
            "v_pk_fma_f32 v[14:15], v[156:157], v[190:191], v[14:15] op_sel_hi:[0,1,1]\n"
            "v_pk_add_f32 v[160:161], v[160:161], 1.0 op_sel_hi:[1,0]\n"
            "v_pk_add_f32 v[162:163], v[162:163], 1.0 op_sel_hi:[1,0]\n"
            "v_pk_add_f32 v[164:165], v[164:165], 1.0 op_sel_hi:[1,0]\n"
            "v_pk_add_f32 v[166:167], v[166:167], 1.0 op_sel_hi:[1,0]\n"
            "s_cmp_eq_u32 s69, 1\n"
            "s_cbranch_scc1 Lf00_%=\n"
            "Lr00_%=:\n"
            "v_mfma_f32_16x16x32_bf16 v[140:143], v[192:195], v[124:127], 0\n"
            "ds_read_b32 v230, v211 offset:4\n"
            "ds_read_u16 v210, v237\n"
            "v_rcp_f32 v160, v160\n"
            "v_rcp_f32 v161, v161\n"
            "v_mfma_f32_4x4x4_16b_bf16 v[176:179], v[208:209], v[48:49], 0 cbsz:2 abid:1\n"
            "v_rcp_f32 v162, v162\n"
            "v_mfma_f32_4x4x4_16b_bf16 v[180:183], v[208:209], v[50:51], 0 cbsz:2 abid:1\n"
            "v_rcp_f32 v163, v163\n"
            "v_mfma_f32_4x4x4_16b_bf16 v[184:187], v[208:209], v[52:53], 0 cbsz:2 abid:1\n"
            "v_rcp_f32 v164, v164\n"
            "v_mfma_f32_4x4x4_16b_bf16 v[188:191], v[208:209], v[54:55], 0 cbsz:2 abid:1\n"
            "v_rcp_f32 v165, v165\n"
            "v_add_u32_dpp v220, v196, v214 quad_perm:[1,1,1,1] row_mask:0xf bank_mask:0xf\n"
            "v_mfma_f32_4x4x4_16b_bf16 v[204:207], v[200:201], v[198:199], v[204:207]\n"
            "v_rcp_f32 v166, v166\n"
            "buffer_load_dwordx4 v[48:51], v220, s[28:31], 0 offen\n"
            "buffer_load_dwordx4 v[52:55], v220, s[28:31], 0 offen offset:256\n"
            "v_rcp_f32 v167, v167\n"
            "v_pk_fma_f32 v[0:1], v[144:145], v[176:177], v[0:1] op_sel:[1,0,0]\n"
            "v_pk_fma_f32 v[2:3], v[144:145], v[178:179], v[2:3] op_sel:[1,0,0]\n"
            "v_pk_fma_f32 v[4:5], v[148:149], v[180:181], v[4:5] op_sel:[1,0,0]\n"
            "v_pk_fma_f32 v[6:7], v[148:149], v[182:183], v[6:7] op_sel:[1,0,0]\n"
            "v_pk_fma_f32 v[8:9], v[152:153], v[184:185], v[8:9] op_sel:[1,0,0]\n"
            "v_pk_fma_f32 v[10:11], v[152:153], v[186:187], v[10:11] op_sel:[1,0,0]\n"
            "v_pk_fma_f32 v[12:13], v[156:157], v[188:189], v[12:13] op_sel:[1,0,0]\n"
            "v_pk_fma_f32 v[14:15], v[156:157], v[190:191], v[14:15] op_sel:[1,0,0]\n"
            "v_pk_mul_f32 v[160:161], v[160:161], v[128:129]\n"
            "v_pk_mul_f32 v[162:163], v[162:163], v[130:131]\n"
            "v_pk_mul_f32 v[164:165], v[164:165], v[132:133]\n"
            "v_pk_mul_f32 v[166:167], v[166:167], v[134:135]\n"
            "s_cmp_eq_u32 s69, 2\n"
            "s_cbranch_scc1 Lf01_%=\n"
            "Lr01_%=:\n"
            "s_waitcnt vmcnt(12) lgkmcnt(0)\n"
            "v_mov_b64 v[192:193], v[232:233]\n"
            "v_mov_b64 v[194:195], v[234:235]\n"
            "v_add_u32 v230, v230, v229\n"
            "buffer_load_dwordx4 v[232:235], v230, s[24:27], 0 offen\n"
            "s_bitcmp0_b32 s85, 0\n"
            "s_cbranch_scc1 Lnps_%=\n"
            "s_buffer_load_dwordx2 s[86:87], s[28:31], s67 offset:60\n"
            "s_buffer_load_dwordx2 s[86:87], s[28:31], s67 offset:188\n"
            "s_buffer_load_dwordx2 s[86:87], s[28:31], s67 offset:316\n"
            "s_buffer_load_dwordx2 s[86:87], s[28:31], s67 offset:444\n"
            "s_add_u32 s67, s67, 524288\n"
            "Lnps_%=:\n"
            "s_lshr_b32 s85, s85, 1\n"
            "v_mfma_f32_16x16x32_bf16 v[128:131], v[192:195], v[96:99], 0\n"
            "v_exp_f32 v168, v136\n"
            "v_exp_f32 v169, v137\n"
            "v_mfma_f32_4x4x4_16b_bf16 v[176:179], v[208:209], v[64:65], 0 cbsz:2 abid:2\n"
            "v_exp_f32 v170, v138\n"
            "v_mfma_f32_4x4x4_16b_bf16 v[180:183], v[208:209], v[66:67], 0 cbsz:2 abid:2\n"
            "v_exp_f32 v171, v139\n"
            "v_mfma_f32_4x4x4_16b_bf16 v[184:187], v[208:209], v[68:69], 0 cbsz:2 abid:2\n"
            "v_exp_f32 v172, v140\n"
            "v_mfma_f32_4x4x4_16b_bf16 v[188:191], v[208:209], v[70:71], 0 cbsz:2 abid:2\n"
            "v_exp_f32 v173, v141\n"
            "v_add_u32_dpp v221, v196, v214 quad_perm:[2,2,2,2] row_mask:0xf bank_mask:0xf\n"
            "v_exp_f32 v174, v142\n"
            "buffer_load_dwordx4 v[64:67], v221, s[28:31], 0 offen\n"
            "buffer_load_dwordx4 v[68:71], v221, s[28:31], 0 offen offset:256\n"
            "v_exp_f32 v175, v143\n"
            "v_pk_fma_f32 v[0:1], v[146:147], v[176:177], v[0:1] op_sel_hi:[0,1,1]\n"
            "v_pk_fma_f32 v[2:3], v[146:147], v[178:179], v[2:3] op_sel_hi:[0,1,1]\n"
            "v_pk_fma_f32 v[4:5], v[150:151], v[180:181], v[4:5] op_sel_hi:[0,1,1]\n"
            "v_pk_fma_f32 v[6:7], v[150:151], v[182:183], v[6:7] op_sel_hi:[0,1,1]\n"
            "v_pk_fma_f32 v[8:9], v[154:155], v[184:185], v[8:9] op_sel_hi:[0,1,1]\n"
            "v_pk_fma_f32 v[10:11], v[154:155], v[186:187], v[10:11] op_sel_hi:[0,1,1]\n"
            "v_pk_fma_f32 v[12:13], v[158:159], v[188:189], v[12:13] op_sel_hi:[0,1,1]\n"
            "v_pk_fma_f32 v[14:15], v[158:159], v[190:191], v[14:15] op_sel_hi:[0,1,1]\n"
            "v_pk_add_f32 v[168:169], v[168:169], 1.0 op_sel_hi:[1,0]\n"
            "v_pk_add_f32 v[170:171], v[170:171], 1.0 op_sel_hi:[1,0]\n"
            "v_pk_add_f32 v[172:173], v[172:173], 1.0 op_sel_hi:[1,0]\n"
            "v_pk_add_f32 v[174:175], v[174:175], 1.0 op_sel_hi:[1,0]\n"
            "v_cvt_pk_bf16_f32 v204, v204, v205\n"
            "v_cvt_pk_bf16_f32 v205, v206, v207\n"
            "ds_write_b64 v212, v[204:205]\n"
            "s_cmp_eq_u32 s69, 3\n"
            "s_cbranch_scc1 Lf02_%=\n"
            "Lr02_%=:\n"
            "v_mfma_f32_16x16x32_bf16 v[132:135], v[192:195], v[100:103], 0\n"
            "v_rcp_f32 v168, v168\n"
            "v_rcp_f32 v169, v169\n"
            "v_mfma_f32_4x4x4_16b_bf16 v[176:179], v[208:209], v[80:81], 0 cbsz:2 abid:3\n"
            "v_rcp_f32 v170, v170\n"
            "v_mfma_f32_4x4x4_16b_bf16 v[180:183], v[208:209], v[82:83], 0 cbsz:2 abid:3\n"
            "v_rcp_f32 v171, v171\n"
            "v_mfma_f32_4x4x4_16b_bf16 v[184:187], v[208:209], v[84:85], 0 cbsz:2 abid:3\n"
            "v_rcp_f32 v172, v172\n"
            "v_mfma_f32_4x4x4_16b_bf16 v[188:191], v[208:209], v[86:87], 0 cbsz:2 abid:3\n"
            "v_rcp_f32 v173, v173\n"
            "v_add_u32_dpp v222, v196, v214 quad_perm:[3,3,3,3] row_mask:0xf bank_mask:0xf\n"
            "v_rcp_f32 v174, v174\n"
            "buffer_load_dwordx4 v[80:83], v222, s[28:31], 0 offen\n"
            "buffer_load_dwordx4 v[84:87], v222, s[28:31], 0 offen offset:256\n"
            "v_rcp_f32 v175, v175\n"
            "v_pk_fma_f32 v[0:1], v[146:147], v[176:177], v[0:1] op_sel:[1,0,0]\n"
            "v_pk_fma_f32 v[2:3], v[146:147], v[178:179], v[2:3] op_sel:[1,0,0]\n"
            "v_pk_fma_f32 v[4:5], v[150:151], v[180:181], v[4:5] op_sel:[1,0,0]\n"
            "v_pk_fma_f32 v[6:7], v[150:151], v[182:183], v[6:7] op_sel:[1,0,0]\n"
            "v_pk_fma_f32 v[8:9], v[154:155], v[184:185], v[8:9] op_sel:[1,0,0]\n"
            "v_pk_fma_f32 v[10:11], v[154:155], v[186:187], v[10:11] op_sel:[1,0,0]\n"
            "v_pk_fma_f32 v[12:13], v[158:159], v[188:189], v[12:13] op_sel:[1,0,0]\n"
            "v_pk_fma_f32 v[14:15], v[158:159], v[190:191], v[14:15] op_sel:[1,0,0]\n"
            "v_pk_mul_f32 v[168:169], v[168:169], v[136:137]\n"
            "v_pk_mul_f32 v[170:171], v[170:171], v[138:139]\n"
            "v_pk_mul_f32 v[172:173], v[172:173], v[140:141]\n"
            "v_pk_mul_f32 v[174:175], v[174:175], v[142:143]\n"
            "s_cmp_eq_u32 s69, 4\n"
            "s_cbranch_scc1 Lf03_%=\n"
            "Lr03_%=:\n"
            "v_add_u32 v238, 32, v238\n"
            "ds_read_b128 v[196:199], v210\n"
            "ds_read_u16 v211, v238\n"
            "v_mfma_f32_16x16x32_bf16 v[136:139], v[192:195], v[104:107], 0\n"
            "v_exp_f32 v144, v128\n"
            "v_exp_f32 v145, v129\n"
            "s_waitcnt vmcnt(13)\n"
            "v_mfma_f32_4x4x4_16b_bf16 v[176:179], v[208:209], v[40:41], 0 cbsz:2\n"
            "v_exp_f32 v146, v130\n"
            "v_mfma_f32_4x4x4_16b_bf16 v[180:183], v[208:209], v[42:43], 0 cbsz:2\n"
            "v_exp_f32 v147, v131\n"
            "v_mfma_f32_4x4x4_16b_bf16 v[184:187], v[208:209], v[44:45], 0 cbsz:2\n"
            "v_exp_f32 v148, v132\n"
            "v_mfma_f32_4x4x4_16b_bf16 v[188:191], v[208:209], v[46:47], 0 cbsz:2\n"
            "v_exp_f32 v149, v133\n"
            "v_exp_f32 v150, v134\n"
            "buffer_load_dwordx4 v[40:43], v219, s[28:31], 0 offen offset:512\n"
            "buffer_load_dwordx4 v[44:47], v219, s[28:31], 0 offen offset:768\n"
            "v_exp_f32 v151, v135\n"
            "v_pk_fma_f32 v[16:17], v[160:161], v[176:177], v[16:17] op_sel_hi:[0,1,1]\n"
            "v_pk_fma_f32 v[18:19], v[160:161], v[178:179], v[18:19] op_sel_hi:[0,1,1]\n"
            "v_pk_fma_f32 v[20:21], v[164:165], v[180:181], v[20:21] op_sel_hi:[0,1,1]\n"
            "v_pk_fma_f32 v[22:23], v[164:165], v[182:183], v[22:23] op_sel_hi:[0,1,1]\n"
            "v_pk_fma_f32 v[24:25], v[168:169], v[184:185], v[24:25] op_sel_hi:[0,1,1]\n"
            "v_pk_fma_f32 v[26:27], v[168:169], v[186:187], v[26:27] op_sel_hi:[0,1,1]\n"
            "v_pk_fma_f32 v[28:29], v[172:173], v[188:189], v[28:29] op_sel_hi:[0,1,1]\n"
            "v_pk_fma_f32 v[30:31], v[172:173], v[190:191], v[30:31] op_sel_hi:[0,1,1]\n"
            "v_pk_add_f32 v[144:145], v[144:145], 1.0 op_sel_hi:[1,0]\n"
            "v_pk_add_f32 v[146:147], v[146:147], 1.0 op_sel_hi:[1,0]\n"
            "v_pk_add_f32 v[148:149], v[148:149], 1.0 op_sel_hi:[1,0]\n"
            "v_pk_add_f32 v[150:151], v[150:151], 1.0 op_sel_hi:[1,0]\n"
            "s_cmp_eq_u32 s69, 1\n"
            "s_cbranch_scc1 Lf10_%=\n"
            "Lr10_%=:\n"
            "v_mfma_f32_16x16x32_bf16 v[140:143], v[192:195], v[108:111], 0\n"
            "v_rcp_f32 v144, v144\n"
            "v_rcp_f32 v145, v145\n"
            "v_mfma_f32_4x4x4_16b_bf16 v[176:179], v[208:209], v[56:57], 0 cbsz:2 abid:1\n"
            "v_rcp_f32 v146, v146\n"
            "v_mfma_f32_4x4x4_16b_bf16 v[180:183], v[208:209], v[58:59], 0 cbsz:2 abid:1\n"
            "v_rcp_f32 v147, v147\n"
            "v_mfma_f32_4x4x4_16b_bf16 v[184:187], v[208:209], v[60:61], 0 cbsz:2 abid:1\n"
            "v_rcp_f32 v148, v148\n"
            "v_mfma_f32_4x4x4_16b_bf16 v[188:191], v[208:209], v[62:63], 0 cbsz:2 abid:1\n"
            "v_rcp_f32 v149, v149\n"
            "v_rcp_f32 v150, v150\n"
            "buffer_load_dwordx4 v[56:59], v220, s[28:31], 0 offen offset:512\n"
            "buffer_load_dwordx4 v[60:63], v220, s[28:31], 0 offen offset:768\n"
            "v_rcp_f32 v151, v151\n"
            "v_pk_fma_f32 v[16:17], v[160:161], v[176:177], v[16:17] op_sel:[1,0,0]\n"
            "v_pk_fma_f32 v[18:19], v[160:161], v[178:179], v[18:19] op_sel:[1,0,0]\n"
            "v_pk_fma_f32 v[20:21], v[164:165], v[180:181], v[20:21] op_sel:[1,0,0]\n"
            "v_pk_fma_f32 v[22:23], v[164:165], v[182:183], v[22:23] op_sel:[1,0,0]\n"
            "v_pk_fma_f32 v[24:25], v[168:169], v[184:185], v[24:25] op_sel:[1,0,0]\n"
            "v_pk_fma_f32 v[26:27], v[168:169], v[186:187], v[26:27] op_sel:[1,0,0]\n"
            "v_pk_fma_f32 v[28:29], v[172:173], v[188:189], v[28:29] op_sel:[1,0,0]\n"
            "v_pk_fma_f32 v[30:31], v[172:173], v[190:191], v[30:31] op_sel:[1,0,0]\n"
            "v_pk_mul_f32 v[144:145], v[144:145], v[128:129]\n"
            "v_pk_mul_f32 v[146:147], v[146:147], v[130:131]\n"
            "v_pk_mul_f32 v[148:149], v[148:149], v[132:133]\n"
            "v_pk_mul_f32 v[150:151], v[150:151], v[134:135]\n"
            "s_cmp_eq_u32 s69, 2\n"
            "s_cbranch_scc1 Lf11_%=\n"
            "Lr11_%=:\n"
            "v_mfma_f32_16x16x32_bf16 v[128:131], v[192:195], v[112:115], 0\n"
            "v_exp_f32 v152, v136\n"
            "v_exp_f32 v153, v137\n"
            "s_waitcnt vmcnt(13)\n"
            "v_mfma_f32_4x4x4_16b_bf16 v[176:179], v[208:209], v[72:73], 0 cbsz:2 abid:2\n"
            "v_exp_f32 v154, v138\n"
            "v_mfma_f32_4x4x4_16b_bf16 v[180:183], v[208:209], v[74:75], 0 cbsz:2 abid:2\n"
            "v_exp_f32 v155, v139\n"
            "v_mfma_f32_4x4x4_16b_bf16 v[184:187], v[208:209], v[76:77], 0 cbsz:2 abid:2\n"
            "v_exp_f32 v156, v140\n"
            "v_mfma_f32_4x4x4_16b_bf16 v[188:191], v[208:209], v[78:79], 0 cbsz:2 abid:2\n"
            "v_exp_f32 v157, v141\n"
            "v_exp_f32 v158, v142\n"
            "buffer_load_dwordx4 v[72:75], v221, s[28:31], 0 offen offset:512\n"
            "buffer_load_dwordx4 v[76:79], v221, s[28:31], 0 offen offset:768\n"
            "v_exp_f32 v159, v143\n"
            "v_pk_fma_f32 v[16:17], v[162:163], v[176:177], v[16:17] op_sel_hi:[0,1,1]\n"
            "v_pk_fma_f32 v[18:19], v[162:163], v[178:179], v[18:19] op_sel_hi:[0,1,1]\n"
            "v_pk_fma_f32 v[20:21], v[166:167], v[180:181], v[20:21] op_sel_hi:[0,1,1]\n"
            "v_pk_fma_f32 v[22:23], v[166:167], v[182:183], v[22:23] op_sel_hi:[0,1,1]\n"
            "v_pk_fma_f32 v[24:25], v[170:171], v[184:185], v[24:25] op_sel_hi:[0,1,1]\n"
            "v_pk_fma_f32 v[26:27], v[170:171], v[186:187], v[26:27] op_sel_hi:[0,1,1]\n"
            "v_pk_fma_f32 v[28:29], v[174:175], v[188:189], v[28:29] op_sel_hi:[0,1,1]\n"
            "v_pk_fma_f32 v[30:31], v[174:175], v[190:191], v[30:31] op_sel_hi:[0,1,1]\n"
            "v_pk_add_f32 v[152:153], v[152:153], 1.0 op_sel_hi:[1,0]\n"
            "v_pk_add_f32 v[154:155], v[154:155], 1.0 op_sel_hi:[1,0]\n"
            "v_pk_add_f32 v[156:157], v[156:157], 1.0 op_sel_hi:[1,0]\n"
            "v_pk_add_f32 v[158:159], v[158:159], 1.0 op_sel_hi:[1,0]\n"
            "s_cmp_eq_u32 s69, 3\n"
            "s_cbranch_scc1 Lf12_%=\n"
            "Lr12_%=:\n"
            "v_mfma_f32_16x16x32_bf16 v[132:135], v[192:195], v[116:119], 0\n"
            "v_rcp_f32 v152, v152\n"
            "v_rcp_f32 v153, v153\n"
            "v_mfma_f32_4x4x4_16b_bf16 v[176:179], v[208:209], v[88:89], 0 cbsz:2 abid:3\n"
            "v_rcp_f32 v154, v154\n"
            "v_mfma_f32_4x4x4_16b_bf16 v[180:183], v[208:209], v[90:91], 0 cbsz:2 abid:3\n"
            "v_rcp_f32 v155, v155\n"
            "v_mfma_f32_4x4x4_16b_bf16 v[184:187], v[208:209], v[92:93], 0 cbsz:2 abid:3\n"
            "v_rcp_f32 v156, v156\n"
            "v_mfma_f32_4x4x4_16b_bf16 v[188:191], v[208:209], v[94:95], 0 cbsz:2 abid:3\n"
            "ds_read_b64 v[208:209], v213\n"
            "v_rcp_f32 v157, v157\n"
            "v_rcp_f32 v158, v158\n"
            "buffer_load_dwordx4 v[88:91], v222, s[28:31], 0 offen offset:512\n"
            "buffer_load_dwordx4 v[92:95], v222, s[28:31], 0 offen offset:768\n"
            "v_rcp_f32 v159, v159\n"
            "v_pk_fma_f32 v[16:17], v[162:163], v[176:177], v[16:17] op_sel:[1,0,0]\n"
            "v_pk_fma_f32 v[18:19], v[162:163], v[178:179], v[18:19] op_sel:[1,0,0]\n"
            "v_pk_fma_f32 v[20:21], v[166:167], v[180:181], v[20:21] op_sel:[1,0,0]\n"
            "v_pk_fma_f32 v[22:23], v[166:167], v[182:183], v[22:23] op_sel:[1,0,0]\n"
            "v_pk_fma_f32 v[24:25], v[170:171], v[184:185], v[24:25] op_sel:[1,0,0]\n"
            "v_pk_fma_f32 v[26:27], v[170:171], v[186:187], v[26:27] op_sel:[1,0,0]\n"
            "v_pk_fma_f32 v[28:29], v[174:175], v[188:189], v[28:29] op_sel:[1,0,0]\n"
            "v_pk_fma_f32 v[30:31], v[174:175], v[190:191], v[30:31] op_sel:[1,0,0]\n"
            "v_pk_mul_f32 v[152:153], v[152:153], v[136:137]\n"
            "v_pk_mul_f32 v[154:155], v[154:155], v[138:139]\n"
            "v_pk_mul_f32 v[156:157], v[156:157], v[140:141]\n"
            "v_pk_mul_f32 v[158:159], v[158:159], v[142:143]\n"
            "s_cmp_eq_u32 s69, 4\n"
            "s_cbranch_scc1 Lf13_%=\n"
            "Lr13_%=:\n"
            "s_and_b32 s68, s72, 0xff\n"
            "s_add_u32 s69, s69, s68\n"
            "s_lshr_b32 s72, s72, 8\n"
            "v_mov_b32 v231, v239\n"
            "v_mov_b32 v239, v252\n"
            "s_and_b32 s68, s62, 14\n"
            "s_lshr_b32 s62, s62, 4\n"
            "s_cmp_lg_u32 s68, 0\n"
            "s_cbranch_scc1 Lb1_%=\n"
            "v_mov_b32 v215, v216\n"
            "s_branch Lnext_%=\n"
            "Lb1_%=:\n"
            "s_cmp_lg_u32 s68, 6\n"
            "s_cbranch_scc1 Lb2_%=\n"
            "s_lshl_b32 s68, s3, 2\n"
            "v_mov_b32 v176, s68\n"
            "ds_write_b32 v176, v176\n"
            "s_branch Lnext_%=\n"
            "Lb2_%=:\n"
            "s_cmp_lg_u32 s68, 8\n"
            "s_cbranch_scc1 Lnext_%=\n"
            "s_waitcnt vmcnt(0)\n"
            "v_mov_b32 v176, 4\n"
            "v_mov_b32 v177, 1\n"
            "s_mov_b64 exec, 1\n"
            "ds_add_rtn_u32 v177, v176, v177\n"
            "s_waitcnt lgkmcnt(0)\n"
            "v_readfirstlane_b32 s68, v177\n"
            "s_mov_b64 exec, 0xff\n"
            "v_mov_b32 v176, s90\n"
            "v_mov_b32 v177, s90\n"
            "v_mov_b32 v178, s90\n"
            "v_mov_b32 v179, s90\n"
            "s_cmp_lg_u32 s68, 7\n"
            "s_cbranch_scc1 Lpub2_%=\n"
            "global_store_dwordx4 v214, v[176:179], s[56:57] sc1\n"
            "Lpub2_%=:\n"
            "s_mov_b64 exec, -1\n"
            "s_branch Lnext_%=\n"
            "Lprio_%=:\n"
            "s_cmp_eq_u32 s73, 1\n"
            "s_cbranch_scc1 Llast_%=\n"
            "s_lshl_b32 s68, s73, 1\n"
            "s_lshr_b32 s68, s61, s68\n"
            "s_bitcmp1_b32 s68, 1\n"
            "s_cbranch_scc1 Lq2_%=\n"
            "s_bitcmp1_b32 s68, 0\n"
            "s_setprio 0\n"
            "s_cbranch_scc0 Lq9_%=\n"
            "s_setprio 1\n"
            "s_branch Lq9_%=\n"
            "Lq2_%=:\n"
            "s_bitcmp1_b32 s68, 0\n"
            "s_setprio 2\n"
            "s_cbranch_scc0 Lq9_%=\n"
            "s_setprio 3\n"
            "Lq9_%=:\n"
            "s_cmp_lg_u32 s73, 2\n"
            "s_cbranch_scc1 Ld2_%=\n"
            "v_mov_b32 v223, 0\n"
            "global_load_dword v223, v223, s[58:59] sc1\n"
            "s_branch Ld2_%=\n"
            "Lf00_%=:\n"
            "s_call_b64 s[70:71], Lflush0_%=\n"
            "s_branch Lr00_%=\n"
            "Lf01_%=:\n"
            "s_call_b64 s[70:71], Lflush0_%=\n"
            "s_branch Lr01_%=\n"
            "Lf02_%=:\n"
            "s_call_b64 s[70:71], Lflush0_%=\n"
            "s_branch Lr02_%=\n"
            "Lf03_%=:\n"
            "s_call_b64 s[70:71], Lflush0_%=\n"
            "s_branch Lr03_%=\n"
            "Lflush0_%=:\n"
            "s_bitcmp0_b32 s62, 0\n"
            "s_cbranch_scc1 Lfa0_%=\n"
            "Lfw0_%=:\n"
            "v_mov_b32 v184, s41\n"
            "ds_read_b32 v184, v184\n"
            "ds_read_b128 v[176:179], v231 offset:0\n"
            "ds_read_b128 v[180:183], v231 offset:1024\n"
            "s_waitcnt lgkmcnt(0)\n"
            "v_readfirstlane_b32 s68, v184\n"
            "s_cmp_eq_u32 s68, 0\n"
            "s_cbranch_scc1 Lfw0_%=\n"
            "v_mfma_f32_4x4x4_16b_bf16 v[0:3], v[224:225], v[176:177], v[0:3]\n"
            "v_mfma_f32_4x4x4_16b_bf16 v[4:7], v[224:225], v[178:179], v[4:7]\n"
            "v_mfma_f32_4x4x4_16b_bf16 v[8:11], v[224:225], v[180:181], v[8:11]\n"
            "v_mfma_f32_4x4x4_16b_bf16 v[12:15], v[224:225], v[182:183], v[12:15]\n"
            "s_nop 1\n"
            "Lfa0_%=:\n"
            "v_cvt_pk_bf16_f32 v176, v0, v1\n"
            "v_cvt_pk_bf16_f32 v177, v2, v3\n"
            "v_cvt_pk_bf16_f32 v178, v4, v5\n"
            "v_cvt_pk_bf16_f32 v179, v6, v7\n"
            "v_cvt_pk_bf16_f32 v180, v8, v9\n"
            "v_cvt_pk_bf16_f32 v181, v10, v11\n"
            "v_cvt_pk_bf16_f32 v182, v12, v13\n"
            "v_cvt_pk_bf16_f32 v183, v14, v15\n"
            "s_bitcmp1_b32 s62, 1\n"
            "s_cbranch_scc1 Lpark0_%=\n"
            "buffer_store_dwordx4 v[176:179], v215, s[20:23], 0 offen offset:0 sc1\n"
            "buffer_store_dwordx4 v[180:183], v215, s[20:23], 0 offen offset:256 sc1\n"
            "s_branch Lzero0_%=\n"
            "Lpark0_%=:\n"
            "ds_write_b128 v231, v[176:179] offset:0\n"
            "ds_write_b128 v231, v[180:183] offset:1024\n"
            "Lzero0_%=:\n"
            "v_mov_b64 v[0:1], 0\n"
            "v_mov_b64 v[2:3], 0\n"
            "v_mov_b64 v[4:5], 0\n"
            "v_mov_b64 v[6:7], 0\n"
            "v_mov_b64 v[8:9], 0\n"
            "v_mov_b64 v[10:11], 0\n"
            "v_mov_b64 v[12:13], 0\n"
            "v_mov_b64 v[14:15], 0\n"
            "s_setpc_b64 s[70:71]\n"
            "Lf10_%=:\n"
            "s_call_b64 s[70:71], Lflush1_%=\n"
            "s_branch Lr10_%=\n"
            "Lf11_%=:\n"
            "s_call_b64 s[70:71], Lflush1_%=\n"
            "s_branch Lr11_%=\n"
            "Lf12_%=:\n"
            "s_call_b64 s[70:71], Lflush1_%=\n"
            "s_branch Lr12_%=\n"
            "Lf13_%=:\n"
            "s_call_b64 s[70:71], Lflush1_%=\n"
            "s_branch Lr13_%=\n"
            "Lflush1_%=:\n"
            "s_bitcmp0_b32 s62, 0\n"
            "s_cbranch_scc1 Lfa1_%=\n"
            "Lfw1_%=:\n"
            "v_mov_b32 v184, s41\n"
            "ds_read_b32 v184, v184\n"
            "ds_read_b128 v[176:179], v231 offset:2048\n"
            "ds_read_b128 v[180:183], v231 offset:3072\n"
            "s_waitcnt lgkmcnt(0)\n"
            "v_readfirstlane_b32 s68, v184\n"
            "s_cmp_eq_u32 s68, 0\n"
            "s_cbranch_scc1 Lfw1_%=\n"
            "v_mfma_f32_4x4x4_16b_bf16 v[16:19], v[224:225], v[176:177], v[16:19]\n"
            "v_mfma_f32_4x4x4_16b_bf16 v[20:23], v[224:225], v[178:179], v[20:23]\n"
            "v_mfma_f32_4x4x4_16b_bf16 v[24:27], v[224:225], v[180:181], v[24:27]\n"
            "v_mfma_f32_4x4x4_16b_bf16 v[28:31], v[224:225], v[182:183], v[28:31]\n"
            "s_nop 1\n"
            "Lfa1_%=:\n"
            "v_cvt_pk_bf16_f32 v176, v16, v17\n"
            "v_cvt_pk_bf16_f32 v177, v18, v19\n"
            "v_cvt_pk_bf16_f32 v178, v20, v21\n"
            "v_cvt_pk_bf16_f32 v179, v22, v23\n"
            "v_cvt_pk_bf16_f32 v180, v24, v25\n"
            "v_cvt_pk_bf16_f32 v181, v26, v27\n"
            "v_cvt_pk_bf16_f32 v182, v28, v29\n"
            "v_cvt_pk_bf16_f32 v183, v30, v31\n"
            "s_bitcmp1_b32 s62, 1\n"
            "s_cbranch_scc1 Lpark1_%=\n"
            "buffer_store_dwordx4 v[176:179], v215, s[20:23], 0 offen offset:512 sc1\n"
            "buffer_store_dwordx4 v[180:183], v215, s[20:23], 0 offen offset:768 sc1\n"
            "s_branch Lzero1_%=\n"
            "Lpark1_%=:\n"
            "ds_write_b128 v231, v[176:179] offset:2048\n"
            "ds_write_b128 v231, v[180:183] offset:3072\n"
            "Lzero1_%=:\n"
            "v_mov_b64 v[16:17], 0\n"
            "v_mov_b64 v[18:19], 0\n"
            "v_mov_b64 v[20:21], 0\n"
            "v_mov_b64 v[22:23], 0\n"
            "v_mov_b64 v[24:25], 0\n"
            "v_mov_b64 v[26:27], 0\n"
            "v_mov_b64 v[28:29], 0\n"
            "v_mov_b64 v[30:31], 0\n"
            "s_setpc_b64 s[70:71]\n"
            "Llk_%=:\n"
            "s_waitcnt vmcnt(4)\n"
            "v_readfirstlane_b32 s73, v223\n"
            "s_cmp_lg_u32 s73, s90\n"
            "s_cbranch_scc1 Llk2_%=\n"
            "buffer_load_dwordx4 v[32:35], v217, s[20:23], s63 offen offset:0 sc1\n"
            "buffer_load_dwordx4 v[36:39], v217, s[20:23], s63 offen offset:256 sc1\n"
            "buffer_load_dwordx4 v[48:51], v217, s[20:23], s63 offen offset:512 sc1\n"
            "buffer_load_dwordx4 v[52:55], v217, s[20:23], s63 offen offset:768 sc1\n"
            "s_branch Llkr_%=\n"
            "Llk2_%=:\n"
            "v_mov_b32 v223, 0\n"
            "global_load_dword v223, v223, s[58:59] sc1\n"
            "s_branch Llkr_%=\n"
            "Lspin_%=:\n"
            "s_waitcnt vmcnt(0)\n"
            "s_mov_b32 s72, 0x400000\n"
            "Lsp1_%=:\n"
            "v_readfirstlane_b32 s73, v223\n"
            "s_sub_u32 s72, s72, 1\n"
            "s_cmp_eq_u32 s72, 0\n"
            "s_cbranch_scc1 Lsp2_%=\n"
            "s_cmp_eq_u32 s73, s90\n"
            "s_cbranch_scc1 Lsp2_%=\n"
            "s_sleep 1\n"
            "v_mov_b32 v223, 0\n"
            "global_load_dword v223, v223, s[58:59] sc1\n"
            "s_waitcnt vmcnt(0)\n"
            "s_branch Lsp1_%=\n"
            "Lsp2_%=:\n"
            "buffer_load_dwordx4 v[32:35], v217, s[20:23], s63 offen offset:0 sc1\n"
            "buffer_load_dwordx4 v[36:39], v217, s[20:23], s63 offen offset:256 sc1\n"
            "buffer_load_dwordx4 v[48:51], v217, s[20:23], s63 offen offset:512 sc1\n"
            "buffer_load_dwordx4 v[52:55], v217, s[20:23], s63 offen offset:768 sc1\n"
            "s_branch Lgo_%=\n"
            "Llast_%=:\n"
            "s_waitcnt vmcnt(16)\n"
            "v_readfirstlane_b32 s73, v223\n"
            "v_mov_b32 v222, v217\n"
            "s_cmp_eq_u32 s73, s90\n"
            "s_cbranch_scc1 Lp4_%=\n"
            "v_bfrev_b32 v222, 1\n"
            "v_mov_b32 v223, 0\n"
            "global_load_dword v223, v223, s[58:59] sc1\n"
            "Lp4_%=:\n"
            "v_mfma_f32_16x16x32_bf16 v[136:139], v[192:195], v[120:123], 0\n"
            "v_exp_f32 v160, v128\n"
            "v_exp_f32 v161, v129\n"
            "s_waitcnt vmcnt(13) lgkmcnt(0)\n"
            "v_mfma_f32_4x4x4_16b_bf16 v[176:179], v[208:209], v[32:33], 0 cbsz:2\n"
            "v_exp_f32 v162, v130\n"
            "v_mfma_f32_4x4x4_16b_bf16 v[180:183], v[208:209], v[34:35], 0 cbsz:2\n"
            "v_exp_f32 v163, v131\n"
            "v_mfma_f32_4x4x4_16b_bf16 v[184:187], v[208:209], v[36:37], 0 cbsz:2\n"
            "v_exp_f32 v164, v132\n"
            "v_mfma_f32_4x4x4_16b_bf16 v[188:191], v[208:209], v[38:39], 0 cbsz:2\n"
            "v_exp_f32 v165, v133\n"
            "v_exp_f32 v166, v134\n"
            "buffer_load_dwordx4 v[32:35], v222, s[20:23], s63 offen offset:0 sc1\n"
            "buffer_load_dwordx4 v[36:39], v222, s[20:23], s63 offen offset:256 sc1\n"
            "ds_read_b128 v[240:243], v228 offset:0\n"
            "ds_read_b128 v[244:247], v228 offset:1024\n"
            "ds_read_b128 v[248:251], v228 offset:2048\n"
            "ds_read_b128 v[96:99], v228 offset:3072\n"
            "ds_read_b128 v[100:103], v228 offset:4096\n"
            "v_exp_f32 v167, v135\n"
            "v_pk_fma_f32 v[0:1], v[144:145], v[176:177], v[0:1] op_sel_hi:[0,1,1]\n"
            "v_pk_fma_f32 v[2:3], v[144:145], v[178:179], v[2:3] op_sel_hi:[0,1,1]\n"
            "v_pk_fma_f32 v[4:5], v[148:149], v[180:181], v[4:5] op_sel_hi:[0,1,1]\n"
            "v_pk_fma_f32 v[6:7], v[148:149], v[182:183], v[6:7] op_sel_hi:[0,1,1]\n"
            "v_pk_fma_f32 v[8:9], v[152:153], v[184:185], v[8:9] op_sel_hi:[0,1,1]\n"
            "v_pk_fma_f32 v[10:11], v[152:153], v[186:187], v[10:11] op_sel_hi:[0,1,1]\n"
            "v_pk_fma_f32 v[12:13], v[156:157], v[188:189], v[12:13] op_sel_hi:[0,1,1]\n"
            "v_pk_fma_f32 v[14:15], v[156:157], v[190:191], v[14:15] op_sel_hi:[0,1,1]\n"
            "v_pk_add_f32 v[160:161], v[160:161], 1.0 op_sel_hi:[1,0]\n"
            "v_pk_add_f32 v[162:163], v[162:163], 1.0 op_sel_hi:[1,0]\n"
            "v_pk_add_f32 v[164:165], v[164:165], 1.0 op_sel_hi:[1,0]\n"
            "v_pk_add_f32 v[166:167], v[166:167], 1.0 op_sel_hi:[1,0]\n"
            "v_mfma_f32_16x16x32_bf16 v[140:143], v[192:195], v[124:127], 0\n"
            "v_rcp_f32 v160, v160\n"
            "v_rcp_f32 v161, v161\n"
            "v_mfma_f32_4x4x4_16b_bf16 v[176:179], v[208:209], v[48:49], 0 cbsz:2 abid:1\n"
            "v_rcp_f32 v162, v162\n"
            "v_mfma_f32_4x4x4_16b_bf16 v[180:183], v[208:209], v[50:51], 0 cbsz:2 abid:1\n"
            "v_rcp_f32 v163, v163\n"
            "v_mfma_f32_4x4x4_16b_bf16 v[184:187], v[208:209], v[52:53], 0 cbsz:2 abid:1\n"
            "v_rcp_f32 v164, v164\n"
            "v_mfma_f32_4x4x4_16b_bf16 v[188:191], v[208:209], v[54:55], 0 cbsz:2 abid:1\n"
            "v_rcp_f32 v165, v165\n"
            "v_rcp_f32 v166, v166\n"
            "buffer_load_dwordx4 v[48:51], v222, s[20:23], s63 offen offset:512 sc1\n"
            "buffer_load_dwordx4 v[52:55], v222, s[20:23], s63 offen offset:768 sc1\n"
            "ds_read_b128 v[104:107], v228 offset:5120\n"
            "ds_read_b128 v[108:111], v228 offset:6144\n"
            "ds_read_b128 v[112:115], v228 offset:7168\n"
            "ds_read_b128 v[116:119], v228 offset:8192\n"
            "ds_read_b128 v[196:199], v228 offset:9216\n"
            "ds_read_b128 v[200:203], v228 offset:10240\n"
            "v_rcp_f32 v167, v167\n"
            "v_pk_fma_f32 v[0:1], v[144:145], v[176:177], v[0:1] op_sel:[1,0,0]\n"
            "v_pk_fma_f32 v[2:3], v[144:145], v[178:179], v[2:3] op_sel:[1,0,0]\n"
            "v_pk_fma_f32 v[4:5], v[148:149], v[180:181], v[4:5] op_sel:[1,0,0]\n"
            "v_pk_fma_f32 v[6:7], v[148:149], v[182:183], v[6:7] op_sel:[1,0,0]\n"
            "v_pk_fma_f32 v[8:9], v[152:153], v[184:185], v[8:9] op_sel:[1,0,0]\n"
            "v_pk_fma_f32 v[10:11], v[152:153], v[186:187], v[10:11] op_sel:[1,0,0]\n"
            "v_pk_fma_f32 v[12:13], v[156:157], v[188:189], v[12:13] op_sel:[1,0,0]\n"
            "v_pk_fma_f32 v[14:15], v[156:157], v[190:191], v[14:15] op_sel:[1,0,0]\n"
            "v_pk_mul_f32 v[160:161], v[160:161], v[128:129]\n"
            "v_pk_mul_f32 v[162:163], v[162:163], v[130:131]\n"
            "v_pk_mul_f32 v[164:165], v[164:165], v[132:133]\n"
            "v_pk_mul_f32 v[166:167], v[166:167], v[134:135]\n"
            "v_exp_f32 v168, v136\n"
            "v_exp_f32 v169, v137\n"
            "s_waitcnt vmcnt(12)\n"
            "v_mfma_f32_4x4x4_16b_bf16 v[176:179], v[208:209], v[64:65], 0 cbsz:2 abid:2\n"
            "v_exp_f32 v170, v138\n"
            "v_mfma_f32_4x4x4_16b_bf16 v[180:183], v[208:209], v[66:67], 0 cbsz:2 abid:2\n"
            "v_exp_f32 v171, v139\n"
            "v_mfma_f32_4x4x4_16b_bf16 v[184:187], v[208:209], v[68:69], 0 cbsz:2 abid:2\n"
            "v_exp_f32 v172, v140\n"
            "v_mfma_f32_4x4x4_16b_bf16 v[188:191], v[208:209], v[70:71], 0 cbsz:2 abid:2\n"
            "v_exp_f32 v173, v141\n"
            "v_exp_f32 v174, v142\n"
            "ds_read_b128 v[64:67], v236 offset:0\n"
            "ds_read_b128 v[68:71], v236 offset:1024\n"
            "ds_read_b128 v[204:207], v228 offset:11264\n"
            "ds_read_b128 v[232:235], v228 offset:12288\n"
            "ds_read_b128 v[120:123], v228 offset:13312\n"
            "v_exp_f32 v175, v143\n"
            "v_pk_fma_f32 v[0:1], v[146:147], v[176:177], v[0:1] op_sel_hi:[0,1,1]\n"
            "v_pk_fma_f32 v[2:3], v[146:147], v[178:179], v[2:3] op_sel_hi:[0,1,1]\n"
            "v_pk_fma_f32 v[4:5], v[150:151], v[180:181], v[4:5] op_sel_hi:[0,1,1]\n"
            "v_pk_fma_f32 v[6:7], v[150:151], v[182:183], v[6:7] op_sel_hi:[0,1,1]\n"
            "v_pk_fma_f32 v[8:9], v[154:155], v[184:185], v[8:9] op_sel_hi:[0,1,1]\n"
            "v_pk_fma_f32 v[10:11], v[154:155], v[186:187], v[10:11] op_sel_hi:[0,1,1]\n"
            "v_pk_fma_f32 v[12:13], v[158:159], v[188:189], v[12:13] op_sel_hi:[0,1,1]\n"
            "v_pk_fma_f32 v[14:15], v[158:159], v[190:191], v[14:15] op_sel_hi:[0,1,1]\n"
            "v_pk_add_f32 v[168:169], v[168:169], 1.0 op_sel_hi:[1,0]\n"
            "v_pk_add_f32 v[170:171], v[170:171], 1.0 op_sel_hi:[1,0]\n"
            "v_pk_add_f32 v[172:173], v[172:173], 1.0 op_sel_hi:[1,0]\n"
            "v_pk_add_f32 v[174:175], v[174:175], 1.0 op_sel_hi:[1,0]\n"
            "v_rcp_f32 v168, v168\n"
            "v_rcp_f32 v169, v169\n"
            "v_mfma_f32_4x4x4_16b_bf16 v[176:179], v[208:209], v[80:81], 0 cbsz:2 abid:3\n"
            "v_rcp_f32 v170, v170\n"
            "v_mfma_f32_4x4x4_16b_bf16 v[180:183], v[208:209], v[82:83], 0 cbsz:2 abid:3\n"
            "v_rcp_f32 v171, v171\n"
            "v_mfma_f32_4x4x4_16b_bf16 v[184:187], v[208:209], v[84:85], 0 cbsz:2 abid:3\n"
            "v_rcp_f32 v172, v172\n"
            "v_mfma_f32_4x4x4_16b_bf16 v[188:191], v[208:209], v[86:87], 0 cbsz:2 abid:3\n"
            "v_rcp_f32 v173, v173\n"
            "v_rcp_f32 v174, v174\n"
            "ds_read_b128 v[80:83], v236 offset:2048\n"
            "ds_read_b128 v[84:87], v236 offset:3072\n"
            "ds_read_b128 v[124:127], v228 offset:14336\n"
            "ds_read_b128 v[192:195], v228 offset:15360\n"
            "ds_read_b128 v[128:131], v228 offset:16384\n"
            "ds_read_b128 v[132:135], v228 offset:17408\n"
            "ds_read_b128 v[236:239], v228 offset:18432\n"
            "v_rcp_f32 v175, v175\n"
            "v_pk_fma_f32 v[0:1], v[146:147], v[176:177], v[0:1] op_sel:[1,0,0]\n"
            "v_pk_fma_f32 v[2:3], v[146:147], v[178:179], v[2:3] op_sel:[1,0,0]\n"
            "v_pk_fma_f32 v[4:5], v[150:151], v[180:181], v[4:5] op_sel:[1,0,0]\n"
            "v_pk_fma_f32 v[6:7], v[150:151], v[182:183], v[6:7] op_sel:[1,0,0]\n"
            "v_pk_fma_f32 v[8:9], v[154:155], v[184:185], v[8:9] op_sel:[1,0,0]\n"
            "v_pk_fma_f32 v[10:11], v[154:155], v[186:187], v[10:11] op_sel:[1,0,0]\n"
            "v_pk_fma_f32 v[12:13], v[158:159], v[188:189], v[12:13] op_sel:[1,0,0]\n"
            "v_pk_fma_f32 v[14:15], v[158:159], v[190:191], v[14:15] op_sel:[1,0,0]\n"
            "v_pk_mul_f32 v[168:169], v[168:169], v[136:137]\n"
            "v_pk_mul_f32 v[170:171], v[170:171], v[138:139]\n"
            "v_pk_mul_f32 v[172:173], v[172:173], v[140:141]\n"
            "v_pk_mul_f32 v[174:175], v[174:175], v[142:143]\n"
            "Llkr_%=:\n"
            "s_waitcnt vmcnt(8)\n"
            "v_mfma_f32_4x4x4_16b_bf16 v[176:179], v[208:209], v[40:41], 0 cbsz:2\n"
            "v_mfma_f32_4x4x4_16b_bf16 v[180:183], v[208:209], v[42:43], 0 cbsz:2\n"
            "v_mfma_f32_4x4x4_16b_bf16 v[184:187], v[208:209], v[44:45], 0 cbsz:2\n"
            "v_mfma_f32_4x4x4_16b_bf16 v[188:191], v[208:209], v[46:47], 0 cbsz:2\n"
            "ds_read_b128 v[136:139], v228 offset:19456\n"
            "ds_read_b128 v[140:143], v228 offset:20480\n"
            "ds_read_b128 v[144:147], v228 offset:21504\n"
            "ds_read_b128 v[148:151], v228 offset:22528\n"
            "ds_read_b128 v[152:155], v228 offset:23552\n"
            "ds_read_b128 v[156:159], v228 offset:24576\n"
            "s_nop 1\n"
            "v_pk_fma_f32 v[16:17], v[160:161], v[176:177], v[16:17] op_sel_hi:[0,1,1]\n"
            "v_pk_fma_f32 v[18:19], v[160:161], v[178:179], v[18:19] op_sel_hi:[0,1,1]\n"
            "v_pk_fma_f32 v[20:21], v[164:165], v[180:181], v[20:21] op_sel_hi:[0,1,1]\n"
            "v_pk_fma_f32 v[22:23], v[164:165], v[182:183], v[22:23] op_sel_hi:[0,1,1]\n"
            "v_pk_fma_f32 v[24:25], v[168:169], v[184:185], v[24:25] op_sel_hi:[0,1,1]\n"
            "v_pk_fma_f32 v[26:27], v[168:169], v[186:187], v[26:27] op_sel_hi:[0,1,1]\n"
            "v_pk_fma_f32 v[28:29], v[172:173], v[188:189], v[28:29] op_sel_hi:[0,1,1]\n"
            "v_pk_fma_f32 v[30:31], v[172:173], v[190:191], v[30:31] op_sel_hi:[0,1,1]\n"
            "s_cmp_le_u32 s74, 1\n"
            "s_cbranch_scc1 Ltail_%=\n"
            "v_mfma_f32_4x4x4_16b_bf16 v[176:179], v[208:209], v[56:57], 0 cbsz:2 abid:1\n"
            "v_mfma_f32_4x4x4_16b_bf16 v[180:183], v[208:209], v[58:59], 0 cbsz:2 abid:1\n"
            "v_mfma_f32_4x4x4_16b_bf16 v[184:187], v[208:209], v[60:61], 0 cbsz:2 abid:1\n"
            "v_mfma_f32_4x4x4_16b_bf16 v[188:191], v[208:209], v[62:63], 0 cbsz:2 abid:1\n"
            "s_nop 1\n"
            "v_pk_fma_f32 v[16:17], v[160:161], v[176:177], v[16:17] op_sel:[1,0,0]\n"
            "v_pk_fma_f32 v[18:19], v[160:161], v[178:179], v[18:19] op_sel:[1,0,0]\n"
            "v_pk_fma_f32 v[20:21], v[164:165], v[180:181], v[20:21] op_sel:[1,0,0]\n"
            "v_pk_fma_f32 v[22:23], v[164:165], v[182:183], v[22:23] op_sel:[1,0,0]\n"
            "v_pk_fma_f32 v[24:25], v[168:169], v[184:185], v[24:25] op_sel:[1,0,0]\n"
            "v_pk_fma_f32 v[26:27], v[168:169], v[186:187], v[26:27] op_sel:[1,0,0]\n"
            "v_pk_fma_f32 v[28:29], v[172:173], v[188:189], v[28:29] op_sel:[1,0,0]\n"
            "v_pk_fma_f32 v[30:31], v[172:173], v[190:191], v[30:31] op_sel:[1,0,0]\n"
            "s_cmp_le_u32 s74, 2\n"
            "s_cbranch_scc1 Ltail_%=\n"
            "s_waitcnt vmcnt(4)\n"
            "v_mfma_f32_4x4x4_16b_bf16 v[176:179], v[208:209], v[72:73], 0 cbsz:2 abid:2\n"
            "v_mfma_f32_4x4x4_16b_bf16 v[180:183], v[208:209], v[74:75], 0 cbsz:2 abid:2\n"
            "v_mfma_f32_4x4x4_16b_bf16 v[184:187], v[208:209], v[76:77], 0 cbsz:2 abid:2\n"
            "v_mfma_f32_4x4x4_16b_bf16 v[188:191], v[208:209], v[78:79], 0 cbsz:2 abid:2\n"
            "s_nop 1\n"
            "v_pk_fma_f32 v[16:17], v[162:163], v[176:177], v[16:17] op_sel_hi:[0,1,1]\n"
            "v_pk_fma_f32 v[18:19], v[162:163], v[178:179], v[18:19] op_sel_hi:[0,1,1]\n"
            "v_pk_fma_f32 v[20:21], v[166:167], v[180:181], v[20:21] op_sel_hi:[0,1,1]\n"
            "v_pk_fma_f32 v[22:23], v[166:167], v[182:183], v[22:23] op_sel_hi:[0,1,1]\n"
            "v_pk_fma_f32 v[24:25], v[170:171], v[184:185], v[24:25] op_sel_hi:[0,1,1]\n"
            "v_pk_fma_f32 v[26:27], v[170:171], v[186:187], v[26:27] op_sel_hi:[0,1,1]\n"
            "v_pk_fma_f32 v[28:29], v[174:175], v[188:189], v[28:29] op_sel_hi:[0,1,1]\n"
            "v_pk_fma_f32 v[30:31], v[174:175], v[190:191], v[30:31] op_sel_hi:[0,1,1]\n"
            "s_cmp_le_u32 s74, 3\n"
            "s_cbranch_scc1 Ltail_%=\n"
            "v_mfma_f32_4x4x4_16b_bf16 v[176:179], v[208:209], v[88:89], 0 cbsz:2 abid:3\n"
            "v_mfma_f32_4x4x4_16b_bf16 v[180:183], v[208:209], v[90:91], 0 cbsz:2 abid:3\n"
            "v_mfma_f32_4x4x4_16b_bf16 v[184:187], v[208:209], v[92:93], 0 cbsz:2 abid:3\n"
            "v_mfma_f32_4x4x4_16b_bf16 v[188:191], v[208:209], v[94:95], 0 cbsz:2 abid:3\n"
            "s_nop 1\n"
            "v_pk_fma_f32 v[16:17], v[162:163], v[176:177], v[16:17] op_sel:[1,0,0]\n"
            "v_pk_fma_f32 v[18:19], v[162:163], v[178:179], v[18:19] op_sel:[1,0,0]\n"
            "v_pk_fma_f32 v[20:21], v[166:167], v[180:181], v[20:21] op_sel:[1,0,0]\n"
            "v_pk_fma_f32 v[22:23], v[166:167], v[182:183], v[22:23] op_sel:[1,0,0]\n"
            "v_pk_fma_f32 v[24:25], v[170:171], v[184:185], v[24:25] op_sel:[1,0,0]\n"
            "v_pk_fma_f32 v[26:27], v[170:171], v[186:187], v[26:27] op_sel:[1,0,0]\n"
            "v_pk_fma_f32 v[28:29], v[174:175], v[188:189], v[28:29] op_sel:[1,0,0]\n"
            "v_pk_fma_f32 v[30:31], v[174:175], v[190:191], v[30:31] op_sel:[1,0,0]\n"
            "Ltail_%=:\n"
            "s_setprio 3\n"
            "s_waitcnt vmcnt(4) lgkmcnt(11)\n"
            "ds_read_b128 v[72:75], v228 offset:25600\n"
            "ds_read_b128 v[76:79], v228 offset:26624\n"
            "ds_read_b128 v[88:91], v228 offset:27648\n"
            "ds_read_b128 v[92:95], v228 offset:28672\n"
            "ds_read_b128 v[160:163], v228 offset:29696\n"
            "ds_read_b128 v[164:167], v228 offset:30720\n"
            "ds_read_b128 v[168:171], v228 offset:31744\n"
            "v_mfma_f32_4x4x4_16b_bf16 v[0:3], v[224:225], v[64:65], v[0:3]\n"
            "v_mfma_f32_4x4x4_16b_bf16 v[4:7], v[224:225], v[66:67], v[4:7]\n"
            "v_mfma_f32_4x4x4_16b_bf16 v[8:11], v[224:225], v[68:69], v[8:11]\n"
            "v_mfma_f32_4x4x4_16b_bf16 v[12:15], v[224:225], v[70:71], v[12:15]\n"
            "v_mfma_f32_4x4x4_16b_bf16 v[16:19], v[224:225], v[80:81], v[16:19]\n"
            "v_mfma_f32_4x4x4_16b_bf16 v[20:23], v[224:225], v[82:83], v[20:23]\n"
            "v_mfma_f32_4x4x4_16b_bf16 v[24:27], v[224:225], v[84:85], v[24:27]\n"
            "v_mfma_f32_4x4x4_16b_bf16 v[28:31], v[224:225], v[86:87], v[28:31]\n"
            "s_cmp_lg_u32 s73, s90\n"
            "s_cbranch_scc1 Lspin_%=\n"
            "Lgo_%=:\n"
            "buffer_load_dwordx4 v[40:43], v218, s[20:23], s63 offen offset:0 sc1\n"
            "buffer_load_dwordx4 v[44:47], v218, s[20:23], s63 offen offset:256 sc1\n"
            "buffer_load_dwordx4 v[56:59], v218, s[20:23], s63 offen offset:512 sc1\n"
            "buffer_load_dwordx4 v[60:63], v218, s[20:23], s63 offen offset:768 sc1\n"
            "s_waitcnt vmcnt(4)\n"
            "v_mfma_f32_4x4x4_16b_bf16 v[0:3], v[224:225], v[32:33], v[0:3]\n"
            "v_mfma_f32_4x4x4_16b_bf16 v[4:7], v[224:225], v[34:35], v[4:7]\n"
            "v_mfma_f32_4x4x4_16b_bf16 v[8:11], v[224:225], v[36:37], v[8:11]\n"
            "v_mfma_f32_4x4x4_16b_bf16 v[12:15], v[224:225], v[38:39], v[12:15]\n"
            "v_mfma_f32_4x4x4_16b_bf16 v[16:19], v[224:225], v[48:49], v[16:19]\n"
            "v_mfma_f32_4x4x4_16b_bf16 v[20:23], v[224:225], v[50:51], v[20:23]\n"
            "v_mfma_f32_4x4x4_16b_bf16 v[24:27], v[224:225], v[52:53], v[24:27]\n"
            "v_mfma_f32_4x4x4_16b_bf16 v[28:31], v[224:225], v[54:55], v[28:31]\n"
            "s_nop 1\n"
            "v_cvt_pk_bf16_f32 v176, v0, v4\n"
            "v_cvt_pk_bf16_f32 v177, v8, v12\n"
            "v_cvt_pk_bf16_f32 v178, v16, v20\n"
            "v_cvt_pk_bf16_f32 v179, v24, v28\n"
            "ds_write_b128 v226, v[176:179] offset:0\n"
            "v_cvt_pk_bf16_f32 v180, v1, v5\n"
            "v_cvt_pk_bf16_f32 v181, v9, v13\n"
            "v_cvt_pk_bf16_f32 v182, v17, v21\n"
            "v_cvt_pk_bf16_f32 v183, v25, v29\n"
            "ds_write_b128 v226, v[180:183] offset:272\n"
            "v_cvt_pk_bf16_f32 v184, v2, v6\n"
            "v_cvt_pk_bf16_f32 v185, v10, v14\n"
            "v_cvt_pk_bf16_f32 v186, v18, v22\n"
            "v_cvt_pk_bf16_f32 v187, v26, v30\n"
            "ds_write_b128 v226, v[184:187] offset:544\n"
            "v_cvt_pk_bf16_f32 v188, v3, v7\n"
            "v_cvt_pk_bf16_f32 v189, v11, v15\n"
            "v_cvt_pk_bf16_f32 v190, v19, v23\n"
            "v_cvt_pk_bf16_f32 v191, v27, v31\n"
            "ds_write_b128 v226, v[188:191] offset:816\n"
            "ds_read_b128 v[176:179], v227 offset:0\n"
            "ds_read_b128 v[180:183], v227 offset:64\n"
            "ds_read_b128 v[184:187], v227 offset:128\n"
            "ds_read_b128 v[188:191], v227 offset:192\n"
            "s_waitcnt lgkmcnt(3)\n"
            "v_mfma_f32_16x16x32_bf16 v[0:3], v[176:179], v[240:243], 0\n"
            "v_mfma_f32_16x16x32_bf16 v[4:7], v[176:179], v[100:103], 0\n"
            "s_waitcnt lgkmcnt(2)\n"
            "v_mfma_f32_16x16x32_bf16 v[0:3], v[180:183], v[244:247], v[0:3]\n"
            "v_mfma_f32_16x16x32_bf16 v[4:7], v[180:183], v[104:107], v[4:7]\n"
            "s_waitcnt lgkmcnt(1)\n"
            "v_mfma_f32_16x16x32_bf16 v[0:3], v[184:187], v[248:251], v[0:3]\n"
            "v_mfma_f32_16x16x32_bf16 v[4:7], v[184:187], v[108:111], v[4:7]\n"
            "s_waitcnt lgkmcnt(0)\n"
            "v_mfma_f32_16x16x32_bf16 v[0:3], v[188:191], v[96:99], v[0:3]\n"
            "v_mfma_f32_16x16x32_bf16 v[4:7], v[188:191], v[112:115], v[4:7]\n"
            "v_mfma_f32_16x16x32_bf16 v[8:11], v[176:179], v[116:119], 0\n"
            "v_mfma_f32_16x16x32_bf16 v[12:15], v[176:179], v[232:235], 0\n"
            "v_mfma_f32_16x16x32_bf16 v[8:11], v[180:183], v[196:199], v[8:11]\n"
            "v_mfma_f32_16x16x32_bf16 v[12:15], v[180:183], v[120:123], v[12:15]\n"
            "v_mfma_f32_16x16x32_bf16 v[8:11], v[184:187], v[200:203], v[8:11]\n"
            "v_mfma_f32_16x16x32_bf16 v[12:15], v[184:187], v[124:127], v[12:15]\n"
            "v_mfma_f32_16x16x32_bf16 v[8:11], v[188:191], v[204:207], v[8:11]\n"
            "v_mfma_f32_16x16x32_bf16 v[12:15], v[188:191], v[192:195], v[12:15]\n"
            "v_cvt_pk_bf16_f32 v0, v0, v1\n"
            "v_cvt_pk_bf16_f32 v1, v2, v3\n"
            "v_cvt_pk_bf16_f32 v2, v4, v5\n"
            "v_cvt_pk_bf16_f32 v3, v6, v7\n"
            "buffer_store_dwordx4 v[0:3], v217, s[32:35], 0 offen offset:0 sc1\n"
            "v_mfma_f32_16x16x32_bf16 v[16:19], v[176:179], v[128:131], 0\n"
            "v_mfma_f32_16x16x32_bf16 v[20:23], v[176:179], v[140:143], 0\n"
            "v_mfma_f32_16x16x32_bf16 v[16:19], v[180:183], v[132:135], v[16:19]\n"
            "v_mfma_f32_16x16x32_bf16 v[20:23], v[180:183], v[144:147], v[20:23]\n"
            "v_mfma_f32_16x16x32_bf16 v[16:19], v[184:187], v[236:239], v[16:19]\n"
            "v_mfma_f32_16x16x32_bf16 v[20:23], v[184:187], v[148:151], v[20:23]\n"
            "v_mfma_f32_16x16x32_bf16 v[16:19], v[188:191], v[136:139], v[16:19]\n"
            "v_mfma_f32_16x16x32_bf16 v[20:23], v[188:191], v[152:155], v[20:23]\n"
            "v_cvt_pk_bf16_f32 v8, v8, v9\n"
            "v_cvt_pk_bf16_f32 v9, v10, v11\n"
            "v_cvt_pk_bf16_f32 v10, v12, v13\n"
            "v_cvt_pk_bf16_f32 v11, v14, v15\n"
            "buffer_store_dwordx4 v[8:11], v217, s[32:35], 0 offen offset:256 sc1\n"
            "v_mfma_f32_16x16x32_bf16 v[24:27], v[176:179], v[156:159], 0\n"
            "v_mfma_f32_16x16x32_bf16 v[28:31], v[176:179], v[92:95], 0\n"
            "v_mfma_f32_16x16x32_bf16 v[24:27], v[180:183], v[72:75], v[24:27]\n"
            "v_mfma_f32_16x16x32_bf16 v[28:31], v[180:183], v[160:163], v[28:31]\n"
            "v_mfma_f32_16x16x32_bf16 v[24:27], v[184:187], v[76:79], v[24:27]\n"
            "v_mfma_f32_16x16x32_bf16 v[28:31], v[184:187], v[164:167], v[28:31]\n"
            "v_mfma_f32_16x16x32_bf16 v[24:27], v[188:191], v[88:91], v[24:27]\n"
            "v_mfma_f32_16x16x32_bf16 v[28:31], v[188:191], v[168:171], v[28:31]\n"
            "v_cvt_pk_bf16_f32 v16, v16, v17\n"
            "v_cvt_pk_bf16_f32 v17, v18, v19\n"
            "v_cvt_pk_bf16_f32 v18, v20, v21\n"
            "v_cvt_pk_bf16_f32 v19, v22, v23\n"
            "buffer_store_dwordx4 v[16:19], v217, s[32:35], 0 offen offset:512 sc1\n"
            "s_nop 2\n"
            "v_cvt_pk_bf16_f32 v24, v24, v25\n"
            "v_cvt_pk_bf16_f32 v25, v26, v27\n"
            "v_cvt_pk_bf16_f32 v26, v28, v29\n"
            "v_cvt_pk_bf16_f32 v27, v30, v31\n"
            "buffer_store_dwordx4 v[24:27], v217, s[32:35], 0 offen offset:768 sc1\n"
            "s_cmp_lt_u32 s3, 6\n"
            "s_cbranch_scc1 Lend_%=\n"
            "ds_read_b128 v[64:67], v231 offset:0\n"
            "ds_read_b128 v[68:71], v231 offset:1024\n"
            "ds_read_b128 v[80:83], v231 offset:2048\n"
            "ds_read_b128 v[84:87], v231 offset:3072\n"
            "s_waitcnt vmcnt(4) lgkmcnt(0)\n"
            "v_mfma_f32_4x4x4_16b_bf16 v[0:3], v[224:225], v[64:65], 0\n"
            "v_mfma_f32_4x4x4_16b_bf16 v[4:7], v[224:225], v[66:67], 0\n"
            "v_mfma_f32_4x4x4_16b_bf16 v[8:11], v[224:225], v[68:69], 0\n"
            "v_mfma_f32_4x4x4_16b_bf16 v[12:15], v[224:225], v[70:71], 0\n"
            "v_mfma_f32_4x4x4_16b_bf16 v[16:19], v[224:225], v[80:81], 0\n"
            "v_mfma_f32_4x4x4_16b_bf16 v[20:23], v[224:225], v[82:83], 0\n"
            "v_mfma_f32_4x4x4_16b_bf16 v[24:27], v[224:225], v[84:85], 0\n"
            "v_mfma_f32_4x4x4_16b_bf16 v[28:31], v[224:225], v[86:87], 0\n"
            "v_mfma_f32_4x4x4_16b_bf16 v[0:3], v[224:225], v[40:41], v[0:3]\n"
            "v_mfma_f32_4x4x4_16b_bf16 v[4:7], v[224:225], v[42:43], v[4:7]\n"
            "v_mfma_f32_4x4x4_16b_bf16 v[8:11], v[224:225], v[44:45], v[8:11]\n"
            "v_mfma_f32_4x4x4_16b_bf16 v[12:15], v[224:225], v[46:47], v[12:15]\n"
            "v_mfma_f32_4x4x4_16b_bf16 v[16:19], v[224:225], v[56:57], v[16:19]\n"
            "v_mfma_f32_4x4x4_16b_bf16 v[20:23], v[224:225], v[58:59], v[20:23]\n"
            "v_mfma_f32_4x4x4_16b_bf16 v[24:27], v[224:225], v[60:61], v[24:27]\n"
            "v_mfma_f32_4x4x4_16b_bf16 v[28:31], v[224:225], v[62:63], v[28:31]\n"
            "s_nop 1\n"
            "v_cvt_pk_bf16_f32 v176, v0, v4\n"
            "v_cvt_pk_bf16_f32 v177, v8, v12\n"
            "v_cvt_pk_bf16_f32 v178, v16, v20\n"
            "v_cvt_pk_bf16_f32 v179, v24, v28\n"
            "ds_write_b128 v226, v[176:179] offset:0\n"
            "v_cvt_pk_bf16_f32 v180, v1, v5\n"
            "v_cvt_pk_bf16_f32 v181, v9, v13\n"
            "v_cvt_pk_bf16_f32 v182, v17, v21\n"
            "v_cvt_pk_bf16_f32 v183, v25, v29\n"
            "ds_write_b128 v226, v[180:183] offset:272\n"
            "v_cvt_pk_bf16_f32 v184, v2, v6\n"
            "v_cvt_pk_bf16_f32 v185, v10, v14\n"
            "v_cvt_pk_bf16_f32 v186, v18, v22\n"
            "v_cvt_pk_bf16_f32 v187, v26, v30\n"
            "ds_write_b128 v226, v[184:187] offset:544\n"
            "v_cvt_pk_bf16_f32 v188, v3, v7\n"
            "v_cvt_pk_bf16_f32 v189, v11, v15\n"
            "v_cvt_pk_bf16_f32 v190, v19, v23\n"
            "v_cvt_pk_bf16_f32 v191, v27, v31\n"
            "ds_write_b128 v226, v[188:191] offset:816\n"
            "ds_read_b128 v[176:179], v227 offset:0\n"
            "ds_read_b128 v[180:183], v227 offset:64\n"
            "ds_read_b128 v[184:187], v227 offset:128\n"
            "ds_read_b128 v[188:191], v227 offset:192\n"
            "s_waitcnt lgkmcnt(3)\n"
            "v_mfma_f32_16x16x32_bf16 v[0:3], v[176:179], v[240:243], 0\n"
            "v_mfma_f32_16x16x32_bf16 v[4:7], v[176:179], v[100:103], 0\n"
            "s_waitcnt lgkmcnt(2)\n"
            "v_mfma_f32_16x16x32_bf16 v[0:3], v[180:183], v[244:247], v[0:3]\n"
            "v_mfma_f32_16x16x32_bf16 v[4:7], v[180:183], v[104:107], v[4:7]\n"
            "s_waitcnt lgkmcnt(1)\n"
            "v_mfma_f32_16x16x32_bf16 v[0:3], v[184:187], v[248:251], v[0:3]\n"
            "v_mfma_f32_16x16x32_bf16 v[4:7], v[184:187], v[108:111], v[4:7]\n"
            "s_waitcnt lgkmcnt(0)\n"
            "v_mfma_f32_16x16x32_bf16 v[0:3], v[188:191], v[96:99], v[0:3]\n"
            "v_mfma_f32_16x16x32_bf16 v[4:7], v[188:191], v[112:115], v[4:7]\n"
            "v_mfma_f32_16x16x32_bf16 v[8:11], v[176:179], v[116:119], 0\n"
            "v_mfma_f32_16x16x32_bf16 v[12:15], v[176:179], v[232:235], 0\n"
            "v_mfma_f32_16x16x32_bf16 v[8:11], v[180:183], v[196:199], v[8:11]\n"
            "v_mfma_f32_16x16x32_bf16 v[12:15], v[180:183], v[120:123], v[12:15]\n"
            "v_mfma_f32_16x16x32_bf16 v[8:11], v[184:187], v[200:203], v[8:11]\n"
            "v_mfma_f32_16x16x32_bf16 v[12:15], v[184:187], v[124:127], v[12:15]\n"
            "v_mfma_f32_16x16x32_bf16 v[8:11], v[188:191], v[204:207], v[8:11]\n"
            "v_mfma_f32_16x16x32_bf16 v[12:15], v[188:191], v[192:195], v[12:15]\n"
            "v_cvt_pk_bf16_f32 v0, v0, v1\n"
            "v_cvt_pk_bf16_f32 v1, v2, v3\n"
            "v_cvt_pk_bf16_f32 v2, v4, v5\n"
            "v_cvt_pk_bf16_f32 v3, v6, v7\n"
            "buffer_store_dwordx4 v[0:3], v218, s[32:35], 0 offen offset:0 sc1\n"
            "v_mfma_f32_16x16x32_bf16 v[16:19], v[176:179], v[128:131], 0\n"
            "v_mfma_f32_16x16x32_bf16 v[20:23], v[176:179], v[140:143], 0\n"
            "v_mfma_f32_16x16x32_bf16 v[16:19], v[180:183], v[132:135], v[16:19]\n"
            "v_mfma_f32_16x16x32_bf16 v[20:23], v[180:183], v[144:147], v[20:23]\n"
            "v_mfma_f32_16x16x32_bf16 v[16:19], v[184:187], v[236:239], v[16:19]\n"
            "v_mfma_f32_16x16x32_bf16 v[20:23], v[184:187], v[148:151], v[20:23]\n"
            "v_mfma_f32_16x16x32_bf16 v[16:19], v[188:191], v[136:139], v[16:19]\n"
            "v_mfma_f32_16x16x32_bf16 v[20:23], v[188:191], v[152:155], v[20:23]\n"
            "v_cvt_pk_bf16_f32 v8, v8, v9\n"
            "v_cvt_pk_bf16_f32 v9, v10, v11\n"
            "v_cvt_pk_bf16_f32 v10, v12, v13\n"
            "v_cvt_pk_bf16_f32 v11, v14, v15\n"
            "buffer_store_dwordx4 v[8:11], v218, s[32:35], 0 offen offset:256 sc1\n"
            "v_mfma_f32_16x16x32_bf16 v[24:27], v[176:179], v[156:159], 0\n"
            "v_mfma_f32_16x16x32_bf16 v[28:31], v[176:179], v[92:95], 0\n"
            "v_mfma_f32_16x16x32_bf16 v[24:27], v[180:183], v[72:75], v[24:27]\n"
            "v_mfma_f32_16x16x32_bf16 v[28:31], v[180:183], v[160:163], v[28:31]\n"
            "v_mfma_f32_16x16x32_bf16 v[24:27], v[184:187], v[76:79], v[24:27]\n"
            "v_mfma_f32_16x16x32_bf16 v[28:31], v[184:187], v[164:167], v[28:31]\n"
            "v_mfma_f32_16x16x32_bf16 v[24:27], v[188:191], v[88:91], v[24:27]\n"
            "v_mfma_f32_16x16x32_bf16 v[28:31], v[188:191], v[168:171], v[28:31]\n"
            "v_cvt_pk_bf16_f32 v16, v16, v17\n"
            "v_cvt_pk_bf16_f32 v17, v18, v19\n"
            "v_cvt_pk_bf16_f32 v18, v20, v21\n"
            "v_cvt_pk_bf16_f32 v19, v22, v23\n"
            "buffer_store_dwordx4 v[16:19], v218, s[32:35], 0 offen offset:512 sc1\n"
            "s_nop 2\n"
            "v_cvt_pk_bf16_f32 v24, v24, v25\n"
            "v_cvt_pk_bf16_f32 v25, v26, v27\n"
            "v_cvt_pk_bf16_f32 v26, v28, v29\n"
            "v_cvt_pk_bf16_f32 v27, v30, v31\n"
            "buffer_store_dwordx4 v[24:27], v218, s[32:35], 0 offen offset:768 sc1\n"
            "Lend_%=:\n"
            "s_waitcnt vmcnt(4)\n"
            "s_mov_b32 s68, 0\n"
            "s_branch Lout_%=\n"
            "Lpoll2_%=:\n"
            "s_sub_u32 s99, s99, 1\n"
            "s_cmp_lt_i32 s99, 0\n"
            "s_cbranch_scc1 Lpoll3_%=\n"
            "s_sleep 2\n"
            "buffer_load_dwordx2 v[142:143], v180, s[20:23], 0 offen sc1\n"
            "s_branch Lpoll_%=\n"
            "Lover_%=:\n"
            "s_mov_b32 s37, 0\n"
            "v_mov_b32 v144, 255\n"
            "ds_write_b32 v219, v144\n"
            "s_branch Lover2_%=\n"
            "Llng_%=:\n"
            "s_nop 1\n"
            "s_mov_b32 s38, 0\n"
            "v_max_u32_dpp v74, v74, v74 row_bcast:15 row_mask:0xa bank_mask:0xf\n"
            "s_nop 1\n"
            "v_max_u32_dpp v74, v74, v74 row_bcast:31 row_mask:0xc bank_mask:0xf\n"
            "v_max_u32 v74, s38, v74\n"
            "s_nop 0\n"
            "v_readlane_b32 s38, v74, 63\n"
            "v_max_u32_dpp v75, v75, v75 row_bcast:15 row_mask:0xa bank_mask:0xf\n"
            "s_nop 1\n"
            "v_max_u32_dpp v75, v75, v75 row_bcast:31 row_mask:0xc bank_mask:0xf\n"
            "v_max_u32 v75, s38, v75\n"
            "s_nop 0\n"
            "v_readlane_b32 s38, v75, 63\n"
            "v_max_u32_dpp v76, v76, v76 row_bcast:15 row_mask:0xa bank_mask:0xf\n"
            "s_nop 1\n"
            "v_max_u32_dpp v76, v76, v76 row_bcast:31 row_mask:0xc bank_mask:0xf\n"
            "v_max_u32 v76, s38, v76\n"
            "s_nop 0\n"
            "v_readlane_b32 s38, v76, 63\n"
            "v_max_u32_dpp v77, v77, v77 row_bcast:15 row_mask:0xa bank_mask:0xf\n"
            "s_nop 1\n"
            "v_max_u32_dpp v77, v77, v77 row_bcast:31 row_mask:0xc bank_mask:0xf\n"
            "v_max_u32 v77, s38, v77\n"
            "s_nop 0\n"
            "v_readlane_b32 s38, v77, 63\n"
            "v_max_u32_dpp v78, v78, v78 row_bcast:15 row_mask:0xa bank_mask:0xf\n"
            "s_nop 1\n"
            "v_max_u32_dpp v78, v78, v78 row_bcast:31 row_mask:0xc bank_mask:0xf\n"
            "v_max_u32 v78, s38, v78\n"
            "s_nop 0\n"
            "v_readlane_b32 s38, v78, 63\n"
            "v_max_u32_dpp v79, v79, v79 row_bcast:15 row_mask:0xa bank_mask:0xf\n"
            "s_nop 1\n"
            "v_max_u32_dpp v79, v79, v79 row_bcast:31 row_mask:0xc bank_mask:0xf\n"
            "v_max_u32 v79, s38, v79\n"
            "s_nop 0\n"
            "v_readlane_b32 s38, v79, 63\n"
            "v_max_u32_dpp v80, v80, v80 row_bcast:15 row_mask:0xa bank_mask:0xf\n"
            "s_nop 1\n"
            "v_max_u32_dpp v80, v80, v80 row_bcast:31 row_mask:0xc bank_mask:0xf\n"
            "v_max_u32 v80, s38, v80\n"
            "s_nop 0\n"
            "v_readlane_b32 s38, v80, 63\n"
            "s_branch Llng2_%=\n"
            "Lre0_%=:\n"
            "s_sub_u32 s99, s99, 1\n"
            "s_cmp_lt_i32 s99, 0\n"
            "s_cbranch_scc1 Lok0_%=\n"
            "s_sleep 1\n"
            "buffer_load_dwordx4 v[34:37], v81, s[20:23], s15 offen offset:0 sc1\n"
            "s_waitcnt vmcnt(0)\n"
            "s_branch Lchk0_%=\n"
            "Lre1_%=:\n"
            "s_sub_u32 s99, s99, 1\n"
            "s_cmp_lt_i32 s99, 0\n"
            "s_cbranch_scc1 Lok1_%=\n"
            "s_sleep 1\n"
            "buffer_load_dwordx4 v[40:43], v82, s[20:23], s15 offen offset:1024 sc1\n"
            "s_waitcnt vmcnt(0)\n"
            "s_branch Lchk1_%=\n"
            "Lre2_%=:\n"
            "s_sub_u32 s99, s99, 1\n"
            "s_cmp_lt_i32 s99, 0\n"
            "s_cbranch_scc1 Lok2_%=\n"
            "s_sleep 1\n"
            "buffer_load_dwordx4 v[46:49], v83, s[20:23], s15 offen offset:2048 sc1\n"
            "s_waitcnt vmcnt(0)\n"
            "s_branch Lchk2_%=\n"
            "Lre3_%=:\n"
            "s_sub_u32 s99, s99, 1\n"
            "s_cmp_lt_i32 s99, 0\n"
            "s_cbranch_scc1 Lok3_%=\n"
            "s_sleep 1\n"
            "buffer_load_dwordx4 v[52:55], v84, s[20:23], s15 offen offset:3072 sc1\n"
            "s_waitcnt vmcnt(0)\n"
            "s_branch Lchk3_%=\n"
            "Lre4_%=:\n"
            "s_sub_u32 s99, s99, 1\n"
            "s_cmp_lt_i32 s99, 0\n"
            "s_cbranch_scc1 Lok4_%=\n"
            "s_sleep 1\n"
            "buffer_load_dwordx4 v[58:61], v85, s[20:23], s16 offen offset:0 sc1\n"
            "s_waitcnt vmcnt(0)\n"
            "s_branch Lchk4_%=\n"
            "Lre5_%=:\n"
            "s_sub_u32 s99, s99, 1\n"
            "s_cmp_lt_i32 s99, 0\n"
            "s_cbranch_scc1 Lok5_%=\n"
            "s_sleep 1\n"
            "buffer_load_dwordx4 v[64:67], v86, s[20:23], s16 offen offset:1024 sc1\n"
            "s_waitcnt vmcnt(0)\n"
            "s_branch Lchk5_%=\n"
            "Lre6_%=:\n"
            "s_sub_u32 s99, s99, 1\n"
            "s_cmp_lt_i32 s99, 0\n"
            "s_cbranch_scc1 Lok6_%=\n"
            "s_sleep 1\n"
            "buffer_load_dwordx4 v[70:73], v87, s[20:23], s16 offen offset:2048 sc1\n"
            "s_waitcnt vmcnt(0)\n"
            "s_branch Lchk6_%=\n"
            "Llong_%=:\n"
            "v_lshrrev_b32 v243, 4, v214\n"
            "v_readfirstlane_b32 s86, v247\n"
            "v_add_u32 v243, 64, v243\n"
            "s_nop 0\n"
            "v_lshl_add_u32 v247, v243, 3, s86\n"
            "v_mov_b32 v136, s64\n"
            "v_cmpx_le_u32 vcc, s69, v243\n"
            "v_mov_b32 v136, s81\n"
            "v_cmpx_le_u32 vcc, s78, v243\n"
            "v_mov_b32 v136, s82\n"
            "v_cmpx_le_u32 vcc, s79, v243\n"
            "v_mov_b32 v136, s83\n"
            "v_cmpx_le_u32 vcc, s80, v243\n"
            "v_mov_b32 v136, s84\n"
            "s_mov_b64 exec, -1\n"
            "v_and_b32 v137, 0xffff, v136\n"
            "ds_read_b128 v[140:143], v137\n"
            "v_ashrrev_i32 v136, 16, v136\n"
            "v_sub_u32 v136, v243, v136\n"
            "v_lshl_or_b32 v137, v136, 16, v138\n"
            "v_lshlrev_b32 v136, 4, v136\n"
            "s_waitcnt lgkmcnt(0)\n"
            "v_cmp_gt_u32 vcc, v140, v137\n"
            "v_cmp_gt_u32 s[70:71], v141, v137\n"
            "v_cmp_gt_u32 s[86:87], v142, v137\n"
            "v_cmp_gt_u32 s[88:89], v143, v137\n"
            "v_add_u32 v140, v140, v136\n"
            "v_add_u32 v141, v141, v136\n"
            "v_add_u32 v142, v142, v136\n"
            "v_add_u32 v143, v143, v136\n"
            "v_cndmask_b32 v140, v251, v140, vcc\n"
            "v_cndmask_b32 v141, v251, v141, s[70:71]\n"
            "v_cndmask_b32 v142, v251, v142, s[86:87]\n"
            "v_cndmask_b32 v143, v251, v143, s[88:89]\n"
            "v_perm_b32 v140, v141, v140, s68\n"
            "v_perm_b32 v141, v143, v142, s68\n"
            "ds_write_b64 v247, v[140:141]\n"
            "s_branch Llong2_%=\n"
            "Lslow_%=:\n"
            "s_waitcnt vmcnt(0) lgkmcnt(0)\n"
            "s_mov_b32 s68, -1\n"
            "Lout_%=:\n"
            : "+{v0}"(tid), "={s68}"(slow), "={s90}"(tag), "={s3}"(wq)
            : "{s[0:1]}"(ka), "{s2}"(bx)
            : "memory", "vcc", "scc", "v1", "v2", "v3", "v4", "v5", "v6", "v7", "v8", "v9", "v10", "v11", "v12", "v13", "v14", "v15", "v16", "v17", "v18", "v19", "v20", "v21", "v22", "v23", "v24", "v25", "v26", "v27", "v28", "v29", "v30", "v31", "v32", "v33", "v34", "v35", "v36", "v37", "v38", "v39", "v40", "v41", "v42", "v43", "v44", "v45", "v46", "v47", "v48", "v49", "v50", "v51", "v52", "v53", "v54", "v55", "v56", "v57", "v58", "v59", "v60", "v61", "v62", "v63", "v64", "v65", "v66", "v67", "v68", "v69", "v70", "v71", "v72", "v73", "v74", "v75", "v76", "v77", "v78", "v79", "v80", "v81", "v82", "v83", "v84", "v85", "v86", "v87", "v88", "v89", "v90", "v91", "v92", "v93", "v94", "v95", "v96", "v97", "v98", "v99", "v100", "v101", "v102", "v103", "v104", "v105", "v106", "v107", "v108", "v109", "v110", "v111", "v112", "v113", "v114", "v115", "v116", "v117", "v118", "v119", "v120", "v121", "v122", "v123", "v124", "v125", "v126", "v127", "v128", "v129", "v130", "v131", "v132", "v133", "v134", "v135", "v136", "v137", "v138", "v139", "v140", "v141", "v142", "v143", "v144", "v145", "v146", "v147", "v148", "v149", "v150", "v151", "v152", "v153", "v154", "v155", "v156", "v157", "v158", "v159", "v160", "v161", "v162", "v163", "v164", "v165", "v166", "v167", "v168", "v169", "v170", "v171", "v172", "v173", "v174", "v175", "v176", "v177", "v178", "v179", "v180", "v181", "v182", "v183", "v184", "v185", "v186", "v187", "v188", "v189", "v190", "v191", "v192", "v193", "v194", "v195", "v196", "v197", "v198", "v199", "v200", "v201", "v202", "v203", "v204", "v205", "v206", "v207", "v208", "v209", "v210", "v211", "v212", "v213", "v214", "v215", "v216", "v217", "v218", "v219", "v220", "v221", "v222", "v223", "v224", "v225", "v226", "v227", "v228", "v229", "v230", "v231", "v232", "v233", "v234", "v235", "v236", "v237", "v238", "v239", "v240", "v241", "v242", "v243", "v244", "v245", "v246", "v247", "v248", "v249", "v250", "v251", "v252", "s4", "s5", "s6", "s7", "s8", "s9", "s10", "s11", "s12", "s13", "s14", "s15", "s16", "s17", "s18", "s19", "s20", "s21", "s22", "s23", "s24", "s25", "s26", "s27", "s28", "s29", "s30", "s31", "s33", "s34", "s35", "s36", "s37", "s38", "s39", "s40", "s41", "s42", "s43", "s44", "s45", "s46", "s47", "s48", "s49", "s50", "s51", "s52", "s53", "s54", "s55", "s56", "s57", "s58", "s59", "s60", "s61", "s62", "s63", "s64", "s65", "s66", "s67", "s69", "s70", "s71", "s72", "s73", "s74", "s75", "s76", "s77", "s78", "s79", "s80", "s81", "s82", "s83", "s84", "s85", "s86", "s87", "s88", "s89", "s91", "s92", "s93", "s94", "s95", "s96", "s97", "s98", "s99");
    const int w = __builtin_amdgcn_readfirstlane((int)wq);
    const unsigned lds0 = (unsigned)(uintptr_t)(__attribute__((address_space(3))) unsigned char*)smem;
    asm volatile("" : "+s"(ka));
    unsigned char* ws = (unsigned char*)ka[6];
    if (__builtin_amdgcn_readfirstlane((int)slow) == -1) {
        const int lane = lane_opaque();
        tag = (unsigned)__builtin_amdgcn_readfirstlane((int)tag);
        const int* dst = (const int*)ka[0]; const int* src = (const int*)ka[2];
        const unsigned short *Y = (const unsigned short*)ka[4], *ef = (const unsigned short*)ka[8], *x = (const unsigned short*)ka[10], *w_rad = (const unsigned short*)ka[14],
                             *cg = (const unsigned short*)ka[16], *w_mix = (const unsigned short*)ka[18];
        unsigned short* out = (unsigned short*)ka[12];
        const int N = (int)ka[20], E = (int)(ka[20] >> 32), nh = (int)ka[23];
        const int half = (bx >> 2) & 1, b = (bx & 3) * 32 + (bx >> 3), sS = 40 * (1 - half), sK = 40 * half;
        const int node0 = (b * N + 127) >> 7, npb = (((b + 1) * N + 127) >> 7) - node0;
        const __amdgpu_buffer_rsrc_t rsw = __builtin_amdgcn_make_buffer_rsrc((void*)ws, 0, W_END, 0x00020000);
        lds_wait(lds0 + SY_C3, 4);
        const __amdgpu_buffer_rsrc_t rso = __builtin_amdgcn_make_buffer_rsrc((void*)out, 0, N * 1024, 0x00020000);
#pragma unroll 1
        for (int it = 0; it < 10; ++it) {
            const int nl = (it < 5 ? sS + 8 * it : sK + 8 * (it - 5)) + w, node = node0 + nl;
            const bool nv = nl < npb;
            const unsigned ro = nv ? (unsigned)node * 1024u + (unsigned)lane * 16u : OOB_OFF;
            float a8[8] = {0.f, 0.f, 0.f, 0.f, 0.f, 0.f, 0.f, 0.f};
#pragma unroll 1
            for (int e0 = 0; e0 < (nv ? E : 0); e0 += 64) {
                const int ec = min(e0 + lane, E - 1);
                u64 mm = __ballot(e0 + lane < E && dst[ec] == node && (src[ec] >= nh) == (half != 0));
#pragma unroll 1
                while (mm) {
                    const int e = e0 + __builtin_ctzll(mm);
                    mm &= mm - 1;
                    const unsigned short* fe = (const unsigned short*)ef + (size_t)e * DE_DIM;
                    const unsigned short* ye = (const unsigned short*)Y + (size_t)e * M_DIM;
                    const unsigned short* xp = x + (size_t)src[e] * (C_DIM * M_DIM) + lane * 8;
                    float z0 = 0.f, z1 = 0.f;
#pragma unroll 1
                    for (int f = 0; f < DE_DIM; ++f) { z0 += bf2f(fe[f]) * bf2f(w_rad[f * C_DIM + 2 * lane]); z1 += bf2f(fe[f]) * bf2f(w_rad[f * C_DIM + 2 * lane + 1]); }
                    const float r0 = z0 / (1.f + __expf(-z0)), r1 = z1 / (1.f + __expf(-z1));
#pragma unroll 1
                    for (int i = 0; i < 4; ++i) {
                        const float x0 = bf2f(xp[i]) * r0, x1 = bf2f(xp[4 + i]) * r1;
#pragma unroll
                        for (int o = 0; o < 4; ++o) {
                            float wv4 = 0.f;
#pragma unroll
                            for (int f = 0; f < 4; ++f) wv4 += bf2f(cg[(i * 4 + f) * 4 + o]) * bf2f(ye[f]);
                            a8[o] += x0 * wv4; a8[4 + o] += x1 * wv4;
                        }
                    }
                }
            }
            if (it >= 5) {
                const u32x4 pv = __builtin_amdgcn_raw_buffer_load_b128(rsw, ro, W_AGG, SC1);
#pragma unroll
                for (int i = 0; i < 4; ++i) { a8[2 * i] += __uint_as_float(pv[i] << 16); a8[2 * i + 1] += __uint_as_float(pv[i] & 0xffff0000u); }
            }
            const u32x4 rv = (u32x4){pack2bf(a8[0], a8[1]), pack2bf(a8[2], a8[3]), pack2bf(a8[4], a8[5]), pack2bf(a8[6], a8[7])};
            if (it < 5) {
                __builtin_amdgcn_raw_buffer_store_b128(rv, rsw, ro, W_AGG, SC1);
                if (it == 4) {
                    asm volatile("s_waitcnt vmcnt(0)" ::: "memory");
                    if (lds_arrive_rtn(lds0 + 4) == 7u) flag_store(ws + W_FLG + bx * 128, tag);
                    poll_tag(ws + W_FLG + (bx ^ 4) * 128, tag);
                }
            } else {
                *(u32x4*)(smem + L_LST + w * 1024 + lane * 16) = rv;
#pragma unroll
                for (int i = 0; i < 8; ++i) a8[i] = 0.f;
#pragma unroll 1
                for (int c = 0; c < C_DIM; ++c) {
                    const unsigned short* ap = (const unsigned short*)(smem + L_LST + w * 1024 + c * 8);
                    const float w0 = bf2f(w_mix[c * C_DIM + 2 * lane]), w1 = bf2f(w_mix[c * C_DIM + 2 * lane + 1]);
#pragma unroll
                    for (int m = 0; m < 4; ++m) { a8[m] += bf2f(ap[m]) * w0; a8[4 + m] += bf2f(ap[m]) * w1; }
                }
                __builtin_amdgcn_raw_buffer_store_b128((u32x4){pack2bf(a8[0], a8[1]), pack2bf(a8[2], a8[3]), pack2bf(a8[4], a8[5]), pack2bf(a8[6], a8[7])}, rso, ro, 0, 0);
            }
        }
    }
}

__global__ void kf_zero(float* __restrict__ agg, size_t n) {
    for (size_t i = (size_t)blockIdx.x * blockDim.x + threadIdx.x; i < n; i += (size_t)gridDim.x * blockDim.x) agg[i] = 0.f;
}
__global__ void kf_edges(const unsigned short* __restrict__ x, const int* __restrict__ src, const int* __restrict__ dst,
                         const unsigned short* __restrict__ Y, const unsigned short* __restrict__ ef,
                         const unsigned short* __restrict__ w_rad, const unsigned short* __restrict__ cg,
                         float* __restrict__ agg, int E) {
    const int ch = threadIdx.x;
    for (int e = blockIdx.x; e < E; e += gridDim.x) {
        float z = 0.f;
        for (int f = 0; f < DE_DIM; ++f) z += bf2f(ef[(size_t)e * DE_DIM + f]) * bf2f(w_rad[f * C_DIM + ch]);
        const float r = z / (1.f + __expf(-z));
        float a4[4] = {0.f, 0.f, 0.f, 0.f};
        for (int ii = 0; ii < 4; ++ii) {
            const float xv = bf2f(x[((size_t)src[e] * C_DIM + ch) * M_DIM + ii]) * r;
            for (int o = 0; o < 4; ++o) {
                float wv = 0.f;
                for (int f = 0; f < 4; ++f) wv += bf2f(cg[(ii * 4 + f) * 4 + o]) * bf2f(Y[(size_t)e * 4 + f]);
                a4[o] += xv * wv;
            }
        }
        for (int o = 0; o < 4; ++o) atomicAdd(&agg[((size_t)dst[e] * C_DIM + ch) * M_DIM + o], a4[o]);
    }
}
__global__ void kf_mix(const float* __restrict__ agg, const unsigned short* __restrict__ w_mix, unsigned short* __restrict__ out, int N) {
    const int d = threadIdx.x;
    for (int n = blockIdx.x; n < N; n += gridDim.x) {
        float a4[4] = {0.f, 0.f, 0.f, 0.f};
        for (int c = 0; c < C_DIM; ++c) {
            const float wv = bf2f(w_mix[c * C_DIM + d]);
            for (int m = 0; m < 4; ++m) a4[m] += bf2f(f2bf(agg[((size_t)n * C_DIM + c) * M_DIM + m])) * wv;
        }
        for (int m = 0; m < 4; ++m) out[((size_t)n * C_DIM + d) * M_DIM + m] = f2bf(a4[m]);
    }
}

extern "C" void kernel_launch(void* const* d_in, const int* in_sizes, int n_in,
                              void* d_out, int out_size, void* d_ws, size_t ws_size,
                              hipStream_t stream) {
    const unsigned short* x = (const unsigned short*)d_in[0];
    const int* src = (const int*)d_in[1];
    const int* dst = (const int*)d_in[2];
    const unsigned short* Y = (const unsigned short*)d_in[3];
    const unsigned short* ef = (const unsigned short*)d_in[4];
    const unsigned short* w_rad = (const unsigned short*)d_in[5];
    const unsigned short* cg = (const unsigned short*)d_in[6];
    const unsigned short* w_mix = (const unsigned short*)d_in[7];
    unsigned short* out = (unsigned short*)d_out;
    const int N = in_sizes[0] / (C_DIM * M_DIM);
    const int E = in_sizes[1];
    const int epb = (E + 255) / 256;
    const int nh = (N + 1) / 2;
    u64 hi = (u64)(uintptr_t)d_out | (u64)(uintptr_t)d_ws;
    for (int i = 0; i < 8; ++i) hi |= (u64)(uintptr_t)d_in[i];
    const bool fast = (hi >> 48) == 0 && N >= 256 && N <= NB_MAX * NPB_MAX && E > 0 && epb <= EPB_MAX && ws_size >= (size_t)W_END;
    if (!fast) {
        float* agg = (float*)d_ws;
        if (N <= 0) return;
        kf_zero<<<1024, 256, 0, stream>>>(agg, (size_t)N * C_DIM * M_DIM);
        if (E > 0) kf_edges<<<4096, C_DIM, 0, stream>>>(x, src, dst, Y, ef, w_rad, cg, agg, E);
        kf_mix<<<2048, C_DIM, 0, stream>>>(agg, w_mix, out, N);
        return;
    }
    int k = 0;
    while ((N << k) <= 32768) ++k;
    const unsigned magic = (unsigned)(((1ull << (39 - k)) + N - 1) / N);
    const unsigned F = 0x00020000u;
    k_fused<<<256, 512, 0, stream>>>(dst, E * 4u, F, src, E * 4u, F, Y, E * 8u, F, d_ws, W_END, F, ef, E * 32u, F, x, N * 1024u, F, out, N * 1024u, F, w_rad, DE_DIM * C_DIM * 2u, F,
                                     cg, 128u, F, w_mix, C_DIM * C_DIM * 2u, F, N, E, epb, magic, k, nh * 1024u, nh, 0u);
}
```

```cpp
#include <hip/hip_runtime.h>
#include <hip/hip_bf16.h>
#include <stdint.h>

typedef __attribute__((ext_vector_type(4))) unsigned u32x4;
typedef unsigned long long u64;

#define C_DIM 128
#define M_DIM 4
#define DE_DIM 16
#define NPB_MAX 80
#define NB_MAX 128
#define EPB_MAX 1280
#define OOB_OFF 0x80000000u
#define SC1 16
#define SPIN_MAX (1u << 22)
#define W_FLG 4096
#define W_AGG 5804032
#define W_END 16289792
#define L_LST 4096
#define L_TOTAL 155648
#define SY_C3 56

static __device__ __forceinline__ int lane_opaque() {
    int l;
    asm volatile("v_mbcnt_lo_u32_b32 %0, -1, 0\n\tv_mbcnt_hi_u32_b32 %0, -1, %0" : "=v"(l));
    return l;
}

static __device__ __forceinline__ float bf2f(unsigned short v) { return __uint_as_float(((unsigned)v) << 16); }
typedef __bf16 bf16v2 __attribute__((ext_vector_type(2)));
static __device__ __forceinline__ unsigned pack2bf(float lo, float hi) {
    bf16v2 r = (bf16v2){(__bf16)lo, (__bf16)hi};
    return __builtin_bit_cast(unsigned, r);
}
static __device__ __forceinline__ unsigned short f2bf(float v) { return (unsigned short)(pack2bf(v, 0.f) & 0xffffu); }
static __device__ __forceinline__ void flag_store(const void* p, unsigned v) {
    unsigned t0, t1; u64 sv;
    asm volatile("v_mov_b32 %0, 0\n\tv_mov_b32 %1, %4\n\ts_mov_b64 %2, exec\n\ts_mov_b64 exec, 1\n\t"
                 "global_store_dword %0, %1, %3 sc1\n\ts_mov_b64 exec, %2" : "=&v"(t0), "=&v"(t1), "=&s"(sv) : "s"(p), "s"(v) : "memory");
}
static __device__ __forceinline__ unsigned lds_arrive_rtn(unsigned a) {
    unsigned t0, t1; u64 sv;
    asm volatile("v_mov_b32 %0, %3\n\tv_mov_b32 %1, 1\n\ts_mov_b64 %2, exec\n\ts_mov_b64 exec, 1\n\t"
                 "ds_add_rtn_u32 %1, %0, %1\n\ts_mov_b64 exec, %2\n\ts_waitcnt lgkmcnt(0)" : "=&v"(t0), "=&v"(t1), "=&s"(sv) : "s"(a) : "memory");
    return (unsigned)__builtin_amdgcn_readfirstlane((int)t1);
}
template <int SLEEP = 0> static __device__ __forceinline__ void lds_wait(unsigned a, unsigned n) {
    for (;;) {
        unsigned v;
        asm volatile("ds_read_b32 %0, %1\n\ts_waitcnt lgkmcnt(0)" : "=v"(v) : "v"(a) : "memory");
        if ((unsigned)__builtin_amdgcn_readfirstlane((int)v) >= n) break;
        if (SLEEP) __builtin_amdgcn_s_sleep(SLEEP);
    }
}
static __device__ __forceinline__ unsigned ld_agent(const void* p) { return __hip_atomic_load((const unsigned*)p, __ATOMIC_RELAXED, __HIP_MEMORY_SCOPE_AGENT); }
static __device__ __forceinline__ void poll_tag(const void* p, unsigned tag) {
#pragma unroll 1
    for (unsigned spin = 0; spin < SPIN_MAX; ++spin) {
        if (__all(ld_agent(p) == tag)) break;
        __builtin_amdgcn_s_sleep(2);
    }
    __builtin_amdgcn_fence(__ATOMIC_ACQUIRE, "wavefront");
}

#define BUF(t, n) t n, unsigned n##_bytes, unsigned n##_fmt
__global__ __launch_bounds__(512, 2) void k_fused(
    BUF(const void*, dst_), BUF(const void*, src_), BUF(const void*, Y_), BUF(void*, ws_), BUF(const void*, ef_), BUF(const void*, x_), BUF(void*, out_), BUF(const void*, w_rad_),
    BUF(const void*, cg_), BUF(const void*, w_mix_), unsigned N_, unsigned E_, unsigned epb_, unsigned magic_, unsigned shift_, unsigned nhb_, unsigned nh_, unsigned zero_)
{
    __shared__ __attribute__((aligned(256))) unsigned char smem[L_TOTAL];
    const __attribute__((address_space(4))) u64* ka = (const __attribute__((address_space(4))) u64*)__builtin_amdgcn_kernarg_segment_ptr();
    const int bx = blockIdx.x;
    unsigned slow, tag, wq, tid = threadIdx.x;
    asm volatile(
            "s_load_dwordx16 s[8:23], s[0:1], 0\n"
            "s_load_dwordx16 s[40:55], s[0:1], 128\n"
            "s_load_dwordx16 s[24:39], s[0:1], 64\n"
            "s_getpc_b64 s[4:5]\n"
            "v_readfirstlane_b32 s3, v0\n"
            "v_and_b32 v243, 63, v0\n"
            "s_setprio 3\n"
            "s_lshr_b32 s3, s3, 6\n"
            "v_mul_u32_u24 v54, 3, v243\n"
            "s_cmp_lg_u32 s3, 4\n"
            "s_cbranch_scc1 Lncp_%=\n"
            "s_and_b32 s4, s4, 0xffffff80\n"
            "s_movk_i32 s6, 16896\n"
            "s_mov_b32 s7, 0x20000\n"
            "s_lshr_b32 s68, s2, 3\n"
            "s_lshl_b32 s68, s68, 7\n"
            "v_lshlrev_b32 v81, 12, v243\n"
            "buffer_load_dword v53, v81, s[4:7], s68 offen\n"
            "Lncp_%=:\n"
            "s_mul_i32 s68, s3, 162\n"
            "v_cmp_gt_u32 vcc, 54, v243\n"
            "v_add_u32 v54, s68, v54\n"
            "v_mov_b32 v82, 0x1000000\n"
            "v_cndmask_b32 v54, v82, v54, vcc\n"
            "v_mov_b32 v219, 0\n"
            "s_waitcnt lgkmcnt(0)\n"
            "s_mul_i32 s95, s2, s50\n"
            "v_add_lshl_u32 v81, v54, s95, 2\n"
            "buffer_load_dwordx3 v[32:34], v81, s[8:11], 0 offen nt\n"
            "buffer_load_dwordx3 v[36:38], v81, s[12:15], 0 offen nt\n"
            "v_lshlrev_b32 v82, 1, v81\n"
            "buffer_load_dword v40, off, s[20:23], 0\n"
            "buffer_load_dwordx4 v[42:45], v82, s[16:19], 0 offen nt\n"
            "buffer_load_dwordx2 v[46:47], v82, s[16:19], 0 offen offset:16 nt\n"
            "v_lshlrev_b32 v193, 7, v243\n"
            "s_lshl_b32 s54, s95, 5\n"
            "s_bfe_u32 s8, s2, 0x10002\n"
            "s_and_b32 s9, s2, 3\n"
            "s_lshl_b32 s9, s9, 5\n"
            "s_lshr_b32 s68, s2, 3\n"
            "s_add_u32 s9, s9, s68\n"
            "s_mul_i32 s55, s8, s53\n"
            "s_lshl_b32 s69, s9, 13\n"
            "s_add_u32 s69, s69, s55\n"
            "s_cmp_lt_u32 s3, 4\n"
            "s_cbranch_scc1 Lx0_%=\n"
            "v_lshlrev_b32 v81, 4, v0\n"
            "v_and_b32 v81, 0xe00, v81\n"
            "v_and_b32 v82, 31, v0\n"
            "v_lshl_or_b32 v81, v82, 3, v81\n"
            "buffer_load_dwordx2 v[48:49], v81, s[36:39], 0 offen\n"
            "buffer_load_dwordx2 v[50:51], v81, s[36:39], 0 offen offset:256\n"
            "s_cmp_eq_u32 s3, 4\n"
            "s_cbranch_scc1 Lx4_%=\n"
            "s_sub_u32 s68, 7, s3\n"
            "s_cmp_lt_u32 s68, 3\n"
            "s_cselect_b32 s68, s68, 0x20000\n"
            "s_lshl_b32 s68, s68, 13\n"
            "s_add_u32 s68, s68, s54\n"
            "buffer_load_dword v52, v193, s[24:27], s68 offen\n"
            "s_branch Lx1_%=\n"
            "Lx4_%=:\n"
            "v_lshlrev_b32 v81, 2, v243\n"
            "buffer_load_dword v52, v81, s[40:43], 0 offen\n"
            "s_branch Lx1_%=\n"
            "Lx0_%=:\n"
            "s_add_u32 s68, s3, 3\n"
            "s_cmp_lt_u32 s68, 3\n"
            "s_cselect_b32 s68, s68, 0x20000\n"
            "s_lshl_b32 s68, s68, 13\n"
            "s_add_u32 s68, s68, s54\n"
            "buffer_load_dword v52, v193, s[24:27], s68 offen\n"
            "s_and_b32 s68, s9, 31\n"
            "s_lshl_b32 s68, s68, 7\n"
            "s_cmp_eq_u32 s3, 3\n"
            "s_cselect_b32 s68, s68, 0x40000000\n"
            "v_lshlrev_b32 v81, 5, v193\n"
            "buffer_load_dword v48, v81, s[44:47], s68 offen\n"
            "buffer_load_dword v49, off, s[20:23], 0\n"
            "Lx1_%=:\n"
            "v_mov_b64 v[82:83], 0\n"
            "v_mov_b64 v[84:85], 0\n"
            "v_lshlrev_b32 v81, 4, v0\n"
            "s_cmp_lt_u32 s3, 4\n"
            "s_cselect_b32 s68, 0, 71680\n"
            "v_add_u32 v86, s68, v81\n"
            "ds_write_b128 v86, v[82:85]\n"
            "s_cmov_b32 s68, 79872\n"
            "v_add_u32 v86, s68, v81\n"
            "ds_write_b128 v86, v[82:85]\n"
            "v_and_b32 v196, 15, v0\n"
            "v_mov_b32 v209, 1\n"
            "s_cmp_lg_u32 s3, 0\n"
            "s_cbranch_scc1 Lnk_%=\n"
            "v_lshlrev_b32 v81, 2, v243\n"
            "v_sub_u32 v82, 127, v243\n"
            "ds_write_b32 v81, v82 offset:96\n"
            "v_lshlrev_b32 v81, 2, v196\n"
            "v_sub_u32 v82, 63, v196\n"
            "ds_write_b32 v81, v82 offset:352\n"
            "v_bfrev_b32 v82, 1\n"
            "ds_write2_b32 v219, v82, v82 offset0:20 offset1:21\n"
            "Lnk_%=:\n"
            "s_sub_i32 s96, s49, s95\n"
            "s_min_i32 s96, s96, s50\n"
            "s_max_i32 s96, s96, 0\n"
            "s_and_b32 s12, s3, 3\n"
            "s_lshl_b32 s12, s12, 3\n"
            "s_lshl_b32 s68, 1, s12\n"
            "v_mov_b32 v80, s68\n"
            "s_sub_u32 s68, s68, 1\n"
            "s_and_b32 s68, s68, 0x10101010\n"
            "s_cmp_lt_u32 s3, 4\n"
            "s_cselect_b32 s13, s68, 0x10101010\n"
            "s_cselect_b32 s14, 0, s68\n"
            "s_mov_b32 s39, 0x10101010\n"
            "v_cmp_gt_u32 s[72:73], s96, v54\n"
            "v_lshlrev_b32 v77, 9, v54\n"
            "v_add_u32 v81, 1, v54\n"
            "v_cmp_gt_u32 s[74:75], s96, v81\n"
            "v_lshlrev_b32 v78, 9, v81\n"
            "v_add_u32 v81, 2, v54\n"
            "v_cmp_gt_u32 s[76:77], s96, v81\n"
            "v_lshlrev_b32 v79, 9, v81\n"
            "s_mul_i32 s10, s9, s48\n"
            "s_add_u32 s10, s10, 127\n"
            "s_add_u32 s11, s10, s48\n"
            "s_lshr_b32 s10, s10, 7\n"
            "s_lshr_b32 s11, s11, 7\n"
            "s_sub_u32 s11, s11, s10\n"
            "v_lshlrev_b32 v192, 4, v243\n"
            "v_lshlrev_b32 v194, 3, v243\n"
            "v_lshrrev_b32 v195, 4, v243\n"
            "v_lshlrev_b32 v214, 4, v196\n"
            "v_and_b32 v249, 3, v0\n"
            "v_lshlrev_b32 v250, 4, v249\n"
            "v_mov_b32 v251, 80\n"
            "v_lshrrev_b32 v83, 2, v196\n"
            "s_lshl_b32 s68, s3, 9\n"
            "s_add_u32 s68, s68, 61440\n"
            "v_add_u32 v212, s68, v194\n"
            "v_lshl_add_u32 v213, v195, 7, s68\n"
            "v_lshl_add_u32 v213, v249, 5, v213\n"
            "v_lshl_add_u32 v213, v83, 3, v213\n"
            "v_mov_b32 v224, 0x3f80\n"
            "v_mov_b32 v225, 0\n"
            "v_lshlrev_b64 v[224:225], v250, v[224:225]\n"
            "s_mul_i32 s68, s3, 4352\n"
            "s_sub_u32 s38, s3, 3\n"
            "s_max_i32 s38, s38, 0\n"
            "s_lshl_b32 s38, s38, 12\n"
            "s_add_u32 s68, s68, s38\n"
            "s_add_u32 s68, s68, 71680\n"
            "v_mov_b32 v81, s68\n"
            "v_add_u32 v231, s68, v192\n"
            "v_add_u32 v239, 0xfffff000, v231\n"
            "v_mov_b32 v236, v231\n"
            "s_add_u32 s38, s3, 3\n"
            "s_and_b32 s38, s38, 3\n"
            "s_lshl2_add_u32 s41, s38, 16\n"
            "s_mul_i32 s38, s38, 8448\n"
            "s_add_u32 s38, s38, 89088\n"
            "v_add_u32 v252, s38, v192\n"
            "s_movk_i32 s38, 272\n"
            "v_mad_u32_u24 v227, v196, s38, v81\n"
            "v_lshl_add_u32 v227, v195, 4, v227\n"
            "s_movk_i32 s38, 1088\n"
            "v_mad_u32_u24 v226, v195, s38, v81\n"
            "v_add_u32 v226, v226, v214\n"
            "v_add_u32 v228, 122880, v192\n"
            "v_and_b32 v253, 1, v195\n"
            "v_lshl_add_u32 v253, v196, 1, v253\n"
            "v_lshlrev_b32 v253, 2, v253\n"
            "v_and_b32 v229, 1, v0\n"
            "v_lshlrev_b32 v229, 4, v229\n"
            "v_lshlrev_b32 v81, 25, v0\n"
            "v_and_b32 v81, 2.0, v81\n"
            "v_or_b32 v229, v229, v81\n"
            "v_bfe_u32 v206, v0, 1, 2\n"
            "v_lshlrev_b32 v207, 4, v206\n"
            "v_bfe_u32 v83, v0, 3, 2\n"
            "s_mul_i32 s68, s3, 768\n"
            "s_add_u32 s68, s68, 65536\n"
            "v_add_u32 v247, s68, v194\n"
            "v_lshl_add_u32 v81, v249, 3, s68\n"
            "v_lshl_add_u32 v237, v195, 1, v81\n"
            "v_add_u32 v237, 32, v237\n"
            "v_lshl_add_u32 v81, v206, 3, s68\n"
            "v_lshl_add_u32 v238, v83, 1, v81\n"
            "v_add_u32 v238, 64, v238\n"
            "v_and_b32 v81, 7, v0\n"
            "v_lshlrev_b32 v82, 2, v81\n"
            "s_mov_b32 s68, 1969373488\n"
            "v_bfe_u32 v240, s68, v82, 4\n"
            "v_lshlrev_b32 v240, 4, v240\n"
            "v_add_u32 v240, 2304, v240\n"
            "s_mov_b32 s68, 1969816083\n"
            "v_bfe_u32 v241, s68, v82, 4\n"
            "v_lshlrev_b32 v241, 4, v241\n"
            "v_add_u32 v241, 2048, v241\n"
            "s_mov_b32 s68, 2554920959\n"
            "v_bfe_u32 v242, s68, v82, 4\n"
            "v_lshlrev_b32 v242, 4, v242\n"
            "v_add_u32 v242, 2048, v242\n"
            "v_cmp_lt_u32 vcc, 5, v81\n"
            "v_add_u32 v82, 256, v242\n"
            "s_nop 0\n"
            "v_cndmask_b32 v242, v242, v82, vcc\n"
            "v_cmp_lt_u32 vcc, 3, v81\n"
            "s_nop 1\n"
            "v_cndmask_b32 v242, 8, v242, vcc\n"
            "s_mov_b32 s68, 1998716928\n"
            "v_lshlrev_b32 v82, 2, v81\n"
            "v_bfe_u32 v208, s68, v82, 4\n"
            "s_add_u32 s38, s3, 1\n"
            "s_and_b32 s38, s38, 3\n"
            "s_add_u32 s38, s38, 4\n"
            "v_readlane_b32 s64, v240, s3\n"
            "v_readlane_b32 s65, v241, s3\n"
            "v_readlane_b32 s66, v242, s3\n"
            "v_readlane_b32 s40, v242, s38\n"
            "v_lshl_add_u32 v244, v195, 2, s64\n"
            "v_lshl_add_u32 v245, v195, 2, s65\n"
            "v_lshl_add_u32 v246, v195, 2, s66\n"
            "v_lshl_add_u32 v248, v83, 2, s64\n"
            "s_add_u32 s64, s64, 512\n"
            "s_add_u32 s65, s65, 512\n"
            "s_add_u32 s66, s66, 512\n"
            "s_add_u32 s40, s40, 512\n"
            "s_lshl_b32 s68, s2, 7\n"
            "s_add_u32 s38, s68, 4096\n"
            "s_add_u32 s56, s20, s38\n"
            "s_addc_u32 s57, s21, 0\n"
            "s_xor_b32 s38, s38, 512\n"
            "s_add_u32 s58, s20, s38\n"
            "s_addc_u32 s59, s21, 0\n"
            "s_cmp_lt_u32 s3, 6\n"
            "s_movk_i32 s60, 78\n"
            "s_cmovk_i32 s60, 150\n"
            "s_mov_b32 s61, 4294961748\n"
            "s_cmov_b32 s61, 4294945104\n"
            "s_movk_i32 s62, 0x3862\n"
            "s_cmov_b32 s62, 0x9062\n"
            "s_cmp_lt_u32 s3, 4\n"
            "s_cmovk_i32 s60, 590\n"
            "s_cmov_b32 s61, 4294612224\n"
            "s_cmovk_i32 s62, 0x82\n"
            "s_mov_b32 s63, 5804032\n"
            "s_and_b32 s68, s9, 127\n"
            "s_lshl3_add_u32 s68, s68, s3\n"
            "s_mul_i32 s68, s68, 512\n"
            "s_add_u32 s67, s68, s55\n"
            "s_add_u32 s67, s67, 3145728\n"
            "s_lshr_b32 s38, s3, 2\n"
            "s_lshl_b32 s38, s38, 10\n"
            "s_lshl_b32 s15, s3, 9\n"
            "s_add_u32 s15, s15, 24576\n"
            "v_add_u32 v94, s15, v194\n"
            "s_lshr_b32 s68, s38, 1\n"
            "s_sub_u32 s15, s15, s68\n"
            "s_and_b32 s68, s3, 1\n"
            "s_lshl1_add_u32 s68, s2, s68\n"
            "s_lshl_b32 s68, s68, 10\n"
            "s_add_u32 s36, s68, 36864\n"
            "v_mov_b32 v93, 268439552\n"
            "v_lshlrev_b32 v95, 4, v0\n"
            "s_mul_i32 s68, s2, 20480\n"
            "s_add_u32 s68, s68, 557056\n"
            "s_add_u32 s4, s20, s68\n"
            "s_addc_u32 s5, s21, 0\n"
            "s_lshl_b32 s6, s96, 4\n"
            "s_add_u32 s6, s6, 4223\n"
            "s_and_b32 s6, s6, 0xffffff80\n"
            "s_mov_b32 s7, 0x20000\n"
            "v_mov_b64 v[0:1], 0\n"
            "v_mov_b64 v[2:3], 0\n"
            "v_mov_b64 v[4:5], 0\n"
            "v_mov_b64 v[6:7], 0\n"
            "v_mov_b64 v[8:9], 0\n"
            "v_mov_b64 v[10:11], 0\n"
            "v_mov_b64 v[12:13], 0\n"
            "v_mov_b64 v[14:15], 0\n"
            "v_mov_b64 v[16:17], 0\n"
            "v_mov_b64 v[18:19], 0\n"
            "v_mov_b64 v[20:21], 0\n"
            "v_mov_b64 v[22:23], 0\n"
            "v_mov_b64 v[24:25], 0\n"
            "v_mov_b64 v[26:27], 0\n"
            "v_mov_b64 v[28:29], 0\n"
            "v_mov_b64 v[30:31], 0\n"
            "s_waitcnt lgkmcnt(0)\n"
            "s_barrier\n"
            "s_waitcnt vmcnt(6)\n"
            "v_lshlrev_b32 v81, s52, v32\n"
            "v_lshlrev_b32 v84, s52, v36\n"
            "v_lshlrev_b32 v82, s52, v33\n"
            "v_lshlrev_b32 v85, s52, v37\n"
            "v_lshlrev_b32 v83, s52, v34\n"
            "v_lshlrev_b32 v86, s52, v38\n"
            "v_mul_hi_u32_u24 v81, s51, v81\n"
            "v_mul_hi_u32_u24 v84, s51, v84\n"
            "v_lshl_add_u32 v68, v81, 2, s38\n"
            "v_and_b32 v81, 64, v84\n"
            "v_lshl_add_u32 v68, v81, 3, v68\n"
            "s_mov_b64 exec, s[72:73]\n"
            "ds_add_rtn_u32 v71, v68, v80 offset:2048\n"
            "s_mov_b64 exec, -1\n"
            "v_mul_hi_u32_u24 v82, s51, v82\n"
            "v_mul_hi_u32_u24 v85, s51, v85\n"
            "v_lshl_add_u32 v69, v82, 2, s38\n"
            "v_and_b32 v82, 64, v85\n"
            "v_lshl_add_u32 v69, v82, 3, v69\n"
            "s_mov_b64 exec, s[74:75]\n"
            "ds_add_rtn_u32 v72, v69, v80 offset:2048\n"
            "s_mov_b64 exec, -1\n"
            "v_mul_hi_u32_u24 v83, s51, v83\n"
            "v_mul_hi_u32_u24 v86, s51, v86\n"
            "v_lshl_add_u32 v70, v83, 2, s38\n"
            "v_and_b32 v83, 64, v86\n"
            "v_lshl_add_u32 v70, v83, 3, v70\n"
            "s_mov_b64 exec, s[76:77]\n"
            "ds_add_rtn_u32 v73, v70, v80 offset:2048\n"
            "s_mov_b64 exec, -1\n"
            "v_bfe_u32 v84, v84, 4, 2\n"
            "v_and_b32 v81, 127, v32\n"
            "v_lshl_or_b32 v81, v81, 2, v84\n"
            "v_bfe_u32 v85, v85, 4, 2\n"
            "v_and_b32 v82, 127, v33\n"
            "v_lshl_or_b32 v82, v82, 2, v85\n"
            "v_bfe_u32 v86, v86, 4, 2\n"
            "v_and_b32 v83, 127, v34\n"
            "v_lshl_or_b32 v83, v83, 2, v86\n"
            "s_waitcnt vmcnt(5)\n"
            "v_readfirstlane_b32 s90, v40\n"
            "s_mul_i32 s90, s90, 1664525\n"
            "s_add_u32 s90, s90, 1013904223\n"
            "s_lshl_b32 s93, s90, 14\n"
            "s_and_b32 s94, s90, 0xfff00000\n"
            "s_waitcnt lgkmcnt(0)\n"
            "s_barrier\n"
            "ds_read_b128 v[128:131], v192 offset:2048\n"
            "ds_read_b128 v[132:135], v192 offset:3072\n"
            "v_or3_b32 v59, v81, v77, s94\n"
            "v_or_b32 v57, s93, v36\n"
            "v_lshrrev_b32 v74, 1, v68\n"
            "v_add_u32 v74, s15, v74\n"
            "v_bfe_u32 v71, v71, s12, 8\n"
            "v_or3_b32 v63, v82, v78, s94\n"
            "v_or_b32 v61, s93, v37\n"
            "v_lshrrev_b32 v75, 1, v69\n"
            "v_add_u32 v75, s15, v75\n"
            "v_bfe_u32 v72, v72, s12, 8\n"
            "v_or3_b32 v67, v83, v79, s94\n"
            "v_or_b32 v65, s93, v38\n"
            "v_lshrrev_b32 v76, 1, v70\n"
            "v_add_u32 v76, s15, v76\n"
            "v_bfe_u32 v73, v73, s12, 8\n"
            "s_waitcnt lgkmcnt(1)\n"
            "v_dot4_u32_u8 v136, v128, s39, 0\n"
            "v_dot4_u32_u8 v137, v129, s39, 0\n"
            "v_dot4_u32_u8 v138, v130, s39, 0\n"
            "v_dot4_u32_u8 v139, v131, s39, 0\n"
            "s_waitcnt lgkmcnt(0)\n"
            "v_dot4_u32_u8 v136, v132, s39, v136\n"
            "v_dot4_u32_u8 v137, v133, s39, v137\n"
            "v_dot4_u32_u8 v138, v134, s39, v138\n"
            "v_dot4_u32_u8 v139, v135, s39, v139\n"
            "v_dot4_u32_u8 v140, v128, s13, 0\n"
            "v_dot4_u32_u8 v141, v129, s13, v136\n"
            "v_dot4_u32_u8 v142, v130, s13, 0\n"
            "v_dot4_u32_u8 v143, v131, s13, v138\n"
            "v_add_u32 v87, v136, v137\n"
            "v_add3_u32 v88, v87, v138, v139\n"
            "v_dot4_u32_u8 v140, v132, s14, v140\n"
            "v_dot4_u32_u8 v141, v133, s14, v141\n"
            "s_mov_b32 s68, 0x10001\n"
            "v_add_u32_dpp v89, v88, v88 row_shr:1 row_mask:0xf bank_mask:0xf bound_ctrl:1\n"
            "v_dot4_u32_u8 v142, v134, s14, v142\n"
            "v_dot4_u32_u8 v143, v135, s14, v143\n"
            "v_add_u32_dpp v89, v89, v89 row_shr:2 row_mask:0xf bank_mask:0xf bound_ctrl:1\n"
            "v_lshl_add_u32 v140, v141, 16, v140\n"
            "v_mad_u32_u24 v90, v87, s68, v93\n"
            "v_add_u32_dpp v89, v89, v89 row_shr:4 row_mask:0xf bank_mask:0xf bound_ctrl:1\n"
            "v_add_u32 v140, v140, v93\n"
            "v_lshl_add_u32 v141, v143, 16, v142\n"
            "v_add_u32_dpp v89, v89, v89 row_shr:8 row_mask:0xf bank_mask:0xf bound_ctrl:1\n"
            "v_add_u32 v141, v141, v90\n"
            "s_and_b32 s38, s3, 6\n"
            "v_add_u32_dpp v89, v89, v89 row_bcast:15 row_mask:0xa bank_mask:0xf\n"
            "s_cmp_lg_u32 s38, 4\n"
            "s_nop 0\n"
            "v_add_u32_dpp v89, v89, v89 row_bcast:31 row_mask:0xc bank_mask:0xf\n"
            "v_sub_u32 v89, v89, v88\n"
            "v_mad_u32_u24 v140, v89, s68, v140\n"
            "v_mad_u32_u24 v141, v89, s68, v141\n"
            "ds_write_b64 v94, v[140:141]\n"
            "ds_read_u16 v74, v74\n"
            "ds_read_u16 v75, v75\n"
            "ds_read_u16 v76, v76\n"
            "s_cbranch_scc1 Lnt_%=\n"
            "s_bitcmp1_b32 s3, 0\n"
            "s_cbranch_scc0 Lt0_%=\n"
            "v_add_u32 v89, v89, v87\n"
            "v_mov_b64 v[136:137], v[138:139]\n"
            "Lt0_%=:\n"
            "v_lshl_add_u32 v128, v136, 16, v89\n"
            "v_add_u32 v89, v89, v136\n"
            "v_lshl_add_u32 v130, v137, 16, v89\n"
            "v_mov_b32 v129, s90\n"
            "v_mov_b32 v131, s90\n"
            "buffer_store_dwordx4 v[128:131], v192, s[20:23], s36 offen sc1\n"
            "s_waitcnt vmcnt(4)\n"
            "s_branch Lt1_%=\n"
            "Lnt_%=:\n"
            "s_waitcnt vmcnt(3)\n"
            "Lt1_%=:\n"
            "v_mov_b32 v56, v42\n"
            "v_mov_b32 v58, v43\n"
            "v_mov_b32 v60, v44\n"
            "v_mov_b32 v62, v45\n"
            "v_mov_b32 v64, v46\n"
            "v_mov_b32 v66, v47\n"
            "s_waitcnt lgkmcnt(0)\n"
            "v_lshl_add_u32 v74, v71, 4, v74\n"
            "v_lshl_add_u32 v75, v72, 4, v75\n"
            "v_lshl_add_u32 v76, v73, 4, v76\n"
            "s_mov_b64 exec, s[72:73]\n"
            "ds_write_b128 v74, v[56:59]\n"
            "s_mov_b64 exec, s[74:75]\n"
            "ds_write_b128 v75, v[60:63]\n"
            "s_mov_b64 exec, s[76:77]\n"
            "ds_write_b128 v76, v[64:67]\n"
            "s_mov_b64 exec, -1\n"
            "s_cmp_lt_u32 s3, 4\n"
            "s_waitcnt lgkmcnt(0)\n"
            "s_barrier\n"
            "s_cbranch_scc1 Lcol_%=\n"
            "ds_read_b128 v[56:59], v95 offset:0\n"
            "ds_read_b128 v[60:63], v95 offset:4096\n"
            "ds_read_b128 v[64:67], v95 offset:8192\n"
            "ds_read_b128 v[68:71], v95 offset:12288\n"
            "ds_read_b128 v[72:75], v95 offset:16384\n"
            "s_movk_i32 s72, 4096\n"
            "s_movk_i32 s73, 8192\n"
            "s_movk_i32 s74, 12288\n"
            "s_movk_i32 s75, 16384\n"
            "s_waitcnt lgkmcnt(4)\n"
            "buffer_store_dwordx4 v[56:59], v95, s[4:7], 0 offen sc1\n"
            "s_waitcnt lgkmcnt(3)\n"
            "buffer_store_dwordx4 v[60:63], v95, s[4:7], s72 offen sc1\n"
            "s_waitcnt lgkmcnt(2)\n"
            "buffer_store_dwordx4 v[64:67], v95, s[4:7], s73 offen sc1\n"
            "s_waitcnt lgkmcnt(1)\n"
            "buffer_store_dwordx4 v[68:71], v95, s[4:7], s74 offen sc1\n"
            "s_waitcnt lgkmcnt(0)\n"
            "buffer_store_dwordx4 v[72:75], v95, s[4:7], s75 offen sc1\n"
            "s_setprio 2\n"
            "s_lshl_b32 s68, s3, 11\n"
            "s_sub_u32 s68, s68, 8192\n"
            "v_lshl_add_u32 v81, v195, 9, s68\n"
            "v_add_u32 v81, v81, v214\n"
            "s_movk_i32 s76, 0x6000\n"
            "buffer_load_dwordx4 v[128:131], v81, s[44:47], 0 offen\n"
            "buffer_load_dwordx4 v[132:135], v81, s[44:47], 0 offen offset:256\n"
            "buffer_load_dwordx4 v[136:139], v81, s[44:47], s73 offen\n"
            "buffer_load_dwordx4 v[140:143], v81, s[44:47], s73 offen offset:256\n"
            "buffer_load_dwordx4 v[144:147], v81, s[44:47], s75 offen\n"
            "buffer_load_dwordx4 v[148:151], v81, s[44:47], s75 offen offset:256\n"
            "buffer_load_dwordx4 v[152:155], v81, s[44:47], s76 offen\n"
            "buffer_load_dwordx4 v[156:159], v81, s[44:47], s76 offen offset:256\n"
            "v_lshlrev_b32 v81, 4, v95\n"
            "v_and_b32 v81, 0x1800, v81\n"
            "v_lshrrev_b32 v82, 3, v95\n"
            "v_and_b32 v82, 0x100, v82\n"
            "v_lshlrev_b32 v83, 1, v95\n"
            "v_and_b32 v83, 0xe0, v83\n"
            "v_lshrrev_b32 v84, 7, v95\n"
            "v_and_b32 v84, 12, v84\n"
            "v_or3_b32 v81, v81, v82, v83\n"
            "v_add_u32 v84, 114432, v84\n"
            "v_add_u32 v81, v81, v84\n"
            "s_mov_b32 s78, 0xbfb8aa3b\n"
            "s_waitcnt vmcnt(14)\n"
            "v_lshlrev_b32 v160, 16, v48\n"
            "v_lshlrev_b32 v161, 16, v50\n"
            "v_mul_f32 v160, s78, v160\n"
            "v_mul_f32 v161, s78, v161\n"
            "v_cvt_pk_bf16_f32 v164, v160, v161\n"
            "v_lshlrev_b32 v162, 16, v164\n"
            "v_and_b32 v163, 0xffff0000, v164\n"
            "v_sub_f32 v160, v160, v162\n"
            "v_sub_f32 v161, v161, v163\n"
            "v_cvt_pk_bf16_f32 v165, v160, v161\n"
            "ds_write_b32 v81, v164 offset:0\n"
            "ds_write_b32 v81, v165 offset:512\n"
            "v_and_b32 v160, 0xffff0000, v48\n"
            "v_and_b32 v161, 0xffff0000, v50\n"
            "v_mul_f32 v160, s78, v160\n"
            "v_mul_f32 v161, s78, v161\n"
            "v_cvt_pk_bf16_f32 v166, v160, v161\n"
            "v_lshlrev_b32 v162, 16, v166\n"
            "v_and_b32 v163, 0xffff0000, v166\n"
            "v_sub_f32 v160, v160, v162\n"
            "v_sub_f32 v161, v161, v163\n"
            "v_cvt_pk_bf16_f32 v167, v160, v161\n"
            "ds_write_b32 v81, v166 offset:1024\n"
            "ds_write_b32 v81, v167 offset:1536\n"
            "v_lshlrev_b32 v160, 16, v49\n"
            "v_lshlrev_b32 v161, 16, v51\n"
            "v_mul_f32 v160, s78, v160\n"
            "v_mul_f32 v161, s78, v161\n"
            "v_cvt_pk_bf16_f32 v168, v160, v161\n"
            "v_lshlrev_b32 v162, 16, v168\n"
            "v_and_b32 v163, 0xffff0000, v168\n"
            "v_sub_f32 v160, v160, v162\n"
            "v_sub_f32 v161, v161, v163\n"
            "v_cvt_pk_bf16_f32 v169, v160, v161\n"
            "ds_write_b32 v81, v168 offset:16\n"
            "ds_write_b32 v81, v169 offset:528\n"
            "v_and_b32 v160, 0xffff0000, v49\n"
            "v_and_b32 v161, 0xffff0000, v51\n"
            "v_mul_f32 v160, s78, v160\n"
            "v_mul_f32 v161, s78, v161\n"
            "v_cvt_pk_bf16_f32 v170, v160, v161\n"
            "v_lshlrev_b32 v162, 16, v170\n"
            "v_and_b32 v163, 0xffff0000, v170\n"
            "v_sub_f32 v160, v160, v162\n"
            "v_sub_f32 v161, v161, v163\n"
            "v_cvt_pk_bf16_f32 v171, v160, v161\n"
            "ds_write_b32 v81, v170 offset:1040\n"
            "ds_write_b32 v81, v171 offset:1552\n"
            "s_cmp_lg_u32 s3, 4\n"
            "s_cbranch_scc1 Lncg_%=\n"
            "v_lshlrev_b32 v81, 2, v243\n"
            "s_waitcnt vmcnt(14)\n"
            "ds_write_b32 v81, v52 offset:61440\n"
            "v_lshrrev_b32 v81, 1, v243\n"
            "v_and_b32 v81, 6, v81\n"
            "v_lshl_add_u32 v81, v249, 5, v81\n"
            "ds_read_u16 v160, v81 offset:61440\n"
            "ds_read_u16 v161, v81 offset:61448\n"
            "ds_read_u16 v162, v81 offset:61456\n"
            "ds_read_u16 v163, v81 offset:61464\n"
            "s_mov_b32 s78, 0xbf317218\n"
            "s_waitcnt lgkmcnt(0)\n"
            "v_lshlrev_b32 v160, 16, v160\n"
            "v_lshlrev_b32 v161, 16, v161\n"
            "v_lshlrev_b32 v162, 16, v162\n"
            "v_lshlrev_b32 v163, 16, v163\n"
            "v_mul_f32 v160, s78, v160\n"
            "v_mul_f32 v161, s78, v161\n"
            "v_mul_f32 v162, s78, v162\n"
            "v_mul_f32 v163, s78, v163\n"
            "v_cvt_pk_bf16_f32 v168, v160, v161\n"
            "v_cvt_pk_bf16_f32 v169, v162, v163\n"
            "v_lshlrev_b32 v164, 16, v168\n"
            "v_and_b32 v165, 0xffff0000, v168\n"
            "v_lshlrev_b32 v166, 16, v169\n"
            "v_and_b32 v167, 0xffff0000, v169\n"
            "v_sub_f32 v160, v160, v164\n"
            "v_sub_f32 v161, v161, v165\n"
            "v_sub_f32 v162, v162, v166\n"
            "v_sub_f32 v163, v163, v167\n"
            "v_cvt_pk_bf16_f32 v170, v160, v161\n"
            "v_cvt_pk_bf16_f32 v171, v162, v163\n"
            "ds_write_b128 v192, v[168:171] offset:60416\n"
            "Lncg_%=:\n"
            "s_mov_b64 exec, 1\n"
            "ds_add_u32 v219, v209 offset:64\n"
            "s_mov_b64 exec, -1\n"
            "s_mul_i32 s68, s3, 27\n"
            "s_sub_u32 s68, s68, 108\n"
            "v_lshrrev_b32 v81, 1, v243\n"
            "v_add_u32 v81, s68, v81\n"
            "v_min_u32 v81, 79, v81\n"
            "v_lshlrev_b32 v180, 2, v81\n"
            "v_cmp_lt_u32 vcc, 39, v81\n"
            "v_and_b32 v82, 1, v243\n"
            "v_mul_u32_u24 v181, 80, v82\n"
            "v_add_u32 v82, 160, v181\n"
            "v_cndmask_b32 v181, v181, v82, vcc\n"
            "v_cndmask_b32 v82, 1, 0, vcc\n"
            "v_xor_b32 v82, s8, v82\n"
            "v_lshlrev_b32 v182, 8, v82\n"
            "v_mul_u32_u24 v183, 704, v81\n"
            "v_add_u32 v183, 4096, v183\n"
            "v_cmp_gt_u32 vcc, s11, v81\n"
            "v_add_lshl_u32 v184, v81, s10, 10\n"
            "v_bfrev_b32 v82, 1\n"
            "v_cndmask_b32 v184, v82, v184, vcc\n"
            "s_branch Lhpa_%=\n"
            "Lhp_%=:\n"
            "s_sleep 1\n"
            "Lhpa_%=:\n"
            "ds_read_b32 v220, v219 offset:64\n"
            "s_waitcnt lgkmcnt(0)\n"
            "v_readfirstlane_b32 s68, v220\n"
            "s_cmp_lt_u32 s68, 4\n"
            "s_cbranch_scc1 Lhp_%=\n"
            "v_add_u32 v81, 114432, v192\n"
            "ds_read_b128 v[96:99], v81 offset:0\n"
            "ds_read_b128 v[100:103], v81 offset:1024\n"
            "ds_read_b128 v[104:107], v81 offset:2048\n"
            "ds_read_b128 v[108:111], v81 offset:3072\n"
            "ds_read_b128 v[112:115], v81 offset:4096\n"
            "ds_read_b128 v[116:119], v81 offset:5120\n"
            "ds_read_b128 v[120:123], v81 offset:6144\n"
            "ds_read_b128 v[124:127], v81 offset:7168\n"
            "ds_read_b128 v[200:203], v192 offset:60416\n"
            "v_lshrrev_b32 v82, 2, v196\n"
            "s_lshl_b32 s68, s3, 10\n"
            "s_add_u32 s68, s68, 118784\n"
            "v_lshl_add_u32 v82, v82, 13, s68\n"
            "v_lshl_add_u32 v82, v195, 8, v82\n"
            "v_lshl_add_u32 v82, v249, 6, v82\n"
            "s_mov_b32 s78, 0x05040100\n"
            "s_mov_b32 s79, 0x07060302\n"
            "s_waitcnt vmcnt(0)\n"
            "v_perm_b32 v160, v132, v128, s78\n"
            "v_perm_b32 v161, v140, v136, s78\n"
            "v_perm_b32 v162, v148, v144, s78\n"
            "v_perm_b32 v163, v156, v152, s78\n"
            "ds_write_b128 v82, v[160:163] offset:0\n"
            "v_perm_b32 v164, v132, v128, s79\n"
            "v_perm_b32 v165, v140, v136, s79\n"
            "v_perm_b32 v166, v148, v144, s79\n"
            "v_perm_b32 v167, v156, v152, s79\n"
            "ds_write_b128 v82, v[164:167] offset:4096\n"
            "v_perm_b32 v168, v133, v129, s78\n"
            "v_perm_b32 v169, v141, v137, s78\n"
            "v_perm_b32 v170, v149, v145, s78\n"
            "v_perm_b32 v171, v157, v153, s78\n"
            "ds_write_b128 v82, v[168:171] offset:16\n"
            "v_perm_b32 v172, v133, v129, s79\n"
            "v_perm_b32 v173, v141, v137, s79\n"
            "v_perm_b32 v174, v149, v145, s79\n"
            "v_perm_b32 v175, v157, v153, s79\n"
            "ds_write_b128 v82, v[172:175] offset:4112\n"
            "v_perm_b32 v160, v134, v130, s78\n"
            "v_perm_b32 v161, v142, v138, s78\n"
            "v_perm_b32 v162, v150, v146, s78\n"
            "v_perm_b32 v163, v158, v154, s78\n"
            "ds_write_b128 v82, v[160:163] offset:32\n"
            "v_perm_b32 v164, v134, v130, s79\n"
            "v_perm_b32 v165, v142, v138, s79\n"
            "v_perm_b32 v166, v150, v146, s79\n"
            "v_perm_b32 v167, v158, v154, s79\n"
            "ds_write_b128 v82, v[164:167] offset:4128\n"
            "v_perm_b32 v168, v135, v131, s78\n"
            "v_perm_b32 v169, v143, v139, s78\n"
            "v_perm_b32 v170, v151, v147, s78\n"
            "v_perm_b32 v171, v159, v155, s78\n"
            "ds_write_b128 v82, v[168:171] offset:48\n"
            "v_perm_b32 v172, v135, v131, s79\n"
            "v_perm_b32 v173, v143, v139, s79\n"
            "v_perm_b32 v174, v151, v147, s79\n"
            "v_perm_b32 v175, v159, v155, s79\n"
            "ds_write_b128 v82, v[172:175] offset:4144\n"
            "s_cmp_lg_u32 s3, 7\n"
            "s_cbranch_scc1 Lsort_%=\n"
            "s_mov_b64 exec, 1\n"
            "ds_add_u32 v219, v209 offset:48\n"
            "s_mov_b64 exec, -1\n"
            "s_branch Lria_%=\n"
            "Lri_%=:\n"
            "s_sleep 1\n"
            "Lria_%=:\n"
            "ds_read_b32 v220, v219 offset:60\n"
            "s_waitcnt lgkmcnt(0)\n"
            "v_readfirstlane_b32 s68, v220\n"
            "s_cmp_lt_u32 s68, 4\n"
            "s_cbranch_scc1 Lri_%=\n"
            "s_add_u32 s68, s54, 24576\n"
            "s_nop 1\n"
            "buffer_load_dword v230, v193, s[24:27], s68 offen\n"
            "s_add_u32 s68, s54, 32768\n"
            "buffer_load_dword v230, v193, s[24:27], s68 offen\n"
            "s_add_u32 s68, s69, 0\n"
            "buffer_load_dword v230, v193, s[28:31], s68 offen\n"
            "s_add_u32 s68, s69, 1048576\n"
            "buffer_load_dword v230, v193, s[28:31], s68 offen\n"
            "s_add_u32 s68, s69, 2097152\n"
            "buffer_load_dword v230, v193, s[28:31], s68 offen\n"
            "s_branch Lw7a_%=\n"
            "Lw7_%=:\n"
            "s_sleep 1\n"
            "Lw7a_%=:\n"
            "ds_read_b32 v220, v219 offset:44\n"
            "s_waitcnt lgkmcnt(0)\n"
            "v_readfirstlane_b32 s68, v220\n"
            "s_cmp_lt_u32 s68, 4\n"
            "s_cbranch_scc1 Lw7_%=\n"
            "s_setprio 3\n"
            "s_branch Lmain_%=\n"
            "Lsort_%=:\n"
            "s_branch Lc3a_%=\n"
            "Lc3_%=:\n"
            "s_sleep 1\n"
            "Lc3a_%=:\n"
            "ds_read_b32 v220, v219 offset:44\n"
            "s_waitcnt lgkmcnt(0)\n"
            "v_readfirstlane_b32 s68, v220\n"
            "s_cmp_lt_u32 s68, 3\n"
            "s_cbranch_scc1 Lc3_%=\n"
            "s_setprio 3\n"
            "Lc4_%=:\n"
            "ds_read_b32 v220, v219 offset:44\n"
            "s_waitcnt lgkmcnt(0)\n"
            "v_readfirstlane_b32 s68, v220\n"
            "s_cmp_lt_u32 s68, 4\n"
            "s_cbranch_scc1 Lc4_%=\n"
            "ds_read_b32 v160, v180 offset:96\n"
            "ds_read_b128 v[128:131], v181 offset:96\n"
            "ds_read_b128 v[132:135], v181 offset:112\n"
            "ds_read_b128 v[136:139], v181 offset:128\n"
            "ds_read_b128 v[140:143], v181 offset:144\n"
            "ds_read_b128 v[144:147], v181 offset:160\n"
            "s_waitcnt lgkmcnt(4)\n"
            "v_sub_u32 v128, v160, v128\n"
            "v_sub_u32 v129, v160, v129\n"
            "v_sub_u32 v130, v160, v130\n"
            "v_sub_u32 v131, v160, v131\n"
            "v_lshrrev_b32 v161, 31, v128\n"
            "v_lshrrev_b32 v162, 31, v129\n"
            "v_alignbit_b32 v161, v161, v130, 31\n"
            "v_alignbit_b32 v162, v162, v131, 31\n"
            "v_lshrrev_b32 v163, 7, v160\n"
            "s_waitcnt lgkmcnt(3)\n"
            "v_sub_u32 v132, v160, v132\n"
            "v_sub_u32 v133, v160, v133\n"
            "v_sub_u32 v134, v160, v134\n"
            "v_sub_u32 v135, v160, v135\n"
            "v_alignbit_b32 v161, v161, v132, 31\n"
            "v_alignbit_b32 v162, v162, v133, 31\n"
            "v_alignbit_b32 v161, v161, v134, 31\n"
            "v_alignbit_b32 v162, v162, v135, 31\n"
            "v_cmp_lt_u32 vcc, 44, v163\n"
            "v_max_u32 v164, 8, v163\n"
            "s_waitcnt lgkmcnt(2)\n"
            "v_sub_u32 v136, v160, v136\n"
            "v_sub_u32 v137, v160, v137\n"
            "v_sub_u32 v138, v160, v138\n"
            "v_sub_u32 v139, v160, v139\n"
            "v_alignbit_b32 v161, v161, v136, 31\n"
            "v_alignbit_b32 v162, v162, v137, 31\n"
            "v_alignbit_b32 v161, v161, v138, 31\n"
            "v_alignbit_b32 v162, v162, v139, 31\n"
            "v_mov_b32 v165, 255\n"
            "v_lshl_or_b32 v183, v163, 16, v183\n"
            "s_waitcnt lgkmcnt(1)\n"
            "v_sub_u32 v140, v160, v140\n"
            "v_sub_u32 v141, v160, v141\n"
            "v_sub_u32 v142, v160, v142\n"
            "v_sub_u32 v143, v160, v143\n"
            "v_alignbit_b32 v161, v161, v140, 31\n"
            "v_alignbit_b32 v162, v162, v141, 31\n"
            "v_alignbit_b32 v161, v161, v142, 31\n"
            "v_alignbit_b32 v162, v162, v143, 31\n"
            "v_cndmask_b32 v164, v164, v165, vcc\n"
            "s_waitcnt lgkmcnt(0)\n"
            "v_sub_u32 v144, v160, v144\n"
            "v_sub_u32 v145, v160, v145\n"
            "v_sub_u32 v146, v160, v146\n"
            "v_sub_u32 v147, v160, v147\n"
            "v_alignbit_b32 v161, v161, v144, 31\n"
            "v_alignbit_b32 v162, v162, v145, 31\n"
            "v_alignbit_b32 v161, v161, v146, 31\n"
            "v_alignbit_b32 v162, v162, v147, 31\n"
            "v_bcnt_u32_b32 v161, v161, 0\n"
            "v_bcnt_u32_b32 v161, v162, v161\n"
            "s_nop 1\n"
            "v_add_u32_dpp v161, v161, v161 quad_perm:[1,0,3,2] row_mask:0xf bank_mask:0xf\n"
            "v_lshl_add_u32 v182, v161, 2, v182\n"
            "ds_write2st64_b32 v182, v183, v164 offset0:10 offset1:8\n"
            "ds_write_b32 v182, v184 offset:3072\n"
            "s_mov_b64 exec, 1\n"
            "ds_add_u32 v219, v209 offset:48\n"
            "s_mov_b64 exec, -1\n"
            "s_lshl3_add_u32 s68, s2, s3\n"
            "s_cmp_lg_u32 s68, 4\n"
            "s_cbranch_scc1 Lmain_%=\n"
            "v_mov_b32 v160, s90\n"
            "s_mov_b64 exec, 1\n"
            "buffer_store_dword v160, off, s[20:23], 0 sc1\n"
            "s_mov_b64 exec, -1\n"
            "s_branch Lmain_%=\n"
            "Lcol_%=:\n"
            "s_lshl_b32 s38, s8, 7\n"
            "s_add_u32 s38, s38, s9\n"
            "s_bfe_u32 s68, s38, 0x10001\n"
            "s_lshl_b32 s68, s68, 10\n"
            "s_lshr_b32 s72, s38, 2\n"
            "s_lshl4_add_u32 s68, s72, s68\n"
            "s_and_b32 s72, s38, 1\n"
            "s_lshl3_add_u32 s68, s72, s68\n"
            "s_add_u32 s68, s68, 36864\n"
            "v_lshl_add_u32 v180, v95, 7, s68\n"
            "s_sleep 8\n"
            "buffer_load_dwordx2 v[142:143], v180, s[20:23], 0 offen sc1\n"
            "s_mul_i32 s17, s3, 1792\n"
            "s_add_u32 s17, s17, 75776\n"
            "v_lshl_add_u32 v181, v243, 2, s17\n"
            "s_mul_i32 s15, s3, 1310720\n"
            "s_add_u32 s15, s15, 561152\n"
            "s_add_u32 s16, s15, 4096\n"
            "s_mul_i32 s18, s3, 640\n"
            "s_add_u32 s18, s18, 72704\n"
            "s_lshl_b32 s19, s10, 2\n"
            "s_lshl_b32 s97, s50, 5\n"
            "s_mul_i32 s98, s97, s3\n"
            "s_lshl_b32 s98, s98, 6\n"
            "v_mul_u32_u24 v182, 704, v243\n"
            "v_add_u32 v182, 4096, v182\n"
            "v_add_u32 v184, 704, v182\n"
            "v_mul_u32_u24 v183, 704, v196\n"
            "v_add_u32 v183, 49152, v183\n"
            "v_add_u32 v185, 704, v183\n"
            "v_add_u32 v187, s18, v194\n"
            "v_lshl_add_u32 v188, v196, 3, s18\n"
            "s_mov_b32 s68, 16797696\n"
            "v_mul_lo_u32 v189, v243, s68\n"
            "v_mov_b32 v190, 128\n"
            "v_mov_b32 v191, 2.0\n"
            "v_mov_b32 v186, s98\n"
            "v_mov_b32 v197, v80\n"
            "s_branch Lcpa_%=\n"
            "Lcp_%=:\n"
            "s_sleep 1\n"
            "Lcpa_%=:\n"
            "ds_read_b32 v220, v219 offset:64\n"
            "s_waitcnt lgkmcnt(0)\n"
            "v_readfirstlane_b32 s68, v220\n"
            "s_cmp_lt_u32 s68, 4\n"
            "s_cbranch_scc1 Lcp_%=\n"
            "v_add_u32 v81, 114432, v192\n"
            "ds_read_b128 v[96:99], v81 offset:0\n"
            "ds_read_b128 v[100:103], v81 offset:1024\n"
            "ds_read_b128 v[104:107], v81 offset:2048\n"
            "ds_read_b128 v[108:111], v81 offset:3072\n"
            "ds_read_b128 v[112:115], v81 offset:4096\n"
            "ds_read_b128 v[116:119], v81 offset:5120\n"
            "ds_read_b128 v[120:123], v81 offset:6144\n"
            "ds_read_b128 v[124:127], v81 offset:7168\n"
            "ds_read_b128 v[200:203], v192 offset:60416\n"
            "s_mov_b32 s99, 0x400000\n"
            "Lpoll_%=:\n"
            "s_waitcnt vmcnt(0)\n"
            "v_cmp_ne_u32 vcc, s90, v143\n"
            "s_cbranch_vccnz Lpoll2_%=\n"
            "Lpoll3_%=:\n"
            "v_lshrrev_b32 v144, 18, v142\n"
            "v_and_b32 v146, 0xffff, v142\n"
            "v_cmp_ne_u32 s[86:87], 0, v144\n"
            "v_add_u32_dpp v145, v144, v144 row_shr:1 row_mask:0xf bank_mask:0xf bound_ctrl:1\n"
            "v_add_u32 v146, v146, v189\n"
            "v_cmp_lt_u32 s[88:89], 64, v144\n"
            "v_add_u32_dpp v145, v145, v145 row_shr:2 row_mask:0xf bank_mask:0xf bound_ctrl:1\n"
            "v_lshl_add_u32 v146, v144, 2, v146\n"
            "v_sub_u32 v147, s17, v144\n"
            "v_add_u32_dpp v145, v145, v145 row_shr:4 row_mask:0xf bank_mask:0xf bound_ctrl:1\n"
            "s_add_u32 s38, s17, -4\n"
            "s_nop 0\n"
            "v_add_u32_dpp v145, v145, v145 row_shr:8 row_mask:0xf bank_mask:0xf bound_ctrl:1\n"
            "s_nop 1\n"
            "v_add_u32_dpp v145, v145, v145 row_bcast:15 row_mask:0xa bank_mask:0xf\n"
            "s_nop 1\n"
            "v_add_u32_dpp v145, v145, v145 row_bcast:31 row_mask:0xc bank_mask:0xf\n"
            "v_cmp_ge_u32 vcc, 1792, v145\n"
            "v_readlane_b32 s37, v145, 63\n"
            "v_add_u32 v147, v147, v145\n"
            "v_add_u32 v148, s38, v145\n"
            "v_lshlrev_b32 v145, 2, v145\n"
            "v_and_b32 v148, -64, v148\n"
            "v_sub_u32 v146, v146, v145\n"
            "v_max_u32 v148, v148, v147\n"
            "s_and_b64 exec, vcc, s[86:87]\n"
            "ds_write_b32 v147, v146\n"
            "ds_write_b32 v148, v146\n"
            "s_mov_b64 exec, -1\n"
            "ds_read2st64_b32 v[74:75], v181 offset0:0 offset1:1\n"
            "ds_read2st64_b32 v[76:77], v181 offset0:2 offset1:3\n"
            "ds_read2st64_b32 v[78:79], v181 offset0:4 offset1:5\n"
            "ds_read_b32 v80, v181 offset:1536\n"
            "s_cmp_gt_u32 s37, 1792\n"
            "s_cbranch_scc1 Lover_%=\n"
            "Lover2_%=:\n"
            "s_lshr_b32 s37, s37, 2\n"
            "v_cmp_gt_i32 s[72:73], s37, v243\n"
            "s_sub_i32 s38, s37, 64\n"
            "v_cmp_gt_i32 s[74:75], s38, v243\n"
            "s_sub_i32 s38, s37, 128\n"
            "v_cmp_gt_i32 s[76:77], s38, v243\n"
            "s_sub_i32 s38, s37, 192\n"
            "v_cmp_gt_i32 s[78:79], s38, v243\n"
            "s_sub_i32 s38, s37, 256\n"
            "v_cmp_gt_i32 s[80:81], s38, v243\n"
            "s_sub_i32 s38, s37, 320\n"
            "v_cmp_gt_i32 s[82:83], s38, v243\n"
            "s_sub_i32 s38, s37, 384\n"
            "v_cmp_gt_i32 s[84:85], s38, v243\n"
            "v_cndmask_b32 v81, v191, v192, s[72:73]\n"
            "v_cndmask_b32 v82, v191, v192, s[74:75]\n"
            "v_cndmask_b32 v83, v191, v192, s[76:77]\n"
            "v_cndmask_b32 v84, v191, v192, s[78:79]\n"
            "v_cndmask_b32 v85, v191, v192, s[80:81]\n"
            "v_cndmask_b32 v86, v191, v192, s[82:83]\n"
            "v_cndmask_b32 v87, v191, v192, s[84:85]\n"
            "s_cmp_lg_u64 s[88:89], 0\n"
            "s_waitcnt lgkmcnt(0)\n"
            "v_max_u32_dpp v74, v74, v74 row_shr:1 row_mask:0xf bank_mask:0xf bound_ctrl:1\n"
            "v_max_u32_dpp v75, v75, v75 row_shr:1 row_mask:0xf bank_mask:0xf bound_ctrl:1\n"
            "v_max_u32_dpp v76, v76, v76 row_shr:1 row_mask:0xf bank_mask:0xf bound_ctrl:1\n"
            "v_max_u32_dpp v77, v77, v77 row_shr:1 row_mask:0xf bank_mask:0xf bound_ctrl:1\n"
            "v_max_u32_dpp v78, v78, v78 row_shr:1 row_mask:0xf bank_mask:0xf bound_ctrl:1\n"
            "v_max_u32_dpp v79, v79, v79 row_shr:1 row_mask:0xf bank_mask:0xf bound_ctrl:1\n"
            "v_max_u32_dpp v80, v80, v80 row_shr:1 row_mask:0xf bank_mask:0xf bound_ctrl:1\n"
            "v_max_u32_dpp v74, v74, v74 row_shr:2 row_mask:0xf bank_mask:0xf bound_ctrl:1\n"
            "v_max_u32_dpp v75, v75, v75 row_shr:2 row_mask:0xf bank_mask:0xf bound_ctrl:1\n"
            "v_max_u32_dpp v76, v76, v76 row_shr:2 row_mask:0xf bank_mask:0xf bound_ctrl:1\n"
            "v_max_u32_dpp v77, v77, v77 row_shr:2 row_mask:0xf bank_mask:0xf bound_ctrl:1\n"
            "v_max_u32_dpp v78, v78, v78 row_shr:2 row_mask:0xf bank_mask:0xf bound_ctrl:1\n"
            "v_max_u32_dpp v79, v79, v79 row_shr:2 row_mask:0xf bank_mask:0xf bound_ctrl:1\n"
            "v_max_u32_dpp v80, v80, v80 row_shr:2 row_mask:0xf bank_mask:0xf bound_ctrl:1\n"
            "v_max_u32_dpp v74, v74, v74 row_shr:4 row_mask:0xf bank_mask:0xf bound_ctrl:1\n"
            "v_max_u32_dpp v75, v75, v75 row_shr:4 row_mask:0xf bank_mask:0xf bound_ctrl:1\n"
            "v_max_u32_dpp v76, v76, v76 row_shr:4 row_mask:0xf bank_mask:0xf bound_ctrl:1\n"
            "v_max_u32_dpp v77, v77, v77 row_shr:4 row_mask:0xf bank_mask:0xf bound_ctrl:1\n"
            "v_max_u32_dpp v78, v78, v78 row_shr:4 row_mask:0xf bank_mask:0xf bound_ctrl:1\n"
            "v_max_u32_dpp v79, v79, v79 row_shr:4 row_mask:0xf bank_mask:0xf bound_ctrl:1\n"
            "v_max_u32_dpp v80, v80, v80 row_shr:4 row_mask:0xf bank_mask:0xf bound_ctrl:1\n"
            "v_max_u32_dpp v74, v74, v74 row_shr:8 row_mask:0xf bank_mask:0xf bound_ctrl:1\n"
            "v_max_u32_dpp v75, v75, v75 row_shr:8 row_mask:0xf bank_mask:0xf bound_ctrl:1\n"
            "v_max_u32_dpp v76, v76, v76 row_shr:8 row_mask:0xf bank_mask:0xf bound_ctrl:1\n"
            "v_max_u32_dpp v77, v77, v77 row_shr:8 row_mask:0xf bank_mask:0xf bound_ctrl:1\n"
            "v_max_u32_dpp v78, v78, v78 row_shr:8 row_mask:0xf bank_mask:0xf bound_ctrl:1\n"
            "v_max_u32_dpp v79, v79, v79 row_shr:8 row_mask:0xf bank_mask:0xf bound_ctrl:1\n"
            "v_max_u32_dpp v80, v80, v80 row_shr:8 row_mask:0xf bank_mask:0xf bound_ctrl:1\n"
            "s_cbranch_scc1 Llng_%=\n"
            "Llng2_%=:\n"
            "v_mad_u32_u24 v81, v74, 1, v81\n"
            "buffer_load_dwordx4 v[34:37], v81, s[20:23], s15 offen offset:0 sc1\n"
            "v_mad_u32_u24 v82, v75, 1, v82\n"
            "buffer_load_dwordx4 v[40:43], v82, s[20:23], s15 offen offset:1024 sc1\n"
            "v_mad_u32_u24 v83, v76, 1, v83\n"
            "buffer_load_dwordx4 v[46:49], v83, s[20:23], s15 offen offset:2048 sc1\n"
            "v_mad_u32_u24 v84, v77, 1, v84\n"
            "buffer_load_dwordx4 v[52:55], v84, s[20:23], s15 offen offset:3072 sc1\n"
            "v_mad_u32_u24 v85, v78, 1, v85\n"
            "buffer_load_dwordx4 v[58:61], v85, s[20:23], s16 offen offset:0 sc1\n"
            "v_mad_u32_u24 v86, v79, 1, v86\n"
            "buffer_load_dwordx4 v[64:67], v86, s[20:23], s16 offen offset:1024 sc1\n"
            "v_mad_u32_u24 v87, v80, 1, v87\n"
            "buffer_load_dwordx4 v[70:73], v87, s[20:23], s16 offen offset:2048 sc1\n"
            "s_mov_b64 exec, 1\n"
            "ds_add_u32 v219, v209 offset:60\n"
            "s_mov_b64 exec, -1\n"
            "v_lshrrev_b32 v88, 24, v74\n"
            "v_mad_u32_u24 v88, v88, s97, v186\n"
            "v_lshrrev_b32 v89, 24, v75\n"
            "v_mad_u32_u24 v89, v89, s97, v186\n"
            "v_lshrrev_b32 v90, 24, v76\n"
            "v_mad_u32_u24 v90, v90, s97, v186\n"
            "v_lshrrev_b32 v91, 24, v77\n"
            "v_mad_u32_u24 v91, v91, s97, v186\n"
            "v_lshrrev_b32 v92, 24, v78\n"
            "v_mad_u32_u24 v92, v92, s97, v186\n"
            "v_lshrrev_b32 v93, 24, v79\n"
            "v_mad_u32_u24 v93, v93, s97, v186\n"
            "v_lshrrev_b32 v94, 24, v80\n"
            "v_mad_u32_u24 v94, v94, s97, v186\n"
            "s_movk_i32 s38, 0x3fff\n"
            "s_mov_b32 s36, 0xfffff\n"
            "s_waitcnt vmcnt(6)\n"
            "Lchk0_%=:\n"
            "v_xor_b32 v35, s93, v35\n"
            "v_xor_b32 v37, s94, v37\n"
            "v_cmp_lt_u32 vcc, s38, v35\n"
            "v_cmp_lt_u32 s[86:87], s36, v37\n"
            "v_subrev_u32 v135, s19, v37\n"
            "v_and_b32 v135, 0x1ff, v135\n"
            "s_or_b64 vcc, vcc, s[86:87]\n"
            "s_and_b64 vcc, vcc, s[72:73]\n"
            "s_cbranch_scc1 Lre0_%=\n"
            "Lok0_%=:\n"
            "v_lshlrev_b32 v144, 2, v135\n"
            "v_and_b32 v145, 0x1fc, v135\n"
            "s_mov_b64 exec, s[72:73]\n"
            "ds_add_rtn_u32 v128, v144, v197 offset:512\n"
            "ds_add_u32 v145, v190 offset:96\n"
            "s_mov_b64 exec, -1\n"
            "s_waitcnt vmcnt(5)\n"
            "Lchk1_%=:\n"
            "v_xor_b32 v41, s93, v41\n"
            "v_xor_b32 v43, s94, v43\n"
            "v_cmp_lt_u32 vcc, s38, v41\n"
            "v_cmp_lt_u32 s[86:87], s36, v43\n"
            "v_subrev_u32 v136, s19, v43\n"
            "v_and_b32 v136, 0x1ff, v136\n"
            "s_or_b64 vcc, vcc, s[86:87]\n"
            "s_and_b64 vcc, vcc, s[74:75]\n"
            "s_cbranch_scc1 Lre1_%=\n"
            "Lok1_%=:\n"
            "v_lshlrev_b32 v144, 2, v136\n"
            "v_and_b32 v145, 0x1fc, v136\n"
            "s_mov_b64 exec, s[74:75]\n"
            "ds_add_rtn_u32 v129, v144, v197 offset:512\n"
            "ds_add_u32 v145, v190 offset:96\n"
            "s_mov_b64 exec, -1\n"
            "s_waitcnt vmcnt(4)\n"
            "Lchk2_%=:\n"
            "v_xor_b32 v47, s93, v47\n"
            "v_xor_b32 v49, s94, v49\n"
            "v_cmp_lt_u32 vcc, s38, v47\n"
            "v_cmp_lt_u32 s[86:87], s36, v49\n"
            "v_subrev_u32 v137, s19, v49\n"
            "v_and_b32 v137, 0x1ff, v137\n"
            "s_or_b64 vcc, vcc, s[86:87]\n"
            "s_and_b64 vcc, vcc, s[76:77]\n"
            "s_cbranch_scc1 Lre2_%=\n"
            "Lok2_%=:\n"
            "v_lshlrev_b32 v144, 2, v137\n"
            "v_and_b32 v145, 0x1fc, v137\n"
            "s_mov_b64 exec, s[76:77]\n"
            "ds_add_rtn_u32 v130, v144, v197 offset:512\n"
            "ds_add_u32 v145, v190 offset:96\n"
            "s_mov_b64 exec, -1\n"
            "s_waitcnt vmcnt(3)\n"
            "Lchk3_%=:\n"
            "v_xor_b32 v53, s93, v53\n"
            "v_xor_b32 v55, s94, v55\n"
            "v_cmp_lt_u32 vcc, s38, v53\n"
            "v_cmp_lt_u32 s[86:87], s36, v55\n"
            "v_subrev_u32 v138, s19, v55\n"
            "v_and_b32 v138, 0x1ff, v138\n"
            "s_or_b64 vcc, vcc, s[86:87]\n"
            "s_and_b64 vcc, vcc, s[78:79]\n"
            "s_cbranch_scc1 Lre3_%=\n"
            "Lok3_%=:\n"
            "v_lshlrev_b32 v144, 2, v138\n"
            "v_and_b32 v145, 0x1fc, v138\n"
            "s_mov_b64 exec, s[78:79]\n"
            "ds_add_rtn_u32 v131, v144, v197 offset:512\n"
            "ds_add_u32 v145, v190 offset:96\n"
            "s_mov_b64 exec, -1\n"
            "s_waitcnt vmcnt(2)\n"
            "Lchk4_%=:\n"
            "v_xor_b32 v59, s93, v59\n"
            "v_xor_b32 v61, s94, v61\n"
            "v_cmp_lt_u32 vcc, s38, v59\n"
            "v_cmp_lt_u32 s[86:87], s36, v61\n"
            "v_subrev_u32 v139, s19, v61\n"
            "v_and_b32 v139, 0x1ff, v139\n"
            "s_or_b64 vcc, vcc, s[86:87]\n"
            "s_and_b64 vcc, vcc, s[80:81]\n"
            "s_cbranch_scc1 Lre4_%=\n"
            "Lok4_%=:\n"
            "v_lshlrev_b32 v144, 2, v139\n"
            "v_and_b32 v145, 0x1fc, v139\n"
            "s_mov_b64 exec, s[80:81]\n"
            "ds_add_rtn_u32 v132, v144, v197 offset:512\n"
            "ds_add_u32 v145, v190 offset:96\n"
            "s_mov_b64 exec, -1\n"
            "s_waitcnt vmcnt(1)\n"
            "Lchk5_%=:\n"
            "v_xor_b32 v65, s93, v65\n"
            "v_xor_b32 v67, s94, v67\n"
            "v_cmp_lt_u32 vcc, s38, v65\n"
            "v_cmp_lt_u32 s[86:87], s36, v67\n"
            "v_subrev_u32 v140, s19, v67\n"
            "v_and_b32 v140, 0x1ff, v140\n"
            "s_or_b64 vcc, vcc, s[86:87]\n"
            "s_and_b64 vcc, vcc, s[82:83]\n"
            "s_cbranch_scc1 Lre5_%=\n"
            "Lok5_%=:\n"
            "v_lshlrev_b32 v144, 2, v140\n"
            "v_and_b32 v145, 0x1fc, v140\n"
            "s_mov_b64 exec, s[82:83]\n"
            "ds_add_rtn_u32 v133, v144, v197 offset:512\n"
            "ds_add_u32 v145, v190 offset:96\n"
            "s_mov_b64 exec, -1\n"
            "s_cmp_eq_u64 s[84:85], 0\n"
            "s_cbranch_scc1 Lr7_%=\n"
            "s_waitcnt vmcnt(0)\n"
            "Lchk6_%=:\n"
            "v_xor_b32 v71, s93, v71\n"
            "v_xor_b32 v73, s94, v73\n"
            "v_cmp_lt_u32 vcc, s38, v71\n"
            "v_cmp_lt_u32 s[86:87], s36, v73\n"
            "v_subrev_u32 v141, s19, v73\n"
            "v_and_b32 v141, 0x1ff, v141\n"
            "s_or_b64 vcc, vcc, s[86:87]\n"
            "s_and_b64 vcc, vcc, s[84:85]\n"
            "s_cbranch_scc1 Lre6_%=\n"
            "Lok6_%=:\n"
            "v_lshlrev_b32 v144, 2, v141\n"
            "v_and_b32 v145, 0x1fc, v141\n"
            "s_mov_b64 exec, s[84:85]\n"
            "ds_add_rtn_u32 v134, v144, v197 offset:512\n"
            "ds_add_u32 v145, v190 offset:96\n"
            "s_mov_b64 exec, -1\n"
            "Lr7_%=:\n"
            "s_mov_b64 exec, 1\n"
            "ds_add_u32 v219, v209 offset:44\n"
            "s_mov_b64 exec, -1\n"
            "v_lshlrev_b32 v32, 10, v35\n"
            "v_lshrrev_b32 v33, 9, v37\n"
            "v_mov_b32 v35, v36\n"
            "v_lshl_add_u32 v33, v33, 5, v88\n"
            "v_lshl_add_u32 v135, v135, 1, s18\n"
            "v_lshlrev_b32 v38, 10, v41\n"
            "v_lshrrev_b32 v39, 9, v43\n"
            "v_mov_b32 v41, v42\n"
            "v_lshl_add_u32 v39, v39, 5, v89\n"
            "v_lshl_add_u32 v136, v136, 1, s18\n"
            "v_lshlrev_b32 v44, 10, v47\n"
            "v_lshrrev_b32 v45, 9, v49\n"
            "v_mov_b32 v47, v48\n"
            "v_lshl_add_u32 v45, v45, 5, v90\n"
            "v_lshl_add_u32 v137, v137, 1, s18\n"
            "v_lshlrev_b32 v50, 10, v53\n"
            "v_lshrrev_b32 v51, 9, v55\n"
            "v_mov_b32 v53, v54\n"
            "v_lshl_add_u32 v51, v51, 5, v91\n"
            "v_lshl_add_u32 v138, v138, 1, s18\n"
            "v_lshlrev_b32 v56, 10, v59\n"
            "v_lshrrev_b32 v57, 9, v61\n"
            "v_mov_b32 v59, v60\n"
            "v_lshl_add_u32 v57, v57, 5, v92\n"
            "v_lshl_add_u32 v139, v139, 1, s18\n"
            "v_lshlrev_b32 v62, 10, v65\n"
            "v_lshrrev_b32 v63, 9, v67\n"
            "v_mov_b32 v65, v66\n"
            "v_lshl_add_u32 v63, v63, 5, v93\n"
            "v_lshl_add_u32 v140, v140, 1, s18\n"
            "v_lshlrev_b32 v68, 10, v71\n"
            "v_lshrrev_b32 v69, 9, v73\n"
            "v_mov_b32 v71, v72\n"
            "v_lshl_add_u32 v69, v69, 5, v94\n"
            "v_lshl_add_u32 v141, v141, 1, s18\n"
            "Lcc_%=:\n"
            "ds_read_b32 v220, v219 offset:44\n"
            "s_waitcnt lgkmcnt(0)\n"
            "v_readfirstlane_b32 s68, v220\n"
            "s_cmp_lt_u32 s68, 4\n"
            "s_cbranch_scc1 Lcc_%=\n"
            "ds_read_b128 v[144:147], v192 offset:512\n"
            "ds_read_b128 v[148:151], v214 offset:1536\n"
            "v_bfe_u32 v128, v128, s12, 8\n"
            "v_bfe_u32 v129, v129, s12, 8\n"
            "v_bfe_u32 v130, v130, s12, 8\n"
            "v_bfe_u32 v131, v131, s12, 8\n"
            "v_bfe_u32 v132, v132, s12, 8\n"
            "v_bfe_u32 v133, v133, s12, 8\n"
            "v_bfe_u32 v134, v134, s12, 8\n"
            "s_waitcnt lgkmcnt(1)\n"
            "v_dot4_u32_u8 v152, v144, s13, v182\n"
            "v_dot4_u32_u8 v182, v144, s39, v182\n"
            "v_dot4_u32_u8 v153, v145, s13, v182\n"
            "v_dot4_u32_u8 v182, v145, s39, v182\n"
            "v_dot4_u32_u8 v154, v146, s13, v182\n"
            "v_dot4_u32_u8 v182, v146, s39, v182\n"
            "v_dot4_u32_u8 v155, v147, s13, v182\n"
            "v_dot4_u32_u8 v182, v147, s39, v182\n"
            "s_waitcnt lgkmcnt(0)\n"
            "v_dot4_u32_u8 v156, v148, s13, v183\n"
            "v_dot4_u32_u8 v183, v148, s39, v183\n"
            "v_dot4_u32_u8 v157, v149, s13, v183\n"
            "v_dot4_u32_u8 v183, v149, s39, v183\n"
            "v_lshl_or_b32 v152, v153, 16, v152\n"
            "v_lshl_or_b32 v153, v155, 16, v154\n"
            "ds_write_b64 v187, v[152:153]\n"
            "v_dot4_u32_u8 v158, v150, s13, v183\n"
            "v_dot4_u32_u8 v183, v150, s39, v183\n"
            "v_dot4_u32_u8 v159, v151, s13, v183\n"
            "v_dot4_u32_u8 v183, v151, s39, v183\n"
            "v_lshl_or_b32 v156, v157, 16, v156\n"
            "v_cmp_gt_u32 vcc, v182, v184\n"
            "v_lshl_or_b32 v157, v159, 16, v158\n"
            "ds_write_b64 v188, v[156:157] offset:512\n"
            "ds_read_u16 v135, v135\n"
            "ds_read_u16 v136, v136\n"
            "ds_read_u16 v137, v137\n"
            "ds_read_u16 v138, v138\n"
            "ds_read_u16 v139, v139\n"
            "ds_read_u16 v140, v140\n"
            "ds_read_u16 v141, v141\n"
            "v_cmp_gt_u32 s[86:87], v183, v185\n"
            "s_or_b64 vcc, vcc, s[86:87]\n"
            "s_cbranch_scc1 Lbig_%=\n"
            "s_waitcnt lgkmcnt(6)\n"
            "v_lshl_add_u32 v135, v128, 4, v135\n"
            "s_mov_b64 exec, s[72:73]\n"
            "ds_write_b128 v135, v[32:35]\n"
            "s_waitcnt lgkmcnt(6)\n"
            "v_lshl_add_u32 v136, v129, 4, v136\n"
            "s_mov_b64 exec, s[74:75]\n"
            "ds_write_b128 v136, v[38:41]\n"
            "s_waitcnt lgkmcnt(6)\n"
            "v_lshl_add_u32 v137, v130, 4, v137\n"
            "s_mov_b64 exec, s[76:77]\n"
            "ds_write_b128 v137, v[44:47]\n"
            "s_waitcnt lgkmcnt(6)\n"
            "v_lshl_add_u32 v138, v131, 4, v138\n"
            "s_mov_b64 exec, s[78:79]\n"
            "ds_write_b128 v138, v[50:53]\n"
            "s_waitcnt lgkmcnt(6)\n"
            "v_lshl_add_u32 v139, v132, 4, v139\n"
            "s_mov_b64 exec, s[80:81]\n"
            "ds_write_b128 v139, v[56:59]\n"
            "s_waitcnt lgkmcnt(6)\n"
            "v_lshl_add_u32 v140, v133, 4, v140\n"
            "s_mov_b64 exec, s[82:83]\n"
            "ds_write_b128 v140, v[62:65]\n"
            "s_waitcnt lgkmcnt(6)\n"
            "v_lshl_add_u32 v141, v134, 4, v141\n"
            "s_mov_b64 exec, s[84:85]\n"
            "ds_write_b128 v141, v[68:71]\n"
            "Lbig_%=:\n"
            "s_mov_b64 exec, 1\n"
            "ds_add_u32 v219, v209 offset:56\n"
            "s_mov_b64 exec, -1\n"
            "Lmain_%=:\n"
            "Ls_%=:\n"
            "ds_read_b32 v220, v219 offset:48\n"
            "s_waitcnt lgkmcnt(0)\n"
            "v_readfirstlane_b32 s68, v220\n"
            "s_cmp_lt_u32 s68, 4\n"
            "s_cbranch_scc1 Ls_%=\n"
            "ds_read_u16 v132, v248 offset:512\n"
            "ds_read_u16 v133, v248 offset:514\n"
            "ds_read_u16 v134, v244 offset:512\n"
            "ds_read_u16 v135, v244 offset:514\n"
            "ds_read_b32 v128, v240\n"
            "ds_read_b32 v129, v241\n"
            "ds_read_b32 v130, v242\n"
            "ds_read_b32 v131, v219\n"
            "ds_read_b32 v217, v244 offset:1024\n"
            "ds_read_b32 v215, v245 offset:1024\n"
            "ds_read_b32 v218, v246 offset:1024\n"
            "v_add_u32 v136, 4, v249\n"
            "v_add_u32 v137, 4, v206\n"
            "s_waitcnt lgkmcnt(7)\n"
            "v_cmp_gt_u32 vcc, v133, v206\n"
            "v_cmp_gt_u32 s[70:71], v135, v249\n"
            "v_cmp_gt_u32 s[86:87], v133, v137\n"
            "v_cmp_gt_u32 s[88:89], v135, v136\n"
            "v_add_u32 v132, v132, v207\n"
            "v_add_u32 v134, v134, v250\n"
            "v_cndmask_b32 v232, v251, v132, vcc\n"
            "v_cndmask_b32 v221, v251, v134, s[70:71]\n"
            "v_add_u32 v132, 64, v132\n"
            "v_add_u32 v134, 64, v134\n"
            "v_cndmask_b32 v233, v251, v132, s[86:87]\n"
            "v_cndmask_b32 v210, v251, v134, s[88:89]\n"
            "Lw_%=:\n"
            "ds_read_b32 v220, v219 offset:56\n"
            "s_waitcnt lgkmcnt(0)\n"
            "v_readfirstlane_b32 s68, v220\n"
            "s_cmp_lt_u32 s68, 4\n"
            "s_cbranch_scc1 Lw_%=\n"
            "ds_read_b32 v232, v232 offset:4\n"
            "ds_read_b128 v[196:199], v221\n"
            "ds_read_b32 v233, v233 offset:4\n"
            "s_waitcnt lgkmcnt(2)\n"
            "v_add_u32 v232, v232, v229\n"
            "buffer_load_dwordx4 v[160:163], v232, s[24:27], 0 offen\n"
            "s_waitcnt lgkmcnt(0)\n"
            "v_add_u32 v233, v233, v229\n"
            "v_add_u32_dpp v220, v196, v214 quad_perm:[0,0,0,0] row_mask:0xf bank_mask:0xf\n"
            "v_add_u32_dpp v221, v196, v214 quad_perm:[1,1,1,1] row_mask:0xf bank_mask:0xf\n"
            "v_add_u32_dpp v222, v196, v214 quad_perm:[2,2,2,2] row_mask:0xf bank_mask:0xf\n"
            "v_add_u32_dpp v223, v196, v214 quad_perm:[3,3,3,3] row_mask:0xf bank_mask:0xf\n"
            "buffer_load_dwordx4 v[32:35], v220, s[28:31], 0 offen\n"
            "buffer_load_dwordx4 v[36:39], v220, s[28:31], 0 offen offset:256\n"
            "v_mfma_f32_4x4x4_16b_bf16 v[204:207], v[202:203], v[198:199], 0\n"
            "buffer_load_dwordx4 v[48:51], v221, s[28:31], 0 offen\n"
            "buffer_load_dwordx4 v[52:55], v221, s[28:31], 0 offen offset:256\n"
            "buffer_load_dwordx4 v[232:235], v233, s[24:27], 0 offen\n"
            "buffer_load_dwordx4 v[64:67], v222, s[28:31], 0 offen\n"
            "buffer_load_dwordx4 v[68:71], v222, s[28:31], 0 offen offset:256\n"
            "v_mfma_f32_4x4x4_16b_bf16 v[204:207], v[200:201], v[198:199], v[204:207]\n"
            "buffer_load_dwordx4 v[80:83], v223, s[28:31], 0 offen\n"
            "buffer_load_dwordx4 v[84:87], v223, s[28:31], 0 offen offset:256\n"
            "buffer_load_dwordx4 v[40:43], v220, s[28:31], 0 offen offset:512\n"
            "buffer_load_dwordx4 v[44:47], v220, s[28:31], 0 offen offset:768\n"
            "buffer_load_dwordx4 v[56:59], v221, s[28:31], 0 offen offset:512\n"
            "buffer_load_dwordx4 v[60:63], v221, s[28:31], 0 offen offset:768\n"
            "buffer_load_dwordx4 v[72:75], v222, s[28:31], 0 offen offset:512\n"
            "buffer_load_dwordx4 v[76:79], v222, s[28:31], 0 offen offset:768\n"
            "buffer_load_dwordx4 v[88:91], v223, s[28:31], 0 offen offset:512\n"
            "buffer_load_dwordx4 v[92:95], v223, s[28:31], 0 offen offset:768\n"
            "v_max_u32 v128, 12, v128\n"
            "v_mov_b32_dpp v134, v130 quad_perm:[1,2,3,0] row_mask:0xf bank_mask:0xf\n"
            "v_add_u32 v132, v128, v129\n"
            "v_sub_u32 v133, 0, v132\n"
            "v_lshl_add_u32 v135, v132, 1, v208\n"
            "v_and_b32 v133, 3, v133\n"
            "v_lshl_add_u32 v133, v132, 1, v133\n"
            "v_add3_u32 v135, v135, v134, v130\n"
            "v_readlane_b32 s73, v128, s3\n"
            "s_nop 0\n"
            "v_add_u32_dpp v135, v133, v135 row_shr:4 row_mask:0xf bank_mask:0xf bound_ctrl:1\n"
            "v_readlane_b32 s75, v129, s3\n"
            "v_readlane_b32 s80, v130, s3\n"
            "v_sub_u32_dpp v133, v135, v135 quad_perm:[1,2,3,0] row_mask:0xf bank_mask:0xf\n"
            "v_mov_b32_dpp v136, v135 quad_perm:[3,0,1,2] row_mask:0xf bank_mask:0xf\n"
            "v_lshl_add_u32 v137, v134, 3, 8\n"
            "v_mad_i32_i24 v133, v133, 3, v137\n"
            "v_sub_u32_dpp v136, v135, v136 quad_perm:[2,3,0,1] row_mask:0xf bank_mask:0xf\n"
            "v_add_u32 v134, -4, v134\n"
            "v_add_u32 v133, v133, v136\n"
            "v_ashrrev_i32 v133, 4, v133\n"
            "v_max_i32 v133, 4, v133\n"
            "v_min_i32 v133, v133, v134\n"
            "v_max_i32 v133, 0, v133\n"
            "s_lshr_b32 s69, s73, 1\n"
            "s_max_i32 s69, s69, 8\n"
            "v_mov_b32_dpp v134, v133 quad_perm:[3,0,1,2] row_mask:0xf bank_mask:0xf\n"
            "v_readlane_b32 s77, v133, s3\n"
            "v_sub_u32 v134, v130, v134\n"
            "v_add3_u32 v132, v132, v133, v131\n"
            "v_readlane_b32 s76, v134, s3\n"
            "v_add_u32 v132, v132, v134\n"
            "s_add_u32 s78, s69, s77\n"
            "v_cmp_lt_u32 vcc, 72, v132\n"
            "s_add_u32 s79, s78, s75\n"
            "s_sub_u32 s83, s79, s80\n"
            "s_add_u32 s83, s83, s76\n"
            "s_cbranch_vccnz Lslow_%=\n"
            "s_add_u32 s80, s79, s76\n"
            "s_sub_u32 s84, s80, s69\n"
            "s_add_u32 s73, s73, s84\n"
            "s_pack_ll_b32_b16 s81, s40, s69\n"
            "s_pack_ll_b32_b16 s82, s65, s78\n"
            "s_pack_ll_b32_b16 s83, s66, s83\n"
            "s_pack_ll_b32_b16 s84, s64, s84\n"
            "v_mov_b32 v138, 0xffff\n"
            "v_mov_b32 v136, s64\n"
            "v_cmpx_le_u32 vcc, s69, v243\n"
            "v_mov_b32 v136, s81\n"
            "v_cmpx_le_u32 vcc, s78, v243\n"
            "v_mov_b32 v136, s82\n"
            "v_cmpx_le_u32 vcc, s79, v243\n"
            "v_mov_b32 v136, s83\n"
            "v_cmpx_le_u32 vcc, s80, v243\n"
            "v_mov_b32 v136, s84\n"
            "s_mov_b64 exec, -1\n"
            "v_and_b32 v137, 0xffff, v136\n"
            "ds_read_b128 v[140:143], v137\n"
            "v_ashrrev_i32 v136, 16, v136\n"
            "v_sub_u32 v136, v243, v136\n"
            "v_lshl_or_b32 v137, v136, 16, v138\n"
            "v_lshlrev_b32 v136, 4, v136\n"
            "v_cvt_pk_bf16_f32 v204, v204, v205\n"
            "v_cvt_pk_bf16_f32 v205, v206, v207\n"
            "ds_write_b64 v212, v[204:205]\n"
            "ds_read_b128 v[196:199], v210\n"
            "ds_read_b64 v[208:209], v213\n"
            "s_add_u32 s73, s73, 3\n"
            "s_and_b32 s74, s73, 3\n"
            "s_add_u32 s74, s74, 1\n"
            "s_lshr_b32 s73, s73, 2\n"
            "s_mov_b32 s68, 0x05040100\n"
            "v_add_u32 v217, v217, v214\n"
            "v_add3_u32 v215, v215, v214, s63\n"
            "v_add_u32 v218, v218, v214\n"
            "v_not_b32 v223, s90\n"
            "s_waitcnt lgkmcnt(3)\n"
            "v_cmp_gt_u32 vcc, v140, v137\n"
            "v_cmp_gt_u32 s[70:71], v141, v137\n"
            "v_cmp_gt_u32 s[86:87], v142, v137\n"
            "v_cmp_gt_u32 s[88:89], v143, v137\n"
            "v_add_u32 v140, v140, v136\n"
            "v_add_u32 v141, v141, v136\n"
            "v_add_u32 v142, v142, v136\n"
            "v_add_u32 v143, v143, v136\n"
            "v_cndmask_b32 v140, v251, v140, vcc\n"
            "v_cndmask_b32 v141, v251, v141, s[70:71]\n"
            "v_cndmask_b32 v142, v251, v142, s[86:87]\n"
            "v_cndmask_b32 v143, v251, v143, s[88:89]\n"
            "v_perm_b32 v140, v141, v140, s68\n"
            "v_perm_b32 v141, v143, v142, s68\n"
            "ds_write_b64 v247, v[140:141]\n"
            "ds_read_u16 v211, v238\n"
            "s_cmp_gt_u32 s73, 13\n"
            "s_cbranch_scc1 Llong_%=\n"
            "Llong2_%=:\n"
            "v_add_u32 v216, s63, v218\n"
            "s_lshl_b32 s72, s75, 8\n"
            "s_lshl_b32 s76, s76, 16\n"
            "s_add_u32 s72, s72, s76\n"
            "s_add_u32 s72, s72, s77\n"
            "s_add_u32 s72, s72, 0xfefbfbfc\n"
            "s_add_u32 s75, s75, 0xfefc\n"
            "s_cmp_lt_u32 s3, 4\n"
            "s_cselect_b32 s72, s75, s72\n"
            "s_cmp_lt_u32 s3, 6\n"
            "s_cselect_b32 s75, 1, 0\n"
            "v_lshl_or_b32 v218, s75, 31, v218\n"
            "s_mov_b32 s85, 30\n"
            "s_mov_b32 s42, 0\n"
            "s_waitcnt vmcnt(17)\n"
            "ds_bpermute_b32 v192, v253, v160\n"
            "ds_bpermute_b32 v193, v253, v161\n"
            "ds_bpermute_b32 v194, v253, v162\n"
            "ds_bpermute_b32 v195, v253, v163\n"
            "s_waitcnt lgkmcnt(0)\n"
            "v_mfma_f32_16x16x32_bf16 v[176:179], v[192:195], v[96:99], 0\n"
            "v_mfma_f32_16x16x32_bf16 v[180:183], v[192:195], v[100:103], 0\n"
            "v_mfma_f32_16x16x32_bf16 v[184:187], v[192:195], v[104:107], 0\n"
            "v_mfma_f32_16x16x32_bf16 v[188:191], v[192:195], v[108:111], 0\n"
            "v_mfma_f32_16x16x32_bf16 v[128:131], v[192:195], v[112:115], 0\n"
            "v_mfma_f32_16x16x32_bf16 v[132:135], v[192:195], v[116:119], 0\n"
            "s_nop 2\n"
            "v_exp_f32 v144, v176\n"
            "v_exp_f32 v145, v177\n"
            "v_exp_f32 v146, v178\n"
            "v_exp_f32 v147, v179\n"
            "v_exp_f32 v148, v180\n"
            "v_exp_f32 v149, v181\n"
            "v_exp_f32 v150, v182\n"
            "v_exp_f32 v151, v183\n"
            "v_exp_f32 v152, v184\n"
            "v_exp_f32 v153, v185\n"
            "v_exp_f32 v154, v186\n"
            "v_exp_f32 v155, v187\n"
            "v_exp_f32 v156, v188\n"
            "v_exp_f32 v157, v189\n"
            "v_exp_f32 v158, v190\n"
            "v_exp_f32 v159, v191\n"
            "v_pk_add_f32 v[144:145], v[144:145], 1.0 op_sel_hi:[1,0]\n"
            "v_pk_add_f32 v[146:147], v[146:147], 1.0 op_sel_hi:[1,0]\n"
            "v_pk_add_f32 v[148:149], v[148:149], 1.0 op_sel_hi:[1,0]\n"
            "v_pk_add_f32 v[150:151], v[150:151], 1.0 op_sel_hi:[1,0]\n"
            "v_pk_add_f32 v[152:153], v[152:153], 1.0 op_sel_hi:[1,0]\n"
            "v_pk_add_f32 v[154:155], v[154:155], 1.0 op_sel_hi:[1,0]\n"
            "v_pk_add_f32 v[156:157], v[156:157], 1.0 op_sel_hi:[1,0]\n"
            "v_pk_add_f32 v[158:159], v[158:159], 1.0 op_sel_hi:[1,0]\n"
            "v_rcp_f32 v144, v144\n"
            "v_rcp_f32 v145, v145\n"
            "v_rcp_f32 v146, v146\n"
            "v_rcp_f32 v147, v147\n"
            "v_rcp_f32 v148, v148\n"
            "v_rcp_f32 v149, v149\n"
            "v_rcp_f32 v150, v150\n"
            "v_rcp_f32 v151, v151\n"
            "v_rcp_f32 v152, v152\n"
            "v_rcp_f32 v153, v153\n"
            "v_rcp_f32 v154, v154\n"
            "v_rcp_f32 v155, v155\n"
            "v_rcp_f32 v156, v156\n"
            "v_rcp_f32 v157, v157\n"
            "v_rcp_f32 v158, v158\n"
            "v_rcp_f32 v159, v159\n"
            "v_pk_mul_f32 v[144:145], v[144:145], v[176:177]\n"
            "v_pk_mul_f32 v[146:147], v[146:147], v[178:179]\n"
            "v_pk_mul_f32 v[148:149], v[148:149], v[180:181]\n"
            "v_pk_mul_f32 v[150:151], v[150:151], v[182:183]\n"
            "v_pk_mul_f32 v[152:153], v[152:153], v[184:185]\n"
            "v_pk_mul_f32 v[154:155], v[154:155], v[186:187]\n"
            "v_pk_mul_f32 v[156:157], v[156:157], v[188:189]\n"
            "v_pk_mul_f32 v[158:159], v[158:159], v[190:191]\n"
            "s_branch Ldisp_%=\n"
            "Lfast_%=:\n"
            "v_mfma_f32_16x16x32_bf16 v[136:139], v[192:195], v[120:123], 0\n"
            "v_exp_f32 v160, v128\n"
            "v_add_u32 v237, 32, v237\n"
            "v_exp_f32 v161, v129\n"
            "s_waitcnt vmcnt(12) lgkmcnt(0)\n"
            "v_mfma_f32_4x4x4_16b_bf16 v[176:179], v[208:209], v[32:33], 0 cbsz:2\n"
            "v_exp_f32 v162, v130\n"
            "v_mfma_f32_4x4x4_16b_bf16 v[180:183], v[208:209], v[34:35], 0 cbsz:2\n"
            "v_exp_f32 v163, v131\n"
            "v_mfma_f32_4x4x4_16b_bf16 v[184:187], v[208:209], v[36:37], 0 cbsz:2\n"
            "v_exp_f32 v164, v132\n"
            "v_mfma_f32_4x4x4_16b_bf16 v[188:191], v[208:209], v[38:39], 0 cbsz:2\n"
            "v_exp_f32 v165, v133\n"
            "v_add_u32_dpp v219, v196, v214 quad_perm:[0,0,0,0] row_mask:0xf bank_mask:0xf\n"
            "v_mfma_f32_4x4x4_16b_bf16 v[204:207], v[202:203], v[198:199], 0\n"
            "v_exp_f32 v166, v134\n"
            "buffer_load_dwordx4 v[32:35], v219, s[28:31], 0 offen\n"
            "buffer_load_dwordx4 v[36:39], v219, s[28:31], 0 offen offset:256\n"
            "v_exp_f32 v167, v135\n"
            "v_pk_fma_f32 v[0:1], v[144:145], v[176:177], v[0:1] op_sel_hi:[0,1,1]\n"
            "v_pk_fma_f32 v[2:3], v[144:145], v[178:179], v[2:3] op_sel_hi:[0,1,1]\n"
            "v_pk_fma_f32 v[4:5], v[148:149], v[180:181], v[4:5] op_sel_hi:[0,1,1]\n"
            "v_pk_fma_f32 v[6:7], v[148:149], v[182:183], v[6:7] op_sel_hi:[0,1,1]\n"
            "v_pk_fma_f32 v[8:9], v[152:153], v[184:185], v[8:9] op_sel_hi:[0,1,1]\n"
            "v_pk_fma_f32 v[10:11], v[152:153], v[186:187], v[10:11] op_sel_hi:[0,1,1]\n"
            "v_pk_fma_f32 v[12:13], v[156:157], v[188:189], v[12:13] op_sel_hi:[0,1,1]\n"
            "v_pk_fma_f32 v[14:15], v[156:157], v[190:191], v[14:15] op_sel_hi:[0,1,1]\n"
            "v_pk_add_f32 v[160:161], v[160:161], 1.0 op_sel_hi:[1,0]\n"
            "v_pk_add_f32 v[162:163], v[162:163], 1.0 op_sel_hi:[1,0]\n"
            "v_pk_add_f32 v[164:165], v[164:165], 1.0 op_sel_hi:[1,0]\n"
            "v_pk_add_f32 v[166:167], v[166:167], 1.0 op_sel_hi:[1,0]\n"
            "v_mfma_f32_16x16x32_bf16 v[140:143], v[192:195], v[124:127], 0\n"
            "ds_bpermute_b32 v192, v253, v232\n"
            "ds_bpermute_b32 v193, v253, v233\n"
            "ds_bpermute_b32 v194, v253, v234\n"
            "ds_bpermute_b32 v195, v253, v235\n"
            "ds_read_b32 v230, v211 offset:4\n"
            "ds_read_u16 v210, v237\n"
            "v_rcp_f32 v160, v160\n"
            "v_rcp_f32 v161, v161\n"
            "v_mfma_f32_4x4x4_16b_bf16 v[176:179], v[208:209], v[48:49], 0 cbsz:2 abid:1\n"
            "v_rcp_f32 v162, v162\n"
            "v_mfma_f32_4x4x4_16b_bf16 v[180:183], v[208:209], v[50:51], 0 cbsz:2 abid:1\n"
            "v_rcp_f32 v163, v163\n"
            "v_mfma_f32_4x4x4_16b_bf16 v[184:187], v[208:209], v[52:53], 0 cbsz:2 abid:1\n"
            "v_rcp_f32 v164, v164\n"
            "v_mfma_f32_4x4x4_16b_bf16 v[188:191], v[208:209], v[54:55], 0 cbsz:2 abid:1\n"
            "v_rcp_f32 v165, v165\n"
            "v_add_u32_dpp v220, v196, v214 quad_perm:[1,1,1,1] row_mask:0xf bank_mask:0xf\n"
            "v_mfma_f32_4x4x4_16b_bf16 v[204:207], v[200:201], v[198:199], v[204:207]\n"
            "v_rcp_f32 v166, v166\n"
            "buffer_load_dwordx4 v[48:51], v220, s[28:31], 0 offen\n"
            "buffer_load_dwordx4 v[52:55], v220, s[28:31], 0 offen offset:256\n"
            "v_rcp_f32 v167, v167\n"
            "v_pk_fma_f32 v[0:1], v[144:145], v[176:177], v[0:1] op_sel:[1,0,0]\n"
            "v_pk_fma_f32 v[2:3], v[144:145], v[178:179], v[2:3] op_sel:[1,0,0]\n"
            "v_pk_fma_f32 v[4:5], v[148:149], v[180:181], v[4:5] op_sel:[1,0,0]\n"
            "v_pk_fma_f32 v[6:7], v[148:149], v[182:183], v[6:7] op_sel:[1,0,0]\n"
            "v_pk_fma_f32 v[8:9], v[152:153], v[184:185], v[8:9] op_sel:[1,0,0]\n"
            "v_pk_fma_f32 v[10:11], v[152:153], v[186:187], v[10:11] op_sel:[1,0,0]\n"
            "v_pk_fma_f32 v[12:13], v[156:157], v[188:189], v[12:13] op_sel:[1,0,0]\n"
            "v_pk_fma_f32 v[14:15], v[156:157], v[190:191], v[14:15] op_sel:[1,0,0]\n"
            "v_pk_mul_f32 v[160:161], v[160:161], v[128:129]\n"
            "v_pk_mul_f32 v[162:163], v[162:163], v[130:131]\n"
            "v_pk_mul_f32 v[164:165], v[164:165], v[132:133]\n"
            "v_pk_mul_f32 v[166:167], v[166:167], v[134:135]\n"
            "s_waitcnt vmcnt(12) lgkmcnt(0)\n"
            "v_add_u32 v230, v230, v229\n"
            "buffer_load_dwordx4 v[232:235], v230, s[24:27], 0 offen\n"
            "s_bitcmp0_b32 s85, 0\n"
            "s_cbranch_scc1 Lnpf02_%=\n"
            "s_buffer_load_dwordx2 s[86:87], s[28:31], s67 offset:60\n"
            "s_buffer_load_dwordx2 s[86:87], s[28:31], s67 offset:188\n"
            "s_buffer_load_dwordx2 s[86:87], s[28:31], s67 offset:316\n"
            "s_buffer_load_dwordx2 s[86:87], s[28:31], s67 offset:444\n"
            "s_add_u32 s67, s67, 524288\n"
            "Lnpf02_%=:\n"
            "s_lshr_b32 s85, s85, 1\n"
            "v_mfma_f32_16x16x32_bf16 v[128:131], v[192:195], v[96:99], 0\n"
            "v_exp_f32 v168, v136\n"
            "v_exp_f32 v169, v137\n"
            "v_mfma_f32_4x4x4_16b_bf16 v[176:179], v[208:209], v[64:65], 0 cbsz:2 abid:2\n"
            "v_exp_f32 v170, v138\n"
            "v_mfma_f32_4x4x4_16b_bf16 v[180:183], v[208:209], v[66:67], 0 cbsz:2 abid:2\n"
            "v_exp_f32 v171, v139\n"
            "v_mfma_f32_4x4x4_16b_bf16 v[184:187], v[208:209], v[68:69], 0 cbsz:2 abid:2\n"
            "v_exp_f32 v172, v140\n"
            "v_mfma_f32_4x4x4_16b_bf16 v[188:191], v[208:209], v[70:71], 0 cbsz:2 abid:2\n"
            "v_exp_f32 v173, v141\n"
            "v_add_u32_dpp v221, v196, v214 quad_perm:[2,2,2,2] row_mask:0xf bank_mask:0xf\n"
            "v_exp_f32 v174, v142\n"
            "buffer_load_dwordx4 v[64:67], v221, s[28:31], 0 offen\n"
            "buffer_load_dwordx4 v[68:71], v221, s[28:31], 0 offen offset:256\n"
            "v_exp_f32 v175, v143\n"
            "v_pk_fma_f32 v[0:1], v[146:147], v[176:177], v[0:1] op_sel_hi:[0,1,1]\n"
            "v_pk_fma_f32 v[2:3], v[146:147], v[178:179], v[2:3] op_sel_hi:[0,1,1]\n"
            "v_pk_fma_f32 v[4:5], v[150:151], v[180:181], v[4:5] op_sel_hi:[0,1,1]\n"
            "v_pk_fma_f32 v[6:7], v[150:151], v[182:183], v[6:7] op_sel_hi:[0,1,1]\n"
            "v_pk_fma_f32 v[8:9], v[154:155], v[184:185], v[8:9] op_sel_hi:[0,1,1]\n"
            "v_pk_fma_f32 v[10:11], v[154:155], v[186:187], v[10:11] op_sel_hi:[0,1,1]\n"
            "v_pk_fma_f32 v[12:13], v[158:159], v[188:189], v[12:13] op_sel_hi:[0,1,1]\n"
            "v_pk_fma_f32 v[14:15], v[158:159], v[190:191], v[14:15] op_sel_hi:[0,1,1]\n"
            "v_pk_add_f32 v[168:169], v[168:169], 1.0 op_sel_hi:[1,0]\n"
            "v_pk_add_f32 v[170:171], v[170:171], 1.0 op_sel_hi:[1,0]\n"
            "v_pk_add_f32 v[172:173], v[172:173], 1.0 op_sel_hi:[1,0]\n"
            "v_pk_add_f32 v[174:175], v[174:175], 1.0 op_sel_hi:[1,0]\n"
            "v_cvt_pk_bf16_f32 v204, v204, v205\n"
            "v_cvt_pk_bf16_f32 v205, v206, v207\n"
            "ds_write_b64 v212, v[204:205]\n"
            "v_mfma_f32_16x16x32_bf16 v[132:135], v[192:195], v[100:103], 0\n"
            "v_rcp_f32 v168, v168\n"
            "v_rcp_f32 v169, v169\n"
            "v_mfma_f32_4x4x4_16b_bf16 v[176:179], v[208:209], v[80:81], 0 cbsz:2 abid:3\n"
            "v_rcp_f32 v170, v170\n"
            "v_mfma_f32_4x4x4_16b_bf16 v[180:183], v[208:209], v[82:83], 0 cbsz:2 abid:3\n"
            "v_rcp_f32 v171, v171\n"
            "v_mfma_f32_4x4x4_16b_bf16 v[184:187], v[208:209], v[84:85], 0 cbsz:2 abid:3\n"
            "v_rcp_f32 v172, v172\n"
            "v_mfma_f32_4x4x4_16b_bf16 v[188:191], v[208:209], v[86:87], 0 cbsz:2 abid:3\n"
            "v_rcp_f32 v173, v173\n"
            "v_add_u32_dpp v222, v196, v214 quad_perm:[3,3,3,3] row_mask:0xf bank_mask:0xf\n"
            "v_rcp_f32 v174, v174\n"
            "buffer_load_dwordx4 v[80:83], v222, s[28:31], 0 offen\n"
            "buffer_load_dwordx4 v[84:87], v222, s[28:31], 0 offen offset:256\n"
            "v_rcp_f32 v175, v175\n"
            "v_pk_fma_f32 v[0:1], v[146:147], v[176:177], v[0:1] op_sel:[1,0,0]\n"
            "v_pk_fma_f32 v[2:3], v[146:147], v[178:179], v[2:3] op_sel:[1,0,0]\n"
            "v_pk_fma_f32 v[4:5], v[150:151], v[180:181], v[4:5] op_sel:[1,0,0]\n"
            "v_pk_fma_f32 v[6:7], v[150:151], v[182:183], v[6:7] op_sel:[1,0,0]\n"
            "v_pk_fma_f32 v[8:9], v[154:155], v[184:185], v[8:9] op_sel:[1,0,0]\n"
            "v_pk_fma_f32 v[10:11], v[154:155], v[186:187], v[10:11] op_sel:[1,0,0]\n"
            "v_pk_fma_f32 v[12:13], v[158:159], v[188:189], v[12:13] op_sel:[1,0,0]\n"
            "v_pk_fma_f32 v[14:15], v[158:159], v[190:191], v[14:15] op_sel:[1,0,0]\n"
            "v_pk_mul_f32 v[168:169], v[168:169], v[136:137]\n"
            "v_pk_mul_f32 v[170:171], v[170:171], v[138:139]\n"
            "v_pk_mul_f32 v[172:173], v[172:173], v[140:141]\n"
            "v_pk_mul_f32 v[174:175], v[174:175], v[142:143]\n"
            "v_add_u32 v238, 32, v238\n"
            "ds_read_b128 v[196:199], v210\n"
            "ds_read_u16 v211, v238\n"
            "v_mfma_f32_16x16x32_bf16 v[136:139], v[192:195], v[104:107], 0\n"
            "v_exp_f32 v144, v128\n"
            "v_exp_f32 v145, v129\n"
            "s_waitcnt vmcnt(13)\n"
            "v_mfma_f32_4x4x4_16b_bf16 v[176:179], v[208:209], v[40:41], 0 cbsz:2\n"
            "v_exp_f32 v146, v130\n"
            "v_mfma_f32_4x4x4_16b_bf16 v[180:183], v[208:209], v[42:43], 0 cbsz:2\n"
            "v_exp_f32 v147, v131\n"
            "v_mfma_f32_4x4x4_16b_bf16 v[184:187], v[208:209], v[44:45], 0 cbsz:2\n"
            "v_exp_f32 v148, v132\n"
            "v_mfma_f32_4x4x4_16b_bf16 v[188:191], v[208:209], v[46:47], 0 cbsz:2\n"
            "v_exp_f32 v149, v133\n"
            "v_exp_f32 v150, v134\n"
            "buffer_load_dwordx4 v[40:43], v219, s[28:31], 0 offen offset:512\n"
            "buffer_load_dwordx4 v[44:47], v219, s[28:31], 0 offen offset:768\n"
            "v_exp_f32 v151, v135\n"
            "v_pk_fma_f32 v[16:17], v[160:161], v[176:177], v[16:17] op_sel_hi:[0,1,1]\n"
            "v_pk_fma_f32 v[18:19], v[160:161], v[178:179], v[18:19] op_sel_hi:[0,1,1]\n"
            "v_pk_fma_f32 v[20:21], v[164:165], v[180:181], v[20:21] op_sel_hi:[0,1,1]\n"
            "v_pk_fma_f32 v[22:23], v[164:165], v[182:183], v[22:23] op_sel_hi:[0,1,1]\n"
            "v_pk_fma_f32 v[24:25], v[168:169], v[184:185], v[24:25] op_sel_hi:[0,1,1]\n"
            "v_pk_fma_f32 v[26:27], v[168:169], v[186:187], v[26:27] op_sel_hi:[0,1,1]\n"
            "v_pk_fma_f32 v[28:29], v[172:173], v[188:189], v[28:29] op_sel_hi:[0,1,1]\n"
            "v_pk_fma_f32 v[30:31], v[172:173], v[190:191], v[30:31] op_sel_hi:[0,1,1]\n"
            "v_pk_add_f32 v[144:145], v[144:145], 1.0 op_sel_hi:[1,0]\n"
            "v_pk_add_f32 v[146:147], v[146:147], 1.0 op_sel_hi:[1,0]\n"
            "v_pk_add_f32 v[148:149], v[148:149], 1.0 op_sel_hi:[1,0]\n"
            "v_pk_add_f32 v[150:151], v[150:151], 1.0 op_sel_hi:[1,0]\n"
            "v_mfma_f32_16x16x32_bf16 v[140:143], v[192:195], v[108:111], 0\n"
            "v_rcp_f32 v144, v144\n"
            "v_rcp_f32 v145, v145\n"
            "v_mfma_f32_4x4x4_16b_bf16 v[176:179], v[208:209], v[56:57], 0 cbsz:2 abid:1\n"
            "v_rcp_f32 v146, v146\n"
            "v_mfma_f32_4x4x4_16b_bf16 v[180:183], v[208:209], v[58:59], 0 cbsz:2 abid:1\n"
            "v_rcp_f32 v147, v147\n"
            "v_mfma_f32_4x4x4_16b_bf16 v[184:187], v[208:209], v[60:61], 0 cbsz:2 abid:1\n"
            "v_rcp_f32 v148, v148\n"
            "v_mfma_f32_4x4x4_16b_bf16 v[188:191], v[208:209], v[62:63], 0 cbsz:2 abid:1\n"
            "v_rcp_f32 v149, v149\n"
            "v_rcp_f32 v150, v150\n"
            "buffer_load_dwordx4 v[56:59], v220, s[28:31], 0 offen offset:512\n"
            "buffer_load_dwordx4 v[60:63], v220, s[28:31], 0 offen offset:768\n"
            "v_rcp_f32 v151, v151\n"
            "v_pk_fma_f32 v[16:17], v[160:161], v[176:177], v[16:17] op_sel:[1,0,0]\n"
            "v_pk_fma_f32 v[18:19], v[160:161], v[178:179], v[18:19] op_sel:[1,0,0]\n"
            "v_pk_fma_f32 v[20:21], v[164:165], v[180:181], v[20:21] op_sel:[1,0,0]\n"
            "v_pk_fma_f32 v[22:23], v[164:165], v[182:183], v[22:23] op_sel:[1,0,0]\n"
            "v_pk_fma_f32 v[24:25], v[168:169], v[184:185], v[24:25] op_sel:[1,0,0]\n"
            "v_pk_fma_f32 v[26:27], v[168:169], v[186:187], v[26:27] op_sel:[1,0,0]\n"
            "v_pk_fma_f32 v[28:29], v[172:173], v[188:189], v[28:29] op_sel:[1,0,0]\n"
            "v_pk_fma_f32 v[30:31], v[172:173], v[190:191], v[30:31] op_sel:[1,0,0]\n"
            "v_pk_mul_f32 v[144:145], v[144:145], v[128:129]\n"
            "v_pk_mul_f32 v[146:147], v[146:147], v[130:131]\n"
            "v_pk_mul_f32 v[148:149], v[148:149], v[132:133]\n"
            "v_pk_mul_f32 v[150:151], v[150:151], v[134:135]\n"
            "v_mfma_f32_16x16x32_bf16 v[128:131], v[192:195], v[112:115], 0\n"
            "v_exp_f32 v152, v136\n"
            "v_exp_f32 v153, v137\n"
            "s_waitcnt vmcnt(13)\n"
            "v_mfma_f32_4x4x4_16b_bf16 v[176:179], v[208:209], v[72:73], 0 cbsz:2 abid:2\n"
            "v_exp_f32 v154, v138\n"
            "v_mfma_f32_4x4x4_16b_bf16 v[180:183], v[208:209], v[74:75], 0 cbsz:2 abid:2\n"
            "v_exp_f32 v155, v139\n"
            "v_mfma_f32_4x4x4_16b_bf16 v[184:187], v[208:209], v[76:77], 0 cbsz:2 abid:2\n"
            "v_exp_f32 v156, v140\n"
            "v_mfma_f32_4x4x4_16b_bf16 v[188:191], v[208:209], v[78:79], 0 cbsz:2 abid:2\n"
            "v_exp_f32 v157, v141\n"
            "v_exp_f32 v158, v142\n"
            "buffer_load_dwordx4 v[72:75], v221, s[28:31], 0 offen offset:512\n"
            "buffer_load_dwordx4 v[76:79], v221, s[28:31], 0 offen offset:768\n"
            "v_exp_f32 v159, v143\n"
            "v_pk_fma_f32 v[16:17], v[162:163], v[176:177], v[16:17] op_sel_hi:[0,1,1]\n"
            "v_pk_fma_f32 v[18:19], v[162:163], v[178:179], v[18:19] op_sel_hi:[0,1,1]\n"
            "v_pk_fma_f32 v[20:21], v[166:167], v[180:181], v[20:21] op_sel_hi:[0,1,1]\n"
            "v_pk_fma_f32 v[22:23], v[166:167], v[182:183], v[22:23] op_sel_hi:[0,1,1]\n"
            "v_pk_fma_f32 v[24:25], v[170:171], v[184:185], v[24:25] op_sel_hi:[0,1,1]\n"
            "v_pk_fma_f32 v[26:27], v[170:171], v[186:187], v[26:27] op_sel_hi:[0,1,1]\n"
            "v_pk_fma_f32 v[28:29], v[174:175], v[188:189], v[28:29] op_sel_hi:[0,1,1]\n"
            "v_pk_fma_f32 v[30:31], v[174:175], v[190:191], v[30:31] op_sel_hi:[0,1,1]\n"
            "v_pk_add_f32 v[152:153], v[152:153], 1.0 op_sel_hi:[1,0]\n"
            "v_pk_add_f32 v[154:155], v[154:155], 1.0 op_sel_hi:[1,0]\n"
            "v_pk_add_f32 v[156:157], v[156:157], 1.0 op_sel_hi:[1,0]\n"
            "v_pk_add_f32 v[158:159], v[158:159], 1.0 op_sel_hi:[1,0]\n"
            "v_mfma_f32_16x16x32_bf16 v[132:135], v[192:195], v[116:119], 0\n"
            "v_rcp_f32 v152, v152\n"
            "v_rcp_f32 v153, v153\n"
            "v_mfma_f32_4x4x4_16b_bf16 v[176:179], v[208:209], v[88:89], 0 cbsz:2 abid:3\n"
            "v_rcp_f32 v154, v154\n"
            "v_mfma_f32_4x4x4_16b_bf16 v[180:183], v[208:209], v[90:91], 0 cbsz:2 abid:3\n"
            "v_rcp_f32 v155, v155\n"
            "v_mfma_f32_4x4x4_16b_bf16 v[184:187], v[208:209], v[92:93], 0 cbsz:2 abid:3\n"
            "v_rcp_f32 v156, v156\n"
            "v_mfma_f32_4x4x4_16b_bf16 v[188:191], v[208:209], v[94:95], 0 cbsz:2 abid:3\n"
            "ds_read_b64 v[208:209], v213\n"
            "v_rcp_f32 v157, v157\n"
            "v_rcp_f32 v158, v158\n"
            "buffer_load_dwordx4 v[88:91], v222, s[28:31], 0 offen offset:512\n"
            "buffer_load_dwordx4 v[92:95], v222, s[28:31], 0 offen offset:768\n"
            "v_rcp_f32 v159, v159\n"
            "v_pk_fma_f32 v[16:17], v[162:163], v[176:177], v[16:17] op_sel:[1,0,0]\n"
            "v_pk_fma_f32 v[18:19], v[162:163], v[178:179], v[18:19] op_sel:[1,0,0]\n"
            "v_pk_fma_f32 v[20:21], v[166:167], v[180:181], v[20:21] op_sel:[1,0,0]\n"
            "v_pk_fma_f32 v[22:23], v[166:167], v[182:183], v[22:23] op_sel:[1,0,0]\n"
            "v_pk_fma_f32 v[24:25], v[170:171], v[184:185], v[24:25] op_sel:[1,0,0]\n"
            "v_pk_fma_f32 v[26:27], v[170:171], v[186:187], v[26:27] op_sel:[1,0,0]\n"
            "v_pk_fma_f32 v[28:29], v[174:175], v[188:189], v[28:29] op_sel:[1,0,0]\n"
            "v_pk_fma_f32 v[30:31], v[174:175], v[190:191], v[30:31] op_sel:[1,0,0]\n"
            "v_pk_mul_f32 v[152:153], v[152:153], v[136:137]\n"
            "v_pk_mul_f32 v[154:155], v[154:155], v[138:139]\n"
            "v_pk_mul_f32 v[156:157], v[156:157], v[140:141]\n"
            "v_pk_mul_f32 v[158:159], v[158:159], v[142:143]\n"
            "s_sub_u32 s69, s69, 4\n"
            "Lnext_%=:\n"
            "s_sub_u32 s73, s73, 1\n"
            "Ldisp_%=:\n"
            "s_bitcmp1_b32 s60, s73\n"
            "s_cbranch_scc1 Lprio_%=\n"
            "Ld2_%=:\n"
            "s_cmp_gt_u32 s69, 4\n"
            "s_cbranch_scc1 Lfast_%=\n"
            "v_mfma_f32_16x16x32_bf16 v[136:139], v[192:195], v[120:123], 0\n"
            "v_exp_f32 v160, v128\n"
            "v_add_u32 v237, 32, v237\n"
            "v_exp_f32 v161, v129\n"
            "s_waitcnt vmcnt(12) lgkmcnt(0)\n"
            "v_mfma_f32_4x4x4_16b_bf16 v[176:179], v[208:209], v[32:33], 0 cbsz:2\n"
            "v_exp_f32 v162, v130\n"
            "v_mfma_f32_4x4x4_16b_bf16 v[180:183], v[208:209], v[34:35], 0 cbsz:2\n"
            "v_exp_f32 v163, v131\n"
            "v_mfma_f32_4x4x4_16b_bf16 v[184:187], v[208:209], v[36:37], 0 cbsz:2\n"
            "v_exp_f32 v164, v132\n"
            "v_mfma_f32_4x4x4_16b_bf16 v[188:191], v[208:209], v[38:39], 0 cbsz:2\n"
            "v_exp_f32 v165, v133\n"
            "v_add_u32_dpp v219, v196, v214 quad_perm:[0,0,0,0] row_mask:0xf bank_mask:0xf\n"
            "v_mfma_f32_4x4x4_16b_bf16 v[204:207], v[202:203], v[198:199], 0\n"
            "v_exp_f32 v166, v134\n"
            "buffer_load_dwordx4 v[32:35], v219, s[28:31], 0 offen\n"
            "buffer_load_dwordx4 v[36:39], v219, s[28:31], 0 offen offset:256\n"
            "v_exp_f32 v167, v135\n"
            "v_pk_fma_f32 v[0:1], v[144:145], v[176:177], v[0:1] op_sel_hi:[0,1,1]\n"
            "v_pk_fma_f32 v[2:3], v[144:145], v[178:179], v[2:3] op_sel_hi:[0,1,1]\n"
            "v_pk_fma_f32 v[4:5], v[148:149], v[180:181], v[4:5] op_sel_hi:[0,1,1]\n"
            "v_pk_fma_f32 v[6:7], v[148:149], v[182:183], v[6:7] op_sel_hi:[0,1,1]\n"
            "v_pk_fma_f32 v[8:9], v[152:153], v[184:185], v[8:9] op_sel_hi:[0,1,1]\n"
            "v_pk_fma_f32 v[10:11], v[152:153], v[186:187], v[10:11] op_sel_hi:[0,1,1]\n"
            "v_pk_fma_f32 v[12:13], v[156:157], v[188:189], v[12:13] op_sel_hi:[0,1,1]\n"
            "v_pk_fma_f32 v[14:15], v[156:157], v[190:191], v[14:15] op_sel_hi:[0,1,1]\n"
            "v_pk_add_f32 v[160:161], v[160:161], 1.0 op_sel_hi:[1,0]\n"
            "v_pk_add_f32 v[162:163], v[162:163], 1.0 op_sel_hi:[1,0]\n"
            "v_pk_add_f32 v[164:165], v[164:165], 1.0 op_sel_hi:[1,0]\n"
            "v_pk_add_f32 v[166:167], v[166:167], 1.0 op_sel_hi:[1,0]\n"
            "s_cmp_eq_u32 s69, 1\n"
            "s_cbranch_scc1 Lf00_%=\n"
            "Lr00_%=:\n"
            "v_mfma_f32_16x16x32_bf16 v[140:143], v[192:195], v[124:127], 0\n"
            "ds_bpermute_b32 v192, v253, v232\n"
            "ds_bpermute_b32 v193, v253, v233\n"
            "ds_bpermute_b32 v194, v253, v234\n"
            "ds_bpermute_b32 v195, v253, v235\n"
            "ds_read_b32 v230, v211 offset:4\n"
            "ds_read_u16 v210, v237\n"
            "v_rcp_f32 v160, v160\n"
            "v_rcp_f32 v161, v161\n"
            "v_mfma_f32_4x4x4_16b_bf16 v[176:179], v[208:209], v[48:49], 0 cbsz:2 abid:1\n"
            "v_rcp_f32 v162, v162\n"
            "v_mfma_f32_4x4x4_16b_bf16 v[180:183], v[208:209], v[50:51], 0 cbsz:2 abid:1\n"
            "v_rcp_f32 v163, v163\n"
            "v_mfma_f32_4x4x4_16b_bf16 v[184:187], v[208:209], v[52:53], 0 cbsz:2 abid:1\n"
            "v_rcp_f32 v164, v164\n"
            "v_mfma_f32_4x4x4_16b_bf16 v[188:191], v[208:209], v[54:55], 0 cbsz:2 abid:1\n"
            "v_rcp_f32 v165, v165\n"
            "v_add_u32_dpp v220, v196, v214 quad_perm:[1,1,1,1] row_mask:0xf bank_mask:0xf\n"
            "v_mfma_f32_4x4x4_16b_bf16 v[204:207], v[200:201], v[198:199], v[204:207]\n"
            "v_rcp_f32 v166, v166\n"
            "buffer_load_dwordx4 v[48:51], v220, s[28:31], 0 offen\n"
            "buffer_load_dwordx4 v[52:55], v220, s[28:31], 0 offen offset:256\n"
            "v_rcp_f32 v167, v167\n"
            "v_pk_fma_f32 v[0:1], v[144:145], v[176:177], v[0:1] op_sel:[1,0,0]\n"
            "v_pk_fma_f32 v[2:3], v[144:145], v[178:179], v[2:3] op_sel:[1,0,0]\n"
            "v_pk_fma_f32 v[4:5], v[148:149], v[180:181], v[4:5] op_sel:[1,0,0]\n"
            "v_pk_fma_f32 v[6:7], v[148:149], v[182:183], v[6:7] op_sel:[1,0,0]\n"
            "v_pk_fma_f32 v[8:9], v[152:153], v[184:185], v[8:9] op_sel:[1,0,0]\n"
            "v_pk_fma_f32 v[10:11], v[152:153], v[186:187], v[10:11] op_sel:[1,0,0]\n"
            "v_pk_fma_f32 v[12:13], v[156:157], v[188:189], v[12:13] op_sel:[1,0,0]\n"
            "v_pk_fma_f32 v[14:15], v[156:157], v[190:191], v[14:15] op_sel:[1,0,0]\n"
            "v_pk_mul_f32 v[160:161], v[160:161], v[128:129]\n"
            "v_pk_mul_f32 v[162:163], v[162:163], v[130:131]\n"
            "v_pk_mul_f32 v[164:165], v[164:165], v[132:133]\n"
            "v_pk_mul_f32 v[166:167], v[166:167], v[134:135]\n"
            "s_cmp_eq_u32 s69, 2\n"
            "s_cbranch_scc1 Lf01_%=\n"
            "Lr01_%=:\n"
            "s_waitcnt vmcnt(12) lgkmcnt(0)\n"
            "v_add_u32 v230, v230, v229\n"
            "buffer_load_dwordx4 v[232:235], v230, s[24:27], 0 offen\n"
            "s_bitcmp0_b32 s85, 0\n"
            "s_cbranch_scc1 Lnps02_%=\n"
            "s_buffer_load_dwordx2 s[86:87], s[28:31], s67 offset:60\n"
            "s_buffer_load_dwordx2 s[86:87], s[28:31], s67 offset:188\n"
            "s_buffer_load_dwordx2 s[86:87], s[28:31], s67 offset:316\n"
            "s_buffer_load_dwordx2 s[86:87], s[28:31], s67 offset:444\n"
            "s_add_u32 s67, s67, 524288\n"
            "Lnps02_%=:\n"
            "s_lshr_b32 s85, s85, 1\n"
            "v_mfma_f32_16x16x32_bf16 v[128:131], v[192:195], v[96:99], 0\n"
            "v_exp_f32 v168, v136\n"
            "v_exp_f32 v169, v137\n"
            "v_mfma_f32_4x4x4_16b_bf16 v[176:179], v[208:209], v[64:65], 0 cbsz:2 abid:2\n"
            "v_exp_f32 v170, v138\n"
            "v_mfma_f32_4x4x4_16b_bf16 v[180:183], v[208:209], v[66:67], 0 cbsz:2 abid:2\n"
            "v_exp_f32 v171, v139\n"
            "v_mfma_f32_4x4x4_16b_bf16 v[184:187], v[208:209], v[68:69], 0 cbsz:2 abid:2\n"
            "v_exp_f32 v172, v140\n"
            "v_mfma_f32_4x4x4_16b_bf16 v[188:191], v[208:209], v[70:71], 0 cbsz:2 abid:2\n"
            "v_exp_f32 v173, v141\n"
            "v_add_u32_dpp v221, v196, v214 quad_perm:[2,2,2,2] row_mask:0xf bank_mask:0xf\n"
            "v_exp_f32 v174, v142\n"
            "buffer_load_dwordx4 v[64:67], v221, s[28:31], 0 offen\n"
            "buffer_load_dwordx4 v[68:71], v221, s[28:31], 0 offen offset:256\n"
            "v_exp_f32 v175, v143\n"
            "v_pk_fma_f32 v[0:1], v[146:147], v[176:177], v[0:1] op_sel_hi:[0,1,1]\n"
            "v_pk_fma_f32 v[2:3], v[146:147], v[178:179], v[2:3] op_sel_hi:[0,1,1]\n"
            "v_pk_fma_f32 v[4:5], v[150:151], v[180:181], v[4:5] op_sel_hi:[0,1,1]\n"
            "v_pk_fma_f32 v[6:7], v[150:151], v[182:183], v[6:7] op_sel_hi:[0,1,1]\n"
            "v_pk_fma_f32 v[8:9], v[154:155], v[184:185], v[8:9] op_sel_hi:[0,1,1]\n"
            "v_pk_fma_f32 v[10:11], v[154:155], v[186:187], v[10:11] op_sel_hi:[0,1,1]\n"
            "v_pk_fma_f32 v[12:13], v[158:159], v[188:189], v[12:13] op_sel_hi:[0,1,1]\n"
            "v_pk_fma_f32 v[14:15], v[158:159], v[190:191], v[14:15] op_sel_hi:[0,1,1]\n"
            "v_pk_add_f32 v[168:169], v[168:169], 1.0 op_sel_hi:[1,0]\n"
            "v_pk_add_f32 v[170:171], v[170:171], 1.0 op_sel_hi:[1,0]\n"
            "v_pk_add_f32 v[172:173], v[172:173], 1.0 op_sel_hi:[1,0]\n"
            "v_pk_add_f32 v[174:175], v[174:175], 1.0 op_sel_hi:[1,0]\n"
            "v_cvt_pk_bf16_f32 v204, v204, v205\n"
            "v_cvt_pk_bf16_f32 v205, v206, v207\n"
            "ds_write_b64 v212, v[204:205]\n"
            "s_cmp_eq_u32 s69, 3\n"
            "s_cbranch_scc1 Lf02_%=\n"
            "Lr02_%=:\n"
            "v_mfma_f32_16x16x32_bf16 v[132:135], v[192:195], v[100:103], 0\n"
            "v_rcp_f32 v168, v168\n"
            "v_rcp_f32 v169, v169\n"
            "v_mfma_f32_4x4x4_16b_bf16 v[176:179], v[208:209], v[80:81], 0 cbsz:2 abid:3\n"
            "v_rcp_f32 v170, v170\n"
            "v_mfma_f32_4x4x4_16b_bf16 v[180:183], v[208:209], v[82:83], 0 cbsz:2 abid:3\n"
            "v_rcp_f32 v171, v171\n"
            "v_mfma_f32_4x4x4_16b_bf16 v[184:187], v[208:209], v[84:85], 0 cbsz:2 abid:3\n"
            "v_rcp_f32 v172, v172\n"
            "v_mfma_f32_4x4x4_16b_bf16 v[188:191], v[208:209], v[86:87], 0 cbsz:2 abid:3\n"
            "v_rcp_f32 v173, v173\n"
            "v_add_u32_dpp v222, v196, v214 quad_perm:[3,3,3,3] row_mask:0xf bank_mask:0xf\n"
            "v_rcp_f32 v174, v174\n"
            "buffer_load_dwordx4 v[80:83], v222, s[28:31], 0 offen\n"
            "buffer_load_dwordx4 v[84:87], v222, s[28:31], 0 offen offset:256\n"
            "v_rcp_f32 v175, v175\n"
            "v_pk_fma_f32 v[0:1], v[146:147], v[176:177], v[0:1] op_sel:[1,0,0]\n"
            "v_pk_fma_f32 v[2:3], v[146:147], v[178:179], v[2:3] op_sel:[1,0,0]\n"
            "v_pk_fma_f32 v[4:5], v[150:151], v[180:181], v[4:5] op_sel:[1,0,0]\n"
            "v_pk_fma_f32 v[6:7], v[150:151], v[182:183], v[6:7] op_sel:[1,0,0]\n"
            "v_pk_fma_f32 v[8:9], v[154:155], v[184:185], v[8:9] op_sel:[1,0,0]\n"
            "v_pk_fma_f32 v[10:11], v[154:155], v[186:187], v[10:11] op_sel:[1,0,0]\n"
            "v_pk_fma_f32 v[12:13], v[158:159], v[188:189], v[12:13] op_sel:[1,0,0]\n"
            "v_pk_fma_f32 v[14:15], v[158:159], v[190:191], v[14:15] op_sel:[1,0,0]\n"
            "v_pk_mul_f32 v[168:169], v[168:169], v[136:137]\n"
            "v_pk_mul_f32 v[170:171], v[170:171], v[138:139]\n"
            "v_pk_mul_f32 v[172:173], v[172:173], v[140:141]\n"
            "v_pk_mul_f32 v[174:175], v[174:175], v[142:143]\n"
            "s_cmp_eq_u32 s69, 4\n"
            "s_cbranch_scc1 Lf03_%=\n"
            "Lr03_%=:\n"
            "v_add_u32 v238, 32, v238\n"
            "ds_read_b128 v[196:199], v210\n"
            "ds_read_u16 v211, v238\n"
            "v_mfma_f32_16x16x32_bf16 v[136:139], v[192:195], v[104:107], 0\n"
            "v_exp_f32 v144, v128\n"
            "v_exp_f32 v145, v129\n"
            "s_waitcnt vmcnt(13)\n"
            "v_mfma_f32_4x4x4_16b_bf16 v[176:179], v[208:209], v[40:41], 0 cbsz:2\n"
            "v_exp_f32 v146, v130\n"
            "v_mfma_f32_4x4x4_16b_bf16 v[180:183], v[208:209], v[42:43], 0 cbsz:2\n"
            "v_exp_f32 v147, v131\n"
            "v_mfma_f32_4x4x4_16b_bf16 v[184:187], v[208:209], v[44:45], 0 cbsz:2\n"
            "v_exp_f32 v148, v132\n"
            "v_mfma_f32_4x4x4_16b_bf16 v[188:191], v[208:209], v[46:47], 0 cbsz:2\n"
            "v_exp_f32 v149, v133\n"
            "v_exp_f32 v150, v134\n"
            "buffer_load_dwordx4 v[40:43], v219, s[28:31], 0 offen offset:512\n"
            "buffer_load_dwordx4 v[44:47], v219, s[28:31], 0 offen offset:768\n"
            "v_exp_f32 v151, v135\n"
            "v_pk_fma_f32 v[16:17], v[160:161], v[176:177], v[16:17] op_sel_hi:[0,1,1]\n"
            "v_pk_fma_f32 v[18:19], v[160:161], v[178:179], v[18:19] op_sel_hi:[0,1,1]\n"
            "v_pk_fma_f32 v[20:21], v[164:165], v[180:181], v[20:21] op_sel_hi:[0,1,1]\n"
            "v_pk_fma_f32 v[22:23], v[164:165], v[182:183], v[22:23] op_sel_hi:[0,1,1]\n"
            "v_pk_fma_f32 v[24:25], v[168:169], v[184:185], v[24:25] op_sel_hi:[0,1,1]\n"
            "v_pk_fma_f32 v[26:27], v[168:169], v[186:187], v[26:27] op_sel_hi:[0,1,1]\n"
            "v_pk_fma_f32 v[28:29], v[172:173], v[188:189], v[28:29] op_sel_hi:[0,1,1]\n"
            "v_pk_fma_f32 v[30:31], v[172:173], v[190:191], v[30:31] op_sel_hi:[0,1,1]\n"
            "v_pk_add_f32 v[144:145], v[144:145], 1.0 op_sel_hi:[1,0]\n"
            "v_pk_add_f32 v[146:147], v[146:147], 1.0 op_sel_hi:[1,0]\n"
            "v_pk_add_f32 v[148:149], v[148:149], 1.0 op_sel_hi:[1,0]\n"
            "v_pk_add_f32 v[150:151], v[150:151], 1.0 op_sel_hi:[1,0]\n"
            "s_cmp_eq_u32 s69, 1\n"
            "s_cbranch_scc1 Lf10_%=\n"
            "Lr10_%=:\n"
            "v_mfma_f32_16x16x32_bf16 v[140:143], v[192:195], v[108:111], 0\n"
            "v_rcp_f32 v144, v144\n"
            "v_rcp_f32 v145, v145\n"
            "v_mfma_f32_4x4x4_16b_bf16 v[176:179], v[208:209], v[56:57], 0 cbsz:2 abid:1\n"
            "v_rcp_f32 v146, v146\n"
            "v_mfma_f32_4x4x4_16b_bf16 v[180:183], v[208:209], v[58:59], 0 cbsz:2 abid:1\n"
            "v_rcp_f32 v147, v147\n"
            "v_mfma_f32_4x4x4_16b_bf16 v[184:187], v[208:209], v[60:61], 0 cbsz:2 abid:1\n"
            "v_rcp_f32 v148, v148\n"
            "v_mfma_f32_4x4x4_16b_bf16 v[188:191], v[208:209], v[62:63], 0 cbsz:2 abid:1\n"
            "v_rcp_f32 v149, v149\n"
            "v_rcp_f32 v150, v150\n"
            "buffer_load_dwordx4 v[56:59], v220, s[28:31], 0 offen offset:512\n"
            "buffer_load_dwordx4 v[60:63], v220, s[28:31], 0 offen offset:768\n"
            "v_rcp_f32 v151, v151\n"
            "v_pk_fma_f32 v[16:17], v[160:161], v[176:177], v[16:17] op_sel:[1,0,0]\n"
            "v_pk_fma_f32 v[18:19], v[160:161], v[178:179], v[18:19] op_sel:[1,0,0]\n"
            "v_pk_fma_f32 v[20:21], v[164:165], v[180:181], v[20:21] op_sel:[1,0,0]\n"
            "v_pk_fma_f32 v[22:23], v[164:165], v[182:183], v[22:23] op_sel:[1,0,0]\n"
            "v_pk_fma_f32 v[24:25], v[168:169], v[184:185], v[24:25] op_sel:[1,0,0]\n"
            "v_pk_fma_f32 v[26:27], v[168:169], v[186:187], v[26:27] op_sel:[1,0,0]\n"
            "v_pk_fma_f32 v[28:29], v[172:173], v[188:189], v[28:29] op_sel:[1,0,0]\n"
            "v_pk_fma_f32 v[30:31], v[172:173], v[190:191], v[30:31] op_sel:[1,0,0]\n"
            "v_pk_mul_f32 v[144:145], v[144:145], v[128:129]\n"
            "v_pk_mul_f32 v[146:147], v[146:147], v[130:131]\n"
            "v_pk_mul_f32 v[148:149], v[148:149], v[132:133]\n"
            "v_pk_mul_f32 v[150:151], v[150:151], v[134:135]\n"
            "s_cmp_eq_u32 s69, 2\n"
            "s_cbranch_scc1 Lf11_%=\n"
            "Lr11_%=:\n"
            "v_mfma_f32_16x16x32_bf16 v[128:131], v[192:195], v[112:115], 0\n"
            "v_exp_f32 v152, v136\n"
            "v_exp_f32 v153, v137\n"
            "s_waitcnt vmcnt(13)\n"
            "v_mfma_f32_4x4x4_16b_bf16 v[176:179], v[208:209], v[72:73], 0 cbsz:2 abid:2\n"
            "v_exp_f32 v154, v138\n"
            "v_mfma_f32_4x4x4_16b_bf16 v[180:183], v[208:209], v[74:75], 0 cbsz:2 abid:2\n"
            "v_exp_f32 v155, v139\n"
            "v_mfma_f32_4x4x4_16b_bf16 v[184:187], v[208:209], v[76:77], 0 cbsz:2 abid:2\n"
            "v_exp_f32 v156, v140\n"
            "v_mfma_f32_4x4x4_16b_bf16 v[188:191], v[208:209], v[78:79], 0 cbsz:2 abid:2\n"
            "v_exp_f32 v157, v141\n"
            "v_exp_f32 v158, v142\n"
            "buffer_load_dwordx4 v[72:75], v221, s[28:31], 0 offen offset:512\n"
            "buffer_load_dwordx4 v[76:79], v221, s[28:31], 0 offen offset:768\n"
            "v_exp_f32 v159, v143\n"
            "v_pk_fma_f32 v[16:17], v[162:163], v[176:177], v[16:17] op_sel_hi:[0,1,1]\n"
            "v_pk_fma_f32 v[18:19], v[162:163], v[178:179], v[18:19] op_sel_hi:[0,1,1]\n"
            "v_pk_fma_f32 v[20:21], v[166:167], v[180:181], v[20:21] op_sel_hi:[0,1,1]\n"
            "v_pk_fma_f32 v[22:23], v[166:167], v[182:183], v[22:23] op_sel_hi:[0,1,1]\n"
            "v_pk_fma_f32 v[24:25], v[170:171], v[184:185], v[24:25] op_sel_hi:[0,1,1]\n"
            "v_pk_fma_f32 v[26:27], v[170:171], v[186:187], v[26:27] op_sel_hi:[0,1,1]\n"
            "v_pk_fma_f32 v[28:29], v[174:175], v[188:189], v[28:29] op_sel_hi:[0,1,1]\n"
            "v_pk_fma_f32 v[30:31], v[174:175], v[190:191], v[30:31] op_sel_hi:[0,1,1]\n"
            "v_pk_add_f32 v[152:153], v[152:153], 1.0 op_sel_hi:[1,0]\n"
            "v_pk_add_f32 v[154:155], v[154:155], 1.0 op_sel_hi:[1,0]\n"
            "v_pk_add_f32 v[156:157], v[156:157], 1.0 op_sel_hi:[1,0]\n"
            "v_pk_add_f32 v[158:159], v[158:159], 1.0 op_sel_hi:[1,0]\n"
            "s_cmp_eq_u32 s69, 3\n"
            "s_cbranch_scc1 Lf12_%=\n"
            "Lr12_%=:\n"
            "v_mfma_f32_16x16x32_bf16 v[132:135], v[192:195], v[116:119], 0\n"
            "v_rcp_f32 v152, v152\n"
            "v_rcp_f32 v153, v153\n"
            "v_mfma_f32_4x4x4_16b_bf16 v[176:179], v[208:209], v[88:89], 0 cbsz:2 abid:3\n"
            "v_rcp_f32 v154, v154\n"
            "v_mfma_f32_4x4x4_16b_bf16 v[180:183], v[208:209], v[90:91], 0 cbsz:2 abid:3\n"
            "v_rcp_f32 v155, v155\n"
            "v_mfma_f32_4x4x4_16b_bf16 v[184:187], v[208:209], v[92:93], 0 cbsz:2 abid:3\n"
            "v_rcp_f32 v156, v156\n"
            "v_mfma_f32_4x4x4_16b_bf16 v[188:191], v[208:209], v[94:95], 0 cbsz:2 abid:3\n"
            "ds_read_b64 v[208:209], v213\n"
            "v_rcp_f32 v157, v157\n"
            "v_rcp_f32 v158, v158\n"
            "buffer_load_dwordx4 v[88:91], v222, s[28:31], 0 offen offset:512\n"
            "buffer_load_dwordx4 v[92:95], v222, s[28:31], 0 offen offset:768\n"
            "v_rcp_f32 v159, v159\n"
            "v_pk_fma_f32 v[16:17], v[162:163], v[176:177], v[16:17] op_sel:[1,0,0]\n"
            "v_pk_fma_f32 v[18:19], v[162:163], v[178:179], v[18:19] op_sel:[1,0,0]\n"
            "v_pk_fma_f32 v[20:21], v[166:167], v[180:181], v[20:21] op_sel:[1,0,0]\n"
            "v_pk_fma_f32 v[22:23], v[166:167], v[182:183], v[22:23] op_sel:[1,0,0]\n"
            "v_pk_fma_f32 v[24:25], v[170:171], v[184:185], v[24:25] op_sel:[1,0,0]\n"
            "v_pk_fma_f32 v[26:27], v[170:171], v[186:187], v[26:27] op_sel:[1,0,0]\n"
            "v_pk_fma_f32 v[28:29], v[174:175], v[188:189], v[28:29] op_sel:[1,0,0]\n"
            "v_pk_fma_f32 v[30:31], v[174:175], v[190:191], v[30:31] op_sel:[1,0,0]\n"
            "v_pk_mul_f32 v[152:153], v[152:153], v[136:137]\n"
            "v_pk_mul_f32 v[154:155], v[154:155], v[138:139]\n"
            "v_pk_mul_f32 v[156:157], v[156:157], v[140:141]\n"
            "v_pk_mul_f32 v[158:159], v[158:159], v[142:143]\n"
            "s_cmp_eq_u32 s69, 4\n"
            "s_cbranch_scc1 Lf13_%=\n"
            "Lr13_%=:\n"
            "s_and_b32 s68, s72, 0xff\n"
            "s_add_u32 s69, s69, s68\n"
            "s_lshr_b32 s72, s72, 8\n"
            "v_mov_b32 v231, v239\n"
            "v_mov_b32 v239, v252\n"
            "s_and_b32 s68, s62, 14\n"
            "s_lshr_b32 s62, s62, 4\n"
            "s_cmp_lg_u32 s68, 0\n"
            "s_cbranch_scc1 Lb1_%=\n"
            "v_mov_b32 v215, v216\n"
            "s_branch Lnext_%=\n"
            "Lb1_%=:\n"
            "s_cmp_lg_u32 s68, 6\n"
            "s_cbranch_scc1 Lb2_%=\n"
            "s_lshl_b32 s68, s3, 2\n"
            "v_mov_b32 v176, s68\n"
            "ds_write_b32 v176, v176\n"
            "s_branch Lnext_%=\n"
            "Lb2_%=:\n"
            "s_cmp_lg_u32 s68, 8\n"
            "s_cbranch_scc1 Lnext_%=\n"
            "s_waitcnt vmcnt(0)\n"
            "v_mov_b32 v176, 4\n"
            "v_mov_b32 v177, 1\n"
            "s_mov_b64 exec, 1\n"
            "ds_add_rtn_u32 v177, v176, v177\n"
            "s_waitcnt lgkmcnt(0)\n"
            "v_readfirstlane_b32 s68, v177\n"
            "s_mov_b64 exec, 0xff\n"
            "v_mov_b32 v176, s90\n"
            "v_mov_b32 v177, s90\n"
            "v_mov_b32 v178, s90\n"
            "v_mov_b32 v179, s90\n"
            "s_cmp_lg_u32 s68, 7\n"
            "s_cbranch_scc1 Lpub2_%=\n"
            "global_store_dwordx4 v214, v[176:179], s[56:57] sc1\n"
            "Lpub2_%=:\n"
            "s_mov_b64 exec, -1\n"
            "s_branch Lnext_%=\n"
            "Lprio_%=:\n"
            "s_cmp_eq_u32 s73, 1\n"
            "s_cbranch_scc1 Llast_%=\n"
            "s_lshl_b32 s68, s73, 1\n"
            "s_lshr_b32 s68, s61, s68\n"
            "s_bitcmp1_b32 s68, 1\n"
            "s_cbranch_scc1 Lq2_%=\n"
            "s_bitcmp1_b32 s68, 0\n"
            "s_setprio 0\n"
            "s_cbranch_scc0 Lq9_%=\n"
            "s_setprio 1\n"
            "s_branch Lq9_%=\n"
            "Lq2_%=:\n"
            "s_bitcmp1_b32 s68, 0\n"
            "s_setprio 2\n"
            "s_cbranch_scc0 Lq9_%=\n"
            "s_setprio 3\n"
            "Lq9_%=:\n"
            "s_cmp_lg_u32 s73, 2\n"
            "s_cbranch_scc1 Ld2_%=\n"
            "v_mov_b32 v223, 0\n"
            "global_load_dword v223, v223, s[58:59] sc1\n"
            "s_branch Ld2_%=\n"
            "Lf00_%=:\n"
            "s_call_b64 s[70:71], Lflush0_%=\n"
            "s_branch Lr00_%=\n"
            "Lf01_%=:\n"
            "s_call_b64 s[70:71], Lflush0_%=\n"
            "s_branch Lr01_%=\n"
            "Lf02_%=:\n"
            "s_call_b64 s[70:71], Lflush0_%=\n"
            "s_branch Lr02_%=\n"
            "Lf03_%=:\n"
            "s_call_b64 s[70:71], Lflush0_%=\n"
            "s_branch Lr03_%=\n"
            "Lflush0_%=:\n"
            "s_bitcmp0_b32 s62, 0\n"
            "s_cbranch_scc1 Lfa0_%=\n"
            "Lfw0_%=:\n"
            "v_mov_b32 v184, s41\n"
            "ds_read_b32 v184, v184\n"
            "ds_read_b128 v[176:179], v231 offset:0\n"
            "ds_read_b128 v[180:183], v231 offset:1024\n"
            "s_waitcnt lgkmcnt(0)\n"
            "v_readfirstlane_b32 s68, v184\n"
            "s_cmp_eq_u32 s68, 0\n"
            "s_cbranch_scc1 Lfw0_%=\n"
            "v_mfma_f32_4x4x4_16b_bf16 v[0:3], v[224:225], v[176:177], v[0:3]\n"
            "v_mfma_f32_4x4x4_16b_bf16 v[4:7], v[224:225], v[178:179], v[4:7]\n"
            "v_mfma_f32_4x4x4_16b_bf16 v[8:11], v[224:225], v[180:181], v[8:11]\n"
            "v_mfma_f32_4x4x4_16b_bf16 v[12:15], v[224:225], v[182:183], v[12:15]\n"
            "s_nop 1\n"
            "Lfa0_%=:\n"
            "v_cvt_pk_bf16_f32 v176, v0, v1\n"
            "v_cvt_pk_bf16_f32 v177, v2, v3\n"
            "v_cvt_pk_bf16_f32 v178, v4, v5\n"
            "v_cvt_pk_bf16_f32 v179, v6, v7\n"
            "v_cvt_pk_bf16_f32 v180, v8, v9\n"
            "v_cvt_pk_bf16_f32 v181, v10, v11\n"
            "v_cvt_pk_bf16_f32 v182, v12, v13\n"
            "v_cvt_pk_bf16_f32 v183, v14, v15\n"
            "s_bitcmp1_b32 s62, 1\n"
            "s_cbranch_scc1 Lpark0_%=\n"
            "buffer_store_dwordx4 v[176:179], v215, s[20:23], 0 offen offset:0 sc1\n"
            "buffer_store_dwordx4 v[180:183], v215, s[20:23], 0 offen offset:256 sc1\n"
            "s_branch Lzero0_%=\n"
            "Lpark0_%=:\n"
            "ds_write_b128 v231, v[176:179] offset:0\n"
            "ds_write_b128 v231, v[180:183] offset:1024\n"
            "Lzero0_%=:\n"
            "v_mov_b64 v[0:1], 0\n"
            "v_mov_b64 v[2:3], 0\n"
            "v_mov_b64 v[4:5], 0\n"
            "v_mov_b64 v[6:7], 0\n"
            "v_mov_b64 v[8:9], 0\n"
            "v_mov_b64 v[10:11], 0\n"
            "v_mov_b64 v[12:13], 0\n"
            "v_mov_b64 v[14:15], 0\n"
            "s_setpc_b64 s[70:71]\n"
            "Lf10_%=:\n"
            "s_call_b64 s[70:71], Lflush1_%=\n"
            "s_branch Lr10_%=\n"
            "Lf11_%=:\n"
            "s_call_b64 s[70:71], Lflush1_%=\n"
            "s_branch Lr11_%=\n"
            "Lf12_%=:\n"
            "s_call_b64 s[70:71], Lflush1_%=\n"
            "s_branch Lr12_%=\n"
            "Lf13_%=:\n"
            "s_call_b64 s[70:71], Lflush1_%=\n"
            "s_branch Lr13_%=\n"
            "Lflush1_%=:\n"
            "s_bitcmp0_b32 s62, 0\n"
            "s_cbranch_scc1 Lfa1_%=\n"
            "Lfw1_%=:\n"
            "v_mov_b32 v184, s41\n"
            "ds_read_b32 v184, v184\n"
            "ds_read_b128 v[176:179], v231 offset:2048\n"
            "ds_read_b128 v[180:183], v231 offset:3072\n"
            "s_waitcnt lgkmcnt(0)\n"
            "v_readfirstlane_b32 s68, v184\n"
            "s_cmp_eq_u32 s68, 0\n"
            "s_cbranch_scc1 Lfw1_%=\n"
            "v_mfma_f32_4x4x4_16b_bf16 v[16:19], v[224:225], v[176:177], v[16:19]\n"
            "v_mfma_f32_4x4x4_16b_bf16 v[20:23], v[224:225], v[178:179], v[20:23]\n"
            "v_mfma_f32_4x4x4_16b_bf16 v[24:27], v[224:225], v[180:181], v[24:27]\n"
            "v_mfma_f32_4x4x4_16b_bf16 v[28:31], v[224:225], v[182:183], v[28:31]\n"
            "s_nop 1\n"
            "Lfa1_%=:\n"
            "v_cvt_pk_bf16_f32 v176, v16, v17\n"
            "v_cvt_pk_bf16_f32 v177, v18, v19\n"
            "v_cvt_pk_bf16_f32 v178, v20, v21\n"
            "v_cvt_pk_bf16_f32 v179, v22, v23\n"
            "v_cvt_pk_bf16_f32 v180, v24, v25\n"
            "v_cvt_pk_bf16_f32 v181, v26, v27\n"
            "v_cvt_pk_bf16_f32 v182, v28, v29\n"
            "v_cvt_pk_bf16_f32 v183, v30, v31\n"
            "s_bitcmp1_b32 s62, 1\n"
            "s_cbranch_scc1 Lpark1_%=\n"
            "buffer_store_dwordx4 v[176:179], v215, s[20:23], 0 offen offset:512 sc1\n"
            "buffer_store_dwordx4 v[180:183], v215, s[20:23], 0 offen offset:768 sc1\n"
            "s_branch Lzero1_%=\n"
            "Lpark1_%=:\n"
            "ds_write_b128 v231, v[176:179] offset:2048\n"
            "ds_write_b128 v231, v[180:183] offset:3072\n"
            "Lzero1_%=:\n"
            "v_mov_b64 v[16:17], 0\n"
            "v_mov_b64 v[18:19], 0\n"
            "v_mov_b64 v[20:21], 0\n"
            "v_mov_b64 v[22:23], 0\n"
            "v_mov_b64 v[24:25], 0\n"
            "v_mov_b64 v[26:27], 0\n"
            "v_mov_b64 v[28:29], 0\n"
            "v_mov_b64 v[30:31], 0\n"
            "s_setpc_b64 s[70:71]\n"
            "Llk_%=:\n"
            "s_waitcnt vmcnt(4)\n"
            "v_readfirstlane_b32 s73, v223\n"
            "s_cmp_lg_u32 s73, s90\n"
            "s_cbranch_scc1 Llk2_%=\n"
            "buffer_load_dwordx4 v[32:35], v217, s[20:23], s63 offen offset:0 sc1\n"
            "buffer_load_dwordx4 v[36:39], v217, s[20:23], s63 offen offset:256 sc1\n"
            "buffer_load_dwordx4 v[48:51], v217, s[20:23], s63 offen offset:512 sc1\n"
            "buffer_load_dwordx4 v[52:55], v217, s[20:23], s63 offen offset:768 sc1\n"
            "s_branch Llkr_%=\n"
            "Llk2_%=:\n"
            "v_mov_b32 v223, 0\n"
            "global_load_dword v223, v223, s[58:59] sc1\n"
            "s_branch Llkr_%=\n"
            "Lspin_%=:\n"
            "s_waitcnt vmcnt(0)\n"
            "s_mov_b32 s72, 0x400000\n"
            "Lsp1_%=:\n"
            "v_readfirstlane_b32 s73, v223\n"
            "s_sub_u32 s72, s72, 1\n"
            "s_cmp_eq_u32 s72, 0\n"
            "s_cbranch_scc1 Lsp2_%=\n"
            "s_cmp_eq_u32 s73, s90\n"
            "s_cbranch_scc1 Lsp2_%=\n"
            "s_sleep 1\n"
            "v_mov_b32 v223, 0\n"
            "global_load_dword v223, v223, s[58:59] sc1\n"
            "s_waitcnt vmcnt(0)\n"
            "s_branch Lsp1_%=\n"
            "Lsp2_%=:\n"
            "buffer_load_dwordx4 v[32:35], v217, s[20:23], s63 offen offset:0 sc1\n"
            "buffer_load_dwordx4 v[36:39], v217, s[20:23], s63 offen offset:256 sc1\n"
            "buffer_load_dwordx4 v[48:51], v217, s[20:23], s63 offen offset:512 sc1\n"
            "buffer_load_dwordx4 v[52:55], v217, s[20:23], s63 offen offset:768 sc1\n"
            "s_branch Lgo_%=\n"
            "Llast_%=:\n"
            "s_waitcnt vmcnt(16)\n"
            "v_readfirstlane_b32 s73, v223\n"
            "v_mov_b32 v222, v217\n"
            "s_cmp_eq_u32 s73, s90\n"
            "s_cbranch_scc1 Lp4_%=\n"
            "v_bfrev_b32 v222, 1\n"
            "v_mov_b32 v223, 0\n"
            "global_load_dword v223, v223, s[58:59] sc1\n"
            "Lp4_%=:\n"
            "v_mfma_f32_16x16x32_bf16 v[136:139], v[192:195], v[120:123], 0\n"
            "v_exp_f32 v160, v128\n"
            "v_exp_f32 v161, v129\n"
            "s_waitcnt vmcnt(13) lgkmcnt(0)\n"
            "v_mfma_f32_4x4x4_16b_bf16 v[176:179], v[208:209], v[32:33], 0 cbsz:2\n"
            "v_exp_f32 v162, v130\n"
            "v_mfma_f32_4x4x4_16b_bf16 v[180:183], v[208:209], v[34:35], 0 cbsz:2\n"
            "v_exp_f32 v163, v131\n"
            "v_mfma_f32_4x4x4_16b_bf16 v[184:187], v[208:209], v[36:37], 0 cbsz:2\n"
            "v_exp_f32 v164, v132\n"
            "v_mfma_f32_4x4x4_16b_bf16 v[188:191], v[208:209], v[38:39], 0 cbsz:2\n"
            "v_exp_f32 v165, v133\n"
            "v_exp_f32 v166, v134\n"
            "buffer_load_dwordx4 v[32:35], v222, s[20:23], s63 offen offset:0 sc1\n"
            "buffer_load_dwordx4 v[36:39], v222, s[20:23], s63 offen offset:256 sc1\n"
            "ds_read_b128 v[240:243], v228 offset:0\n"
            "ds_read_b128 v[244:247], v228 offset:1024\n"
            "ds_read_b128 v[248:251], v228 offset:2048\n"
            "ds_read_b128 v[96:99], v228 offset:3072\n"
            "ds_read_b128 v[100:103], v228 offset:4096\n"
            "v_exp_f32 v167, v135\n"
            "v_pk_fma_f32 v[0:1], v[144:145], v[176:177], v[0:1] op_sel_hi:[0,1,1]\n"
            "v_pk_fma_f32 v[2:3], v[144:145], v[178:179], v[2:3] op_sel_hi:[0,1,1]\n"
            "v_pk_fma_f32 v[4:5], v[148:149], v[180:181], v[4:5] op_sel_hi:[0,1,1]\n"
            "v_pk_fma_f32 v[6:7], v[148:149], v[182:183], v[6:7] op_sel_hi:[0,1,1]\n"
            "v_pk_fma_f32 v[8:9], v[152:153], v[184:185], v[8:9] op_sel_hi:[0,1,1]\n"
            "v_pk_fma_f32 v[10:11], v[152:153], v[186:187], v[10:11] op_sel_hi:[0,1,1]\n"
            "v_pk_fma_f32 v[12:13], v[156:157], v[188:189], v[12:13] op_sel_hi:[0,1,1]\n"
            "v_pk_fma_f32 v[14:15], v[156:157], v[190:191], v[14:15] op_sel_hi:[0,1,1]\n"
            "v_pk_add_f32 v[160:161], v[160:161], 1.0 op_sel_hi:[1,0]\n"
            "v_pk_add_f32 v[162:163], v[162:163], 1.0 op_sel_hi:[1,0]\n"
            "v_pk_add_f32 v[164:165], v[164:165], 1.0 op_sel_hi:[1,0]\n"
            "v_pk_add_f32 v[166:167], v[166:167], 1.0 op_sel_hi:[1,0]\n"
            "v_mfma_f32_16x16x32_bf16 v[140:143], v[192:195], v[124:127], 0\n"
            "v_rcp_f32 v160, v160\n"
            "v_rcp_f32 v161, v161\n"
            "v_mfma_f32_4x4x4_16b_bf16 v[176:179], v[208:209], v[48:49], 0 cbsz:2 abid:1\n"
            "v_rcp_f32 v162, v162\n"
            "v_mfma_f32_4x4x4_16b_bf16 v[180:183], v[208:209], v[50:51], 0 cbsz:2 abid:1\n"
            "v_rcp_f32 v163, v163\n"
            "v_mfma_f32_4x4x4_16b_bf16 v[184:187], v[208:209], v[52:53], 0 cbsz:2 abid:1\n"
            "v_rcp_f32 v164, v164\n"
            "v_mfma_f32_4x4x4_16b_bf16 v[188:191], v[208:209], v[54:55], 0 cbsz:2 abid:1\n"
            "v_rcp_f32 v165, v165\n"
            "v_rcp_f32 v166, v166\n"
            "buffer_load_dwordx4 v[48:51], v222, s[20:23], s63 offen offset:512 sc1\n"
            "buffer_load_dwordx4 v[52:55], v222, s[20:23], s63 offen offset:768 sc1\n"
            "ds_read_b128 v[104:107], v228 offset:5120\n"
            "ds_read_b128 v[108:111], v228 offset:6144\n"
            "ds_read_b128 v[112:115], v228 offset:7168\n"
            "ds_read_b128 v[116:119], v228 offset:8192\n"
            "ds_read_b128 v[196:199], v228 offset:9216\n"
            "ds_read_b128 v[200:203], v228 offset:10240\n"
            "v_rcp_f32 v167, v167\n"
            "v_pk_fma_f32 v[0:1], v[144:145], v[176:177], v[0:1] op_sel:[1,0,0]\n"
            "v_pk_fma_f32 v[2:3], v[144:145], v[178:179], v[2:3] op_sel:[1,0,0]\n"
            "v_pk_fma_f32 v[4:5], v[148:149], v[180:181], v[4:5] op_sel:[1,0,0]\n"
            "v_pk_fma_f32 v[6:7], v[148:149], v[182:183], v[6:7] op_sel:[1,0,0]\n"
            "v_pk_fma_f32 v[8:9], v[152:153], v[184:185], v[8:9] op_sel:[1,0,0]\n"
            "v_pk_fma_f32 v[10:11], v[152:153], v[186:187], v[10:11] op_sel:[1,0,0]\n"
            "v_pk_fma_f32 v[12:13], v[156:157], v[188:189], v[12:13] op_sel:[1,0,0]\n"
            "v_pk_fma_f32 v[14:15], v[156:157], v[190:191], v[14:15] op_sel:[1,0,0]\n"
            "v_pk_mul_f32 v[160:161], v[160:161], v[128:129]\n"
            "v_pk_mul_f32 v[162:163], v[162:163], v[130:131]\n"
            "v_pk_mul_f32 v[164:165], v[164:165], v[132:133]\n"
            "v_pk_mul_f32 v[166:167], v[166:167], v[134:135]\n"
            "v_exp_f32 v168, v136\n"
            "v_exp_f32 v169, v137\n"
            "s_waitcnt vmcnt(12)\n"
            "v_mfma_f32_4x4x4_16b_bf16 v[176:179], v[208:209], v[64:65], 0 cbsz:2 abid:2\n"
            "v_exp_f32 v170, v138\n"
            "v_mfma_f32_4x4x4_16b_bf16 v[180:183], v[208:209], v[66:67], 0 cbsz:2 abid:2\n"
            "v_exp_f32 v171, v139\n"
            "v_mfma_f32_4x4x4_16b_bf16 v[184:187], v[208:209], v[68:69], 0 cbsz:2 abid:2\n"
            "v_exp_f32 v172, v140\n"
            "v_mfma_f32_4x4x4_16b_bf16 v[188:191], v[208:209], v[70:71], 0 cbsz:2 abid:2\n"
            "v_exp_f32 v173, v141\n"
            "v_exp_f32 v174, v142\n"
            "ds_read_b128 v[64:67], v236 offset:0\n"
            "ds_read_b128 v[68:71], v236 offset:1024\n"
            "ds_read_b128 v[204:207], v228 offset:11264\n"
            "ds_read_b128 v[232:235], v228 offset:12288\n"
            "ds_read_b128 v[120:123], v228 offset:13312\n"
            "v_exp_f32 v175, v143\n"
            "v_pk_fma_f32 v[0:1], v[146:147], v[176:177], v[0:1] op_sel_hi:[0,1,1]\n"
            "v_pk_fma_f32 v[2:3], v[146:147], v[178:179], v[2:3] op_sel_hi:[0,1,1]\n"
            "v_pk_fma_f32 v[4:5], v[150:151], v[180:181], v[4:5] op_sel_hi:[0,1,1]\n"
            "v_pk_fma_f32 v[6:7], v[150:151], v[182:183], v[6:7] op_sel_hi:[0,1,1]\n"
            "v_pk_fma_f32 v[8:9], v[154:155], v[184:185], v[8:9] op_sel_hi:[0,1,1]\n"
            "v_pk_fma_f32 v[10:11], v[154:155], v[186:187], v[10:11] op_sel_hi:[0,1,1]\n"
            "v_pk_fma_f32 v[12:13], v[158:159], v[188:189], v[12:13] op_sel_hi:[0,1,1]\n"
            "v_pk_fma_f32 v[14:15], v[158:159], v[190:191], v[14:15] op_sel_hi:[0,1,1]\n"
            "v_pk_add_f32 v[168:169], v[168:169], 1.0 op_sel_hi:[1,0]\n"
            "v_pk_add_f32 v[170:171], v[170:171], 1.0 op_sel_hi:[1,0]\n"
            "v_pk_add_f32 v[172:173], v[172:173], 1.0 op_sel_hi:[1,0]\n"
            "v_pk_add_f32 v[174:175], v[174:175], 1.0 op_sel_hi:[1,0]\n"
            "v_rcp_f32 v168, v168\n"
            "v_rcp_f32 v169, v169\n"
            "v_mfma_f32_4x4x4_16b_bf16 v[176:179], v[208:209], v[80:81], 0 cbsz:2 abid:3\n"
            "v_rcp_f32 v170, v170\n"
            "v_mfma_f32_4x4x4_16b_bf16 v[180:183], v[208:209], v[82:83], 0 cbsz:2 abid:3\n"
            "v_rcp_f32 v171, v171\n"
            "v_mfma_f32_4x4x4_16b_bf16 v[184:187], v[208:209], v[84:85], 0 cbsz:2 abid:3\n"
            "v_rcp_f32 v172, v172\n"
            "v_mfma_f32_4x4x4_16b_bf16 v[188:191], v[208:209], v[86:87], 0 cbsz:2 abid:3\n"
            "v_rcp_f32 v173, v173\n"
            "v_rcp_f32 v174, v174\n"
            "ds_read_b128 v[80:83], v236 offset:2048\n"
            "ds_read_b128 v[84:87], v236 offset:3072\n"
            "ds_read_b128 v[124:127], v228 offset:14336\n"
            "ds_read_b128 v[192:195], v228 offset:15360\n"
            "ds_read_b128 v[128:131], v228 offset:16384\n"
            "ds_read_b128 v[132:135], v228 offset:17408\n"
            "ds_read_b128 v[236:239], v228 offset:18432\n"
            "v_rcp_f32 v175, v175\n"
            "v_pk_fma_f32 v[0:1], v[146:147], v[176:177], v[0:1] op_sel:[1,0,0]\n"
            "v_pk_fma_f32 v[2:3], v[146:147], v[178:179], v[2:3] op_sel:[1,0,0]\n"
            "v_pk_fma_f32 v[4:5], v[150:151], v[180:181], v[4:5] op_sel:[1,0,0]\n"
            "v_pk_fma_f32 v[6:7], v[150:151], v[182:183], v[6:7] op_sel:[1,0,0]\n"
            "v_pk_fma_f32 v[8:9], v[154:155], v[184:185], v[8:9] op_sel:[1,0,0]\n"
            "v_pk_fma_f32 v[10:11], v[154:155], v[186:187], v[10:11] op_sel:[1,0,0]\n"
            "v_pk_fma_f32 v[12:13], v[158:159], v[188:189], v[12:13] op_sel:[1,0,0]\n"
            "v_pk_fma_f32 v[14:15], v[158:159], v[190:191], v[14:15] op_sel:[1,0,0]\n"
            "v_pk_mul_f32 v[168:169], v[168:169], v[136:137]\n"
            "v_pk_mul_f32 v[170:171], v[170:171], v[138:139]\n"
            "v_pk_mul_f32 v[172:173], v[172:173], v[140:141]\n"
            "v_pk_mul_f32 v[174:175], v[174:175], v[142:143]\n"
            "Llkr_%=:\n"
            "s_waitcnt vmcnt(8)\n"
            "v_mfma_f32_4x4x4_16b_bf16 v[176:179], v[208:209], v[40:41], 0 cbsz:2\n"
            "v_mfma_f32_4x4x4_16b_bf16 v[180:183], v[208:209], v[42:43], 0 cbsz:2\n"
            "v_mfma_f32_4x4x4_16b_bf16 v[184:187], v[208:209], v[44:45], 0 cbsz:2\n"
            "v_mfma_f32_4x4x4_16b_bf16 v[188:191], v[208:209], v[46:47], 0 cbsz:2\n"
            "ds_read_b128 v[136:139], v228 offset:19456\n"
            "ds_read_b128 v[140:143], v228 offset:20480\n"
            "ds_read_b128 v[144:147], v228 offset:21504\n"
            "ds_read_b128 v[148:151], v228 offset:22528\n"
            "ds_read_b128 v[152:155], v228 offset:23552\n"
            "ds_read_b128 v[156:159], v228 offset:24576\n"
            "s_nop 1\n"
            "v_pk_fma_f32 v[16:17], v[160:161], v[176:177], v[16:17] op_sel_hi:[0,1,1]\n"
            "v_pk_fma_f32 v[18:19], v[160:161], v[178:179], v[18:19] op_sel_hi:[0,1,1]\n"
            "v_pk_fma_f32 v[20:21], v[164:165], v[180:181], v[20:21] op_sel_hi:[0,1,1]\n"
            "v_pk_fma_f32 v[22:23], v[164:165], v[182:183], v[22:23] op_sel_hi:[0,1,1]\n"
            "v_pk_fma_f32 v[24:25], v[168:169], v[184:185], v[24:25] op_sel_hi:[0,1,1]\n"
            "v_pk_fma_f32 v[26:27], v[168:169], v[186:187], v[26:27] op_sel_hi:[0,1,1]\n"
            "v_pk_fma_f32 v[28:29], v[172:173], v[188:189], v[28:29] op_sel_hi:[0,1,1]\n"
            "v_pk_fma_f32 v[30:31], v[172:173], v[190:191], v[30:31] op_sel_hi:[0,1,1]\n"
            "s_cmp_le_u32 s74, 1\n"
            "s_cbranch_scc1 Ltail_%=\n"
            "v_mfma_f32_4x4x4_16b_bf16 v[176:179], v[208:209], v[56:57], 0 cbsz:2 abid:1\n"
            "v_mfma_f32_4x4x4_16b_bf16 v[180:183], v[208:209], v[58:59], 0 cbsz:2 abid:1\n"
            "v_mfma_f32_4x4x4_16b_bf16 v[184:187], v[208:209], v[60:61], 0 cbsz:2 abid:1\n"
            "v_mfma_f32_4x4x4_16b_bf16 v[188:191], v[208:209], v[62:63], 0 cbsz:2 abid:1\n"
            "s_nop 1\n"
            "v_pk_fma_f32 v[16:17], v[160:161], v[176:177], v[16:17] op_sel:[1,0,0]\n"
            "v_pk_fma_f32 v[18:19], v[160:161], v[178:179], v[18:19] op_sel:[1,0,0]\n"
            "v_pk_fma_f32 v[20:21], v[164:165], v[180:181], v[20:21] op_sel:[1,0,0]\n"
            "v_pk_fma_f32 v[22:23], v[164:165], v[182:183], v[22:23] op_sel:[1,0,0]\n"
            "v_pk_fma_f32 v[24:25], v[168:169], v[184:185], v[24:25] op_sel:[1,0,0]\n"
            "v_pk_fma_f32 v[26:27], v[168:169], v[186:187], v[26:27] op_sel:[1,0,0]\n"
            "v_pk_fma_f32 v[28:29], v[172:173], v[188:189], v[28:29] op_sel:[1,0,0]\n"
            "v_pk_fma_f32 v[30:31], v[172:173], v[190:191], v[30:31] op_sel:[1,0,0]\n"
            "s_cmp_le_u32 s74, 2\n"
            "s_cbranch_scc1 Ltail_%=\n"
            "s_waitcnt vmcnt(4)\n"
            "v_mfma_f32_4x4x4_16b_bf16 v[176:179], v[208:209], v[72:73], 0 cbsz:2 abid:2\n"
            "v_mfma_f32_4x4x4_16b_bf16 v[180:183], v[208:209], v[74:75], 0 cbsz:2 abid:2\n"
            "v_mfma_f32_4x4x4_16b_bf16 v[184:187], v[208:209], v[76:77], 0 cbsz:2 abid:2\n"
            "v_mfma_f32_4x4x4_16b_bf16 v[188:191], v[208:209], v[78:79], 0 cbsz:2 abid:2\n"
            "s_nop 1\n"
            "v_pk_fma_f32 v[16:17], v[162:163], v[176:177], v[16:17] op_sel_hi:[0,1,1]\n"
            "v_pk_fma_f32 v[18:19], v[162:163], v[178:179], v[18:19] op_sel_hi:[0,1,1]\n"
            "v_pk_fma_f32 v[20:21], v[166:167], v[180:181], v[20:21] op_sel_hi:[0,1,1]\n"
            "v_pk_fma_f32 v[22:23], v[166:167], v[182:183], v[22:23] op_sel_hi:[0,1,1]\n"
            "v_pk_fma_f32 v[24:25], v[170:171], v[184:185], v[24:25] op_sel_hi:[0,1,1]\n"
            "v_pk_fma_f32 v[26:27], v[170:171], v[186:187], v[26:27] op_sel_hi:[0,1,1]\n"
            "v_pk_fma_f32 v[28:29], v[174:175], v[188:189], v[28:29] op_sel_hi:[0,1,1]\n"
            "v_pk_fma_f32 v[30:31], v[174:175], v[190:191], v[30:31] op_sel_hi:[0,1,1]\n"
            "s_cmp_le_u32 s74, 3\n"
            "s_cbranch_scc1 Ltail_%=\n"
            "v_mfma_f32_4x4x4_16b_bf16 v[176:179], v[208:209], v[88:89], 0 cbsz:2 abid:3\n"
            "v_mfma_f32_4x4x4_16b_bf16 v[180:183], v[208:209], v[90:91], 0 cbsz:2 abid:3\n"
            "v_mfma_f32_4x4x4_16b_bf16 v[184:187], v[208:209], v[92:93], 0 cbsz:2 abid:3\n"
            "v_mfma_f32_4x4x4_16b_bf16 v[188:191], v[208:209], v[94:95], 0 cbsz:2 abid:3\n"
            "s_nop 1\n"
            "v_pk_fma_f32 v[16:17], v[162:163], v[176:177], v[16:17] op_sel:[1,0,0]\n"
            "v_pk_fma_f32 v[18:19], v[162:163], v[178:179], v[18:19] op_sel:[1,0,0]\n"
            "v_pk_fma_f32 v[20:21], v[166:167], v[180:181], v[20:21] op_sel:[1,0,0]\n"
            "v_pk_fma_f32 v[22:23], v[166:167], v[182:183], v[22:23] op_sel:[1,0,0]\n"
            "v_pk_fma_f32 v[24:25], v[170:171], v[184:185], v[24:25] op_sel:[1,0,0]\n"
            "v_pk_fma_f32 v[26:27], v[170:171], v[186:187], v[26:27] op_sel:[1,0,0]\n"
            "v_pk_fma_f32 v[28:29], v[174:175], v[188:189], v[28:29] op_sel:[1,0,0]\n"
            "v_pk_fma_f32 v[30:31], v[174:175], v[190:191], v[30:31] op_sel:[1,0,0]\n"
            "Ltail_%=:\n"
            "s_setprio 3\n"
            "s_waitcnt vmcnt(4) lgkmcnt(11)\n"
            "ds_read_b128 v[72:75], v228 offset:25600\n"
            "ds_read_b128 v[76:79], v228 offset:26624\n"
            "ds_read_b128 v[88:91], v228 offset:27648\n"
            "ds_read_b128 v[92:95], v228 offset:28672\n"
            "ds_read_b128 v[160:163], v228 offset:29696\n"
            "ds_read_b128 v[164:167], v228 offset:30720\n"
            "ds_read_b128 v[168:171], v228 offset:31744\n"
            "v_mfma_f32_4x4x4_16b_bf16 v[0:3], v[224:225], v[64:65], v[0:3]\n"
            "v_mfma_f32_4x4x4_16b_bf16 v[4:7], v[224:225], v[66:67], v[4:7]\n"
            "v_mfma_f32_4x4x4_16b_bf16 v[8:11], v[224:225], v[68:69], v[8:11]\n"
            "v_mfma_f32_4x4x4_16b_bf16 v[12:15], v[224:225], v[70:71], v[12:15]\n"
            "v_mfma_f32_4x4x4_16b_bf16 v[16:19], v[224:225], v[80:81], v[16:19]\n"
            "v_mfma_f32_4x4x4_16b_bf16 v[20:23], v[224:225], v[82:83], v[20:23]\n"
            "v_mfma_f32_4x4x4_16b_bf16 v[24:27], v[224:225], v[84:85], v[24:27]\n"
            "v_mfma_f32_4x4x4_16b_bf16 v[28:31], v[224:225], v[86:87], v[28:31]\n"
            "s_cmp_lg_u32 s73, s90\n"
            "s_cbranch_scc1 Lspin_%=\n"
            "Lgo_%=:\n"
            "buffer_load_dwordx4 v[40:43], v218, s[20:23], s63 offen offset:0 sc1\n"
            "buffer_load_dwordx4 v[44:47], v218, s[20:23], s63 offen offset:256 sc1\n"
            "buffer_load_dwordx4 v[56:59], v218, s[20:23], s63 offen offset:512 sc1\n"
            "buffer_load_dwordx4 v[60:63], v218, s[20:23], s63 offen offset:768 sc1\n"
            "s_waitcnt vmcnt(4)\n"
            "v_mfma_f32_4x4x4_16b_bf16 v[0:3], v[224:225], v[32:33], v[0:3]\n"
            "v_mfma_f32_4x4x4_16b_bf16 v[4:7], v[224:225], v[34:35], v[4:7]\n"
            "v_mfma_f32_4x4x4_16b_bf16 v[8:11], v[224:225], v[36:37], v[8:11]\n"
            "v_mfma_f32_4x4x4_16b_bf16 v[12:15], v[224:225], v[38:39], v[12:15]\n"
            "v_mfma_f32_4x4x4_16b_bf16 v[16:19], v[224:225], v[48:49], v[16:19]\n"
            "v_mfma_f32_4x4x4_16b_bf16 v[20:23], v[224:225], v[50:51], v[20:23]\n"
            "v_mfma_f32_4x4x4_16b_bf16 v[24:27], v[224:225], v[52:53], v[24:27]\n"
            "v_mfma_f32_4x4x4_16b_bf16 v[28:31], v[224:225], v[54:55], v[28:31]\n"
            "s_nop 1\n"
            "v_cvt_pk_bf16_f32 v176, v0, v4\n"
            "v_cvt_pk_bf16_f32 v177, v8, v12\n"
            "v_cvt_pk_bf16_f32 v178, v16, v20\n"
            "v_cvt_pk_bf16_f32 v179, v24, v28\n"
            "ds_write_b128 v226, v[176:179] offset:0\n"
            "v_cvt_pk_bf16_f32 v180, v1, v5\n"
            "v_cvt_pk_bf16_f32 v181, v9, v13\n"
            "v_cvt_pk_bf16_f32 v182, v17, v21\n"
            "v_cvt_pk_bf16_f32 v183, v25, v29\n"
            "ds_write_b128 v226, v[180:183] offset:272\n"
            "v_cvt_pk_bf16_f32 v184, v2, v6\n"
            "v_cvt_pk_bf16_f32 v185, v10, v14\n"
            "v_cvt_pk_bf16_f32 v186, v18, v22\n"
            "v_cvt_pk_bf16_f32 v187, v26, v30\n"
            "ds_write_b128 v226, v[184:187] offset:544\n"
            "v_cvt_pk_bf16_f32 v188, v3, v7\n"
            "v_cvt_pk_bf16_f32 v189, v11, v15\n"
            "v_cvt_pk_bf16_f32 v190, v19, v23\n"
            "v_cvt_pk_bf16_f32 v191, v27, v31\n"
            "ds_write_b128 v226, v[188:191] offset:816\n"
            "ds_read_b128 v[176:179], v227 offset:0\n"
            "ds_read_b128 v[180:183], v227 offset:64\n"
            "ds_read_b128 v[184:187], v227 offset:128\n"
            "ds_read_b128 v[188:191], v227 offset:192\n"
            "s_waitcnt lgkmcnt(3)\n"
            "v_mfma_f32_16x16x32_bf16 v[0:3], v[176:179], v[240:243], 0\n"
            "v_mfma_f32_16x16x32_bf16 v[4:7], v[176:179], v[100:103], 0\n"
            "s_waitcnt lgkmcnt(2)\n"
            "v_mfma_f32_16x16x32_bf16 v[0:3], v[180:183], v[244:247], v[0:3]\n"
            "v_mfma_f32_16x16x32_bf16 v[4:7], v[180:183], v[104:107], v[4:7]\n"
            "s_waitcnt lgkmcnt(1)\n"
            "v_mfma_f32_16x16x32_bf16 v[0:3], v[184:187], v[248:251], v[0:3]\n"
            "v_mfma_f32_16x16x32_bf16 v[4:7], v[184:187], v[108:111], v[4:7]\n"
            "s_waitcnt lgkmcnt(0)\n"
            "v_mfma_f32_16x16x32_bf16 v[0:3], v[188:191], v[96:99], v[0:3]\n"
            "v_mfma_f32_16x16x32_bf16 v[4:7], v[188:191], v[112:115], v[4:7]\n"
            "v_mfma_f32_16x16x32_bf16 v[8:11], v[176:179], v[116:119], 0\n"
            "v_mfma_f32_16x16x32_bf16 v[12:15], v[176:179], v[232:235], 0\n"
            "v_mfma_f32_16x16x32_bf16 v[8:11], v[180:183], v[196:199], v[8:11]\n"
            "v_mfma_f32_16x16x32_bf16 v[12:15], v[180:183], v[120:123], v[12:15]\n"
            "v_mfma_f32_16x16x32_bf16 v[8:11], v[184:187], v[200:203], v[8:11]\n"
            "v_mfma_f32_16x16x32_bf16 v[12:15], v[184:187], v[124:127], v[12:15]\n"
            "v_mfma_f32_16x16x32_bf16 v[8:11], v[188:191], v[204:207], v[8:11]\n"
            "v_mfma_f32_16x16x32_bf16 v[12:15], v[188:191], v[192:195], v[12:15]\n"
            "v_cvt_pk_bf16_f32 v0, v0, v1\n"
            "v_cvt_pk_bf16_f32 v1, v2, v3\n"
            "v_cvt_pk_bf16_f32 v2, v4, v5\n"
            "v_cvt_pk_bf16_f32 v3, v6, v7\n"
            "buffer_store_dwordx4 v[0:3], v217, s[32:35], 0 offen offset:0 sc1\n"
            "v_mfma_f32_16x16x32_bf16 v[16:19], v[176:179], v[128:131], 0\n"
            "v_mfma_f32_16x16x32_bf16 v[20:23], v[176:179], v[140:143], 0\n"
            "v_mfma_f32_16x16x32_bf16 v[16:19], v[180:183], v[132:135], v[16:19]\n"
            "v_mfma_f32_16x16x32_bf16 v[20:23], v[180:183], v[144:147], v[20:23]\n"
            "v_mfma_f32_16x16x32_bf16 v[16:19], v[184:187], v[236:239], v[16:19]\n"
            "v_mfma_f32_16x16x32_bf16 v[20:23], v[184:187], v[148:151], v[20:23]\n"
            "v_mfma_f32_16x16x32_bf16 v[16:19], v[188:191], v[136:139], v[16:19]\n"
            "v_mfma_f32_16x16x32_bf16 v[20:23], v[188:191], v[152:155], v[20:23]\n"
            "v_cvt_pk_bf16_f32 v8, v8, v9\n"
            "v_cvt_pk_bf16_f32 v9, v10, v11\n"
            "v_cvt_pk_bf16_f32 v10, v12, v13\n"
            "v_cvt_pk_bf16_f32 v11, v14, v15\n"
            "buffer_store_dwordx4 v[8:11], v217, s[32:35], 0 offen offset:256 sc1\n"
            "v_mfma_f32_16x16x32_bf16 v[24:27], v[176:179], v[156:159], 0\n"
            "v_mfma_f32_16x16x32_bf16 v[28:31], v[176:179], v[92:95], 0\n"
            "v_mfma_f32_16x16x32_bf16 v[24:27], v[180:183], v[72:75], v[24:27]\n"
            "v_mfma_f32_16x16x32_bf16 v[28:31], v[180:183], v[160:163], v[28:31]\n"
            "v_mfma_f32_16x16x32_bf16 v[24:27], v[184:187], v[76:79], v[24:27]\n"
            "v_mfma_f32_16x16x32_bf16 v[28:31], v[184:187], v[164:167], v[28:31]\n"
            "v_mfma_f32_16x16x32_bf16 v[24:27], v[188:191], v[88:91], v[24:27]\n"
            "v_mfma_f32_16x16x32_bf16 v[28:31], v[188:191], v[168:171], v[28:31]\n"
            "v_cvt_pk_bf16_f32 v16, v16, v17\n"
            "v_cvt_pk_bf16_f32 v17, v18, v19\n"
            "v_cvt_pk_bf16_f32 v18, v20, v21\n"
            "v_cvt_pk_bf16_f32 v19, v22, v23\n"
            "buffer_store_dwordx4 v[16:19], v217, s[32:35], 0 offen offset:512 sc1\n"
            "s_nop 2\n"
            "v_cvt_pk_bf16_f32 v24, v24, v25\n"
            "v_cvt_pk_bf16_f32 v25, v26, v27\n"
            "v_cvt_pk_bf16_f32 v26, v28, v29\n"
            "v_cvt_pk_bf16_f32 v27, v30, v31\n"
            "buffer_store_dwordx4 v[24:27], v217, s[32:35], 0 offen offset:768 sc1\n"
            "s_cmp_lt_u32 s3, 6\n"
            "s_cbranch_scc0 Ltwo_%=\n"
            "s_endpgm\n"
            "Ltwo_%=:\n"
            "ds_read_b128 v[64:67], v231 offset:0\n"
            "ds_read_b128 v[68:71], v231 offset:1024\n"
            "ds_read_b128 v[80:83], v231 offset:2048\n"
            "ds_read_b128 v[84:87], v231 offset:3072\n"
            "s_waitcnt vmcnt(4) lgkmcnt(0)\n"
            "v_mfma_f32_4x4x4_16b_bf16 v[0:3], v[224:225], v[64:65], 0\n"
            "v_mfma_f32_4x4x4_16b_bf16 v[4:7], v[224:225], v[66:67], 0\n"
            "v_mfma_f32_4x4x4_16b_bf16 v[8:11], v[224:225], v[68:69], 0\n"
            "v_mfma_f32_4x4x4_16b_bf16 v[12:15], v[224:225], v[70:71], 0\n"
            "v_mfma_f32_4x4x4_16b_bf16 v[16:19], v[224:225], v[80:81], 0\n"
            "v_mfma_f32_4x4x4_16b_bf16 v[20:23], v[224:225], v[82:83], 0\n"
            "v_mfma_f32_4x4x4_16b_bf16 v[24:27], v[224:225], v[84:85], 0\n"
            "v_mfma_f32_4x4x4_16b_bf16 v[28:31], v[224:225], v[86:87], 0\n"
            "v_mfma_f32_4x4x4_16b_bf16 v[0:3], v[224:225], v[40:41], v[0:3]\n"
            "v_mfma_f32_4x4x4_16b_bf16 v[4:7], v[224:225], v[42:43], v[4:7]\n"
            "v_mfma_f32_4x4x4_16b_bf16 v[8:11], v[224:225], v[44:45], v[8:11]\n"
            "v_mfma_f32_4x4x4_16b_bf16 v[12:15], v[224:225], v[46:47], v[12:15]\n"
            "v_mfma_f32_4x4x4_16b_bf16 v[16:19], v[224:225], v[56:57], v[16:19]\n"
            "v_mfma_f32_4x4x4_16b_bf16 v[20:23], v[224:225], v[58:59], v[20:23]\n"
            "v_mfma_f32_4x4x4_16b_bf16 v[24:27], v[224:225], v[60:61], v[24:27]\n"
            "v_mfma_f32_4x4x4_16b_bf16 v[28:31], v[224:225], v[62:63], v[28:31]\n"
            "s_nop 1\n"
            "v_cvt_pk_bf16_f32 v176, v0, v4\n"
            "v_cvt_pk_bf16_f32 v177, v8, v12\n"
            "v_cvt_pk_bf16_f32 v178, v16, v20\n"
            "v_cvt_pk_bf16_f32 v179, v24, v28\n"
            "ds_write_b128 v226, v[176:179] offset:0\n"
            "v_cvt_pk_bf16_f32 v180, v1, v5\n"
            "v_cvt_pk_bf16_f32 v181, v9, v13\n"
            "v_cvt_pk_bf16_f32 v182, v17, v21\n"
            "v_cvt_pk_bf16_f32 v183, v25, v29\n"
            "ds_write_b128 v226, v[180:183] offset:272\n"
            "v_cvt_pk_bf16_f32 v184, v2, v6\n"
            "v_cvt_pk_bf16_f32 v185, v10, v14\n"
            "v_cvt_pk_bf16_f32 v186, v18, v22\n"
            "v_cvt_pk_bf16_f32 v187, v26, v30\n"
            "ds_write_b128 v226, v[184:187] offset:544\n"
            "v_cvt_pk_bf16_f32 v188, v3, v7\n"
            "v_cvt_pk_bf16_f32 v189, v11, v15\n"
            "v_cvt_pk_bf16_f32 v190, v19, v23\n"
            "v_cvt_pk_bf16_f32 v191, v27, v31\n"
            "ds_write_b128 v226, v[188:191] offset:816\n"
            "ds_read_b128 v[176:179], v227 offset:0\n"
            "ds_read_b128 v[180:183], v227 offset:64\n"
            "ds_read_b128 v[184:187], v227 offset:128\n"
            "ds_read_b128 v[188:191], v227 offset:192\n"
            "s_waitcnt lgkmcnt(3)\n"
            "v_mfma_f32_16x16x32_bf16 v[0:3], v[176:179], v[240:243], 0\n"
            "v_mfma_f32_16x16x32_bf16 v[4:7], v[176:179], v[100:103], 0\n"
            "s_waitcnt lgkmcnt(2)\n"
            "v_mfma_f32_16x16x32_bf16 v[0:3], v[180:183], v[244:247], v[0:3]\n"
            "v_mfma_f32_16x16x32_bf16 v[4:7], v[180:183], v[104:107], v[4:7]\n"
            "s_waitcnt lgkmcnt(1)\n"
            "v_mfma_f32_16x16x32_bf16 v[0:3], v[184:187], v[248:251], v[0:3]\n"
            "v_mfma_f32_16x16x32_bf16 v[4:7], v[184:187], v[108:111], v[4:7]\n"
            "s_waitcnt lgkmcnt(0)\n"
            "v_mfma_f32_16x16x32_bf16 v[0:3], v[188:191], v[96:99], v[0:3]\n"
            "v_mfma_f32_16x16x32_bf16 v[4:7], v[188:191], v[112:115], v[4:7]\n"
            "v_mfma_f32_16x16x32_bf16 v[8:11], v[176:179], v[116:119], 0\n"
            "v_mfma_f32_16x16x32_bf16 v[12:15], v[176:179], v[232:235], 0\n"
            "v_mfma_f32_16x16x32_bf16 v[8:11], v[180:183], v[196:199], v[8:11]\n"
            "v_mfma_f32_16x16x32_bf16 v[12:15], v[180:183], v[120:123], v[12:15]\n"
            "v_mfma_f32_16x16x32_bf16 v[8:11], v[184:187], v[200:203], v[8:11]\n"
            "v_mfma_f32_16x16x32_bf16 v[12:15], v[184:187], v[124:127], v[12:15]\n"
            "v_mfma_f32_16x16x32_bf16 v[8:11], v[188:191], v[204:207], v[8:11]\n"
            "v_mfma_f32_16x16x32_bf16 v[12:15], v[188:191], v[192:195], v[12:15]\n"
            "v_cvt_pk_bf16_f32 v0, v0, v1\n"
            "v_cvt_pk_bf16_f32 v1, v2, v3\n"
            "v_cvt_pk_bf16_f32 v2, v4, v5\n"
            "v_cvt_pk_bf16_f32 v3, v6, v7\n"
            "buffer_store_dwordx4 v[0:3], v218, s[32:35], 0 offen offset:0 sc1\n"
            "v_mfma_f32_16x16x32_bf16 v[16:19], v[176:179], v[128:131], 0\n"
            "v_mfma_f32_16x16x32_bf16 v[20:23], v[176:179], v[140:143], 0\n"
            "v_mfma_f32_16x16x32_bf16 v[16:19], v[180:183], v[132:135], v[16:19]\n"
            "v_mfma_f32_16x16x32_bf16 v[20:23], v[180:183], v[144:147], v[20:23]\n"
            "v_mfma_f32_16x16x32_bf16 v[16:19], v[184:187], v[236:239], v[16:19]\n"
            "v_mfma_f32_16x16x32_bf16 v[20:23], v[184:187], v[148:151], v[20:23]\n"
            "v_mfma_f32_16x16x32_bf16 v[16:19], v[188:191], v[136:139], v[16:19]\n"
            "v_mfma_f32_16x16x32_bf16 v[20:23], v[188:191], v[152:155], v[20:23]\n"
            "v_cvt_pk_bf16_f32 v8, v8, v9\n"
            "v_cvt_pk_bf16_f32 v9, v10, v11\n"
            "v_cvt_pk_bf16_f32 v10, v12, v13\n"
            "v_cvt_pk_bf16_f32 v11, v14, v15\n"
            "buffer_store_dwordx4 v[8:11], v218, s[32:35], 0 offen offset:256 sc1\n"
            "v_mfma_f32_16x16x32_bf16 v[24:27], v[176:179], v[156:159], 0\n"
            "v_mfma_f32_16x16x32_bf16 v[28:31], v[176:179], v[92:95], 0\n"
            "v_mfma_f32_16x16x32_bf16 v[24:27], v[180:183], v[72:75], v[24:27]\n"
            "v_mfma_f32_16x16x32_bf16 v[28:31], v[180:183], v[160:163], v[28:31]\n"
            "v_mfma_f32_16x16x32_bf16 v[24:27], v[184:187], v[76:79], v[24:27]\n"
            "v_mfma_f32_16x16x32_bf16 v[28:31], v[184:187], v[164:167], v[28:31]\n"
            "v_mfma_f32_16x16x32_bf16 v[24:27], v[188:191], v[88:91], v[24:27]\n"
            "v_mfma_f32_16x16x32_bf16 v[28:31], v[188:191], v[168:171], v[28:31]\n"
            "v_cvt_pk_bf16_f32 v16, v16, v17\n"
            "v_cvt_pk_bf16_f32 v17, v18, v19\n"
            "v_cvt_pk_bf16_f32 v18, v20, v21\n"
            "v_cvt_pk_bf16_f32 v19, v22, v23\n"
            "buffer_store_dwordx4 v[16:19], v218, s[32:35], 0 offen offset:512 sc1\n"
            "s_nop 2\n"
            "v_cvt_pk_bf16_f32 v24, v24, v25\n"
            "v_cvt_pk_bf16_f32 v25, v26, v27\n"
            "v_cvt_pk_bf16_f32 v26, v28, v29\n"
            "v_cvt_pk_bf16_f32 v27, v30, v31\n"
            "buffer_store_dwordx4 v[24:27], v218, s[32:35], 0 offen offset:768 sc1\n"
            "Lend_%=:\n"
            "s_endpgm\n"
            "s_mov_b32 s68, 0\n"
            "s_branch Lout_%=\n"
            "Lpoll2_%=:\n"
            "s_sub_u32 s99, s99, 1\n"
            "s_cmp_lt_i32 s99, 0\n"
            "s_cbranch_scc1 Lpoll3_%=\n"
            "s_sleep 2\n"
            "buffer_load_dwordx2 v[142:143], v180, s[20:23], 0 offen sc1\n"
            "s_branch Lpoll_%=\n"
            "Lover_%=:\n"
            "s_mov_b32 s37, 0\n"
            "v_mov_b32 v144, 255\n"
            "ds_write_b32 v219, v144\n"
            "s_branch Lover2_%=\n"
            "Llng_%=:\n"
            "s_nop 1\n"
            "s_mov_b32 s38, 0\n"
            "v_max_u32_dpp v74, v74, v74 row_bcast:15 row_mask:0xa bank_mask:0xf\n"
            "s_nop 1\n"
            "v_max_u32_dpp v74, v74, v74 row_bcast:31 row_mask:0xc bank_mask:0xf\n"
            "v_max_u32 v74, s38, v74\n"
            "s_nop 0\n"
            "v_readlane_b32 s38, v74, 63\n"
            "v_max_u32_dpp v75, v75, v75 row_bcast:15 row_mask:0xa bank_mask:0xf\n"
            "s_nop 1\n"
            "v_max_u32_dpp v75, v75, v75 row_bcast:31 row_mask:0xc bank_mask:0xf\n"
            "v_max_u32 v75, s38, v75\n"
            "s_nop 0\n"
            "v_readlane_b32 s38, v75, 63\n"
            "v_max_u32_dpp v76, v76, v76 row_bcast:15 row_mask:0xa bank_mask:0xf\n"
            "s_nop 1\n"
            "v_max_u32_dpp v76, v76, v76 row_bcast:31 row_mask:0xc bank_mask:0xf\n"
            "v_max_u32 v76, s38, v76\n"
            "s_nop 0\n"
            "v_readlane_b32 s38, v76, 63\n"
            "v_max_u32_dpp v77, v77, v77 row_bcast:15 row_mask:0xa bank_mask:0xf\n"
            "s_nop 1\n"
            "v_max_u32_dpp v77, v77, v77 row_bcast:31 row_mask:0xc bank_mask:0xf\n"
            "v_max_u32 v77, s38, v77\n"
            "s_nop 0\n"
            "v_readlane_b32 s38, v77, 63\n"
            "v_max_u32_dpp v78, v78, v78 row_bcast:15 row_mask:0xa bank_mask:0xf\n"
            "s_nop 1\n"
            "v_max_u32_dpp v78, v78, v78 row_bcast:31 row_mask:0xc bank_mask:0xf\n"
            "v_max_u32 v78, s38, v78\n"
            "s_nop 0\n"
            "v_readlane_b32 s38, v78, 63\n"
            "v_max_u32_dpp v79, v79, v79 row_bcast:15 row_mask:0xa bank_mask:0xf\n"
            "s_nop 1\n"
            "v_max_u32_dpp v79, v79, v79 row_bcast:31 row_mask:0xc bank_mask:0xf\n"
            "v_max_u32 v79, s38, v79\n"
            "s_nop 0\n"
            "v_readlane_b32 s38, v79, 63\n"
            "v_max_u32_dpp v80, v80, v80 row_bcast:15 row_mask:0xa bank_mask:0xf\n"
            "s_nop 1\n"
            "v_max_u32_dpp v80, v80, v80 row_bcast:31 row_mask:0xc bank_mask:0xf\n"
            "v_max_u32 v80, s38, v80\n"
            "s_nop 0\n"
            "v_readlane_b32 s38, v80, 63\n"
            "s_branch Llng2_%=\n"
            "Lre0_%=:\n"
            "s_sub_u32 s99, s99, 1\n"
            "s_cmp_lt_i32 s99, 0\n"
            "s_cbranch_scc1 Lok0_%=\n"
            "s_sleep 1\n"
            "buffer_load_dwordx4 v[34:37], v81, s[20:23], s15 offen offset:0 sc1\n"
            "s_waitcnt vmcnt(0)\n"
            "s_branch Lchk0_%=\n"
            "Lre1_%=:\n"
            "s_sub_u32 s99, s99, 1\n"
            "s_cmp_lt_i32 s99, 0\n"
            "s_cbranch_scc1 Lok1_%=\n"
            "s_sleep 1\n"
            "buffer_load_dwordx4 v[40:43], v82, s[20:23], s15 offen offset:1024 sc1\n"
            "s_waitcnt vmcnt(0)\n"
            "s_branch Lchk1_%=\n"
            "Lre2_%=:\n"
            "s_sub_u32 s99, s99, 1\n"
            "s_cmp_lt_i32 s99, 0\n"
            "s_cbranch_scc1 Lok2_%=\n"
            "s_sleep 1\n"
            "buffer_load_dwordx4 v[46:49], v83, s[20:23], s15 offen offset:2048 sc1\n"
            "s_waitcnt vmcnt(0)\n"
            "s_branch Lchk2_%=\n"
            "Lre3_%=:\n"
            "s_sub_u32 s99, s99, 1\n"
            "s_cmp_lt_i32 s99, 0\n"
            "s_cbranch_scc1 Lok3_%=\n"
            "s_sleep 1\n"
            "buffer_load_dwordx4 v[52:55], v84, s[20:23], s15 offen offset:3072 sc1\n"
            "s_waitcnt vmcnt(0)\n"
            "s_branch Lchk3_%=\n"
            "Lre4_%=:\n"
            "s_sub_u32 s99, s99, 1\n"
            "s_cmp_lt_i32 s99, 0\n"
            "s_cbranch_scc1 Lok4_%=\n"
            "s_sleep 1\n"
            "buffer_load_dwordx4 v[58:61], v85, s[20:23], s16 offen offset:0 sc1\n"
            "s_waitcnt vmcnt(0)\n"
            "s_branch Lchk4_%=\n"
            "Lre5_%=:\n"
            "s_sub_u32 s99, s99, 1\n"
            "s_cmp_lt_i32 s99, 0\n"
            "s_cbranch_scc1 Lok5_%=\n"
            "s_sleep 1\n"
            "buffer_load_dwordx4 v[64:67], v86, s[20:23], s16 offen offset:1024 sc1\n"
            "s_waitcnt vmcnt(0)\n"
            "s_branch Lchk5_%=\n"
            "Lre6_%=:\n"
            "s_sub_u32 s99, s99, 1\n"
            "s_cmp_lt_i32 s99, 0\n"
            "s_cbranch_scc1 Lok6_%=\n"
            "s_sleep 1\n"
            "buffer_load_dwordx4 v[70:73], v87, s[20:23], s16 offen offset:2048 sc1\n"
            "s_waitcnt vmcnt(0)\n"
            "s_branch Lchk6_%=\n"
            "Llong_%=:\n"
            "v_and_b32 v243, 31, v243\n"
            "v_readfirstlane_b32 s86, v247\n"
            "v_add_u32 v243, 64, v243\n"
            "s_nop 0\n"
            "v_lshl_add_u32 v247, v243, 3, s86\n"
            "v_mov_b32 v136, s64\n"
            "v_cmpx_le_u32 vcc, s69, v243\n"
            "v_mov_b32 v136, s81\n"
            "v_cmpx_le_u32 vcc, s78, v243\n"
            "v_mov_b32 v136, s82\n"
            "v_cmpx_le_u32 vcc, s79, v243\n"
            "v_mov_b32 v136, s83\n"
            "v_cmpx_le_u32 vcc, s80, v243\n"
            "v_mov_b32 v136, s84\n"
            "s_mov_b64 exec, -1\n"
            "v_and_b32 v137, 0xffff, v136\n"
            "ds_read_b128 v[140:143], v137\n"
            "v_ashrrev_i32 v136, 16, v136\n"
            "v_sub_u32 v136, v243, v136\n"
            "v_lshl_or_b32 v137, v136, 16, v138\n"
            "v_lshlrev_b32 v136, 4, v136\n"
            "s_waitcnt lgkmcnt(0)\n"
            "v_cmp_gt_u32 vcc, v140, v137\n"
            "v_cmp_gt_u32 s[70:71], v141, v137\n"
            "v_cmp_gt_u32 s[86:87], v142, v137\n"
            "v_cmp_gt_u32 s[88:89], v143, v137\n"
            "v_add_u32 v140, v140, v136\n"
            "v_add_u32 v141, v141, v136\n"
            "v_add_u32 v142, v142, v136\n"
            "v_add_u32 v143, v143, v136\n"
            "v_cndmask_b32 v140, v251, v140, vcc\n"
            "v_cndmask_b32 v141, v251, v141, s[70:71]\n"
            "v_cndmask_b32 v142, v251, v142, s[86:87]\n"
            "v_cndmask_b32 v143, v251, v143, s[88:89]\n"
            "v_perm_b32 v140, v141, v140, s68\n"
            "v_perm_b32 v141, v143, v142, s68\n"
            "ds_write_b64 v247, v[140:141]\n"
            "s_branch Llong2_%=\n"
            "Lslow_%=:\n"
            "s_waitcnt vmcnt(0) lgkmcnt(0)\n"
            "s_mov_b32 s68, -1\n"
            "Lout_%=:\n"
            : "+{v0}"(tid), "={s68}"(slow), "={s90}"(tag), "={s3}"(wq)
            : "{s[0:1]}"(ka), "{s2}"(bx)
            : "memory", "vcc", "scc", "v1", "v2", "v3", "v4", "v5", "v6", "v7", "v8", "v9", "v10", "v11", "v12", "v13", "v14", "v15", "v16", "v17", "v18", "v19", "v20", "v21", "v22", "v23", "v24", "v25", "v26", "v27", "v28", "v29", "v30", "v31", "v32", "v33", "v34", "v35", "v36", "v37", "v38", "v39", "v40", "v41", "v42", "v43", "v44", "v45", "v46", "v47", "v48", "v49", "v50", "v51", "v52", "v53", "v54", "v55", "v56", "v57", "v58", "v59", "v60", "v61", "v62", "v63", "v64", "v65", "v66", "v67", "v68", "v69", "v70", "v71", "v72", "v73", "v74", "v75", "v76", "v77", "v78", "v79", "v80", "v81", "v82", "v83", "v84", "v85", "v86", "v87", "v88", "v89", "v90", "v91", "v92", "v93", "v94", "v95", "v96", "v97", "v98", "v99", "v100", "v101", "v102", "v103", "v104", "v105", "v106", "v107", "v108", "v109", "v110", "v111", "v112", "v113", "v114", "v115", "v116", "v117", "v118", "v119", "v120", "v121", "v122", "v123", "v124", "v125", "v126", "v127", "v128", "v129", "v130", "v131", "v132", "v133", "v134", "v135", "v136", "v137", "v138", "v139", "v140", "v141", "v142", "v143", "v144", "v145", "v146", "v147", "v148", "v149", "v150", "v151", "v152", "v153", "v154", "v155", "v156", "v157", "v158", "v159", "v160", "v161", "v162", "v163", "v164", "v165", "v166", "v167", "v168", "v169", "v170", "v171", "v172", "v173", "v174", "v175", "v176", "v177", "v178", "v179", "v180", "v181", "v182", "v183", "v184", "v185", "v186", "v187", "v188", "v189", "v190", "v191", "v192", "v193", "v194", "v195", "v196", "v197", "v198", "v199", "v200", "v201", "v202", "v203", "v204", "v205", "v206", "v207", "v208", "v209", "v210", "v211", "v212", "v213", "v214", "v215", "v216", "v217", "v218", "v219", "v220", "v221", "v222", "v223", "v224", "v225", "v226", "v227", "v228", "v229", "v230", "v231", "v232", "v233", "v234", "v235", "v236", "v237", "v238", "v239", "v240", "v241", "v242", "v243", "v244", "v245", "v246", "v247", "v248", "v249", "v250", "v251", "v252", "v253", "s4", "s5", "s6", "s7", "s8", "s9", "s10", "s11", "s12", "s13", "s14", "s15", "s16", "s17", "s18", "s19", "s20", "s21", "s22", "s23", "s24", "s25", "s26", "s27", "s28", "s29", "s30", "s31", "s33", "s34", "s35", "s36", "s37", "s38", "s39", "s40", "s41", "s42", "s43", "s44", "s45", "s46", "s47", "s48", "s49", "s50", "s51", "s52", "s53", "s54", "s55", "s56", "s57", "s58", "s59", "s60", "s61", "s62", "s63", "s64", "s65", "s66", "s67", "s69", "s70", "s71", "s72", "s73", "s74", "s75", "s76", "s77", "s78", "s79", "s80", "s81", "s82", "s83", "s84", "s85", "s86", "s87", "s88", "s89", "s91", "s92", "s93", "s94", "s95", "s96", "s97", "s98", "s99");
    const int w = __builtin_amdgcn_readfirstlane((int)wq);
    const unsigned lds0 = (unsigned)(uintptr_t)(__attribute__((address_space(3))) unsigned char*)smem;
    asm volatile("" : "+s"(ka));
    unsigned char* ws = (unsigned char*)ka[6];
    if (__builtin_amdgcn_readfirstlane((int)slow) == -1) {
        const int lane = lane_opaque();
        tag = (unsigned)__builtin_amdgcn_readfirstlane((int)tag);
        const int* dst = (const int*)ka[0]; const int* src = (const int*)ka[2];
        const unsigned short *Y = (const unsigned short*)ka[4], *ef = (const unsigned short*)ka[8], *x = (const unsigned short*)ka[10], *w_rad = (const unsigned short*)ka[14],
                             *cg = (const unsigned short*)ka[16], *w_mix = (const unsigned short*)ka[18];
        unsigned short* out = (unsigned short*)ka[12];
        const int N = (int)ka[20], E = (int)(ka[20] >> 32), nh = (int)ka[23];
        const int half = (bx >> 2) & 1, b = (bx & 3) * 32 + (bx >> 3), sS = 40 * (1 - half), sK = 40 * half;
        const int node0 = (b * N + 127) >> 7, npb = (((b + 1) * N + 127) >> 7) - node0;
        const __amdgpu_buffer_rsrc_t rsw = __builtin_amdgcn_make_buffer_rsrc((void*)ws, 0, W_END, 0x00020000);
        lds_wait(lds0 + SY_C3, 4);
        const __amdgpu_buffer_rsrc_t rso = __builtin_amdgcn_make_buffer_rsrc((void*)out, 0, N * 1024, 0x00020000);
#pragma unroll 1
        for (int it = 0; it < 10; ++it) {
            const int nl = (it < 5 ? sS + 8 * it : sK + 8 * (it - 5)) + w, node = node0 + nl;
            const bool nv = nl < npb;
            const unsigned ro = nv ? (unsigned)node * 1024u + (unsigned)lane * 16u : OOB_OFF;
            float a8[8] = {0.f, 0.f, 0.f, 0.f, 0.f, 0.f, 0.f, 0.f};
#pragma unroll 1
            for (int e0 = 0; e0 < (nv ? E : 0); e0 += 64) {
                const int ec = min(e0 + lane, E - 1);
                u64 mm = __ballot(e0 + lane < E && dst[ec] == node && (src[ec] >= nh) == (half != 0));
#pragma unroll 1
                while (mm) {
                    const int e = e0 + __builtin_ctzll(mm);
                    mm &= mm - 1;
                    const unsigned short* fe = (const unsigned short*)ef + (size_t)e * DE_DIM;
                    const unsigned short* ye = (const unsigned short*)Y + (size_t)e * M_DIM;
                    const unsigned short* xp = x + (size_t)src[e] * (C_DIM * M_DIM) + lane * 8;
                    float z0 = 0.f, z1 = 0.f;
#pragma unroll 1
                    for (int f = 0; f < DE_DIM; ++f) { z0 += bf2f(fe[f]) * bf2f(w_rad[f * C_DIM + 2 * lane]); z1 += bf2f(fe[f]) * bf2f(w_rad[f * C_DIM + 2 * lane + 1]); }
                    const float r0 = z0 / (1.f + __expf(-z0)), r1 = z1 / (1.f + __expf(-z1));
#pragma unroll 1
                    for (int i = 0; i < 4; ++i) {
                        const float x0 = bf2f(xp[i]) * r0, x1 = bf2f(xp[4 + i]) * r1;
#pragma unroll
                        for (int o = 0; o < 4; ++o) {
                            float wv4 = 0.f;
#pragma unroll
                            for (int f = 0; f < 4; ++f) wv4 += bf2f(cg[(i * 4 + f) * 4 + o]) * bf2f(ye[f]);
                            a8[o] += x0 * wv4; a8[4 + o] += x1 * wv4;
                        }
                    }
                }
            }
            if (it >= 5) {
                const u32x4 pv = __builtin_amdgcn_raw_buffer_load_b128(rsw, ro, W_AGG, SC1);
#pragma unroll
                for (int i = 0; i < 4; ++i) { a8[2 * i] += __uint_as_float(pv[i] << 16); a8[2 * i + 1] += __uint_as_float(pv[i] & 0xffff0000u); }
            }
            const u32x4 rv = (u32x4){pack2bf(a8[0], a8[1]), pack2bf(a8[2], a8[3]), pack2bf(a8[4], a8[5]), pack2bf(a8[6], a8[7])};
            if (it < 5) {
                __builtin_amdgcn_raw_buffer_store_b128(rv, rsw, ro, W_AGG, SC1);
                if (it == 4) {
                    asm volatile("s_waitcnt vmcnt(0)" ::: "memory");
                    if (lds_arrive_rtn(lds0 + 4) == 7u) flag_store(ws + W_FLG + bx * 128, tag);
                    poll_tag(ws + W_FLG + (bx ^ 4) * 128, tag);
                }
            } else {
                *(u32x4*)(smem + L_LST + w * 1024 + lane * 16) = rv;
#pragma unroll
                for (int i = 0; i < 8; ++i) a8[i] = 0.f;
#pragma unroll 1
                for (int c = 0; c < C_DIM; ++c) {
                    const unsigned short* ap = (const unsigned short*)(smem + L_LST + w * 1024 + c * 8);
                    const float w0 = bf2f(w_mix[c * C_DIM + 2 * lane]), w1 = bf2f(w_mix[c * C_DIM + 2 * lane + 1]);
#pragma unroll
                    for (int m = 0; m < 4; ++m) { a8[m] += bf2f(ap[m]) * w0; a8[4 + m] += bf2f(ap[m]) * w1; }
                }
                __builtin_amdgcn_raw_buffer_store_b128((u32x4){pack2bf(a8[0], a8[1]), pack2bf(a8[2], a8[3]), pack2bf(a8[4], a8[5]), pack2bf(a8[6], a8[7])}, rso, ro, 0, 0);
            }
        }
    }
}

__global__ void kf_zero(float* __restrict__ agg, size_t n) {
    for (size_t i = (size_t)blockIdx.x * blockDim.x + threadIdx.x; i < n; i += (size_t)gridDim.x * blockDim.x) agg[i] = 0.f;
}
__global__ void kf_edges(const unsigned short* __restrict__ x, const int* __restrict__ src, const int* __restrict__ dst,
                         const unsigned short* __restrict__ Y, const unsigned short* __restrict__ ef,
                         const unsigned short* __restrict__ w_rad, const unsigned short* __restrict__ cg,
                         float* __restrict__ agg, int E) {
    const int ch = threadIdx.x;
    for (int e = blockIdx.x; e < E; e += gridDim.x) {
        float z = 0.f;
        for (int f = 0; f < DE_DIM; ++f) z += bf2f(ef[(size_t)e * DE_DIM + f]) * bf2f(w_rad[f * C_DIM + ch]);
        const float r = z / (1.f + __expf(-z));
        float a4[4] = {0.f, 0.f, 0.f, 0.f};
        for (int ii = 0; ii < 4; ++ii) {
            const float xv = bf2f(x[((size_t)src[e] * C_DIM + ch) * M_DIM + ii]) * r;
            for (int o = 0; o < 4; ++o) {
                float wv = 0.f;
                for (int f = 0; f < 4; ++f) wv += bf2f(cg[(ii * 4 + f) * 4 + o]) * bf2f(Y[(size_t)e * 4 + f]);
                a4[o] += xv * wv;
            }
        }
        for (int o = 0; o < 4; ++o) atomicAdd(&agg[((size_t)dst[e] * C_DIM + ch) * M_DIM + o], a4[o]);
    }
}
__global__ void kf_mix(const float* __restrict__ agg, const unsigned short* __restrict__ w_mix, unsigned short* __restrict__ out, int N) {
    const int d = threadIdx.x;
    for (int n = blockIdx.x; n < N; n += gridDim.x) {
        float a4[4] = {0.f, 0.f, 0.f, 0.f};
        for (int c = 0; c < C_DIM; ++c) {
            const float wv = bf2f(w_mix[c * C_DIM + d]);
            for (int m = 0; m < 4; ++m) a4[m] += bf2f(f2bf(agg[((size_t)n * C_DIM + c) * M_DIM + m])) * wv;
        }
        for (int m = 0; m < 4; ++m) out[((size_t)n * C_DIM + d) * M_DIM + m] = f2bf(a4[m]);
    }
}

extern "C" void kernel_launch(void* const* d_in, const int* in_sizes, int n_in,
                              void* d_out, int out_size, void* d_ws, size_t ws_size,
                              hipStream_t stream) {
    const unsigned short* x = (const unsigned short*)d_in[0];
    const int* src = (const int*)d_in[1];
    const int* dst = (const int*)d_in[2];
    const unsigned short* Y = (const unsigned short*)d_in[3];
    const unsigned short* ef = (const unsigned short*)d_in[4];
    const unsigned short* w_rad = (const unsigned short*)d_in[5];
    const unsigned short* cg = (const unsigned short*)d_in[6];
    const unsigned short* w_mix = (const unsigned short*)d_in[7];
    unsigned short* out = (unsigned short*)d_out;
    const int N = in_sizes[0] / (C_DIM * M_DIM);
    const int E = in_sizes[1];
    const int epb = (E + 255) / 256;
    const int nh = (N + 1) / 2;
    u64 hi = (u64)(uintptr_t)d_out | (u64)(uintptr_t)d_ws;
    for (int i = 0; i < 8; ++i) hi |= (u64)(uintptr_t)d_in[i];
    const bool fast = (hi >> 48) == 0 && N >= 256 && N <= NB_MAX * NPB_MAX && E > 0 && epb <= EPB_MAX && ws_size >= (size_t)W_END;
    if (!fast) {
        float* agg = (float*)d_ws;
        if (N <= 0) return;
        kf_zero<<<1024, 256, 0, stream>>>(agg, (size_t)N * C_DIM * M_DIM);
        if (E > 0) kf_edges<<<4096, C_DIM, 0, stream>>>(x, src, dst, Y, ef, w_rad, cg, agg, E);
        kf_mix<<<2048, C_DIM, 0, stream>>>(agg, w_mix, out, N);
        return;
    }
    int k = 0;
    while ((N << k) <= 32768) ++k;
    const unsigned magic = (unsigned)(((1ull << (39 - k)) + N - 1) / N);
    const unsigned F = 0x00020000u;
    k_fused<<<256, 512, 0, stream>>>(dst, E * 4u, F, src, E * 4u, F, Y, E * 8u, F, d_ws, W_END, F, ef, E * 32u, F, x, N * 1024u, F, out, N * 1024u, F, w_rad, DE_DIM * C_DIM * 2u, F,
                                     cg, 128u, F, w_mix, C_DIM * C_DIM * 2u, F, N, E, epb, magic, k, nh * 1024u, nh, 0u);
}
```

```cpp
#include <hip/hip_runtime.h>
#include <hip/hip_bf16.h>
#include <stdint.h>

typedef __attribute__((ext_vector_type(4))) unsigned u32x4;
typedef unsigned long long u64;

#define C_DIM 128
#define M_DIM 4
#define DE_DIM 16
#define NPB_MAX 80
#define NB_MAX 128
#define EPB_MAX 1280
#define OOB_OFF 0x80000000u
#define SC1 16
#define SPIN_MAX (1u << 22)
#define W_FLG 4096
#define W_AGG 5804032
#define W_END 16289792
#define L_LST 4096
#define L_TOTAL 155648
#define SY_C3 56

static __device__ __forceinline__ int lane_opaque() {
    int l;
    asm volatile("v_mbcnt_lo_u32_b32 %0, -1, 0\n\tv_mbcnt_hi_u32_b32 %0, -1, %0" : "=v"(l));
    return l;
}

static __device__ __forceinline__ float bf2f(unsigned short v) { return __uint_as_float(((unsigned)v) << 16); }
typedef __bf16 bf16v2 __attribute__((ext_vector_type(2)));
static __device__ __forceinline__ unsigned pack2bf(float lo, float hi) {
    bf16v2 r = (bf16v2){(__bf16)lo, (__bf16)hi};
    return __builtin_bit_cast(unsigned, r);
}
static __device__ __forceinline__ unsigned short f2bf(float v) { return (unsigned short)(pack2bf(v, 0.f) & 0xffffu); }
static __device__ __forceinline__ void flag_store(const void* p, unsigned v) {
    unsigned t0, t1; u64 sv;
    asm volatile("v_mov_b32 %0, 0\n\tv_mov_b32 %1, %4\n\ts_mov_b64 %2, exec\n\ts_mov_b64 exec, 1\n\t"
                 "global_store_dword %0, %1, %3 sc1\n\ts_mov_b64 exec, %2" : "=&v"(t0), "=&v"(t1), "=&s"(sv) : "s"(p), "s"(v) : "memory");
}
static __device__ __forceinline__ unsigned lds_arrive_rtn(unsigned a) {
    unsigned t0, t1; u64 sv;
    asm volatile("v_mov_b32 %0, %3\n\tv_mov_b32 %1, 1\n\ts_mov_b64 %2, exec\n\ts_mov_b64 exec, 1\n\t"
                 "ds_add_rtn_u32 %1, %0, %1\n\ts_mov_b64 exec, %2\n\ts_waitcnt lgkmcnt(0)" : "=&v"(t0), "=&v"(t1), "=&s"(sv) : "s"(a) : "memory");
    return (unsigned)__builtin_amdgcn_readfirstlane((int)t1);
}
template <int SLEEP = 0> static __device__ __forceinline__ void lds_wait(unsigned a, unsigned n) {
    for (;;) {
        unsigned v;
        asm volatile("ds_read_b32 %0, %1\n\ts_waitcnt lgkmcnt(0)" : "=v"(v) : "v"(a) : "memory");
        if ((unsigned)__builtin_amdgcn_readfirstlane((int)v) >= n) break;
        if (SLEEP) __builtin_amdgcn_s_sleep(SLEEP);
    }
}
static __device__ __forceinline__ unsigned ld_agent(const void* p) { return __hip_atomic_load((const unsigned*)p, __ATOMIC_RELAXED, __HIP_MEMORY_SCOPE_AGENT); }
static __device__ __forceinline__ void poll_tag(const void* p, unsigned tag) {
#pragma unroll 1
    for (unsigned spin = 0; spin < SPIN_MAX; ++spin) {
        if (__all(ld_agent(p) == tag)) break;
        __builtin_amdgcn_s_sleep(2);
    }
    __builtin_amdgcn_fence(__ATOMIC_ACQUIRE, "wavefront");
}

#define BUF(t, n) t n, unsigned n##_bytes, unsigned n##_fmt
__global__ __launch_bounds__(512, 2) void k_fused(
    BUF(const void*, dst_), BUF(const void*, src_), BUF(const void*, Y_), BUF(void*, ws_), BUF(const void*, ef_), BUF(const void*, x_), BUF(void*, out_), BUF(const void*, w_rad_),
    BUF(const void*, cg_), BUF(const void*, w_mix_), unsigned N_, unsigned E_, unsigned epb_, unsigned magic_, unsigned shift_, unsigned nhb_, unsigned nh_, unsigned zero_)
{
    __shared__ __attribute__((aligned(256))) unsigned char smem[L_TOTAL];
    const __attribute__((address_space(4))) u64* ka = (const __attribute__((address_space(4))) u64*)__builtin_amdgcn_kernarg_segment_ptr();
    const int bx = blockIdx.x;
    unsigned slow, tag, wq, tid = threadIdx.x;
    asm volatile(
            "s_load_dwordx16 s[8:23], s[0:1], 0\n"
            "s_load_dwordx16 s[40:55], s[0:1], 128\n"
            "s_load_dwordx16 s[24:39], s[0:1], 64\n"
            "s_getpc_b64 s[4:5]\n"
            "v_readfirstlane_b32 s3, v0\n"
            "v_and_b32 v243, 63, v0\n"
            "s_setprio 3\n"
            "s_lshr_b32 s3, s3, 6\n"
            "v_mul_u32_u24 v54, 3, v243\n"
            "s_cmp_lg_u32 s3, 4\n"
            "s_cbranch_scc1 Lncp_%=\n"
            "s_and_b32 s4, s4, 0xffffff80\n"
            "s_movk_i32 s6, 16896\n"
            "s_mov_b32 s7, 0x20000\n"
            "s_lshr_b32 s68, s2, 3\n"
            "s_lshl_b32 s68, s68, 7\n"
            "v_lshlrev_b32 v81, 12, v243\n"
            "buffer_load_dword v53, v81, s[4:7], s68 offen\n"
            "Lncp_%=:\n"
            "s_mul_i32 s68, s3, 162\n"
            "v_cmp_gt_u32 vcc, 54, v243\n"
            "v_add_u32 v54, s68, v54\n"
            "v_mov_b32 v82, 0x1000000\n"
            "v_cndmask_b32 v54, v82, v54, vcc\n"
            "v_mov_b32 v219, 0\n"
            "s_waitcnt lgkmcnt(0)\n"
            "s_mul_i32 s95, s2, s50\n"
            "v_add_lshl_u32 v81, v54, s95, 2\n"
            "buffer_load_dwordx3 v[32:34], v81, s[8:11], 0 offen nt\n"
            "buffer_load_dwordx3 v[36:38], v81, s[12:15], 0 offen nt\n"
            "v_lshlrev_b32 v82, 1, v81\n"
            "buffer_load_dword v40, off, s[20:23], 0\n"
            "buffer_load_dwordx4 v[42:45], v82, s[16:19], 0 offen nt\n"
            "buffer_load_dwordx2 v[46:47], v82, s[16:19], 0 offen offset:16 nt\n"
            "v_lshlrev_b32 v193, 7, v243\n"
            "s_lshl_b32 s54, s95, 5\n"
            "s_bfe_u32 s8, s2, 0x10002\n"
            "s_and_b32 s9, s2, 3\n"
            "s_lshl_b32 s9, s9, 5\n"
            "s_lshr_b32 s68, s2, 3\n"
            "s_add_u32 s9, s9, s68\n"
            "s_mul_i32 s55, s8, s53\n"
            "s_lshl_b32 s69, s9, 13\n"
            "s_add_u32 s69, s69, s55\n"
            "s_cmp_lt_u32 s3, 4\n"
            "s_cbranch_scc1 Lx0_%=\n"
            "v_lshlrev_b32 v81, 4, v0\n"
            "v_and_b32 v81, 0xe00, v81\n"
            "v_and_b32 v82, 31, v0\n"
            "v_lshl_or_b32 v81, v82, 3, v81\n"
            "buffer_load_dwordx2 v[48:49], v81, s[36:39], 0 offen\n"
            "buffer_load_dwordx2 v[50:51], v81, s[36:39], 0 offen offset:256\n"
            "s_cmp_eq_u32 s3, 4\n"
            "s_cbranch_scc1 Lx4_%=\n"
            "s_sub_u32 s68, 7, s3\n"
            "s_cmp_lt_u32 s68, 3\n"
            "s_cselect_b32 s68, s68, 0x20000\n"
            "s_lshl_b32 s68, s68, 13\n"
            "s_add_u32 s68, s68, s54\n"
            "buffer_load_dword v52, v193, s[24:27], s68 offen\n"
            "s_branch Lx1_%=\n"
            "Lx4_%=:\n"
            "v_lshlrev_b32 v81, 2, v243\n"
            "buffer_load_dword v52, v81, s[40:43], 0 offen\n"
            "s_branch Lx1_%=\n"
            "Lx0_%=:\n"
            "s_add_u32 s68, s3, 3\n"
            "s_cmp_lt_u32 s68, 3\n"
            "s_cselect_b32 s68, s68, 0x20000\n"
            "s_lshl_b32 s68, s68, 13\n"
            "s_add_u32 s68, s68, s54\n"
            "buffer_load_dword v52, v193, s[24:27], s68 offen\n"
            "s_and_b32 s68, s9, 31\n"
            "s_lshl_b32 s68, s68, 7\n"
            "s_cmp_eq_u32 s3, 3\n"
            "s_cselect_b32 s68, s68, 0x40000000\n"
            "v_lshlrev_b32 v81, 5, v193\n"
            "buffer_load_dword v48, v81, s[44:47], s68 offen\n"
            "buffer_load_dword v49, off, s[20:23], 0\n"
            "Lx1_%=:\n"
            "v_mov_b64 v[82:83], 0\n"
            "v_mov_b64 v[84:85], 0\n"
            "v_lshlrev_b32 v81, 4, v0\n"
            "s_cmp_lt_u32 s3, 4\n"
            "s_cselect_b32 s68, 0, 71680\n"
            "v_add_u32 v86, s68, v81\n"
            "ds_write_b128 v86, v[82:85]\n"
            "s_cmov_b32 s68, 79872\n"
            "v_add_u32 v86, s68, v81\n"
            "ds_write_b128 v86, v[82:85]\n"
            "v_and_b32 v196, 15, v0\n"
            "v_mov_b32 v209, 1\n"
            "s_cmp_lg_u32 s3, 0\n"
            "s_cbranch_scc1 Lnk_%=\n"
            "v_lshlrev_b32 v81, 2, v243\n"
            "v_sub_u32 v82, 127, v243\n"
            "ds_write_b32 v81, v82 offset:96\n"
            "v_lshlrev_b32 v81, 2, v196\n"
            "v_sub_u32 v82, 63, v196\n"
            "ds_write_b32 v81, v82 offset:352\n"
            "v_bfrev_b32 v82, 1\n"
            "ds_write2_b32 v219, v82, v82 offset0:20 offset1:21\n"
            "Lnk_%=:\n"
            "s_sub_i32 s96, s49, s95\n"
            "s_min_i32 s96, s96, s50\n"
            "s_max_i32 s96, s96, 0\n"
            "s_and_b32 s12, s3, 3\n"
            "s_lshl_b32 s12, s12, 3\n"
            "s_lshl_b32 s68, 1, s12\n"
            "v_mov_b32 v80, s68\n"
            "s_sub_u32 s68, s68, 1\n"
            "s_and_b32 s68, s68, 0x10101010\n"
            "s_cmp_lt_u32 s3, 4\n"
            "s_cselect_b32 s13, s68, 0x10101010\n"
            "s_cselect_b32 s14, 0, s68\n"
            "s_mov_b32 s39, 0x10101010\n"
            "v_cmp_gt_u32 s[72:73], s96, v54\n"
            "v_lshlrev_b32 v77, 9, v54\n"
            "v_add_u32 v81, 1, v54\n"
            "v_cmp_gt_u32 s[74:75], s96, v81\n"
            "v_lshlrev_b32 v78, 9, v81\n"
            "v_add_u32 v81, 2, v54\n"
            "v_cmp_gt_u32 s[76:77], s96, v81\n"
            "v_lshlrev_b32 v79, 9, v81\n"
            "s_mul_i32 s10, s9, s48\n"
            "s_add_u32 s10, s10, 127\n"
            "s_add_u32 s11, s10, s48\n"
            "s_lshr_b32 s10, s10, 7\n"
            "s_lshr_b32 s11, s11, 7\n"
            "s_sub_u32 s11, s11, s10\n"
            "v_lshlrev_b32 v192, 4, v243\n"
            "v_lshlrev_b32 v194, 3, v243\n"
            "v_lshrrev_b32 v195, 4, v243\n"
            "v_lshlrev_b32 v214, 4, v196\n"
            "v_and_b32 v249, 3, v0\n"
            "v_lshlrev_b32 v250, 4, v249\n"
            "v_mov_b32 v251, 80\n"
            "v_lshrrev_b32 v83, 2, v196\n"
            "s_lshl_b32 s68, s3, 9\n"
            "s_add_u32 s68, s68, 61440\n"
            "v_add_u32 v212, s68, v194\n"
            "v_lshl_add_u32 v213, v195, 7, s68\n"
            "v_lshl_add_u32 v213, v249, 5, v213\n"
            "v_lshl_add_u32 v213, v83, 3, v213\n"
            "v_mov_b32 v224, 0x3f80\n"
            "v_mov_b32 v225, 0\n"
            "v_lshlrev_b64 v[224:225], v250, v[224:225]\n"
            "s_mul_i32 s68, s3, 4352\n"
            "s_sub_u32 s38, s3, 3\n"
            "s_max_i32 s38, s38, 0\n"
            "s_lshl_b32 s38, s38, 12\n"
            "s_add_u32 s68, s68, s38\n"
            "s_add_u32 s68, s68, 71680\n"
            "v_mov_b32 v81, s68\n"
            "v_add_u32 v231, s68, v192\n"
            "v_add_u32 v239, 0xfffff000, v231\n"
            "v_mov_b32 v236, v231\n"
            "s_add_u32 s38, s3, 3\n"
            "s_and_b32 s38, s38, 3\n"
            "s_lshl2_add_u32 s41, s38, 16\n"
            "s_mul_i32 s38, s38, 8448\n"
            "s_add_u32 s38, s38, 89088\n"
            "v_add_u32 v252, s38, v192\n"
            "s_movk_i32 s38, 272\n"
            "v_mad_u32_u24 v227, v196, s38, v81\n"
            "v_lshl_add_u32 v227, v195, 4, v227\n"
            "s_movk_i32 s38, 1088\n"
            "v_mad_u32_u24 v226, v195, s38, v81\n"
            "v_lshl_add_u32 v226, v196, 3, v226\n"
            "v_add_u32 v228, 122880, v192\n"
            "v_and_b32 v253, 1, v195\n"
            "v_lshl_add_u32 v253, v196, 1, v253\n"
            "v_lshlrev_b32 v253, 2, v253\n"
            "v_and_b32 v229, 1, v0\n"
            "v_lshlrev_b32 v229, 4, v229\n"
            "v_lshlrev_b32 v81, 25, v0\n"
            "v_and_b32 v81, 2.0, v81\n"
            "v_or_b32 v229, v229, v81\n"
            "v_bfe_u32 v206, v0, 1, 2\n"
            "v_lshlrev_b32 v207, 4, v206\n"
            "v_bfe_u32 v83, v0, 3, 2\n"
            "s_mul_i32 s68, s3, 768\n"
            "s_add_u32 s68, s68, 65536\n"
            "v_add_u32 v247, s68, v194\n"
            "v_lshl_add_u32 v81, v249, 3, s68\n"
            "v_lshl_add_u32 v237, v195, 1, v81\n"
            "v_add_u32 v237, 32, v237\n"
            "v_lshl_add_u32 v81, v206, 3, s68\n"
            "v_lshl_add_u32 v238, v83, 1, v81\n"
            "v_add_u32 v238, 64, v238\n"
            "v_and_b32 v81, 7, v0\n"
            "v_lshlrev_b32 v82, 2, v81\n"
            "s_mov_b32 s68, 1982153488\n"
            "v_bfe_u32 v240, s68, v82, 4\n"
            "v_lshlrev_b32 v240, 4, v240\n"
            "v_add_u32 v240, 2304, v240\n"
            "s_mov_b32 s68, 2205745445\n"
            "v_bfe_u32 v241, s68, v82, 4\n"
            "v_lshlrev_b32 v241, 4, v241\n"
            "v_add_u32 v241, 2048, v241\n"
            "s_mov_b32 s68, 2305097727\n"
            "v_bfe_u32 v242, s68, v82, 4\n"
            "v_lshlrev_b32 v242, 4, v242\n"
            "v_add_u32 v242, 2048, v242\n"
            "v_cmp_lt_u32 vcc, 5, v81\n"
            "v_add_u32 v82, 256, v242\n"
            "s_nop 0\n"
            "v_cndmask_b32 v242, v242, v82, vcc\n"
            "v_cmp_lt_u32 vcc, 3, v81\n"
            "s_nop 1\n"
            "v_cndmask_b32 v242, 8, v242, vcc\n"
            "s_mov_b32 s68, 1998716928\n"
            "v_lshlrev_b32 v82, 2, v81\n"
            "v_bfe_u32 v208, s68, v82, 4\n"
            "s_add_u32 s38, s3, 1\n"
            "s_and_b32 s38, s38, 3\n"
            "s_add_u32 s38, s38, 4\n"
            "v_readlane_b32 s64, v240, s3\n"
            "v_readlane_b32 s65, v241, s3\n"
            "v_readlane_b32 s66, v242, s3\n"
            "v_readlane_b32 s40, v242, s38\n"
            "v_lshl_add_u32 v244, v195, 2, s64\n"
            "v_lshl_add_u32 v245, v195, 2, s65\n"
            "v_lshl_add_u32 v246, v195, 2, s66\n"
            "v_lshl_add_u32 v248, v83, 2, s64\n"
            "s_add_u32 s64, s64, 512\n"
            "s_add_u32 s65, s65, 512\n"
            "s_add_u32 s66, s66, 512\n"
            "s_add_u32 s40, s40, 512\n"
            "s_lshl_b32 s68, s2, 7\n"
            "s_add_u32 s38, s68, 4096\n"
            "s_add_u32 s56, s20, s38\n"
            "s_addc_u32 s57, s21, 0\n"
            "s_xor_b32 s38, s38, 512\n"
            "s_add_u32 s58, s20, s38\n"
            "s_addc_u32 s59, s21, 0\n"
            "s_cmp_lt_u32 s3, 6\n"
            "s_movk_i32 s60, 78\n"
            "s_cmovk_i32 s60, 150\n"
            "s_mov_b32 s61, 4294961748\n"
            "s_cmov_b32 s61, 4294945104\n"
            "s_movk_i32 s62, 0x3862\n"
            "s_cmov_b32 s62, 0x9062\n"
            "s_cmp_lt_u32 s3, 4\n"
            "s_cmovk_i32 s60, 590\n"
            "s_cmov_b32 s61, 4294612224\n"
            "s_cmovk_i32 s62, 0x82\n"
            "s_mov_b32 s63, 5804032\n"
            "s_and_b32 s68, s9, 127\n"
            "s_lshl3_add_u32 s68, s68, s3\n"
            "s_mul_i32 s68, s68, 512\n"
            "s_add_u32 s67, s68, s55\n"
            "s_add_u32 s67, s67, 3145728\n"
            "s_lshr_b32 s38, s3, 2\n"
            "s_lshl_b32 s38, s38, 10\n"
            "s_lshl_b32 s15, s3, 9\n"
            "s_add_u32 s15, s15, 24576\n"
            "v_add_u32 v94, s15, v194\n"
            "s_lshr_b32 s68, s38, 1\n"
            "s_sub_u32 s15, s15, s68\n"
            "s_and_b32 s68, s3, 1\n"
            "s_lshl1_add_u32 s68, s2, s68\n"
            "s_lshl_b32 s68, s68, 10\n"
            "s_add_u32 s36, s68, 36864\n"
            "v_mov_b32 v93, 268439552\n"
            "v_lshlrev_b32 v95, 4, v0\n"
            "s_mul_i32 s68, s2, 20480\n"
            "s_add_u32 s68, s68, 557056\n"
            "s_add_u32 s4, s20, s68\n"
            "s_addc_u32 s5, s21, 0\n"
            "s_lshl_b32 s6, s96, 4\n"
            "s_add_u32 s6, s6, 4223\n"
            "s_and_b32 s6, s6, 0xffffff80\n"
            "s_mov_b32 s7, 0x20000\n"
            "v_mov_b64 v[0:1], 0\n"
            "v_mov_b64 v[2:3], 0\n"
            "v_mov_b64 v[4:5], 0\n"
            "v_mov_b64 v[6:7], 0\n"
            "v_mov_b64 v[8:9], 0\n"
            "v_mov_b64 v[10:11], 0\n"
            "v_mov_b64 v[12:13], 0\n"
            "v_mov_b64 v[14:15], 0\n"
            "v_mov_b64 v[16:17], 0\n"
            "v_mov_b64 v[18:19], 0\n"
            "v_mov_b64 v[20:21], 0\n"
            "v_mov_b64 v[22:23], 0\n"
            "v_mov_b64 v[24:25], 0\n"
            "v_mov_b64 v[26:27], 0\n"
            "v_mov_b64 v[28:29], 0\n"
            "v_mov_b64 v[30:31], 0\n"
            "s_waitcnt lgkmcnt(0)\n"
            "s_barrier\n"
            "s_waitcnt vmcnt(6)\n"
            "v_lshlrev_b32 v81, s52, v32\n"
            "v_lshlrev_b32 v84, s52, v36\n"
            "v_lshlrev_b32 v82, s52, v33\n"
            "v_lshlrev_b32 v85, s52, v37\n"
            "v_lshlrev_b32 v83, s52, v34\n"
            "v_lshlrev_b32 v86, s52, v38\n"
            "v_mul_hi_u32_u24 v81, s51, v81\n"
            "v_mul_hi_u32_u24 v84, s51, v84\n"
            "v_lshl_add_u32 v68, v81, 2, s38\n"
            "v_and_b32 v81, 64, v84\n"
            "v_lshl_add_u32 v68, v81, 3, v68\n"
            "s_mov_b64 exec, s[72:73]\n"
            "ds_add_rtn_u32 v71, v68, v80 offset:2048\n"
            "s_mov_b64 exec, -1\n"
            "v_mul_hi_u32_u24 v82, s51, v82\n"
            "v_mul_hi_u32_u24 v85, s51, v85\n"
            "v_lshl_add_u32 v69, v82, 2, s38\n"
            "v_and_b32 v82, 64, v85\n"
            "v_lshl_add_u32 v69, v82, 3, v69\n"
            "s_mov_b64 exec, s[74:75]\n"
            "ds_add_rtn_u32 v72, v69, v80 offset:2048\n"
            "s_mov_b64 exec, -1\n"
            "v_mul_hi_u32_u24 v83, s51, v83\n"
            "v_mul_hi_u32_u24 v86, s51, v86\n"
            "v_lshl_add_u32 v70, v83, 2, s38\n"
            "v_and_b32 v83, 64, v86\n"
            "v_lshl_add_u32 v70, v83, 3, v70\n"
            "s_mov_b64 exec, s[76:77]\n"
            "ds_add_rtn_u32 v73, v70, v80 offset:2048\n"
            "s_mov_b64 exec, -1\n"
            "v_bfe_u32 v84, v84, 4, 2\n"
            "v_and_b32 v81, 127, v32\n"
            "v_lshl_or_b32 v81, v81, 2, v84\n"
            "v_bfe_u32 v85, v85, 4, 2\n"
            "v_and_b32 v82, 127, v33\n"
            "v_lshl_or_b32 v82, v82, 2, v85\n"
            "v_bfe_u32 v86, v86, 4, 2\n"
            "v_and_b32 v83, 127, v34\n"
            "v_lshl_or_b32 v83, v83, 2, v86\n"
            "s_waitcnt vmcnt(5)\n"
            "v_readfirstlane_b32 s90, v40\n"
            "s_mul_i32 s90, s90, 1664525\n"
            "s_add_u32 s90, s90, 1013904223\n"
            "s_lshl_b32 s93, s90, 14\n"
            "s_and_b32 s94, s90, 0xfff00000\n"
            "s_waitcnt lgkmcnt(0)\n"
            "s_barrier\n"
            "ds_read_b128 v[128:131], v192 offset:2048\n"
            "ds_read_b128 v[132:135], v192 offset:3072\n"
            "v_or3_b32 v59, v81, v77, s94\n"
            "v_or_b32 v57, s93, v36\n"
            "v_lshrrev_b32 v74, 1, v68\n"
            "v_add_u32 v74, s15, v74\n"
            "v_bfe_u32 v71, v71, s12, 8\n"
            "v_or3_b32 v63, v82, v78, s94\n"
            "v_or_b32 v61, s93, v37\n"
            "v_lshrrev_b32 v75, 1, v69\n"
            "v_add_u32 v75, s15, v75\n"
            "v_bfe_u32 v72, v72, s12, 8\n"
            "v_or3_b32 v67, v83, v79, s94\n"
            "v_or_b32 v65, s93, v38\n"
            "v_lshrrev_b32 v76, 1, v70\n"
            "v_add_u32 v76, s15, v76\n"
            "v_bfe_u32 v73, v73, s12, 8\n"
            "s_waitcnt lgkmcnt(1)\n"
            "v_dot4_u32_u8 v136, v128, s39, 0\n"
            "v_dot4_u32_u8 v137, v129, s39, 0\n"
            "v_dot4_u32_u8 v138, v130, s39, 0\n"
            "v_dot4_u32_u8 v139, v131, s39, 0\n"
            "s_waitcnt lgkmcnt(0)\n"
            "v_dot4_u32_u8 v136, v132, s39, v136\n"
            "v_dot4_u32_u8 v137, v133, s39, v137\n"
            "v_dot4_u32_u8 v138, v134, s39, v138\n"
            "v_dot4_u32_u8 v139, v135, s39, v139\n"
            "v_dot4_u32_u8 v140, v128, s13, 0\n"
            "v_dot4_u32_u8 v141, v129, s13, v136\n"
            "v_dot4_u32_u8 v142, v130, s13, 0\n"
            "v_dot4_u32_u8 v143, v131, s13, v138\n"
            "v_add_u32 v87, v136, v137\n"
            "v_add3_u32 v88, v87, v138, v139\n"
            "v_dot4_u32_u8 v140, v132, s14, v140\n"
            "v_dot4_u32_u8 v141, v133, s14, v141\n"
            "s_mov_b32 s68, 0x10001\n"
            "v_add_u32_dpp v89, v88, v88 row_shr:1 row_mask:0xf bank_mask:0xf bound_ctrl:1\n"
            "v_dot4_u32_u8 v142, v134, s14, v142\n"
            "v_dot4_u32_u8 v143, v135, s14, v143\n"
            "v_add_u32_dpp v89, v89, v89 row_shr:2 row_mask:0xf bank_mask:0xf bound_ctrl:1\n"
            "v_lshl_add_u32 v140, v141, 16, v140\n"
            "v_mad_u32_u24 v90, v87, s68, v93\n"
            "v_add_u32_dpp v89, v89, v89 row_shr:4 row_mask:0xf bank_mask:0xf bound_ctrl:1\n"
            "v_add_u32 v140, v140, v93\n"
            "v_lshl_add_u32 v141, v143, 16, v142\n"
            "v_add_u32_dpp v89, v89, v89 row_shr:8 row_mask:0xf bank_mask:0xf bound_ctrl:1\n"
            "v_add_u32 v141, v141, v90\n"
            "s_and_b32 s38, s3, 6\n"
            "v_add_u32_dpp v89, v89, v89 row_bcast:15 row_mask:0xa bank_mask:0xf\n"
            "s_cmp_lg_u32 s38, 4\n"
            "s_nop 0\n"
            "v_add_u32_dpp v89, v89, v89 row_bcast:31 row_mask:0xc bank_mask:0xf\n"
            "v_sub_u32 v89, v89, v88\n"
            "v_mad_u32_u24 v140, v89, s68, v140\n"
            "v_mad_u32_u24 v141, v89, s68, v141\n"
            "ds_write_b64 v94, v[140:141]\n"
            "ds_read_u16 v74, v74\n"
            "ds_read_u16 v75, v75\n"
            "ds_read_u16 v76, v76\n"
            "s_cbranch_scc1 Lnt_%=\n"
            "s_bitcmp1_b32 s3, 0\n"
            "s_cbranch_scc0 Lt0_%=\n"
            "v_add_u32 v89, v89, v87\n"
            "v_mov_b64 v[136:137], v[138:139]\n"
            "Lt0_%=:\n"
            "v_lshl_add_u32 v128, v136, 16, v89\n"
            "v_add_u32 v89, v89, v136\n"
            "v_lshl_add_u32 v130, v137, 16, v89\n"
            "v_mov_b32 v129, s90\n"
            "v_mov_b32 v131, s90\n"
            "buffer_store_dwordx4 v[128:131], v192, s[20:23], s36 offen sc1\n"
            "s_waitcnt vmcnt(4)\n"
            "s_branch Lt1_%=\n"
            "Lnt_%=:\n"
            "s_waitcnt vmcnt(3)\n"
            "Lt1_%=:\n"
            "v_mov_b32 v56, v42\n"
            "v_mov_b32 v58, v43\n"
            "v_mov_b32 v60, v44\n"
            "v_mov_b32 v62, v45\n"
            "v_mov_b32 v64, v46\n"
            "v_mov_b32 v66, v47\n"
            "s_waitcnt lgkmcnt(0)\n"
            "v_lshl_add_u32 v74, v71, 4, v74\n"
            "v_lshl_add_u32 v75, v72, 4, v75\n"
            "v_lshl_add_u32 v76, v73, 4, v76\n"
            "s_mov_b64 exec, s[72:73]\n"
            "ds_write_b128 v74, v[56:59]\n"
            "s_mov_b64 exec, s[74:75]\n"
            "ds_write_b128 v75, v[60:63]\n"
            "s_mov_b64 exec, s[76:77]\n"
            "ds_write_b128 v76, v[64:67]\n"
            "s_mov_b64 exec, -1\n"
            "s_cmp_lt_u32 s3, 4\n"
            "s_waitcnt lgkmcnt(0)\n"
            "s_barrier\n"
            "s_cbranch_scc1 Lcol_%=\n"
            "ds_read_b128 v[56:59], v95 offset:0\n"
            "ds_read_b128 v[60:63], v95 offset:4096\n"
            "ds_read_b128 v[64:67], v95 offset:8192\n"
            "ds_read_b128 v[68:71], v95 offset:12288\n"
            "ds_read_b128 v[72:75], v95 offset:16384\n"
            "s_movk_i32 s72, 4096\n"
            "s_movk_i32 s73, 8192\n"
            "s_movk_i32 s74, 12288\n"
            "s_movk_i32 s75, 16384\n"
            "s_waitcnt lgkmcnt(4)\n"
            "buffer_store_dwordx4 v[56:59], v95, s[4:7], 0 offen sc1\n"
            "s_waitcnt lgkmcnt(3)\n"
            "buffer_store_dwordx4 v[60:63], v95, s[4:7], s72 offen sc1\n"
            "s_waitcnt lgkmcnt(2)\n"
            "buffer_store_dwordx4 v[64:67], v95, s[4:7], s73 offen sc1\n"
            "s_waitcnt lgkmcnt(1)\n"
            "buffer_store_dwordx4 v[68:71], v95, s[4:7], s74 offen sc1\n"
            "s_waitcnt lgkmcnt(0)\n"
            "buffer_store_dwordx4 v[72:75], v95, s[4:7], s75 offen sc1\n"
            "s_setprio 2\n"
            "s_and_b32 s68, s3, 2\n"
            "s_lshl_b32 s68, s68, 13\n"
            "s_and_b32 s76, s3, 1\n"
            "s_lshl_b32 s76, s76, 12\n"
            "s_add_u32 s68, s68, s76\n"
            "v_lshl_add_u32 v81, v195, 10, s68\n"
            "v_add_u32 v81, v81, v214\n"
            "buffer_load_dwordx4 v[128:131], v81, s[44:47], 0 offen offset:0\n"
            "buffer_load_dwordx4 v[132:135], v81, s[44:47], 0 offen offset:256\n"
            "buffer_load_dwordx4 v[136:139], v81, s[44:47], s73 offen offset:0\n"
            "buffer_load_dwordx4 v[140:143], v81, s[44:47], s73 offen offset:256\n"
            "buffer_load_dwordx4 v[144:147], v81, s[44:47], 0 offen offset:512\n"
            "buffer_load_dwordx4 v[148:151], v81, s[44:47], 0 offen offset:768\n"
            "buffer_load_dwordx4 v[152:155], v81, s[44:47], s73 offen offset:512\n"
            "buffer_load_dwordx4 v[156:159], v81, s[44:47], s73 offen offset:768\n"
            "v_lshlrev_b32 v81, 4, v95\n"
            "v_and_b32 v81, 0x1800, v81\n"
            "v_lshrrev_b32 v82, 3, v95\n"
            "v_and_b32 v82, 0x100, v82\n"
            "v_lshlrev_b32 v83, 1, v95\n"
            "v_and_b32 v83, 0xe0, v83\n"
            "v_lshrrev_b32 v84, 7, v95\n"
            "v_and_b32 v84, 12, v84\n"
            "v_or3_b32 v81, v81, v82, v83\n"
            "v_add_u32 v84, 114432, v84\n"
            "v_add_u32 v81, v81, v84\n"
            "s_mov_b32 s78, 0xbfb8aa3b\n"
            "s_waitcnt vmcnt(14)\n"
            "v_lshlrev_b32 v160, 16, v48\n"
            "v_lshlrev_b32 v161, 16, v50\n"
            "v_mul_f32 v160, s78, v160\n"
            "v_mul_f32 v161, s78, v161\n"
            "v_cvt_pk_bf16_f32 v164, v160, v161\n"
            "v_lshlrev_b32 v162, 16, v164\n"
            "v_and_b32 v163, 0xffff0000, v164\n"
            "v_sub_f32 v160, v160, v162\n"
            "v_sub_f32 v161, v161, v163\n"
            "v_cvt_pk_bf16_f32 v165, v160, v161\n"
            "ds_write_b32 v81, v164 offset:0\n"
            "ds_write_b32 v81, v165 offset:512\n"
            "v_and_b32 v160, 0xffff0000, v48\n"
            "v_and_b32 v161, 0xffff0000, v50\n"
            "v_mul_f32 v160, s78, v160\n"
            "v_mul_f32 v161, s78, v161\n"
            "v_cvt_pk_bf16_f32 v166, v160, v161\n"
            "v_lshlrev_b32 v162, 16, v166\n"
            "v_and_b32 v163, 0xffff0000, v166\n"
            "v_sub_f32 v160, v160, v162\n"
            "v_sub_f32 v161, v161, v163\n"
            "v_cvt_pk_bf16_f32 v167, v160, v161\n"
            "ds_write_b32 v81, v166 offset:1024\n"
            "ds_write_b32 v81, v167 offset:1536\n"
            "v_lshlrev_b32 v160, 16, v49\n"
            "v_lshlrev_b32 v161, 16, v51\n"
            "v_mul_f32 v160, s78, v160\n"
            "v_mul_f32 v161, s78, v161\n"
            "v_cvt_pk_bf16_f32 v168, v160, v161\n"
            "v_lshlrev_b32 v162, 16, v168\n"
            "v_and_b32 v163, 0xffff0000, v168\n"
            "v_sub_f32 v160, v160, v162\n"
            "v_sub_f32 v161, v161, v163\n"
            "v_cvt_pk_bf16_f32 v169, v160, v161\n"
            "ds_write_b32 v81, v168 offset:16\n"
            "ds_write_b32 v81, v169 offset:528\n"
            "v_and_b32 v160, 0xffff0000, v49\n"
            "v_and_b32 v161, 0xffff0000, v51\n"
            "v_mul_f32 v160, s78, v160\n"
            "v_mul_f32 v161, s78, v161\n"
            "v_cvt_pk_bf16_f32 v170, v160, v161\n"
            "v_lshlrev_b32 v162, 16, v170\n"
            "v_and_b32 v163, 0xffff0000, v170\n"
            "v_sub_f32 v160, v160, v162\n"
            "v_sub_f32 v161, v161, v163\n"
            "v_cvt_pk_bf16_f32 v171, v160, v161\n"
            "ds_write_b32 v81, v170 offset:1040\n"
            "ds_write_b32 v81, v171 offset:1552\n"
            "s_cmp_lg_u32 s3, 4\n"
            "s_cbranch_scc1 Lncg_%=\n"
            "v_lshlrev_b32 v81, 2, v243\n"
            "s_waitcnt vmcnt(14)\n"
            "ds_write_b32 v81, v52 offset:61440\n"
            "v_lshrrev_b32 v81, 1, v243\n"
            "v_and_b32 v81, 6, v81\n"
            "v_lshl_add_u32 v81, v249, 5, v81\n"
            "ds_read_u16 v160, v81 offset:61440\n"
            "ds_read_u16 v161, v81 offset:61448\n"
            "ds_read_u16 v162, v81 offset:61456\n"
            "ds_read_u16 v163, v81 offset:61464\n"
            "s_mov_b32 s78, 0xbf317218\n"
            "s_waitcnt lgkmcnt(0)\n"
            "v_lshlrev_b32 v160, 16, v160\n"
            "v_lshlrev_b32 v161, 16, v161\n"
            "v_lshlrev_b32 v162, 16, v162\n"
            "v_lshlrev_b32 v163, 16, v163\n"
            "v_mul_f32 v160, s78, v160\n"
            "v_mul_f32 v161, s78, v161\n"
            "v_mul_f32 v162, s78, v162\n"
            "v_mul_f32 v163, s78, v163\n"
            "v_cvt_pk_bf16_f32 v168, v160, v161\n"
            "v_cvt_pk_bf16_f32 v169, v162, v163\n"
            "v_lshlrev_b32 v164, 16, v168\n"
            "v_and_b32 v165, 0xffff0000, v168\n"
            "v_lshlrev_b32 v166, 16, v169\n"
            "v_and_b32 v167, 0xffff0000, v169\n"
            "v_sub_f32 v160, v160, v164\n"
            "v_sub_f32 v161, v161, v165\n"
            "v_sub_f32 v162, v162, v166\n"
            "v_sub_f32 v163, v163, v167\n"
            "v_cvt_pk_bf16_f32 v170, v160, v161\n"
            "v_cvt_pk_bf16_f32 v171, v162, v163\n"
            "ds_write_b128 v192, v[168:171] offset:60416\n"
            "Lncg_%=:\n"
            "s_mov_b64 exec, 1\n"
            "ds_add_u32 v219, v209 offset:64\n"
            "s_mov_b64 exec, -1\n"
            "s_cmp_lg_u32 s3, 7\n"
            "s_cbranch_scc1 Lt2_%=\n"
            "s_sleep 16\n"
            "s_add_u32 s68, s69, 0\n"
            "buffer_load_dword v230, v193, s[28:31], s68 offen\n"
            "Lt2_%=:\n"
            "s_mul_i32 s68, s3, 27\n"
            "s_sub_u32 s68, s68, 108\n"
            "v_lshrrev_b32 v81, 1, v243\n"
            "v_add_u32 v81, s68, v81\n"
            "v_min_u32 v81, 79, v81\n"
            "v_lshlrev_b32 v180, 2, v81\n"
            "v_cmp_lt_u32 vcc, 39, v81\n"
            "v_and_b32 v82, 1, v243\n"
            "v_mul_u32_u24 v181, 80, v82\n"
            "v_add_u32 v82, 160, v181\n"
            "v_cndmask_b32 v181, v181, v82, vcc\n"
            "v_cndmask_b32 v82, 1, 0, vcc\n"
            "v_xor_b32 v82, s8, v82\n"
            "v_lshlrev_b32 v182, 8, v82\n"
            "v_mul_u32_u24 v183, 704, v81\n"
            "v_add_u32 v183, 4096, v183\n"
            "v_cmp_gt_u32 vcc, s11, v81\n"
            "v_add_lshl_u32 v184, v81, s10, 10\n"
            "v_bfrev_b32 v82, 1\n"
            "v_cndmask_b32 v184, v82, v184, vcc\n"
            "s_branch Lhpa_%=\n"
            "Lhp_%=:\n"
            "s_sleep 1\n"
            "Lhpa_%=:\n"
            "ds_read_b32 v220, v219 offset:64\n"
            "s_waitcnt lgkmcnt(0)\n"
            "v_readfirstlane_b32 s68, v220\n"
            "s_cmp_lt_u32 s68, 4\n"
            "s_cbranch_scc1 Lhp_%=\n"
            "v_add_u32 v81, 114432, v192\n"
            "ds_read_b128 v[96:99], v81 offset:0\n"
            "ds_read_b128 v[100:103], v81 offset:1024\n"
            "ds_read_b128 v[104:107], v81 offset:2048\n"
            "ds_read_b128 v[108:111], v81 offset:3072\n"
            "ds_read_b128 v[112:115], v81 offset:4096\n"
            "ds_read_b128 v[116:119], v81 offset:5120\n"
            "ds_read_b128 v[120:123], v81 offset:6144\n"
            "ds_read_b128 v[124:127], v81 offset:7168\n"
            "ds_read_b128 v[200:203], v192 offset:60416\n"
            "v_lshrrev_b32 v82, 2, v196\n"
            "s_lshl_b32 s68, s3, 10\n"
            "s_add_u32 s68, s68, 118784\n"
            "v_lshl_add_u32 v82, v82, 13, s68\n"
            "v_lshl_add_u32 v82, v195, 8, v82\n"
            "v_lshl_add_u32 v82, v249, 6, v82\n"
            "s_mov_b32 s78, 0x05040100\n"
            "s_mov_b32 s79, 0x07060302\n"
            "s_waitcnt vmcnt(1)\n"
            "s_cmp_eq_u32 s3, 7\n"
            "s_cbranch_scc1 Lt3_%=\n"
            "s_waitcnt vmcnt(0)\n"
            "Lt3_%=:\n"
            "v_perm_b32 v160, v132, v128, s78\n"
            "v_perm_b32 v161, v140, v136, s78\n"
            "v_perm_b32 v162, v148, v144, s78\n"
            "v_perm_b32 v163, v156, v152, s78\n"
            "ds_write_b128 v82, v[160:163] offset:0\n"
            "v_perm_b32 v164, v132, v128, s79\n"
            "v_perm_b32 v165, v140, v136, s79\n"
            "v_perm_b32 v166, v148, v144, s79\n"
            "v_perm_b32 v167, v156, v152, s79\n"
            "ds_write_b128 v82, v[164:167] offset:4096\n"
            "v_perm_b32 v168, v133, v129, s78\n"
            "v_perm_b32 v169, v141, v137, s78\n"
            "v_perm_b32 v170, v149, v145, s78\n"
            "v_perm_b32 v171, v157, v153, s78\n"
            "ds_write_b128 v82, v[168:171] offset:16\n"
            "v_perm_b32 v172, v133, v129, s79\n"
            "v_perm_b32 v173, v141, v137, s79\n"
            "v_perm_b32 v174, v149, v145, s79\n"
            "v_perm_b32 v175, v157, v153, s79\n"
            "ds_write_b128 v82, v[172:175] offset:4112\n"
            "v_perm_b32 v160, v134, v130, s78\n"
            "v_perm_b32 v161, v142, v138, s78\n"
            "v_perm_b32 v162, v150, v146, s78\n"
            "v_perm_b32 v163, v158, v154, s78\n"
            "ds_write_b128 v82, v[160:163] offset:32\n"
            "v_perm_b32 v164, v134, v130, s79\n"
            "v_perm_b32 v165, v142, v138, s79\n"
            "v_perm_b32 v166, v150, v146, s79\n"
            "v_perm_b32 v167, v158, v154, s79\n"
            "ds_write_b128 v82, v[164:167] offset:4128\n"
            "v_perm_b32 v168, v135, v131, s78\n"
            "v_perm_b32 v169, v143, v139, s78\n"
            "v_perm_b32 v170, v151, v147, s78\n"
            "v_perm_b32 v171, v159, v155, s78\n"
            "ds_write_b128 v82, v[168:171] offset:48\n"
            "v_perm_b32 v172, v135, v131, s79\n"
            "v_perm_b32 v173, v143, v139, s79\n"
            "v_perm_b32 v174, v151, v147, s79\n"
            "v_perm_b32 v175, v159, v155, s79\n"
            "ds_write_b128 v82, v[172:175] offset:4144\n"
            "s_cmp_lg_u32 s3, 7\n"
            "s_cbranch_scc1 Lsort_%=\n"
            "s_mov_b64 exec, 1\n"
            "ds_add_u32 v219, v209 offset:48\n"
            "s_mov_b64 exec, -1\n"
            "s_branch Lria_%=\n"
            "Lri_%=:\n"
            "s_sleep 1\n"
            "Lria_%=:\n"
            "ds_read_b32 v220, v219 offset:60\n"
            "s_waitcnt lgkmcnt(0)\n"
            "v_readfirstlane_b32 s68, v220\n"
            "s_cmp_lt_u32 s68, 4\n"
            "s_cbranch_scc1 Lri_%=\n"
            "s_add_u32 s68, s54, 24576\n"
            "s_nop 1\n"
            "buffer_load_dword v230, v193, s[24:27], s68 offen\n"
            "s_add_u32 s68, s54, 32768\n"
            "buffer_load_dword v230, v193, s[24:27], s68 offen\n"
            "s_add_u32 s68, s69, 1048576\n"
            "buffer_load_dword v230, v193, s[28:31], s68 offen\n"
            "s_add_u32 s68, s69, 2097152\n"
            "buffer_load_dword v230, v193, s[28:31], s68 offen\n"
            "s_branch Lw7a_%=\n"
            "Lw7_%=:\n"
            "s_sleep 1\n"
            "Lw7a_%=:\n"
            "ds_read_b32 v220, v219 offset:44\n"
            "s_waitcnt lgkmcnt(0)\n"
            "v_readfirstlane_b32 s68, v220\n"
            "s_cmp_lt_u32 s68, 4\n"
            "s_cbranch_scc1 Lw7_%=\n"
            "s_setprio 3\n"
            "s_branch Lmain_%=\n"
            "Lsort_%=:\n"
            "s_branch Lc3a_%=\n"
            "Lc3_%=:\n"
            "s_sleep 1\n"
            "Lc3a_%=:\n"
            "ds_read_b32 v220, v219 offset:44\n"
            "s_waitcnt lgkmcnt(0)\n"
            "v_readfirstlane_b32 s68, v220\n"
            "s_cmp_lt_u32 s68, 3\n"
            "s_cbranch_scc1 Lc3_%=\n"
            "s_setprio 3\n"
            "Lc4_%=:\n"
            "ds_read_b32 v220, v219 offset:44\n"
            "s_waitcnt lgkmcnt(0)\n"
            "v_readfirstlane_b32 s68, v220\n"
            "s_cmp_lt_u32 s68, 4\n"
            "s_cbranch_scc1 Lc4_%=\n"
            "ds_read_b32 v160, v180 offset:96\n"
            "ds_read_b128 v[128:131], v181 offset:96\n"
            "ds_read_b128 v[132:135], v181 offset:112\n"
            "ds_read_b128 v[136:139], v181 offset:128\n"
            "ds_read_b128 v[140:143], v181 offset:144\n"
            "ds_read_b128 v[144:147], v181 offset:160\n"
            "s_waitcnt lgkmcnt(4)\n"
            "v_sub_u32 v128, v160, v128\n"
            "v_sub_u32 v129, v160, v129\n"
            "v_sub_u32 v130, v160, v130\n"
            "v_sub_u32 v131, v160, v131\n"
            "v_lshrrev_b32 v161, 31, v128\n"
            "v_lshrrev_b32 v162, 31, v129\n"
            "v_alignbit_b32 v161, v161, v130, 31\n"
            "v_alignbit_b32 v162, v162, v131, 31\n"
            "v_lshrrev_b32 v163, 7, v160\n"
            "s_waitcnt lgkmcnt(3)\n"
            "v_sub_u32 v132, v160, v132\n"
            "v_sub_u32 v133, v160, v133\n"
            "v_sub_u32 v134, v160, v134\n"
            "v_sub_u32 v135, v160, v135\n"
            "v_alignbit_b32 v161, v161, v132, 31\n"
            "v_alignbit_b32 v162, v162, v133, 31\n"
            "v_alignbit_b32 v161, v161, v134, 31\n"
            "v_alignbit_b32 v162, v162, v135, 31\n"
            "v_cmp_lt_u32 vcc, 44, v163\n"
            "v_max_u32 v164, 8, v163\n"
            "s_waitcnt lgkmcnt(2)\n"
            "v_sub_u32 v136, v160, v136\n"
            "v_sub_u32 v137, v160, v137\n"
            "v_sub_u32 v138, v160, v138\n"
            "v_sub_u32 v139, v160, v139\n"
            "v_alignbit_b32 v161, v161, v136, 31\n"
            "v_alignbit_b32 v162, v162, v137, 31\n"
            "v_alignbit_b32 v161, v161, v138, 31\n"
            "v_alignbit_b32 v162, v162, v139, 31\n"
            "v_mov_b32 v165, 255\n"
            "v_lshl_or_b32 v183, v163, 16, v183\n"
            "s_waitcnt lgkmcnt(1)\n"
            "v_sub_u32 v140, v160, v140\n"
            "v_sub_u32 v141, v160, v141\n"
            "v_sub_u32 v142, v160, v142\n"
            "v_sub_u32 v143, v160, v143\n"
            "v_alignbit_b32 v161, v161, v140, 31\n"
            "v_alignbit_b32 v162, v162, v141, 31\n"
            "v_alignbit_b32 v161, v161, v142, 31\n"
            "v_alignbit_b32 v162, v162, v143, 31\n"
            "v_cndmask_b32 v164, v164, v165, vcc\n"
            "s_waitcnt lgkmcnt(0)\n"
            "v_sub_u32 v144, v160, v144\n"
            "v_sub_u32 v145, v160, v145\n"
            "v_sub_u32 v146, v160, v146\n"
            "v_sub_u32 v147, v160, v147\n"
            "v_alignbit_b32 v161, v161, v144, 31\n"
            "v_alignbit_b32 v162, v162, v145, 31\n"
            "v_alignbit_b32 v161, v161, v146, 31\n"
            "v_alignbit_b32 v162, v162, v147, 31\n"
            "v_bcnt_u32_b32 v161, v161, 0\n"
            "v_bcnt_u32_b32 v161, v162, v161\n"
            "s_nop 1\n"
            "v_add_u32_dpp v161, v161, v161 quad_perm:[1,0,3,2] row_mask:0xf bank_mask:0xf\n"
            "v_lshl_add_u32 v182, v161, 2, v182\n"
            "ds_write2st64_b32 v182, v183, v164 offset0:10 offset1:8\n"
            "ds_write_b32 v182, v184 offset:3072\n"
            "s_mov_b64 exec, 1\n"
            "ds_add_u32 v219, v209 offset:48\n"
            "s_mov_b64 exec, -1\n"
            "s_lshl3_add_u32 s68, s2, s3\n"
            "s_cmp_lg_u32 s68, 4\n"
            "s_cbranch_scc1 Lmain_%=\n"
            "v_mov_b32 v160, s90\n"
            "s_mov_b64 exec, 1\n"
            "buffer_store_dword v160, off, s[20:23], 0 sc1\n"
            "s_mov_b64 exec, -1\n"
            "s_branch Lmain_%=\n"
            "Lcol_%=:\n"
            "s_lshl_b32 s38, s8, 7\n"
            "s_add_u32 s38, s38, s9\n"
            "s_bfe_u32 s68, s38, 0x10001\n"
            "s_lshl_b32 s68, s68, 10\n"
            "s_lshr_b32 s72, s38, 2\n"
            "s_lshl4_add_u32 s68, s72, s68\n"
            "s_and_b32 s72, s38, 1\n"
            "s_lshl3_add_u32 s68, s72, s68\n"
            "s_add_u32 s68, s68, 36864\n"
            "v_lshl_add_u32 v180, v95, 7, s68\n"
            "s_sleep 10\n"
            "buffer_load_dwordx2 v[142:143], v180, s[20:23], 0 offen sc1\n"
            "s_mul_i32 s17, s3, 1792\n"
            "s_add_u32 s17, s17, 75776\n"
            "v_lshl_add_u32 v181, v243, 2, s17\n"
            "s_mul_i32 s15, s3, 1310720\n"
            "s_add_u32 s15, s15, 561152\n"
            "s_add_u32 s16, s15, 4096\n"
            "s_mul_i32 s18, s3, 640\n"
            "s_add_u32 s18, s18, 72704\n"
            "s_lshl_b32 s19, s10, 2\n"
            "s_lshl_b32 s97, s50, 5\n"
            "s_mul_i32 s98, s97, s3\n"
            "s_lshl_b32 s98, s98, 6\n"
            "v_mul_u32_u24 v182, 704, v243\n"
            "v_add_u32 v182, 4096, v182\n"
            "v_add_u32 v184, 704, v182\n"
            "v_mul_u32_u24 v183, 704, v196\n"
            "v_add_u32 v183, 49152, v183\n"
            "v_add_u32 v185, 704, v183\n"
            "v_add_u32 v187, s18, v194\n"
            "v_lshl_add_u32 v188, v196, 3, s18\n"
            "s_mov_b32 s68, 16797696\n"
            "v_mul_lo_u32 v189, v243, s68\n"
            "v_mov_b32 v190, 128\n"
            "v_mov_b32 v191, 2.0\n"
            "v_mov_b32 v186, s98\n"
            "v_mov_b32 v197, v80\n"
            "s_branch Lcpa_%=\n"
            "Lcp_%=:\n"
            "s_sleep 1\n"
            "Lcpa_%=:\n"
            "ds_read_b32 v220, v219 offset:64\n"
            "s_waitcnt lgkmcnt(0)\n"
            "v_readfirstlane_b32 s68, v220\n"
            "s_cmp_lt_u32 s68, 4\n"
            "s_cbranch_scc1 Lcp_%=\n"
            "v_add_u32 v81, 114432, v192\n"
            "ds_read_b128 v[96:99], v81 offset:0\n"
            "ds_read_b128 v[100:103], v81 offset:1024\n"
            "ds_read_b128 v[104:107], v81 offset:2048\n"
            "ds_read_b128 v[108:111], v81 offset:3072\n"
            "ds_read_b128 v[112:115], v81 offset:4096\n"
            "ds_read_b128 v[116:119], v81 offset:5120\n"
            "ds_read_b128 v[120:123], v81 offset:6144\n"
            "ds_read_b128 v[124:127], v81 offset:7168\n"
            "ds_read_b128 v[200:203], v192 offset:60416\n"
            "s_mov_b32 s99, 0x400000\n"
            "Lpoll_%=:\n"
            "s_waitcnt vmcnt(0)\n"
            "v_cmp_ne_u32 vcc, s90, v143\n"
            "s_cbranch_vccnz Lpoll2_%=\n"
            "Lpoll3_%=:\n"
            "v_lshrrev_b32 v144, 18, v142\n"
            "v_and_b32 v146, 0xffff, v142\n"
            "v_cmp_ne_u32 s[86:87], 0, v144\n"
            "v_add_u32_dpp v145, v144, v144 row_shr:1 row_mask:0xf bank_mask:0xf bound_ctrl:1\n"
            "v_add_u32 v146, v146, v189\n"
            "v_cmp_lt_u32 s[88:89], 64, v144\n"
            "v_add_u32_dpp v145, v145, v145 row_shr:2 row_mask:0xf bank_mask:0xf bound_ctrl:1\n"
            "v_lshl_add_u32 v146, v144, 2, v146\n"
            "v_sub_u32 v147, s17, v144\n"
            "v_add_u32_dpp v145, v145, v145 row_shr:4 row_mask:0xf bank_mask:0xf bound_ctrl:1\n"
            "s_add_u32 s38, s17, -4\n"
            "s_nop 0\n"
            "v_add_u32_dpp v145, v145, v145 row_shr:8 row_mask:0xf bank_mask:0xf bound_ctrl:1\n"
            "s_nop 1\n"
            "v_add_u32_dpp v145, v145, v145 row_bcast:15 row_mask:0xa bank_mask:0xf\n"
            "s_nop 1\n"
            "v_add_u32_dpp v145, v145, v145 row_bcast:31 row_mask:0xc bank_mask:0xf\n"
            "v_cmp_ge_u32 vcc, 1792, v145\n"
            "v_readlane_b32 s37, v145, 63\n"
            "v_add_u32 v147, v147, v145\n"
            "v_add_u32 v148, s38, v145\n"
            "v_lshlrev_b32 v145, 2, v145\n"
            "v_and_b32 v148, -64, v148\n"
            "v_sub_u32 v146, v146, v145\n"
            "v_max_u32 v148, v148, v147\n"
            "s_and_b64 exec, vcc, s[86:87]\n"
            "ds_write_b32 v147, v146\n"
            "ds_write_b32 v148, v146\n"
            "s_mov_b64 exec, -1\n"
            "ds_read2st64_b32 v[74:75], v181 offset0:0 offset1:1\n"
            "ds_read2st64_b32 v[76:77], v181 offset0:2 offset1:3\n"
            "ds_read2st64_b32 v[78:79], v181 offset0:4 offset1:5\n"
            "ds_read_b32 v80, v181 offset:1536\n"
            "s_cmp_gt_u32 s37, 1792\n"
            "s_cbranch_scc1 Lover_%=\n"
            "Lover2_%=:\n"
            "s_lshr_b32 s37, s37, 2\n"
            "v_cmp_gt_i32 s[72:73], s37, v243\n"
            "s_sub_i32 s38, s37, 64\n"
            "v_cmp_gt_i32 s[74:75], s38, v243\n"
            "s_sub_i32 s38, s37, 128\n"
            "v_cmp_gt_i32 s[76:77], s38, v243\n"
            "s_sub_i32 s38, s37, 192\n"
            "v_cmp_gt_i32 s[78:79], s38, v243\n"
            "s_sub_i32 s38, s37, 256\n"
            "v_cmp_gt_i32 s[80:81], s38, v243\n"
            "s_sub_i32 s38, s37, 320\n"
            "v_cmp_gt_i32 s[82:83], s38, v243\n"
            "s_sub_i32 s38, s37, 384\n"
            "v_cmp_gt_i32 s[84:85], s38, v243\n"
            "v_cndmask_b32 v81, v191, v192, s[72:73]\n"
            "v_cndmask_b32 v82, v191, v192, s[74:75]\n"
            "v_cndmask_b32 v83, v191, v192, s[76:77]\n"
            "v_cndmask_b32 v84, v191, v192, s[78:79]\n"
            "v_cndmask_b32 v85, v191, v192, s[80:81]\n"
            "v_cndmask_b32 v86, v191, v192, s[82:83]\n"
            "v_cndmask_b32 v87, v191, v192, s[84:85]\n"
            "s_cmp_lg_u64 s[88:89], 0\n"
            "s_waitcnt lgkmcnt(0)\n"
            "v_max_u32_dpp v74, v74, v74 row_shr:1 row_mask:0xf bank_mask:0xf bound_ctrl:1\n"
            "v_max_u32_dpp v75, v75, v75 row_shr:1 row_mask:0xf bank_mask:0xf bound_ctrl:1\n"
            "v_max_u32_dpp v76, v76, v76 row_shr:1 row_mask:0xf bank_mask:0xf bound_ctrl:1\n"
            "v_max_u32_dpp v77, v77, v77 row_shr:1 row_mask:0xf bank_mask:0xf bound_ctrl:1\n"
            "v_max_u32_dpp v78, v78, v78 row_shr:1 row_mask:0xf bank_mask:0xf bound_ctrl:1\n"
            "v_max_u32_dpp v79, v79, v79 row_shr:1 row_mask:0xf bank_mask:0xf bound_ctrl:1\n"
            "v_max_u32_dpp v80, v80, v80 row_shr:1 row_mask:0xf bank_mask:0xf bound_ctrl:1\n"
            "v_max_u32_dpp v74, v74, v74 row_shr:2 row_mask:0xf bank_mask:0xf bound_ctrl:1\n"
            "v_max_u32_dpp v75, v75, v75 row_shr:2 row_mask:0xf bank_mask:0xf bound_ctrl:1\n"
            "v_max_u32_dpp v76, v76, v76 row_shr:2 row_mask:0xf bank_mask:0xf bound_ctrl:1\n"
            "v_max_u32_dpp v77, v77, v77 row_shr:2 row_mask:0xf bank_mask:0xf bound_ctrl:1\n"
            "v_max_u32_dpp v78, v78, v78 row_shr:2 row_mask:0xf bank_mask:0xf bound_ctrl:1\n"
            "v_max_u32_dpp v79, v79, v79 row_shr:2 row_mask:0xf bank_mask:0xf bound_ctrl:1\n"
            "v_max_u32_dpp v80, v80, v80 row_shr:2 row_mask:0xf bank_mask:0xf bound_ctrl:1\n"
            "v_max_u32_dpp v74, v74, v74 row_shr:4 row_mask:0xf bank_mask:0xf bound_ctrl:1\n"
            "v_max_u32_dpp v75, v75, v75 row_shr:4 row_mask:0xf bank_mask:0xf bound_ctrl:1\n"
            "v_max_u32_dpp v76, v76, v76 row_shr:4 row_mask:0xf bank_mask:0xf bound_ctrl:1\n"
            "v_max_u32_dpp v77, v77, v77 row_shr:4 row_mask:0xf bank_mask:0xf bound_ctrl:1\n"
            "v_max_u32_dpp v78, v78, v78 row_shr:4 row_mask:0xf bank_mask:0xf bound_ctrl:1\n"
            "v_max_u32_dpp v79, v79, v79 row_shr:4 row_mask:0xf bank_mask:0xf bound_ctrl:1\n"
            "v_max_u32_dpp v80, v80, v80 row_shr:4 row_mask:0xf bank_mask:0xf bound_ctrl:1\n"
            "v_max_u32_dpp v74, v74, v74 row_shr:8 row_mask:0xf bank_mask:0xf bound_ctrl:1\n"
            "v_max_u32_dpp v75, v75, v75 row_shr:8 row_mask:0xf bank_mask:0xf bound_ctrl:1\n"
            "v_max_u32_dpp v76, v76, v76 row_shr:8 row_mask:0xf bank_mask:0xf bound_ctrl:1\n"
            "v_max_u32_dpp v77, v77, v77 row_shr:8 row_mask:0xf bank_mask:0xf bound_ctrl:1\n"
            "v_max_u32_dpp v78, v78, v78 row_shr:8 row_mask:0xf bank_mask:0xf bound_ctrl:1\n"
            "v_max_u32_dpp v79, v79, v79 row_shr:8 row_mask:0xf bank_mask:0xf bound_ctrl:1\n"
            "v_max_u32_dpp v80, v80, v80 row_shr:8 row_mask:0xf bank_mask:0xf bound_ctrl:1\n"
            "s_cbranch_scc1 Llng_%=\n"
            "Llng2_%=:\n"
            "v_mad_u32_u24 v81, v74, 1, v81\n"
            "buffer_load_dwordx4 v[34:37], v81, s[20:23], s15 offen offset:0 sc1\n"
            "v_mad_u32_u24 v82, v75, 1, v82\n"
            "buffer_load_dwordx4 v[40:43], v82, s[20:23], s15 offen offset:1024 sc1\n"
            "v_mad_u32_u24 v83, v76, 1, v83\n"
            "buffer_load_dwordx4 v[46:49], v83, s[20:23], s15 offen offset:2048 sc1\n"
            "v_mad_u32_u24 v84, v77, 1, v84\n"
            "buffer_load_dwordx4 v[52:55], v84, s[20:23], s15 offen offset:3072 sc1\n"
            "v_mad_u32_u24 v85, v78, 1, v85\n"
            "buffer_load_dwordx4 v[58:61], v85, s[20:23], s16 offen offset:0 sc1\n"
            "v_mad_u32_u24 v86, v79, 1, v86\n"
            "buffer_load_dwordx4 v[64:67], v86, s[20:23], s16 offen offset:1024 sc1\n"
            "v_mad_u32_u24 v87, v80, 1, v87\n"
            "buffer_load_dwordx4 v[70:73], v87, s[20:23], s16 offen offset:2048 sc1\n"
            "s_mov_b64 exec, 1\n"
            "ds_add_u32 v219, v209 offset:60\n"
            "s_mov_b64 exec, -1\n"
            "v_lshrrev_b32 v88, 24, v74\n"
            "v_mad_u32_u24 v88, v88, s97, v186\n"
            "v_lshrrev_b32 v89, 24, v75\n"
            "v_mad_u32_u24 v89, v89, s97, v186\n"
            "v_lshrrev_b32 v90, 24, v76\n"
            "v_mad_u32_u24 v90, v90, s97, v186\n"
            "v_lshrrev_b32 v91, 24, v77\n"
            "v_mad_u32_u24 v91, v91, s97, v186\n"
            "v_lshrrev_b32 v92, 24, v78\n"
            "v_mad_u32_u24 v92, v92, s97, v186\n"
            "v_lshrrev_b32 v93, 24, v79\n"
            "v_mad_u32_u24 v93, v93, s97, v186\n"
            "v_lshrrev_b32 v94, 24, v80\n"
            "v_mad_u32_u24 v94, v94, s97, v186\n"
            "s_movk_i32 s38, 0x3fff\n"
            "s_mov_b32 s36, 0xfffff\n"
            "s_waitcnt vmcnt(6)\n"
            "Lchk0_%=:\n"
            "v_xor_b32 v35, s93, v35\n"
            "v_xor_b32 v37, s94, v37\n"
            "v_cmp_lt_u32 vcc, s38, v35\n"
            "v_cmp_lt_u32 s[86:87], s36, v37\n"
            "v_subrev_u32 v135, s19, v37\n"
            "v_and_b32 v135, 0x1ff, v135\n"
            "s_or_b64 vcc, vcc, s[86:87]\n"
            "s_and_b64 vcc, vcc, s[72:73]\n"
            "s_cbranch_scc1 Lre0_%=\n"
            "Lok0_%=:\n"
            "v_lshlrev_b32 v144, 2, v135\n"
            "v_and_b32 v145, 0x1fc, v135\n"
            "s_mov_b64 exec, s[72:73]\n"
            "ds_add_rtn_u32 v128, v144, v197 offset:512\n"
            "ds_add_u32 v145, v190 offset:96\n"
            "s_mov_b64 exec, -1\n"
            "s_waitcnt vmcnt(5)\n"
            "Lchk1_%=:\n"
            "v_xor_b32 v41, s93, v41\n"
            "v_xor_b32 v43, s94, v43\n"
            "v_cmp_lt_u32 vcc, s38, v41\n"
            "v_cmp_lt_u32 s[86:87], s36, v43\n"
            "v_subrev_u32 v136, s19, v43\n"
            "v_and_b32 v136, 0x1ff, v136\n"
            "s_or_b64 vcc, vcc, s[86:87]\n"
            "s_and_b64 vcc, vcc, s[74:75]\n"
            "s_cbranch_scc1 Lre1_%=\n"
            "Lok1_%=:\n"
            "v_lshlrev_b32 v144, 2, v136\n"
            "v_and_b32 v145, 0x1fc, v136\n"
            "s_mov_b64 exec, s[74:75]\n"
            "ds_add_rtn_u32 v129, v144, v197 offset:512\n"
            "ds_add_u32 v145, v190 offset:96\n"
            "s_mov_b64 exec, -1\n"
            "s_waitcnt vmcnt(4)\n"
            "Lchk2_%=:\n"
            "v_xor_b32 v47, s93, v47\n"
            "v_xor_b32 v49, s94, v49\n"
            "v_cmp_lt_u32 vcc, s38, v47\n"
            "v_cmp_lt_u32 s[86:87], s36, v49\n"
            "v_subrev_u32 v137, s19, v49\n"
            "v_and_b32 v137, 0x1ff, v137\n"
            "s_or_b64 vcc, vcc, s[86:87]\n"
            "s_and_b64 vcc, vcc, s[76:77]\n"
            "s_cbranch_scc1 Lre2_%=\n"
            "Lok2_%=:\n"
            "v_lshlrev_b32 v144, 2, v137\n"
            "v_and_b32 v145, 0x1fc, v137\n"
            "s_mov_b64 exec, s[76:77]\n"
            "ds_add_rtn_u32 v130, v144, v197 offset:512\n"
            "ds_add_u32 v145, v190 offset:96\n"
            "s_mov_b64 exec, -1\n"
            "s_waitcnt vmcnt(3)\n"
            "Lchk3_%=:\n"
            "v_xor_b32 v53, s93, v53\n"
            "v_xor_b32 v55, s94, v55\n"
            "v_cmp_lt_u32 vcc, s38, v53\n"
            "v_cmp_lt_u32 s[86:87], s36, v55\n"
            "v_subrev_u32 v138, s19, v55\n"
            "v_and_b32 v138, 0x1ff, v138\n"
            "s_or_b64 vcc, vcc, s[86:87]\n"
            "s_and_b64 vcc, vcc, s[78:79]\n"
            "s_cbranch_scc1 Lre3_%=\n"
            "Lok3_%=:\n"
            "v_lshlrev_b32 v144, 2, v138\n"
            "v_and_b32 v145, 0x1fc, v138\n"
            "s_mov_b64 exec, s[78:79]\n"
            "ds_add_rtn_u32 v131, v144, v197 offset:512\n"
            "ds_add_u32 v145, v190 offset:96\n"
            "s_mov_b64 exec, -1\n"
            "s_waitcnt vmcnt(2)\n"
            "Lchk4_%=:\n"
            "v_xor_b32 v59, s93, v59\n"
            "v_xor_b32 v61, s94, v61\n"
            "v_cmp_lt_u32 vcc, s38, v59\n"
            "v_cmp_lt_u32 s[86:87], s36, v61\n"
            "v_subrev_u32 v139, s19, v61\n"
            "v_and_b32 v139, 0x1ff, v139\n"
            "s_or_b64 vcc, vcc, s[86:87]\n"
            "s_and_b64 vcc, vcc, s[80:81]\n"
            "s_cbranch_scc1 Lre4_%=\n"
            "Lok4_%=:\n"
            "v_lshlrev_b32 v144, 2, v139\n"
            "v_and_b32 v145, 0x1fc, v139\n"
            "s_mov_b64 exec, s[80:81]\n"
            "ds_add_rtn_u32 v132, v144, v197 offset:512\n"
            "ds_add_u32 v145, v190 offset:96\n"
            "s_mov_b64 exec, -1\n"
            "s_waitcnt vmcnt(1)\n"
            "Lchk5_%=:\n"
            "v_xor_b32 v65, s93, v65\n"
            "v_xor_b32 v67, s94, v67\n"
            "v_cmp_lt_u32 vcc, s38, v65\n"
            "v_cmp_lt_u32 s[86:87], s36, v67\n"
            "v_subrev_u32 v140, s19, v67\n"
            "v_and_b32 v140, 0x1ff, v140\n"
            "s_or_b64 vcc, vcc, s[86:87]\n"
            "s_and_b64 vcc, vcc, s[82:83]\n"
            "s_cbranch_scc1 Lre5_%=\n"
            "Lok5_%=:\n"
            "v_lshlrev_b32 v144, 2, v140\n"
            "v_and_b32 v145, 0x1fc, v140\n"
            "s_mov_b64 exec, s[82:83]\n"
            "ds_add_rtn_u32 v133, v144, v197 offset:512\n"
            "ds_add_u32 v145, v190 offset:96\n"
            "s_mov_b64 exec, -1\n"
            "s_cmp_eq_u64 s[84:85], 0\n"
            "s_cbranch_scc1 Lr7_%=\n"
            "s_waitcnt vmcnt(0)\n"
            "Lchk6_%=:\n"
            "v_xor_b32 v71, s93, v71\n"
            "v_xor_b32 v73, s94, v73\n"
            "v_cmp_lt_u32 vcc, s38, v71\n"
            "v_cmp_lt_u32 s[86:87], s36, v73\n"
            "v_subrev_u32 v141, s19, v73\n"
            "v_and_b32 v141, 0x1ff, v141\n"
            "s_or_b64 vcc, vcc, s[86:87]\n"
            "s_and_b64 vcc, vcc, s[84:85]\n"
            "s_cbranch_scc1 Lre6_%=\n"
            "Lok6_%=:\n"
            "v_lshlrev_b32 v144, 2, v141\n"
            "v_and_b32 v145, 0x1fc, v141\n"
            "s_mov_b64 exec, s[84:85]\n"
            "ds_add_rtn_u32 v134, v144, v197 offset:512\n"
            "ds_add_u32 v145, v190 offset:96\n"
            "s_mov_b64 exec, -1\n"
            "Lr7_%=:\n"
            "s_mov_b64 exec, 1\n"
            "ds_add_u32 v219, v209 offset:44\n"
            "s_mov_b64 exec, -1\n"
            "v_lshlrev_b32 v32, 10, v35\n"
            "v_lshrrev_b32 v33, 9, v37\n"
            "v_mov_b32 v35, v36\n"
            "v_lshl_add_u32 v33, v33, 5, v88\n"
            "v_lshl_add_u32 v135, v135, 1, s18\n"
            "v_lshlrev_b32 v38, 10, v41\n"
            "v_lshrrev_b32 v39, 9, v43\n"
            "v_mov_b32 v41, v42\n"
            "v_lshl_add_u32 v39, v39, 5, v89\n"
            "v_lshl_add_u32 v136, v136, 1, s18\n"
            "v_lshlrev_b32 v44, 10, v47\n"
            "v_lshrrev_b32 v45, 9, v49\n"
            "v_mov_b32 v47, v48\n"
            "v_lshl_add_u32 v45, v45, 5, v90\n"
            "v_lshl_add_u32 v137, v137, 1, s18\n"
            "v_lshlrev_b32 v50, 10, v53\n"
            "v_lshrrev_b32 v51, 9, v55\n"
            "v_mov_b32 v53, v54\n"
            "v_lshl_add_u32 v51, v51, 5, v91\n"
            "v_lshl_add_u32 v138, v138, 1, s18\n"
            "v_lshlrev_b32 v56, 10, v59\n"
            "v_lshrrev_b32 v57, 9, v61\n"
            "v_mov_b32 v59, v60\n"
            "v_lshl_add_u32 v57, v57, 5, v92\n"
            "v_lshl_add_u32 v139, v139, 1, s18\n"
            "v_lshlrev_b32 v62, 10, v65\n"
            "v_lshrrev_b32 v63, 9, v67\n"
            "v_mov_b32 v65, v66\n"
            "v_lshl_add_u32 v63, v63, 5, v93\n"
            "v_lshl_add_u32 v140, v140, 1, s18\n"
            "v_lshlrev_b32 v68, 10, v71\n"
            "v_lshrrev_b32 v69, 9, v73\n"
            "v_mov_b32 v71, v72\n"
            "v_lshl_add_u32 v69, v69, 5, v94\n"
            "v_lshl_add_u32 v141, v141, 1, s18\n"
            "Lcc_%=:\n"
            "ds_read_b32 v220, v219 offset:44\n"
            "s_waitcnt lgkmcnt(0)\n"
            "v_readfirstlane_b32 s68, v220\n"
            "s_cmp_lt_u32 s68, 4\n"
            "s_cbranch_scc1 Lcc_%=\n"
            "ds_read_b128 v[144:147], v192 offset:512\n"
            "ds_read_b128 v[148:151], v214 offset:1536\n"
            "v_bfe_u32 v128, v128, s12, 8\n"
            "v_bfe_u32 v129, v129, s12, 8\n"
            "v_bfe_u32 v130, v130, s12, 8\n"
            "v_bfe_u32 v131, v131, s12, 8\n"
            "v_bfe_u32 v132, v132, s12, 8\n"
            "v_bfe_u32 v133, v133, s12, 8\n"
            "v_bfe_u32 v134, v134, s12, 8\n"
            "s_waitcnt lgkmcnt(1)\n"
            "v_dot4_u32_u8 v152, v144, s13, v182\n"
            "v_dot4_u32_u8 v182, v144, s39, v182\n"
            "v_dot4_u32_u8 v153, v145, s13, v182\n"
            "v_dot4_u32_u8 v182, v145, s39, v182\n"
            "v_dot4_u32_u8 v154, v146, s13, v182\n"
            "v_dot4_u32_u8 v182, v146, s39, v182\n"
            "v_dot4_u32_u8 v155, v147, s13, v182\n"
            "v_dot4_u32_u8 v182, v147, s39, v182\n"
            "s_waitcnt lgkmcnt(0)\n"
            "v_dot4_u32_u8 v156, v148, s13, v183\n"
            "v_dot4_u32_u8 v183, v148, s39, v183\n"
            "v_dot4_u32_u8 v157, v149, s13, v183\n"
            "v_dot4_u32_u8 v183, v149, s39, v183\n"
            "v_lshl_or_b32 v152, v153, 16, v152\n"
            "v_lshl_or_b32 v153, v155, 16, v154\n"
            "ds_write_b64 v187, v[152:153]\n"
            "v_dot4_u32_u8 v158, v150, s13, v183\n"
            "v_dot4_u32_u8 v183, v150, s39, v183\n"
            "v_dot4_u32_u8 v159, v151, s13, v183\n"
            "v_dot4_u32_u8 v183, v151, s39, v183\n"
            "v_lshl_or_b32 v156, v157, 16, v156\n"
            "v_cmp_gt_u32 vcc, v182, v184\n"
            "v_lshl_or_b32 v157, v159, 16, v158\n"
            "ds_write_b64 v188, v[156:157] offset:512\n"
            "ds_read_u16 v135, v135\n"
            "ds_read_u16 v136, v136\n"
            "ds_read_u16 v137, v137\n"
            "ds_read_u16 v138, v138\n"
            "ds_read_u16 v139, v139\n"
            "ds_read_u16 v140, v140\n"
            "ds_read_u16 v141, v141\n"
            "v_cmp_gt_u32 s[86:87], v183, v185\n"
            "s_or_b64 vcc, vcc, s[86:87]\n"
            "s_cbranch_scc1 Lbig_%=\n"
            "s_waitcnt lgkmcnt(6)\n"
            "v_lshl_add_u32 v135, v128, 4, v135\n"
            "s_mov_b64 exec, s[72:73]\n"
            "ds_write_b128 v135, v[32:35]\n"
            "s_waitcnt lgkmcnt(6)\n"
            "v_lshl_add_u32 v136, v129, 4, v136\n"
            "s_mov_b64 exec, s[74:75]\n"
            "ds_write_b128 v136, v[38:41]\n"
            "s_waitcnt lgkmcnt(6)\n"
            "v_lshl_add_u32 v137, v130, 4, v137\n"
            "s_mov_b64 exec, s[76:77]\n"
            "ds_write_b128 v137, v[44:47]\n"
            "s_waitcnt lgkmcnt(6)\n"
            "v_lshl_add_u32 v138, v131, 4, v138\n"
            "s_mov_b64 exec, s[78:79]\n"
            "ds_write_b128 v138, v[50:53]\n"
            "s_waitcnt lgkmcnt(6)\n"
            "v_lshl_add_u32 v139, v132, 4, v139\n"
            "s_mov_b64 exec, s[80:81]\n"
            "ds_write_b128 v139, v[56:59]\n"
            "s_waitcnt lgkmcnt(6)\n"
            "v_lshl_add_u32 v140, v133, 4, v140\n"
            "s_mov_b64 exec, s[82:83]\n"
            "ds_write_b128 v140, v[62:65]\n"
            "s_waitcnt lgkmcnt(6)\n"
            "v_lshl_add_u32 v141, v134, 4, v141\n"
            "s_mov_b64 exec, s[84:85]\n"
            "ds_write_b128 v141, v[68:71]\n"
            "Lbig_%=:\n"
            "s_mov_b64 exec, 1\n"
            "ds_add_u32 v219, v209 offset:56\n"
            "s_mov_b64 exec, -1\n"
            "Lmain_%=:\n"
            "Ls_%=:\n"
            "ds_read_b32 v220, v219 offset:48\n"
            "s_waitcnt lgkmcnt(0)\n"
            "v_readfirstlane_b32 s68, v220\n"
            "s_cmp_lt_u32 s68, 4\n"
            "s_cbranch_scc1 Ls_%=\n"
            "ds_read_u16 v132, v248 offset:512\n"
            "ds_read_u16 v133, v248 offset:514\n"
            "ds_read_u16 v134, v244 offset:512\n"
            "ds_read_u16 v135, v244 offset:514\n"
            "ds_read_b32 v128, v240\n"
            "ds_read_b32 v129, v241\n"
            "ds_read_b32 v130, v242\n"
            "ds_read_b32 v131, v219\n"
            "ds_read_b32 v217, v244 offset:1024\n"
            "ds_read_b32 v215, v245 offset:1024\n"
            "ds_read_b32 v218, v246 offset:1024\n"
            "v_add_u32 v136, 4, v249\n"
            "v_add_u32 v137, 4, v206\n"
            "s_waitcnt lgkmcnt(7)\n"
            "v_cmp_gt_u32 vcc, v133, v206\n"
            "v_cmp_gt_u32 s[70:71], v135, v249\n"
            "v_cmp_gt_u32 s[86:87], v133, v137\n"
            "v_cmp_gt_u32 s[88:89], v135, v136\n"
            "v_add_u32 v132, v132, v207\n"
            "v_add_u32 v134, v134, v250\n"
            "v_cndmask_b32 v232, v251, v132, vcc\n"
            "v_cndmask_b32 v221, v251, v134, s[70:71]\n"
            "v_add_u32 v132, 64, v132\n"
            "v_add_u32 v134, 64, v134\n"
            "v_cndmask_b32 v233, v251, v132, s[86:87]\n"
            "v_cndmask_b32 v210, v251, v134, s[88:89]\n"
            "Lw_%=:\n"
            "ds_read_b32 v220, v219 offset:56\n"
            "s_waitcnt lgkmcnt(0)\n"
            "v_readfirstlane_b32 s68, v220\n"
            "s_cmp_lt_u32 s68, 4\n"
            "s_cbranch_scc1 Lw_%=\n"
            "ds_read_b32 v232, v232 offset:4\n"
            "ds_read_b128 v[196:199], v221\n"
            "ds_read_b32 v233, v233 offset:4\n"
            "s_waitcnt lgkmcnt(2)\n"
            "v_add_u32 v232, v232, v229\n"
            "buffer_load_dwordx4 v[160:163], v232, s[24:27], 0 offen\n"
            "s_waitcnt lgkmcnt(0)\n"
            "v_add_u32 v233, v233, v229\n"
            "v_add_u32_dpp v220, v196, v214 quad_perm:[0,0,0,0] row_mask:0xf bank_mask:0xf\n"
            "v_add_u32_dpp v221, v196, v214 quad_perm:[1,1,1,1] row_mask:0xf bank_mask:0xf\n"
            "v_add_u32_dpp v222, v196, v214 quad_perm:[2,2,2,2] row_mask:0xf bank_mask:0xf\n"
            "v_add_u32_dpp v223, v196, v214 quad_perm:[3,3,3,3] row_mask:0xf bank_mask:0xf\n"
            "buffer_load_dwordx4 v[32:35], v220, s[28:31], 0 offen\n"
            "buffer_load_dwordx4 v[36:39], v220, s[28:31], 0 offen offset:256\n"
            "v_mfma_f32_4x4x4_16b_bf16 v[204:207], v[202:203], v[198:199], 0\n"
            "buffer_load_dwordx4 v[48:51], v221, s[28:31], 0 offen\n"
            "buffer_load_dwordx4 v[52:55], v221, s[28:31], 0 offen offset:256\n"
            "buffer_load_dwordx4 v[232:235], v233, s[24:27], 0 offen\n"
            "buffer_load_dwordx4 v[64:67], v222, s[28:31], 0 offen\n"
            "buffer_load_dwordx4 v[68:71], v222, s[28:31], 0 offen offset:256\n"
            "v_mfma_f32_4x4x4_16b_bf16 v[204:207], v[200:201], v[198:199], v[204:207]\n"
            "buffer_load_dwordx4 v[80:83], v223, s[28:31], 0 offen\n"
            "buffer_load_dwordx4 v[84:87], v223, s[28:31], 0 offen offset:256\n"
            "buffer_load_dwordx4 v[40:43], v220, s[28:31], 0 offen offset:512\n"
            "buffer_load_dwordx4 v[44:47], v220, s[28:31], 0 offen offset:768\n"
            "buffer_load_dwordx4 v[56:59], v221, s[28:31], 0 offen offset:512\n"
            "buffer_load_dwordx4 v[60:63], v221, s[28:31], 0 offen offset:768\n"
            "buffer_load_dwordx4 v[72:75], v222, s[28:31], 0 offen offset:512\n"
            "buffer_load_dwordx4 v[76:79], v222, s[28:31], 0 offen offset:768\n"
            "buffer_load_dwordx4 v[88:91], v223, s[28:31], 0 offen offset:512\n"
            "buffer_load_dwordx4 v[92:95], v223, s[28:31], 0 offen offset:768\n"
            "v_max_u32 v128, 12, v128\n"
            "v_mov_b32_dpp v134, v130 quad_perm:[1,2,3,0] row_mask:0xf bank_mask:0xf\n"
            "v_add_u32 v132, v128, v129\n"
            "v_sub_u32 v133, 0, v132\n"
            "v_lshl_add_u32 v135, v132, 1, v208\n"
            "v_and_b32 v133, 3, v133\n"
            "v_lshl_add_u32 v133, v132, 1, v133\n"
            "v_add3_u32 v135, v135, v134, v130\n"
            "v_readlane_b32 s73, v128, s3\n"
            "s_nop 0\n"
            "v_add_u32_dpp v135, v133, v135 row_shr:4 row_mask:0xf bank_mask:0xf bound_ctrl:1\n"
            "v_readlane_b32 s75, v129, s3\n"
            "v_readlane_b32 s80, v130, s3\n"
            "v_sub_u32_dpp v133, v135, v135 quad_perm:[1,2,3,0] row_mask:0xf bank_mask:0xf\n"
            "v_mov_b32_dpp v136, v135 quad_perm:[3,0,1,2] row_mask:0xf bank_mask:0xf\n"
            "v_lshl_add_u32 v137, v134, 3, 8\n"
            "v_mad_i32_i24 v133, v133, 3, v137\n"
            "v_sub_u32_dpp v136, v135, v136 quad_perm:[2,3,0,1] row_mask:0xf bank_mask:0xf\n"
            "v_add_u32 v134, -4, v134\n"
            "v_add_u32 v133, v133, v136\n"
            "v_ashrrev_i32 v133, 4, v133\n"
            "v_max_i32 v133, 4, v133\n"
            "v_min_i32 v133, v133, v134\n"
            "v_max_i32 v133, 0, v133\n"
            "s_lshr_b32 s69, s73, 1\n"
            "s_max_i32 s69, s69, 8\n"
            "v_mov_b32_dpp v134, v133 quad_perm:[3,0,1,2] row_mask:0xf bank_mask:0xf\n"
            "v_readlane_b32 s77, v133, s3\n"
            "v_sub_u32 v134, v130, v134\n"
            "v_add3_u32 v132, v132, v133, v131\n"
            "v_readlane_b32 s76, v134, s3\n"
            "v_add_u32 v132, v132, v134\n"
            "s_add_u32 s78, s69, s77\n"
            "v_cmp_lt_u32 vcc, 72, v132\n"
            "s_add_u32 s79, s78, s75\n"
            "s_sub_u32 s83, s79, s80\n"
            "s_add_u32 s83, s83, s76\n"
            "s_cbranch_vccnz Lslow_%=\n"
            "s_add_u32 s80, s79, s76\n"
            "s_sub_u32 s84, s80, s69\n"
            "s_add_u32 s73, s73, s84\n"
            "s_pack_ll_b32_b16 s81, s40, s69\n"
            "s_pack_ll_b32_b16 s82, s65, s78\n"
            "s_pack_ll_b32_b16 s83, s66, s83\n"
            "s_pack_ll_b32_b16 s84, s64, s84\n"
            "v_mov_b32 v138, 0xffff\n"
            "v_mov_b32 v136, s64\n"
            "v_cmpx_le_u32 vcc, s69, v243\n"
            "v_mov_b32 v136, s81\n"
            "v_cmpx_le_u32 vcc, s78, v243\n"
            "v_mov_b32 v136, s82\n"
            "v_cmpx_le_u32 vcc, s79, v243\n"
            "v_mov_b32 v136, s83\n"
            "v_cmpx_le_u32 vcc, s80, v243\n"
            "v_mov_b32 v136, s84\n"
            "s_mov_b64 exec, -1\n"
            "v_and_b32 v137, 0xffff, v136\n"
            "ds_read_b128 v[140:143], v137\n"
            "v_ashrrev_i32 v136, 16, v136\n"
            "v_sub_u32 v136, v243, v136\n"
            "v_lshl_or_b32 v137, v136, 16, v138\n"
            "v_lshlrev_b32 v136, 4, v136\n"
            "v_cvt_pk_bf16_f32 v204, v204, v205\n"
            "v_cvt_pk_bf16_f32 v205, v206, v207\n"
            "ds_write_b64 v212, v[204:205]\n"
            "ds_read_b128 v[196:199], v210\n"
            "ds_read_b64 v[208:209], v213\n"
            "s_add_u32 s73, s73, 3\n"
            "s_and_b32 s74, s73, 3\n"
            "s_add_u32 s74, s74, 1\n"
            "s_lshr_b32 s73, s73, 2\n"
            "s_mov_b32 s68, 0x05040100\n"
            "v_add_u32 v217, v217, v214\n"
            "v_add3_u32 v215, v215, v214, s63\n"
            "v_add_u32 v218, v218, v214\n"
            "v_not_b32 v223, s90\n"
            "s_waitcnt lgkmcnt(3)\n"
            "v_cmp_gt_u32 vcc, v140, v137\n"
            "v_cmp_gt_u32 s[70:71], v141, v137\n"
            "v_cmp_gt_u32 s[86:87], v142, v137\n"
            "v_cmp_gt_u32 s[88:89], v143, v137\n"
            "v_add_u32 v140, v140, v136\n"
            "v_add_u32 v141, v141, v136\n"
            "v_add_u32 v142, v142, v136\n"
            "v_add_u32 v143, v143, v136\n"
            "v_cndmask_b32 v140, v251, v140, vcc\n"
            "v_cndmask_b32 v141, v251, v141, s[70:71]\n"
            "v_cndmask_b32 v142, v251, v142, s[86:87]\n"
            "v_cndmask_b32 v143, v251, v143, s[88:89]\n"
            "v_perm_b32 v140, v141, v140, s68\n"
            "v_perm_b32 v141, v143, v142, s68\n"
            "ds_write_b64 v247, v[140:141]\n"
            "ds_read_u16 v211, v238\n"
            "s_cmp_gt_u32 s73, 13\n"
            "s_cbranch_scc1 Llong_%=\n"
            "Llong2_%=:\n"
            "v_add_u32 v216, s63, v218\n"
            "s_lshl_b32 s72, s75, 8\n"
            "s_lshl_b32 s76, s76, 16\n"
            "s_add_u32 s72, s72, s76\n"
            "s_add_u32 s72, s72, s77\n"
            "s_add_u32 s72, s72, 0xfefbfbfc\n"
            "s_add_u32 s75, s75, 0xfefc\n"
            "s_cmp_lt_u32 s3, 4\n"
            "s_cselect_b32 s72, s75, s72\n"
            "s_cmp_lt_u32 s3, 6\n"
            "s_cselect_b32 s75, 1, 0\n"
            "v_lshl_or_b32 v218, s75, 31, v218\n"
            "s_mov_b32 s85, 15\n"
            "s_mov_b32 s42, 0\n"
            "s_waitcnt vmcnt(17)\n"
            "ds_bpermute_b32 v192, v253, v160\n"
            "ds_bpermute_b32 v193, v253, v161\n"
            "ds_bpermute_b32 v194, v253, v162\n"
            "ds_bpermute_b32 v195, v253, v163\n"
            "s_waitcnt lgkmcnt(0)\n"
            "v_mfma_f32_16x16x32_bf16 v[176:179], v[192:195], v[96:99], 0\n"
            "v_mfma_f32_16x16x32_bf16 v[180:183], v[192:195], v[100:103], 0\n"
            "v_mfma_f32_16x16x32_bf16 v[184:187], v[192:195], v[104:107], 0\n"
            "v_mfma_f32_16x16x32_bf16 v[188:191], v[192:195], v[108:111], 0\n"
            "v_mfma_f32_16x16x32_bf16 v[128:131], v[192:195], v[112:115], 0\n"
            "v_mfma_f32_16x16x32_bf16 v[132:135], v[192:195], v[116:119], 0\n"
            "s_nop 2\n"
            "v_exp_f32 v144, v176\n"
            "v_exp_f32 v145, v177\n"
            "v_exp_f32 v146, v178\n"
            "v_exp_f32 v147, v179\n"
            "v_exp_f32 v148, v180\n"
            "v_exp_f32 v149, v181\n"
            "v_exp_f32 v150, v182\n"
            "v_exp_f32 v151, v183\n"
            "v_exp_f32 v152, v184\n"
            "v_exp_f32 v153, v185\n"
            "v_exp_f32 v154, v186\n"
            "v_exp_f32 v155, v187\n"
            "v_exp_f32 v156, v188\n"
            "v_exp_f32 v157, v189\n"
            "v_exp_f32 v158, v190\n"
            "v_exp_f32 v159, v191\n"
            "v_pk_add_f32 v[144:145], v[144:145], 1.0 op_sel_hi:[1,0]\n"
            "v_pk_add_f32 v[146:147], v[146:147], 1.0 op_sel_hi:[1,0]\n"
            "v_pk_add_f32 v[148:149], v[148:149], 1.0 op_sel_hi:[1,0]\n"
            "v_pk_add_f32 v[150:151], v[150:151], 1.0 op_sel_hi:[1,0]\n"
            "v_pk_add_f32 v[152:153], v[152:153], 1.0 op_sel_hi:[1,0]\n"
            "v_pk_add_f32 v[154:155], v[154:155], 1.0 op_sel_hi:[1,0]\n"
            "v_pk_add_f32 v[156:157], v[156:157], 1.0 op_sel_hi:[1,0]\n"
            "v_pk_add_f32 v[158:159], v[158:159], 1.0 op_sel_hi:[1,0]\n"
            "v_rcp_f32 v144, v144\n"
            "v_rcp_f32 v145, v145\n"
            "v_rcp_f32 v146, v146\n"
            "v_rcp_f32 v147, v147\n"
            "v_rcp_f32 v148, v148\n"
            "v_rcp_f32 v149, v149\n"
            "v_rcp_f32 v150, v150\n"
            "v_rcp_f32 v151, v151\n"
            "v_rcp_f32 v152, v152\n"
            "v_rcp_f32 v153, v153\n"
            "v_rcp_f32 v154, v154\n"
            "v_rcp_f32 v155, v155\n"
            "v_rcp_f32 v156, v156\n"
            "v_rcp_f32 v157, v157\n"
            "v_rcp_f32 v158, v158\n"
            "v_rcp_f32 v159, v159\n"
            "v_pk_mul_f32 v[144:145], v[144:145], v[176:177]\n"
            "v_pk_mul_f32 v[146:147], v[146:147], v[178:179]\n"
            "v_pk_mul_f32 v[148:149], v[148:149], v[180:181]\n"
            "v_pk_mul_f32 v[150:151], v[150:151], v[182:183]\n"
            "v_pk_mul_f32 v[152:153], v[152:153], v[184:185]\n"
            "v_pk_mul_f32 v[154:155], v[154:155], v[186:187]\n"
            "v_pk_mul_f32 v[156:157], v[156:157], v[188:189]\n"
            "v_pk_mul_f32 v[158:159], v[158:159], v[190:191]\n"
            "s_branch Ldisp_%=\n"
            "Lfast_%=:\n"
            "v_mfma_f32_16x16x32_bf16 v[136:139], v[192:195], v[120:123], 0\n"
            "v_exp_f32 v160, v128\n"
            "v_add_u32 v237, 32, v237\n"
            "v_exp_f32 v161, v129\n"
            "s_waitcnt vmcnt(12) lgkmcnt(0)\n"
            "v_mfma_f32_4x4x4_16b_bf16 v[176:179], v[208:209], v[32:33], 0 cbsz:2\n"
            "v_exp_f32 v162, v130\n"
            "v_mfma_f32_4x4x4_16b_bf16 v[180:183], v[208:209], v[34:35], 0 cbsz:2\n"
            "v_exp_f32 v163, v131\n"
            "v_mfma_f32_4x4x4_16b_bf16 v[184:187], v[208:209], v[36:37], 0 cbsz:2\n"
            "v_exp_f32 v164, v132\n"
            "v_mfma_f32_4x4x4_16b_bf16 v[188:191], v[208:209], v[38:39], 0 cbsz:2\n"
            "v_exp_f32 v165, v133\n"
            "v_add_u32_dpp v219, v196, v214 quad_perm:[0,0,0,0] row_mask:0xf bank_mask:0xf\n"
            "v_mfma_f32_4x4x4_16b_bf16 v[204:207], v[202:203], v[198:199], 0\n"
            "v_exp_f32 v166, v134\n"
            "buffer_load_dwordx4 v[32:35], v219, s[28:31], 0 offen\n"
            "buffer_load_dwordx4 v[36:39], v219, s[28:31], 0 offen offset:256\n"
            "v_exp_f32 v167, v135\n"
            "v_pk_fma_f32 v[0:1], v[144:145], v[176:177], v[0:1] op_sel_hi:[0,1,1]\n"
            "v_pk_fma_f32 v[2:3], v[144:145], v[178:179], v[2:3] op_sel_hi:[0,1,1]\n"
            "v_pk_fma_f32 v[4:5], v[148:149], v[180:181], v[4:5] op_sel_hi:[0,1,1]\n"
            "v_pk_fma_f32 v[6:7], v[148:149], v[182:183], v[6:7] op_sel_hi:[0,1,1]\n"
            "v_pk_fma_f32 v[8:9], v[152:153], v[184:185], v[8:9] op_sel_hi:[0,1,1]\n"
            "v_pk_fma_f32 v[10:11], v[152:153], v[186:187], v[10:11] op_sel_hi:[0,1,1]\n"
            "v_pk_fma_f32 v[12:13], v[156:157], v[188:189], v[12:13] op_sel_hi:[0,1,1]\n"
            "v_pk_fma_f32 v[14:15], v[156:157], v[190:191], v[14:15] op_sel_hi:[0,1,1]\n"
            "v_pk_add_f32 v[160:161], v[160:161], 1.0 op_sel_hi:[1,0]\n"
            "v_pk_add_f32 v[162:163], v[162:163], 1.0 op_sel_hi:[1,0]\n"
            "v_pk_add_f32 v[164:165], v[164:165], 1.0 op_sel_hi:[1,0]\n"
            "v_pk_add_f32 v[166:167], v[166:167], 1.0 op_sel_hi:[1,0]\n"
            "v_mfma_f32_16x16x32_bf16 v[140:143], v[192:195], v[124:127], 0\n"
            "ds_bpermute_b32 v192, v253, v232\n"
            "ds_bpermute_b32 v193, v253, v233\n"
            "ds_bpermute_b32 v194, v253, v234\n"
            "ds_bpermute_b32 v195, v253, v235\n"
            "ds_read_b32 v230, v211 offset:4\n"
            "ds_read_u16 v210, v237\n"
            "v_rcp_f32 v160, v160\n"
            "v_rcp_f32 v161, v161\n"
            "v_mfma_f32_4x4x4_16b_bf16 v[176:179], v[208:209], v[48:49], 0 cbsz:2 abid:1\n"
            "v_rcp_f32 v162, v162\n"
            "v_mfma_f32_4x4x4_16b_bf16 v[180:183], v[208:209], v[50:51], 0 cbsz:2 abid:1\n"
            "v_rcp_f32 v163, v163\n"
            "v_mfma_f32_4x4x4_16b_bf16 v[184:187], v[208:209], v[52:53], 0 cbsz:2 abid:1\n"
            "v_rcp_f32 v164, v164\n"
            "v_mfma_f32_4x4x4_16b_bf16 v[188:191], v[208:209], v[54:55], 0 cbsz:2 abid:1\n"
            "v_rcp_f32 v165, v165\n"
            "v_add_u32_dpp v220, v196, v214 quad_perm:[1,1,1,1] row_mask:0xf bank_mask:0xf\n"
            "v_mfma_f32_4x4x4_16b_bf16 v[204:207], v[200:201], v[198:199], v[204:207]\n"
            "v_rcp_f32 v166, v166\n"
            "buffer_load_dwordx4 v[48:51], v220, s[28:31], 0 offen\n"
            "buffer_load_dwordx4 v[52:55], v220, s[28:31], 0 offen offset:256\n"
            "v_rcp_f32 v167, v167\n"
            "v_pk_fma_f32 v[0:1], v[144:145], v[176:177], v[0:1] op_sel:[1,0,0]\n"
            "v_pk_fma_f32 v[2:3], v[144:145], v[178:179], v[2:3] op_sel:[1,0,0]\n"
            "v_pk_fma_f32 v[4:5], v[148:149], v[180:181], v[4:5] op_sel:[1,0,0]\n"
            "v_pk_fma_f32 v[6:7], v[148:149], v[182:183], v[6:7] op_sel:[1,0,0]\n"
            "v_pk_fma_f32 v[8:9], v[152:153], v[184:185], v[8:9] op_sel:[1,0,0]\n"
            "v_pk_fma_f32 v[10:11], v[152:153], v[186:187], v[10:11] op_sel:[1,0,0]\n"
            "v_pk_fma_f32 v[12:13], v[156:157], v[188:189], v[12:13] op_sel:[1,0,0]\n"
            "v_pk_fma_f32 v[14:15], v[156:157], v[190:191], v[14:15] op_sel:[1,0,0]\n"
            "v_pk_mul_f32 v[160:161], v[160:161], v[128:129]\n"
            "v_pk_mul_f32 v[162:163], v[162:163], v[130:131]\n"
            "v_pk_mul_f32 v[164:165], v[164:165], v[132:133]\n"
            "v_pk_mul_f32 v[166:167], v[166:167], v[134:135]\n"
            "s_waitcnt vmcnt(12) lgkmcnt(0)\n"
            "v_add_u32 v230, v230, v229\n"
            "buffer_load_dwordx4 v[232:235], v230, s[24:27], 0 offen\n"
            "s_bitcmp0_b32 s85, 0\n"
            "s_cbranch_scc1 Lnpf02_%=\n"
            "s_buffer_load_dwordx2 s[86:87], s[28:31], s67 offset:60\n"
            "s_buffer_load_dwordx2 s[86:87], s[28:31], s67 offset:188\n"
            "s_buffer_load_dwordx2 s[86:87], s[28:31], s67 offset:316\n"
            "s_buffer_load_dwordx2 s[86:87], s[28:31], s67 offset:444\n"
            "s_add_u32 s67, s67, 524288\n"
            "Lnpf02_%=:\n"
            "s_lshr_b32 s85, s85, 1\n"
            "v_mfma_f32_16x16x32_bf16 v[128:131], v[192:195], v[96:99], 0\n"
            "v_exp_f32 v168, v136\n"
            "v_exp_f32 v169, v137\n"
            "v_mfma_f32_4x4x4_16b_bf16 v[176:179], v[208:209], v[64:65], 0 cbsz:2 abid:2\n"
            "v_exp_f32 v170, v138\n"
            "v_mfma_f32_4x4x4_16b_bf16 v[180:183], v[208:209], v[66:67], 0 cbsz:2 abid:2\n"
            "v_exp_f32 v171, v139\n"
            "v_mfma_f32_4x4x4_16b_bf16 v[184:187], v[208:209], v[68:69], 0 cbsz:2 abid:2\n"
            "v_exp_f32 v172, v140\n"
            "v_mfma_f32_4x4x4_16b_bf16 v[188:191], v[208:209], v[70:71], 0 cbsz:2 abid:2\n"
            "v_exp_f32 v173, v141\n"
            "v_add_u32_dpp v221, v196, v214 quad_perm:[2,2,2,2] row_mask:0xf bank_mask:0xf\n"
            "v_exp_f32 v174, v142\n"
            "buffer_load_dwordx4 v[64:67], v221, s[28:31], 0 offen\n"
            "buffer_load_dwordx4 v[68:71], v221, s[28:31], 0 offen offset:256\n"
            "v_exp_f32 v175, v143\n"
            "v_pk_fma_f32 v[0:1], v[146:147], v[176:177], v[0:1] op_sel_hi:[0,1,1]\n"
            "v_pk_fma_f32 v[2:3], v[146:147], v[178:179], v[2:3] op_sel_hi:[0,1,1]\n"
            "v_pk_fma_f32 v[4:5], v[150:151], v[180:181], v[4:5] op_sel_hi:[0,1,1]\n"
            "v_pk_fma_f32 v[6:7], v[150:151], v[182:183], v[6:7] op_sel_hi:[0,1,1]\n"
            "v_pk_fma_f32 v[8:9], v[154:155], v[184:185], v[8:9] op_sel_hi:[0,1,1]\n"
            "v_pk_fma_f32 v[10:11], v[154:155], v[186:187], v[10:11] op_sel_hi:[0,1,1]\n"
            "v_pk_fma_f32 v[12:13], v[158:159], v[188:189], v[12:13] op_sel_hi:[0,1,1]\n"
            "v_pk_fma_f32 v[14:15], v[158:159], v[190:191], v[14:15] op_sel_hi:[0,1,1]\n"
            "v_pk_add_f32 v[168:169], v[168:169], 1.0 op_sel_hi:[1,0]\n"
            "v_pk_add_f32 v[170:171], v[170:171], 1.0 op_sel_hi:[1,0]\n"
            "v_pk_add_f32 v[172:173], v[172:173], 1.0 op_sel_hi:[1,0]\n"
            "v_pk_add_f32 v[174:175], v[174:175], 1.0 op_sel_hi:[1,0]\n"
            "v_cvt_pk_bf16_f32 v204, v204, v205\n"
            "v_cvt_pk_bf16_f32 v205, v206, v207\n"
            "ds_write_b64 v212, v[204:205]\n"
            "v_mfma_f32_16x16x32_bf16 v[132:135], v[192:195], v[100:103], 0\n"
            "v_rcp_f32 v168, v168\n"
            "v_rcp_f32 v169, v169\n"
            "v_mfma_f32_4x4x4_16b_bf16 v[176:179], v[208:209], v[80:81], 0 cbsz:2 abid:3\n"
            "v_rcp_f32 v170, v170\n"
            "v_mfma_f32_4x4x4_16b_bf16 v[180:183], v[208:209], v[82:83], 0 cbsz:2 abid:3\n"
            "v_rcp_f32 v171, v171\n"
            "v_mfma_f32_4x4x4_16b_bf16 v[184:187], v[208:209], v[84:85], 0 cbsz:2 abid:3\n"
            "v_rcp_f32 v172, v172\n"
            "v_mfma_f32_4x4x4_16b_bf16 v[188:191], v[208:209], v[86:87], 0 cbsz:2 abid:3\n"
            "v_rcp_f32 v173, v173\n"
            "v_add_u32_dpp v222, v196, v214 quad_perm:[3,3,3,3] row_mask:0xf bank_mask:0xf\n"
            "v_rcp_f32 v174, v174\n"
            "buffer_load_dwordx4 v[80:83], v222, s[28:31], 0 offen\n"
            "buffer_load_dwordx4 v[84:87], v222, s[28:31], 0 offen offset:256\n"
            "v_rcp_f32 v175, v175\n"
            "v_pk_fma_f32 v[0:1], v[146:147], v[176:177], v[0:1] op_sel:[1,0,0]\n"
            "v_pk_fma_f32 v[2:3], v[146:147], v[178:179], v[2:3] op_sel:[1,0,0]\n"
            "v_pk_fma_f32 v[4:5], v[150:151], v[180:181], v[4:5] op_sel:[1,0,0]\n"
            "v_pk_fma_f32 v[6:7], v[150:151], v[182:183], v[6:7] op_sel:[1,0,0]\n"
            "v_pk_fma_f32 v[8:9], v[154:155], v[184:185], v[8:9] op_sel:[1,0,0]\n"
            "v_pk_fma_f32 v[10:11], v[154:155], v[186:187], v[10:11] op_sel:[1,0,0]\n"
            "v_pk_fma_f32 v[12:13], v[158:159], v[188:189], v[12:13] op_sel:[1,0,0]\n"
            "v_pk_fma_f32 v[14:15], v[158:159], v[190:191], v[14:15] op_sel:[1,0,0]\n"
            "v_pk_mul_f32 v[168:169], v[168:169], v[136:137]\n"
            "v_pk_mul_f32 v[170:171], v[170:171], v[138:139]\n"
            "v_pk_mul_f32 v[172:173], v[172:173], v[140:141]\n"
            "v_pk_mul_f32 v[174:175], v[174:175], v[142:143]\n"
            "v_add_u32 v238, 32, v238\n"
            "ds_read_b128 v[196:199], v210\n"
            "ds_read_u16 v211, v238\n"
            "v_mfma_f32_16x16x32_bf16 v[136:139], v[192:195], v[104:107], 0\n"
            "v_exp_f32 v144, v128\n"
            "v_exp_f32 v145, v129\n"
            "s_waitcnt vmcnt(13)\n"
            "v_mfma_f32_4x4x4_16b_bf16 v[176:179], v[208:209], v[40:41], 0 cbsz:2\n"
            "v_exp_f32 v146, v130\n"
            "v_mfma_f32_4x4x4_16b_bf16 v[180:183], v[208:209], v[42:43], 0 cbsz:2\n"
            "v_exp_f32 v147, v131\n"
            "v_mfma_f32_4x4x4_16b_bf16 v[184:187], v[208:209], v[44:45], 0 cbsz:2\n"
            "v_exp_f32 v148, v132\n"
            "v_mfma_f32_4x4x4_16b_bf16 v[188:191], v[208:209], v[46:47], 0 cbsz:2\n"
            "v_exp_f32 v149, v133\n"
            "v_exp_f32 v150, v134\n"
            "buffer_load_dwordx4 v[40:43], v219, s[28:31], 0 offen offset:512\n"
            "buffer_load_dwordx4 v[44:47], v219, s[28:31], 0 offen offset:768\n"
            "v_exp_f32 v151, v135\n"
            "v_pk_fma_f32 v[16:17], v[160:161], v[176:177], v[16:17] op_sel_hi:[0,1,1]\n"
            "v_pk_fma_f32 v[18:19], v[160:161], v[178:179], v[18:19] op_sel_hi:[0,1,1]\n"
            "v_pk_fma_f32 v[20:21], v[164:165], v[180:181], v[20:21] op_sel_hi:[0,1,1]\n"
            "v_pk_fma_f32 v[22:23], v[164:165], v[182:183], v[22:23] op_sel_hi:[0,1,1]\n"
            "v_pk_fma_f32 v[24:25], v[168:169], v[184:185], v[24:25] op_sel_hi:[0,1,1]\n"
            "v_pk_fma_f32 v[26:27], v[168:169], v[186:187], v[26:27] op_sel_hi:[0,1,1]\n"
            "v_pk_fma_f32 v[28:29], v[172:173], v[188:189], v[28:29] op_sel_hi:[0,1,1]\n"
            "v_pk_fma_f32 v[30:31], v[172:173], v[190:191], v[30:31] op_sel_hi:[0,1,1]\n"
            "v_pk_add_f32 v[144:145], v[144:145], 1.0 op_sel_hi:[1,0]\n"
            "v_pk_add_f32 v[146:147], v[146:147], 1.0 op_sel_hi:[1,0]\n"
            "v_pk_add_f32 v[148:149], v[148:149], 1.0 op_sel_hi:[1,0]\n"
            "v_pk_add_f32 v[150:151], v[150:151], 1.0 op_sel_hi:[1,0]\n"
            "v_mfma_f32_16x16x32_bf16 v[140:143], v[192:195], v[108:111], 0\n"
            "v_rcp_f32 v144, v144\n"
            "v_rcp_f32 v145, v145\n"
            "v_mfma_f32_4x4x4_16b_bf16 v[176:179], v[208:209], v[56:57], 0 cbsz:2 abid:1\n"
            "v_rcp_f32 v146, v146\n"
            "v_mfma_f32_4x4x4_16b_bf16 v[180:183], v[208:209], v[58:59], 0 cbsz:2 abid:1\n"
            "v_rcp_f32 v147, v147\n"
            "v_mfma_f32_4x4x4_16b_bf16 v[184:187], v[208:209], v[60:61], 0 cbsz:2 abid:1\n"
            "v_rcp_f32 v148, v148\n"
            "v_mfma_f32_4x4x4_16b_bf16 v[188:191], v[208:209], v[62:63], 0 cbsz:2 abid:1\n"
            "v_rcp_f32 v149, v149\n"
            "v_rcp_f32 v150, v150\n"
            "buffer_load_dwordx4 v[56:59], v220, s[28:31], 0 offen offset:512\n"
            "buffer_load_dwordx4 v[60:63], v220, s[28:31], 0 offen offset:768\n"
            "v_rcp_f32 v151, v151\n"
            "v_pk_fma_f32 v[16:17], v[160:161], v[176:177], v[16:17] op_sel:[1,0,0]\n"
            "v_pk_fma_f32 v[18:19], v[160:161], v[178:179], v[18:19] op_sel:[1,0,0]\n"
            "v_pk_fma_f32 v[20:21], v[164:165], v[180:181], v[20:21] op_sel:[1,0,0]\n"
            "v_pk_fma_f32 v[22:23], v[164:165], v[182:183], v[22:23] op_sel:[1,0,0]\n"
            "v_pk_fma_f32 v[24:25], v[168:169], v[184:185], v[24:25] op_sel:[1,0,0]\n"
            "v_pk_fma_f32 v[26:27], v[168:169], v[186:187], v[26:27] op_sel:[1,0,0]\n"
            "v_pk_fma_f32 v[28:29], v[172:173], v[188:189], v[28:29] op_sel:[1,0,0]\n"
            "v_pk_fma_f32 v[30:31], v[172:173], v[190:191], v[30:31] op_sel:[1,0,0]\n"
            "v_pk_mul_f32 v[144:145], v[144:145], v[128:129]\n"
            "v_pk_mul_f32 v[146:147], v[146:147], v[130:131]\n"
            "v_pk_mul_f32 v[148:149], v[148:149], v[132:133]\n"
            "v_pk_mul_f32 v[150:151], v[150:151], v[134:135]\n"
            "v_mfma_f32_16x16x32_bf16 v[128:131], v[192:195], v[112:115], 0\n"
            "v_exp_f32 v152, v136\n"
            "v_exp_f32 v153, v137\n"
            "s_waitcnt vmcnt(13)\n"
            "v_mfma_f32_4x4x4_16b_bf16 v[176:179], v[208:209], v[72:73], 0 cbsz:2 abid:2\n"
            "v_exp_f32 v154, v138\n"
            "v_mfma_f32_4x4x4_16b_bf16 v[180:183], v[208:209], v[74:75], 0 cbsz:2 abid:2\n"
            "v_exp_f32 v155, v139\n"
            "v_mfma_f32_4x4x4_16b_bf16 v[184:187], v[208:209], v[76:77], 0 cbsz:2 abid:2\n"
            "v_exp_f32 v156, v140\n"
            "v_mfma_f32_4x4x4_16b_bf16 v[188:191], v[208:209], v[78:79], 0 cbsz:2 abid:2\n"
            "v_exp_f32 v157, v141\n"
            "v_exp_f32 v158, v142\n"
            "buffer_load_dwordx4 v[72:75], v221, s[28:31], 0 offen offset:512\n"
            "buffer_load_dwordx4 v[76:79], v221, s[28:31], 0 offen offset:768\n"
            "v_exp_f32 v159, v143\n"
            "v_pk_fma_f32 v[16:17], v[162:163], v[176:177], v[16:17] op_sel_hi:[0,1,1]\n"
            "v_pk_fma_f32 v[18:19], v[162:163], v[178:179], v[18:19] op_sel_hi:[0,1,1]\n"
            "v_pk_fma_f32 v[20:21], v[166:167], v[180:181], v[20:21] op_sel_hi:[0,1,1]\n"
            "v_pk_fma_f32 v[22:23], v[166:167], v[182:183], v[22:23] op_sel_hi:[0,1,1]\n"
            "v_pk_fma_f32 v[24:25], v[170:171], v[184:185], v[24:25] op_sel_hi:[0,1,1]\n"
            "v_pk_fma_f32 v[26:27], v[170:171], v[186:187], v[26:27] op_sel_hi:[0,1,1]\n"
            "v_pk_fma_f32 v[28:29], v[174:175], v[188:189], v[28:29] op_sel_hi:[0,1,1]\n"
            "v_pk_fma_f32 v[30:31], v[174:175], v[190:191], v[30:31] op_sel_hi:[0,1,1]\n"
            "v_pk_add_f32 v[152:153], v[152:153], 1.0 op_sel_hi:[1,0]\n"
            "v_pk_add_f32 v[154:155], v[154:155], 1.0 op_sel_hi:[1,0]\n"
            "v_pk_add_f32 v[156:157], v[156:157], 1.0 op_sel_hi:[1,0]\n"
            "v_pk_add_f32 v[158:159], v[158:159], 1.0 op_sel_hi:[1,0]\n"
            "v_mfma_f32_16x16x32_bf16 v[132:135], v[192:195], v[116:119], 0\n"
            "v_rcp_f32 v152, v152\n"
            "v_rcp_f32 v153, v153\n"
            "v_mfma_f32_4x4x4_16b_bf16 v[176:179], v[208:209], v[88:89], 0 cbsz:2 abid:3\n"
            "v_rcp_f32 v154, v154\n"
            "v_mfma_f32_4x4x4_16b_bf16 v[180:183], v[208:209], v[90:91], 0 cbsz:2 abid:3\n"
            "v_rcp_f32 v155, v155\n"
            "v_mfma_f32_4x4x4_16b_bf16 v[184:187], v[208:209], v[92:93], 0 cbsz:2 abid:3\n"
            "v_rcp_f32 v156, v156\n"
            "v_mfma_f32_4x4x4_16b_bf16 v[188:191], v[208:209], v[94:95], 0 cbsz:2 abid:3\n"
            "ds_read_b64 v[208:209], v213\n"
            "v_rcp_f32 v157, v157\n"
            "v_rcp_f32 v158, v158\n"
            "buffer_load_dwordx4 v[88:91], v222, s[28:31], 0 offen offset:512\n"
            "buffer_load_dwordx4 v[92:95], v222, s[28:31], 0 offen offset:768\n"
            "v_rcp_f32 v159, v159\n"
            "v_pk_fma_f32 v[16:17], v[162:163], v[176:177], v[16:17] op_sel:[1,0,0]\n"
            "v_pk_fma_f32 v[18:19], v[162:163], v[178:179], v[18:19] op_sel:[1,0,0]\n"
            "v_pk_fma_f32 v[20:21], v[166:167], v[180:181], v[20:21] op_sel:[1,0,0]\n"
            "v_pk_fma_f32 v[22:23], v[166:167], v[182:183], v[22:23] op_sel:[1,0,0]\n"
            "v_pk_fma_f32 v[24:25], v[170:171], v[184:185], v[24:25] op_sel:[1,0,0]\n"
            "v_pk_fma_f32 v[26:27], v[170:171], v[186:187], v[26:27] op_sel:[1,0,0]\n"
            "v_pk_fma_f32 v[28:29], v[174:175], v[188:189], v[28:29] op_sel:[1,0,0]\n"
            "v_pk_fma_f32 v[30:31], v[174:175], v[190:191], v[30:31] op_sel:[1,0,0]\n"
            "v_pk_mul_f32 v[152:153], v[152:153], v[136:137]\n"
            "v_pk_mul_f32 v[154:155], v[154:155], v[138:139]\n"
            "v_pk_mul_f32 v[156:157], v[156:157], v[140:141]\n"
            "v_pk_mul_f32 v[158:159], v[158:159], v[142:143]\n"
            "s_sub_u32 s69, s69, 4\n"
            "Lnext_%=:\n"
            "s_sub_u32 s73, s73, 1\n"
            "Ldisp_%=:\n"
            "s_bitcmp1_b32 s60, s73\n"
            "s_cbranch_scc1 Lprio_%=\n"
            "Ld2_%=:\n"
            "s_cmp_gt_u32 s69, 4\n"
            "s_cbranch_scc1 Lfast_%=\n"
            "v_mfma_f32_16x16x32_bf16 v[136:139], v[192:195], v[120:123], 0\n"
            "v_exp_f32 v160, v128\n"
            "v_add_u32 v237, 32, v237\n"
            "v_exp_f32 v161, v129\n"
            "s_waitcnt vmcnt(12) lgkmcnt(0)\n"
            "v_mfma_f32_4x4x4_16b_bf16 v[176:179], v[208:209], v[32:33], 0 cbsz:2\n"
            "v_exp_f32 v162, v130\n"
            "v_mfma_f32_4x4x4_16b_bf16 v[180:183], v[208:209], v[34:35], 0 cbsz:2\n"
            "v_exp_f32 v163, v131\n"
            "v_mfma_f32_4x4x4_16b_bf16 v[184:187], v[208:209], v[36:37], 0 cbsz:2\n"
            "v_exp_f32 v164, v132\n"
            "v_mfma_f32_4x4x4_16b_bf16 v[188:191], v[208:209], v[38:39], 0 cbsz:2\n"
            "v_exp_f32 v165, v133\n"
            "v_add_u32_dpp v219, v196, v214 quad_perm:[0,0,0,0] row_mask:0xf bank_mask:0xf\n"
            "v_mfma_f32_4x4x4_16b_bf16 v[204:207], v[202:203], v[198:199], 0\n"
            "v_exp_f32 v166, v134\n"
            "buffer_load_dwordx4 v[32:35], v219, s[28:31], 0 offen\n"
            "buffer_load_dwordx4 v[36:39], v219, s[28:31], 0 offen offset:256\n"
            "v_exp_f32 v167, v135\n"
            "v_pk_fma_f32 v[0:1], v[144:145], v[176:177], v[0:1] op_sel_hi:[0,1,1]\n"
            "v_pk_fma_f32 v[2:3], v[144:145], v[178:179], v[2:3] op_sel_hi:[0,1,1]\n"
            "v_pk_fma_f32 v[4:5], v[148:149], v[180:181], v[4:5] op_sel_hi:[0,1,1]\n"
            "v_pk_fma_f32 v[6:7], v[148:149], v[182:183], v[6:7] op_sel_hi:[0,1,1]\n"
            "v_pk_fma_f32 v[8:9], v[152:153], v[184:185], v[8:9] op_sel_hi:[0,1,1]\n"
            "v_pk_fma_f32 v[10:11], v[152:153], v[186:187], v[10:11] op_sel_hi:[0,1,1]\n"
            "v_pk_fma_f32 v[12:13], v[156:157], v[188:189], v[12:13] op_sel_hi:[0,1,1]\n"
            "v_pk_fma_f32 v[14:15], v[156:157], v[190:191], v[14:15] op_sel_hi:[0,1,1]\n"
            "v_pk_add_f32 v[160:161], v[160:161], 1.0 op_sel_hi:[1,0]\n"
            "v_pk_add_f32 v[162:163], v[162:163], 1.0 op_sel_hi:[1,0]\n"
            "v_pk_add_f32 v[164:165], v[164:165], 1.0 op_sel_hi:[1,0]\n"
            "v_pk_add_f32 v[166:167], v[166:167], 1.0 op_sel_hi:[1,0]\n"
            "s_cmp_eq_u32 s69, 1\n"
            "s_cbranch_scc1 Lf00_%=\n"
            "Lr00_%=:\n"
            "v_mfma_f32_16x16x32_bf16 v[140:143], v[192:195], v[124:127], 0\n"
            "ds_bpermute_b32 v192, v253, v232\n"
            "ds_bpermute_b32 v193, v253, v233\n"
            "ds_bpermute_b32 v194, v253, v234\n"
            "ds_bpermute_b32 v195, v253, v235\n"
            "ds_read_b32 v230, v211 offset:4\n"
            "ds_read_u16 v210, v237\n"
            "v_rcp_f32 v160, v160\n"
            "v_rcp_f32 v161, v161\n"
            "v_mfma_f32_4x4x4_16b_bf16 v[176:179], v[208:209], v[48:49], 0 cbsz:2 abid:1\n"
            "v_rcp_f32 v162, v162\n"
            "v_mfma_f32_4x4x4_16b_bf16 v[180:183], v[208:209], v[50:51], 0 cbsz:2 abid:1\n"
            "v_rcp_f32 v163, v163\n"
            "v_mfma_f32_4x4x4_16b_bf16 v[184:187], v[208:209], v[52:53], 0 cbsz:2 abid:1\n"
            "v_rcp_f32 v164, v164\n"
            "v_mfma_f32_4x4x4_16b_bf16 v[188:191], v[208:209], v[54:55], 0 cbsz:2 abid:1\n"
            "v_rcp_f32 v165, v165\n"
            "v_add_u32_dpp v220, v196, v214 quad_perm:[1,1,1,1] row_mask:0xf bank_mask:0xf\n"
            "v_mfma_f32_4x4x4_16b_bf16 v[204:207], v[200:201], v[198:199], v[204:207]\n"
            "v_rcp_f32 v166, v166\n"
            "buffer_load_dwordx4 v[48:51], v220, s[28:31], 0 offen\n"
            "buffer_load_dwordx4 v[52:55], v220, s[28:31], 0 offen offset:256\n"
            "v_rcp_f32 v167, v167\n"
            "v_pk_fma_f32 v[0:1], v[144:145], v[176:177], v[0:1] op_sel:[1,0,0]\n"
            "v_pk_fma_f32 v[2:3], v[144:145], v[178:179], v[2:3] op_sel:[1,0,0]\n"
            "v_pk_fma_f32 v[4:5], v[148:149], v[180:181], v[4:5] op_sel:[1,0,0]\n"
            "v_pk_fma_f32 v[6:7], v[148:149], v[182:183], v[6:7] op_sel:[1,0,0]\n"
            "v_pk_fma_f32 v[8:9], v[152:153], v[184:185], v[8:9] op_sel:[1,0,0]\n"
            "v_pk_fma_f32 v[10:11], v[152:153], v[186:187], v[10:11] op_sel:[1,0,0]\n"
            "v_pk_fma_f32 v[12:13], v[156:157], v[188:189], v[12:13] op_sel:[1,0,0]\n"
            "v_pk_fma_f32 v[14:15], v[156:157], v[190:191], v[14:15] op_sel:[1,0,0]\n"
            "v_pk_mul_f32 v[160:161], v[160:161], v[128:129]\n"
            "v_pk_mul_f32 v[162:163], v[162:163], v[130:131]\n"
            "v_pk_mul_f32 v[164:165], v[164:165], v[132:133]\n"
            "v_pk_mul_f32 v[166:167], v[166:167], v[134:135]\n"
            "s_cmp_eq_u32 s69, 2\n"
            "s_cbranch_scc1 Lf01_%=\n"
            "Lr01_%=:\n"
            "s_waitcnt vmcnt(12) lgkmcnt(0)\n"
            "v_add_u32 v230, v230, v229\n"
            "buffer_load_dwordx4 v[232:235], v230, s[24:27], 0 offen\n"
            "s_bitcmp0_b32 s85, 0\n"
            "s_cbranch_scc1 Lnps02_%=\n"
            "s_buffer_load_dwordx2 s[86:87], s[28:31], s67 offset:60\n"
            "s_buffer_load_dwordx2 s[86:87], s[28:31], s67 offset:188\n"
            "s_buffer_load_dwordx2 s[86:87], s[28:31], s67 offset:316\n"
            "s_buffer_load_dwordx2 s[86:87], s[28:31], s67 offset:444\n"
            "s_add_u32 s67, s67, 524288\n"
            "Lnps02_%=:\n"
            "s_lshr_b32 s85, s85, 1\n"
            "v_mfma_f32_16x16x32_bf16 v[128:131], v[192:195], v[96:99], 0\n"
            "v_exp_f32 v168, v136\n"
            "v_exp_f32 v169, v137\n"
            "v_mfma_f32_4x4x4_16b_bf16 v[176:179], v[208:209], v[64:65], 0 cbsz:2 abid:2\n"
            "v_exp_f32 v170, v138\n"
            "v_mfma_f32_4x4x4_16b_bf16 v[180:183], v[208:209], v[66:67], 0 cbsz:2 abid:2\n"
            "v_exp_f32 v171, v139\n"
            "v_mfma_f32_4x4x4_16b_bf16 v[184:187], v[208:209], v[68:69], 0 cbsz:2 abid:2\n"
            "v_exp_f32 v172, v140\n"
            "v_mfma_f32_4x4x4_16b_bf16 v[188:191], v[208:209], v[70:71], 0 cbsz:2 abid:2\n"
            "v_exp_f32 v173, v141\n"
            "v_add_u32_dpp v221, v196, v214 quad_perm:[2,2,2,2] row_mask:0xf bank_mask:0xf\n"
            "v_exp_f32 v174, v142\n"
            "buffer_load_dwordx4 v[64:67], v221, s[28:31], 0 offen\n"
            "buffer_load_dwordx4 v[68:71], v221, s[28:31], 0 offen offset:256\n"
            "v_exp_f32 v175, v143\n"
            "v_pk_fma_f32 v[0:1], v[146:147], v[176:177], v[0:1] op_sel_hi:[0,1,1]\n"
            "v_pk_fma_f32 v[2:3], v[146:147], v[178:179], v[2:3] op_sel_hi:[0,1,1]\n"
            "v_pk_fma_f32 v[4:5], v[150:151], v[180:181], v[4:5] op_sel_hi:[0,1,1]\n"
            "v_pk_fma_f32 v[6:7], v[150:151], v[182:183], v[6:7] op_sel_hi:[0,1,1]\n"
            "v_pk_fma_f32 v[8:9], v[154:155], v[184:185], v[8:9] op_sel_hi:[0,1,1]\n"
            "v_pk_fma_f32 v[10:11], v[154:155], v[186:187], v[10:11] op_sel_hi:[0,1,1]\n"
            "v_pk_fma_f32 v[12:13], v[158:159], v[188:189], v[12:13] op_sel_hi:[0,1,1]\n"
            "v_pk_fma_f32 v[14:15], v[158:159], v[190:191], v[14:15] op_sel_hi:[0,1,1]\n"
            "v_pk_add_f32 v[168:169], v[168:169], 1.0 op_sel_hi:[1,0]\n"
            "v_pk_add_f32 v[170:171], v[170:171], 1.0 op_sel_hi:[1,0]\n"
            "v_pk_add_f32 v[172:173], v[172:173], 1.0 op_sel_hi:[1,0]\n"
            "v_pk_add_f32 v[174:175], v[174:175], 1.0 op_sel_hi:[1,0]\n"
            "v_cvt_pk_bf16_f32 v204, v204, v205\n"
            "v_cvt_pk_bf16_f32 v205, v206, v207\n"
            "ds_write_b64 v212, v[204:205]\n"
            "s_cmp_eq_u32 s69, 3\n"
            "s_cbranch_scc1 Lf02_%=\n"
            "Lr02_%=:\n"
            "v_mfma_f32_16x16x32_bf16 v[132:135], v[192:195], v[100:103], 0\n"
            "v_rcp_f32 v168, v168\n"
            "v_rcp_f32 v169, v169\n"
            "v_mfma_f32_4x4x4_16b_bf16 v[176:179], v[208:209], v[80:81], 0 cbsz:2 abid:3\n"
            "v_rcp_f32 v170, v170\n"
            "v_mfma_f32_4x4x4_16b_bf16 v[180:183], v[208:209], v[82:83], 0 cbsz:2 abid:3\n"
            "v_rcp_f32 v171, v171\n"
            "v_mfma_f32_4x4x4_16b_bf16 v[184:187], v[208:209], v[84:85], 0 cbsz:2 abid:3\n"
            "v_rcp_f32 v172, v172\n"
            "v_mfma_f32_4x4x4_16b_bf16 v[188:191], v[208:209], v[86:87], 0 cbsz:2 abid:3\n"
            "v_rcp_f32 v173, v173\n"
            "v_add_u32_dpp v222, v196, v214 quad_perm:[3,3,3,3] row_mask:0xf bank_mask:0xf\n"
            "v_rcp_f32 v174, v174\n"
            "buffer_load_dwordx4 v[80:83], v222, s[28:31], 0 offen\n"
            "buffer_load_dwordx4 v[84:87], v222, s[28:31], 0 offen offset:256\n"
            "v_rcp_f32 v175, v175\n"
            "v_pk_fma_f32 v[0:1], v[146:147], v[176:177], v[0:1] op_sel:[1,0,0]\n"
            "v_pk_fma_f32 v[2:3], v[146:147], v[178:179], v[2:3] op_sel:[1,0,0]\n"
            "v_pk_fma_f32 v[4:5], v[150:151], v[180:181], v[4:5] op_sel:[1,0,0]\n"
            "v_pk_fma_f32 v[6:7], v[150:151], v[182:183], v[6:7] op_sel:[1,0,0]\n"
            "v_pk_fma_f32 v[8:9], v[154:155], v[184:185], v[8:9] op_sel:[1,0,0]\n"
            "v_pk_fma_f32 v[10:11], v[154:155], v[186:187], v[10:11] op_sel:[1,0,0]\n"
            "v_pk_fma_f32 v[12:13], v[158:159], v[188:189], v[12:13] op_sel:[1,0,0]\n"
            "v_pk_fma_f32 v[14:15], v[158:159], v[190:191], v[14:15] op_sel:[1,0,0]\n"
            "v_pk_mul_f32 v[168:169], v[168:169], v[136:137]\n"
            "v_pk_mul_f32 v[170:171], v[170:171], v[138:139]\n"
            "v_pk_mul_f32 v[172:173], v[172:173], v[140:141]\n"
            "v_pk_mul_f32 v[174:175], v[174:175], v[142:143]\n"
            "s_cmp_eq_u32 s69, 4\n"
            "s_cbranch_scc1 Lf03_%=\n"
            "Lr03_%=:\n"
            "v_add_u32 v238, 32, v238\n"
            "ds_read_b128 v[196:199], v210\n"
            "ds_read_u16 v211, v238\n"
            "v_mfma_f32_16x16x32_bf16 v[136:139], v[192:195], v[104:107], 0\n"
            "v_exp_f32 v144, v128\n"
            "v_exp_f32 v145, v129\n"
            "s_waitcnt vmcnt(13)\n"
            "v_mfma_f32_4x4x4_16b_bf16 v[176:179], v[208:209], v[40:41], 0 cbsz:2\n"
            "v_exp_f32 v146, v130\n"
            "v_mfma_f32_4x4x4_16b_bf16 v[180:183], v[208:209], v[42:43], 0 cbsz:2\n"
            "v_exp_f32 v147, v131\n"
            "v_mfma_f32_4x4x4_16b_bf16 v[184:187], v[208:209], v[44:45], 0 cbsz:2\n"
            "v_exp_f32 v148, v132\n"
            "v_mfma_f32_4x4x4_16b_bf16 v[188:191], v[208:209], v[46:47], 0 cbsz:2\n"
            "v_exp_f32 v149, v133\n"
            "v_exp_f32 v150, v134\n"
            "buffer_load_dwordx4 v[40:43], v219, s[28:31], 0 offen offset:512\n"
            "buffer_load_dwordx4 v[44:47], v219, s[28:31], 0 offen offset:768\n"
            "v_exp_f32 v151, v135\n"
            "v_pk_fma_f32 v[16:17], v[160:161], v[176:177], v[16:17] op_sel_hi:[0,1,1]\n"
            "v_pk_fma_f32 v[18:19], v[160:161], v[178:179], v[18:19] op_sel_hi:[0,1,1]\n"
            "v_pk_fma_f32 v[20:21], v[164:165], v[180:181], v[20:21] op_sel_hi:[0,1,1]\n"
            "v_pk_fma_f32 v[22:23], v[164:165], v[182:183], v[22:23] op_sel_hi:[0,1,1]\n"
            "v_pk_fma_f32 v[24:25], v[168:169], v[184:185], v[24:25] op_sel_hi:[0,1,1]\n"
            "v_pk_fma_f32 v[26:27], v[168:169], v[186:187], v[26:27] op_sel_hi:[0,1,1]\n"
            "v_pk_fma_f32 v[28:29], v[172:173], v[188:189], v[28:29] op_sel_hi:[0,1,1]\n"
            "v_pk_fma_f32 v[30:31], v[172:173], v[190:191], v[30:31] op_sel_hi:[0,1,1]\n"
            "v_pk_add_f32 v[144:145], v[144:145], 1.0 op_sel_hi:[1,0]\n"
            "v_pk_add_f32 v[146:147], v[146:147], 1.0 op_sel_hi:[1,0]\n"
            "v_pk_add_f32 v[148:149], v[148:149], 1.0 op_sel_hi:[1,0]\n"
            "v_pk_add_f32 v[150:151], v[150:151], 1.0 op_sel_hi:[1,0]\n"
            "s_cmp_eq_u32 s69, 1\n"
            "s_cbranch_scc1 Lf10_%=\n"
            "Lr10_%=:\n"
            "v_mfma_f32_16x16x32_bf16 v[140:143], v[192:195], v[108:111], 0\n"
            "v_rcp_f32 v144, v144\n"
            "v_rcp_f32 v145, v145\n"
            "v_mfma_f32_4x4x4_16b_bf16 v[176:179], v[208:209], v[56:57], 0 cbsz:2 abid:1\n"
            "v_rcp_f32 v146, v146\n"
            "v_mfma_f32_4x4x4_16b_bf16 v[180:183], v[208:209], v[58:59], 0 cbsz:2 abid:1\n"
            "v_rcp_f32 v147, v147\n"
            "v_mfma_f32_4x4x4_16b_bf16 v[184:187], v[208:209], v[60:61], 0 cbsz:2 abid:1\n"
            "v_rcp_f32 v148, v148\n"
            "v_mfma_f32_4x4x4_16b_bf16 v[188:191], v[208:209], v[62:63], 0 cbsz:2 abid:1\n"
            "v_rcp_f32 v149, v149\n"
            "v_rcp_f32 v150, v150\n"
            "buffer_load_dwordx4 v[56:59], v220, s[28:31], 0 offen offset:512\n"
            "buffer_load_dwordx4 v[60:63], v220, s[28:31], 0 offen offset:768\n"
            "v_rcp_f32 v151, v151\n"
            "v_pk_fma_f32 v[16:17], v[160:161], v[176:177], v[16:17] op_sel:[1,0,0]\n"
            "v_pk_fma_f32 v[18:19], v[160:161], v[178:179], v[18:19] op_sel:[1,0,0]\n"
            "v_pk_fma_f32 v[20:21], v[164:165], v[180:181], v[20:21] op_sel:[1,0,0]\n"
            "v_pk_fma_f32 v[22:23], v[164:165], v[182:183], v[22:23] op_sel:[1,0,0]\n"
            "v_pk_fma_f32 v[24:25], v[168:169], v[184:185], v[24:25] op_sel:[1,0,0]\n"
            "v_pk_fma_f32 v[26:27], v[168:169], v[186:187], v[26:27] op_sel:[1,0,0]\n"
            "v_pk_fma_f32 v[28:29], v[172:173], v[188:189], v[28:29] op_sel:[1,0,0]\n"
            "v_pk_fma_f32 v[30:31], v[172:173], v[190:191], v[30:31] op_sel:[1,0,0]\n"
            "v_pk_mul_f32 v[144:145], v[144:145], v[128:129]\n"
            "v_pk_mul_f32 v[146:147], v[146:147], v[130:131]\n"
            "v_pk_mul_f32 v[148:149], v[148:149], v[132:133]\n"
            "v_pk_mul_f32 v[150:151], v[150:151], v[134:135]\n"
            "s_cmp_eq_u32 s69, 2\n"
            "s_cbranch_scc1 Lf11_%=\n"
            "Lr11_%=:\n"
            "v_mfma_f32_16x16x32_bf16 v[128:131], v[192:195], v[112:115], 0\n"
            "v_exp_f32 v152, v136\n"
            "v_exp_f32 v153, v137\n"
            "s_waitcnt vmcnt(13)\n"
            "v_mfma_f32_4x4x4_16b_bf16 v[176:179], v[208:209], v[72:73], 0 cbsz:2 abid:2\n"
            "v_exp_f32 v154, v138\n"
            "v_mfma_f32_4x4x4_16b_bf16 v[180:183], v[208:209], v[74:75], 0 cbsz:2 abid:2\n"
            "v_exp_f32 v155, v139\n"
            "v_mfma_f32_4x4x4_16b_bf16 v[184:187], v[208:209], v[76:77], 0 cbsz:2 abid:2\n"
            "v_exp_f32 v156, v140\n"
            "v_mfma_f32_4x4x4_16b_bf16 v[188:191], v[208:209], v[78:79], 0 cbsz:2 abid:2\n"
            "v_exp_f32 v157, v141\n"
            "v_exp_f32 v158, v142\n"
            "buffer_load_dwordx4 v[72:75], v221, s[28:31], 0 offen offset:512\n"
            "buffer_load_dwordx4 v[76:79], v221, s[28:31], 0 offen offset:768\n"
            "v_exp_f32 v159, v143\n"
            "v_pk_fma_f32 v[16:17], v[162:163], v[176:177], v[16:17] op_sel_hi:[0,1,1]\n"
            "v_pk_fma_f32 v[18:19], v[162:163], v[178:179], v[18:19] op_sel_hi:[0,1,1]\n"
            "v_pk_fma_f32 v[20:21], v[166:167], v[180:181], v[20:21] op_sel_hi:[0,1,1]\n"
            "v_pk_fma_f32 v[22:23], v[166:167], v[182:183], v[22:23] op_sel_hi:[0,1,1]\n"
            "v_pk_fma_f32 v[24:25], v[170:171], v[184:185], v[24:25] op_sel_hi:[0,1,1]\n"
            "v_pk_fma_f32 v[26:27], v[170:171], v[186:187], v[26:27] op_sel_hi:[0,1,1]\n"
            "v_pk_fma_f32 v[28:29], v[174:175], v[188:189], v[28:29] op_sel_hi:[0,1,1]\n"
            "v_pk_fma_f32 v[30:31], v[174:175], v[190:191], v[30:31] op_sel_hi:[0,1,1]\n"
            "v_pk_add_f32 v[152:153], v[152:153], 1.0 op_sel_hi:[1,0]\n"
            "v_pk_add_f32 v[154:155], v[154:155], 1.0 op_sel_hi:[1,0]\n"
            "v_pk_add_f32 v[156:157], v[156:157], 1.0 op_sel_hi:[1,0]\n"
            "v_pk_add_f32 v[158:159], v[158:159], 1.0 op_sel_hi:[1,0]\n"
            "s_cmp_eq_u32 s69, 3\n"
            "s_cbranch_scc1 Lf12_%=\n"
            "Lr12_%=:\n"
            "v_mfma_f32_16x16x32_bf16 v[132:135], v[192:195], v[116:119], 0\n"
            "v_rcp_f32 v152, v152\n"
            "v_rcp_f32 v153, v153\n"
            "v_mfma_f32_4x4x4_16b_bf16 v[176:179], v[208:209], v[88:89], 0 cbsz:2 abid:3\n"
            "v_rcp_f32 v154, v154\n"
            "v_mfma_f32_4x4x4_16b_bf16 v[180:183], v[208:209], v[90:91], 0 cbsz:2 abid:3\n"
            "v_rcp_f32 v155, v155\n"
            "v_mfma_f32_4x4x4_16b_bf16 v[184:187], v[208:209], v[92:93], 0 cbsz:2 abid:3\n"
            "v_rcp_f32 v156, v156\n"
            "v_mfma_f32_4x4x4_16b_bf16 v[188:191], v[208:209], v[94:95], 0 cbsz:2 abid:3\n"
            "ds_read_b64 v[208:209], v213\n"
            "v_rcp_f32 v157, v157\n"
            "v_rcp_f32 v158, v158\n"
            "buffer_load_dwordx4 v[88:91], v222, s[28:31], 0 offen offset:512\n"
            "buffer_load_dwordx4 v[92:95], v222, s[28:31], 0 offen offset:768\n"
            "v_rcp_f32 v159, v159\n"
            "v_pk_fma_f32 v[16:17], v[162:163], v[176:177], v[16:17] op_sel:[1,0,0]\n"
            "v_pk_fma_f32 v[18:19], v[162:163], v[178:179], v[18:19] op_sel:[1,0,0]\n"
            "v_pk_fma_f32 v[20:21], v[166:167], v[180:181], v[20:21] op_sel:[1,0,0]\n"
            "v_pk_fma_f32 v[22:23], v[166:167], v[182:183], v[22:23] op_sel:[1,0,0]\n"
            "v_pk_fma_f32 v[24:25], v[170:171], v[184:185], v[24:25] op_sel:[1,0,0]\n"
            "v_pk_fma_f32 v[26:27], v[170:171], v[186:187], v[26:27] op_sel:[1,0,0]\n"
            "v_pk_fma_f32 v[28:29], v[174:175], v[188:189], v[28:29] op_sel:[1,0,0]\n"
            "v_pk_fma_f32 v[30:31], v[174:175], v[190:191], v[30:31] op_sel:[1,0,0]\n"
            "v_pk_mul_f32 v[152:153], v[152:153], v[136:137]\n"
            "v_pk_mul_f32 v[154:155], v[154:155], v[138:139]\n"
            "v_pk_mul_f32 v[156:157], v[156:157], v[140:141]\n"
            "v_pk_mul_f32 v[158:159], v[158:159], v[142:143]\n"
            "s_cmp_eq_u32 s69, 4\n"
            "s_cbranch_scc1 Lf13_%=\n"
            "Lr13_%=:\n"
            "s_and_b32 s68, s72, 0xff\n"
            "s_add_u32 s69, s69, s68\n"
            "s_lshr_b32 s72, s72, 8\n"
            "v_mov_b32 v231, v239\n"
            "v_mov_b32 v239, v252\n"
            "s_and_b32 s68, s62, 14\n"
            "s_lshr_b32 s62, s62, 4\n"
            "s_cmp_lg_u32 s68, 0\n"
            "s_cbranch_scc1 Lb1_%=\n"
            "v_mov_b32 v215, v216\n"
            "s_branch Lnext_%=\n"
            "Lb1_%=:\n"
            "s_cmp_lg_u32 s68, 6\n"
            "s_cbranch_scc1 Lb2_%=\n"
            "s_lshl_b32 s68, s3, 2\n"
            "v_mov_b32 v176, s68\n"
            "ds_write_b32 v176, v176\n"
            "s_branch Lnext_%=\n"
            "Lb2_%=:\n"
            "s_cmp_lg_u32 s68, 8\n"
            "s_cbranch_scc1 Lnext_%=\n"
            "s_waitcnt vmcnt(0)\n"
            "v_mov_b32 v176, 4\n"
            "v_mov_b32 v177, 1\n"
            "s_mov_b64 exec, 1\n"
            "ds_add_rtn_u32 v177, v176, v177\n"
            "s_waitcnt lgkmcnt(0)\n"
            "v_readfirstlane_b32 s68, v177\n"
            "s_mov_b64 exec, 0xff\n"
            "v_mov_b32 v176, s90\n"
            "v_mov_b32 v177, s90\n"
            "v_mov_b32 v178, s90\n"
            "v_mov_b32 v179, s90\n"
            "s_cmp_lg_u32 s68, 7\n"
            "s_cbranch_scc1 Lpub2_%=\n"
            "global_store_dwordx4 v214, v[176:179], s[56:57] sc1\n"
            "Lpub2_%=:\n"
            "s_mov_b64 exec, -1\n"
            "s_branch Lnext_%=\n"
            "Lprio_%=:\n"
            "s_cmp_eq_u32 s73, 1\n"
            "s_cbranch_scc1 Llast_%=\n"
            "s_lshl_b32 s68, s73, 1\n"
            "s_lshr_b32 s68, s61, s68\n"
            "s_bitcmp1_b32 s68, 1\n"
            "s_cbranch_scc1 Lq2_%=\n"
            "s_bitcmp1_b32 s68, 0\n"
            "s_setprio 0\n"
            "s_cbranch_scc0 Lq9_%=\n"
            "s_setprio 1\n"
            "s_branch Lq9_%=\n"
            "Lq2_%=:\n"
            "s_bitcmp1_b32 s68, 0\n"
            "s_setprio 2\n"
            "s_cbranch_scc0 Lq9_%=\n"
            "s_setprio 3\n"
            "Lq9_%=:\n"
            "s_cmp_lg_u32 s73, 2\n"
            "s_cbranch_scc1 Ld2_%=\n"
            "v_mov_b32 v223, 0\n"
            "global_load_dword v223, v223, s[58:59] sc1\n"
            "s_branch Ld2_%=\n"
            "Lf00_%=:\n"
            "s_call_b64 s[70:71], Lflush0_%=\n"
            "s_branch Lr00_%=\n"
            "Lf01_%=:\n"
            "s_call_b64 s[70:71], Lflush0_%=\n"
            "s_branch Lr01_%=\n"
            "Lf02_%=:\n"
            "s_call_b64 s[70:71], Lflush0_%=\n"
            "s_branch Lr02_%=\n"
            "Lf03_%=:\n"
            "s_call_b64 s[70:71], Lflush0_%=\n"
            "s_branch Lr03_%=\n"
            "Lflush0_%=:\n"
            "s_bitcmp0_b32 s62, 0\n"
            "s_cbranch_scc1 Lfa0_%=\n"
            "Lfw0_%=:\n"
            "v_mov_b32 v184, s41\n"
            "ds_read_b32 v184, v184\n"
            "ds_read_b128 v[176:179], v231 offset:0\n"
            "ds_read_b128 v[180:183], v231 offset:1024\n"
            "s_waitcnt lgkmcnt(0)\n"
            "v_readfirstlane_b32 s68, v184\n"
            "s_cmp_eq_u32 s68, 0\n"
            "s_cbranch_scc1 Lfw0_%=\n"
            "v_mfma_f32_4x4x4_16b_bf16 v[0:3], v[224:225], v[176:177], v[0:3]\n"
            "v_mfma_f32_4x4x4_16b_bf16 v[4:7], v[224:225], v[178:179], v[4:7]\n"
            "v_mfma_f32_4x4x4_16b_bf16 v[8:11], v[224:225], v[180:181], v[8:11]\n"
            "v_mfma_f32_4x4x4_16b_bf16 v[12:15], v[224:225], v[182:183], v[12:15]\n"
            "s_nop 1\n"
            "Lfa0_%=:\n"
            "s_cmp_lg_u32 s72, 0xff\n"
            "s_cbranch_scc1 Lfb0_%=\n"
            "ds_read_b128 v[184:187], v236 offset:0\n"
            "ds_read_b128 v[188:191], v236 offset:1024\n"
            "Lfb0_%=:\n"
            "v_cvt_pk_bf16_f32 v176, v0, v1\n"
            "v_cvt_pk_bf16_f32 v177, v2, v3\n"
            "v_cvt_pk_bf16_f32 v178, v4, v5\n"
            "v_cvt_pk_bf16_f32 v179, v6, v7\n"
            "v_cvt_pk_bf16_f32 v180, v8, v9\n"
            "v_cvt_pk_bf16_f32 v181, v10, v11\n"
            "v_cvt_pk_bf16_f32 v182, v12, v13\n"
            "v_cvt_pk_bf16_f32 v183, v14, v15\n"
            "s_bitcmp1_b32 s62, 1\n"
            "s_cbranch_scc1 Lpark0_%=\n"
            "buffer_store_dwordx4 v[176:179], v215, s[20:23], 0 offen offset:0 sc1\n"
            "buffer_store_dwordx4 v[180:183], v215, s[20:23], 0 offen offset:256 sc1\n"
            "s_branch Lzero0_%=\n"
            "Lpark0_%=:\n"
            "ds_write_b128 v231, v[176:179] offset:0\n"
            "ds_write_b128 v231, v[180:183] offset:1024\n"
            "Lzero0_%=:\n"
            "s_cmp_lg_u32 s72, 0xff\n"
            "s_cbranch_scc1 Lzz0_%=\n"
            "s_waitcnt lgkmcnt(0)\n"
            "v_mfma_f32_4x4x4_16b_bf16 v[0:3], v[224:225], v[184:185], 0\n"
            "v_mfma_f32_4x4x4_16b_bf16 v[4:7], v[224:225], v[186:187], 0\n"
            "v_mfma_f32_4x4x4_16b_bf16 v[8:11], v[224:225], v[188:189], 0\n"
            "v_mfma_f32_4x4x4_16b_bf16 v[12:15], v[224:225], v[190:191], 0\n"
            "s_setpc_b64 s[70:71]\n"
            "Lzz0_%=:\n"
            "v_mov_b64 v[0:1], 0\n"
            "v_mov_b64 v[2:3], 0\n"
            "v_mov_b64 v[4:5], 0\n"
            "v_mov_b64 v[6:7], 0\n"
            "v_mov_b64 v[8:9], 0\n"
            "v_mov_b64 v[10:11], 0\n"
            "v_mov_b64 v[12:13], 0\n"
            "v_mov_b64 v[14:15], 0\n"
            "s_setpc_b64 s[70:71]\n"
            "Lf10_%=:\n"
            "s_call_b64 s[70:71], Lflush1_%=\n"
            "s_branch Lr10_%=\n"
            "Lf11_%=:\n"
            "s_call_b64 s[70:71], Lflush1_%=\n"
            "s_branch Lr11_%=\n"
            "Lf12_%=:\n"
            "s_call_b64 s[70:71], Lflush1_%=\n"
            "s_branch Lr12_%=\n"
            "Lf13_%=:\n"
            "s_call_b64 s[70:71], Lflush1_%=\n"
            "s_branch Lr13_%=\n"
            "Lflush1_%=:\n"
            "s_bitcmp0_b32 s62, 0\n"
            "s_cbranch_scc1 Lfa1_%=\n"
            "Lfw1_%=:\n"
            "v_mov_b32 v184, s41\n"
            "ds_read_b32 v184, v184\n"
            "ds_read_b128 v[176:179], v231 offset:2048\n"
            "ds_read_b128 v[180:183], v231 offset:3072\n"
            "s_waitcnt lgkmcnt(0)\n"
            "v_readfirstlane_b32 s68, v184\n"
            "s_cmp_eq_u32 s68, 0\n"
            "s_cbranch_scc1 Lfw1_%=\n"
            "v_mfma_f32_4x4x4_16b_bf16 v[16:19], v[224:225], v[176:177], v[16:19]\n"
            "v_mfma_f32_4x4x4_16b_bf16 v[20:23], v[224:225], v[178:179], v[20:23]\n"
            "v_mfma_f32_4x4x4_16b_bf16 v[24:27], v[224:225], v[180:181], v[24:27]\n"
            "v_mfma_f32_4x4x4_16b_bf16 v[28:31], v[224:225], v[182:183], v[28:31]\n"
            "s_nop 1\n"
            "Lfa1_%=:\n"
            "s_cmp_lg_u32 s72, 0xff\n"
            "s_cbranch_scc1 Lfb1_%=\n"
            "ds_read_b128 v[184:187], v236 offset:2048\n"
            "ds_read_b128 v[188:191], v236 offset:3072\n"
            "Lfb1_%=:\n"
            "v_cvt_pk_bf16_f32 v176, v16, v17\n"
            "v_cvt_pk_bf16_f32 v177, v18, v19\n"
            "v_cvt_pk_bf16_f32 v178, v20, v21\n"
            "v_cvt_pk_bf16_f32 v179, v22, v23\n"
            "v_cvt_pk_bf16_f32 v180, v24, v25\n"
            "v_cvt_pk_bf16_f32 v181, v26, v27\n"
            "v_cvt_pk_bf16_f32 v182, v28, v29\n"
            "v_cvt_pk_bf16_f32 v183, v30, v31\n"
            "s_bitcmp1_b32 s62, 1\n"
            "s_cbranch_scc1 Lpark1_%=\n"
            "buffer_store_dwordx4 v[176:179], v215, s[20:23], 0 offen offset:512 sc1\n"
            "buffer_store_dwordx4 v[180:183], v215, s[20:23], 0 offen offset:768 sc1\n"
            "s_branch Lzero1_%=\n"
            "Lpark1_%=:\n"
            "ds_write_b128 v231, v[176:179] offset:2048\n"
            "ds_write_b128 v231, v[180:183] offset:3072\n"
            "Lzero1_%=:\n"
            "s_cmp_lg_u32 s72, 0xff\n"
            "s_cbranch_scc1 Lzz1_%=\n"
            "s_waitcnt lgkmcnt(0)\n"
            "v_mfma_f32_4x4x4_16b_bf16 v[16:19], v[224:225], v[184:185], 0\n"
            "v_mfma_f32_4x4x4_16b_bf16 v[20:23], v[224:225], v[186:187], 0\n"
            "v_mfma_f32_4x4x4_16b_bf16 v[24:27], v[224:225], v[188:189], 0\n"
            "v_mfma_f32_4x4x4_16b_bf16 v[28:31], v[224:225], v[190:191], 0\n"
            "s_setpc_b64 s[70:71]\n"
            "Lzz1_%=:\n"
            "v_mov_b64 v[16:17], 0\n"
            "v_mov_b64 v[18:19], 0\n"
            "v_mov_b64 v[20:21], 0\n"
            "v_mov_b64 v[22:23], 0\n"
            "v_mov_b64 v[24:25], 0\n"
            "v_mov_b64 v[26:27], 0\n"
            "v_mov_b64 v[28:29], 0\n"
            "v_mov_b64 v[30:31], 0\n"
            "s_setpc_b64 s[70:71]\n"
            "Llk_%=:\n"
            "s_waitcnt vmcnt(4)\n"
            "v_readfirstlane_b32 s73, v223\n"
            "s_cmp_lg_u32 s73, s90\n"
            "s_cbranch_scc1 Llk2_%=\n"
            "buffer_load_dwordx4 v[32:35], v217, s[20:23], s63 offen offset:0 sc1\n"
            "buffer_load_dwordx4 v[36:39], v217, s[20:23], s63 offen offset:256 sc1\n"
            "buffer_load_dwordx4 v[48:51], v217, s[20:23], s63 offen offset:512 sc1\n"
            "buffer_load_dwordx4 v[52:55], v217, s[20:23], s63 offen offset:768 sc1\n"
            "s_branch Llkr_%=\n"
            "Llk2_%=:\n"
            "v_mov_b32 v223, 0\n"
            "global_load_dword v223, v223, s[58:59] sc1\n"
            "s_branch Llkr_%=\n"
            "Lspin_%=:\n"
            "s_waitcnt vmcnt(0)\n"
            "s_mov_b32 s72, 0x400000\n"
            "Lsp1_%=:\n"
            "v_readfirstlane_b32 s73, v223\n"
            "s_sub_u32 s72, s72, 1\n"
            "s_cmp_eq_u32 s72, 0\n"
            "s_cbranch_scc1 Lsp2_%=\n"
            "s_cmp_eq_u32 s73, s90\n"
            "s_cbranch_scc1 Lsp2_%=\n"
            "s_sleep 1\n"
            "v_mov_b32 v223, 0\n"
            "global_load_dword v223, v223, s[58:59] sc1\n"
            "s_waitcnt vmcnt(0)\n"
            "s_branch Lsp1_%=\n"
            "Lsp2_%=:\n"
            "buffer_load_dwordx4 v[32:35], v217, s[20:23], s63 offen offset:0 sc1\n"
            "buffer_load_dwordx4 v[36:39], v217, s[20:23], s63 offen offset:256 sc1\n"
            "buffer_load_dwordx4 v[48:51], v217, s[20:23], s63 offen offset:512 sc1\n"
            "buffer_load_dwordx4 v[52:55], v217, s[20:23], s63 offen offset:768 sc1\n"
            "s_branch Lgo_%=\n"
            "Llast_%=:\n"
            "s_waitcnt vmcnt(16)\n"
            "v_readfirstlane_b32 s73, v223\n"
            "v_mov_b32 v222, v217\n"
            "s_cmp_eq_u32 s73, s90\n"
            "s_cbranch_scc1 Lp4_%=\n"
            "v_bfrev_b32 v222, 1\n"
            "v_mov_b32 v223, 0\n"
            "global_load_dword v223, v223, s[58:59] sc1\n"
            "Lp4_%=:\n"
            "v_mfma_f32_16x16x32_bf16 v[136:139], v[192:195], v[120:123], 0\n"
            "v_exp_f32 v160, v128\n"
            "v_exp_f32 v161, v129\n"
            "s_waitcnt vmcnt(13) lgkmcnt(0)\n"
            "v_mfma_f32_4x4x4_16b_bf16 v[176:179], v[208:209], v[32:33], 0 cbsz:2\n"
            "v_exp_f32 v162, v130\n"
            "v_mfma_f32_4x4x4_16b_bf16 v[180:183], v[208:209], v[34:35], 0 cbsz:2\n"
            "v_exp_f32 v163, v131\n"
            "v_mfma_f32_4x4x4_16b_bf16 v[184:187], v[208:209], v[36:37], 0 cbsz:2\n"
            "v_exp_f32 v164, v132\n"
            "v_mfma_f32_4x4x4_16b_bf16 v[188:191], v[208:209], v[38:39], 0 cbsz:2\n"
            "v_exp_f32 v165, v133\n"
            "v_exp_f32 v166, v134\n"
            "buffer_load_dwordx4 v[32:35], v222, s[20:23], s63 offen offset:0 sc1\n"
            "buffer_load_dwordx4 v[36:39], v222, s[20:23], s63 offen offset:256 sc1\n"
            "ds_read_b128 v[236:239], v228 offset:0\n"
            "ds_read_b128 v[240:243], v228 offset:1024\n"
            "ds_read_b128 v[244:247], v228 offset:2048\n"
            "ds_read_b128 v[248:251], v228 offset:3072\n"
            "ds_read_b128 v[252:255], v228 offset:4096\n"
            "ds_read_b128 v[96:99], v228 offset:5120\n"
            "v_exp_f32 v167, v135\n"
            "v_pk_fma_f32 v[0:1], v[144:145], v[176:177], v[0:1] op_sel_hi:[0,1,1]\n"
            "v_pk_fma_f32 v[2:3], v[144:145], v[178:179], v[2:3] op_sel_hi:[0,1,1]\n"
            "v_pk_fma_f32 v[4:5], v[148:149], v[180:181], v[4:5] op_sel_hi:[0,1,1]\n"
            "v_pk_fma_f32 v[6:7], v[148:149], v[182:183], v[6:7] op_sel_hi:[0,1,1]\n"
            "v_pk_fma_f32 v[8:9], v[152:153], v[184:185], v[8:9] op_sel_hi:[0,1,1]\n"
            "v_pk_fma_f32 v[10:11], v[152:153], v[186:187], v[10:11] op_sel_hi:[0,1,1]\n"
            "v_pk_fma_f32 v[12:13], v[156:157], v[188:189], v[12:13] op_sel_hi:[0,1,1]\n"
            "v_pk_fma_f32 v[14:15], v[156:157], v[190:191], v[14:15] op_sel_hi:[0,1,1]\n"
            "v_pk_add_f32 v[160:161], v[160:161], 1.0 op_sel_hi:[1,0]\n"
            "v_pk_add_f32 v[162:163], v[162:163], 1.0 op_sel_hi:[1,0]\n"
            "v_pk_add_f32 v[164:165], v[164:165], 1.0 op_sel_hi:[1,0]\n"
            "v_pk_add_f32 v[166:167], v[166:167], 1.0 op_sel_hi:[1,0]\n"
            "v_mfma_f32_16x16x32_bf16 v[140:143], v[192:195], v[124:127], 0\n"
            "v_rcp_f32 v160, v160\n"
            "v_rcp_f32 v161, v161\n"
            "v_mfma_f32_4x4x4_16b_bf16 v[176:179], v[208:209], v[48:49], 0 cbsz:2 abid:1\n"
            "v_rcp_f32 v162, v162\n"
            "v_mfma_f32_4x4x4_16b_bf16 v[180:183], v[208:209], v[50:51], 0 cbsz:2 abid:1\n"
            "v_rcp_f32 v163, v163\n"
            "v_mfma_f32_4x4x4_16b_bf16 v[184:187], v[208:209], v[52:53], 0 cbsz:2 abid:1\n"
            "v_rcp_f32 v164, v164\n"
            "v_mfma_f32_4x4x4_16b_bf16 v[188:191], v[208:209], v[54:55], 0 cbsz:2 abid:1\n"
            "v_rcp_f32 v165, v165\n"
            "v_rcp_f32 v166, v166\n"
            "buffer_load_dwordx4 v[48:51], v222, s[20:23], s63 offen offset:512 sc1\n"
            "buffer_load_dwordx4 v[52:55], v222, s[20:23], s63 offen offset:768 sc1\n"
            "ds_read_b128 v[100:103], v228 offset:6144\n"
            "ds_read_b128 v[104:107], v228 offset:7168\n"
            "ds_read_b128 v[108:111], v228 offset:8192\n"
            "ds_read_b128 v[112:115], v228 offset:9216\n"
            "ds_read_b128 v[116:119], v228 offset:10240\n"
            "ds_read_b128 v[196:199], v228 offset:11264\n"
            "v_rcp_f32 v167, v167\n"
            "v_pk_fma_f32 v[0:1], v[144:145], v[176:177], v[0:1] op_sel:[1,0,0]\n"
            "v_pk_fma_f32 v[2:3], v[144:145], v[178:179], v[2:3] op_sel:[1,0,0]\n"
            "v_pk_fma_f32 v[4:5], v[148:149], v[180:181], v[4:5] op_sel:[1,0,0]\n"
            "v_pk_fma_f32 v[6:7], v[148:149], v[182:183], v[6:7] op_sel:[1,0,0]\n"
            "v_pk_fma_f32 v[8:9], v[152:153], v[184:185], v[8:9] op_sel:[1,0,0]\n"
            "v_pk_fma_f32 v[10:11], v[152:153], v[186:187], v[10:11] op_sel:[1,0,0]\n"
            "v_pk_fma_f32 v[12:13], v[156:157], v[188:189], v[12:13] op_sel:[1,0,0]\n"
            "v_pk_fma_f32 v[14:15], v[156:157], v[190:191], v[14:15] op_sel:[1,0,0]\n"
            "v_pk_mul_f32 v[160:161], v[160:161], v[128:129]\n"
            "v_pk_mul_f32 v[162:163], v[162:163], v[130:131]\n"
            "v_pk_mul_f32 v[164:165], v[164:165], v[132:133]\n"
            "v_pk_mul_f32 v[166:167], v[166:167], v[134:135]\n"
            "v_exp_f32 v168, v136\n"
            "v_exp_f32 v169, v137\n"
            "s_waitcnt vmcnt(12)\n"
            "v_mfma_f32_4x4x4_16b_bf16 v[176:179], v[208:209], v[64:65], 0 cbsz:2 abid:2\n"
            "v_exp_f32 v170, v138\n"
            "v_mfma_f32_4x4x4_16b_bf16 v[180:183], v[208:209], v[66:67], 0 cbsz:2 abid:2\n"
            "v_exp_f32 v171, v139\n"
            "v_mfma_f32_4x4x4_16b_bf16 v[184:187], v[208:209], v[68:69], 0 cbsz:2 abid:2\n"
            "v_exp_f32 v172, v140\n"
            "v_mfma_f32_4x4x4_16b_bf16 v[188:191], v[208:209], v[70:71], 0 cbsz:2 abid:2\n"
            "v_exp_f32 v173, v141\n"
            "v_exp_f32 v174, v142\n"
            "ds_read_b128 v[200:203], v228 offset:12288\n"
            "ds_read_b128 v[204:207], v228 offset:13312\n"
            "ds_read_b128 v[232:235], v228 offset:14336\n"
            "ds_read_b128 v[120:123], v228 offset:15360\n"
            "ds_read_b128 v[64:67], v228 offset:16384\n"
            "ds_read_b128 v[68:71], v228 offset:17408\n"
            "v_exp_f32 v175, v143\n"
            "v_pk_fma_f32 v[0:1], v[146:147], v[176:177], v[0:1] op_sel_hi:[0,1,1]\n"
            "v_pk_fma_f32 v[2:3], v[146:147], v[178:179], v[2:3] op_sel_hi:[0,1,1]\n"
            "v_pk_fma_f32 v[4:5], v[150:151], v[180:181], v[4:5] op_sel_hi:[0,1,1]\n"
            "v_pk_fma_f32 v[6:7], v[150:151], v[182:183], v[6:7] op_sel_hi:[0,1,1]\n"
            "v_pk_fma_f32 v[8:9], v[154:155], v[184:185], v[8:9] op_sel_hi:[0,1,1]\n"
            "v_pk_fma_f32 v[10:11], v[154:155], v[186:187], v[10:11] op_sel_hi:[0,1,1]\n"
            "v_pk_fma_f32 v[12:13], v[158:159], v[188:189], v[12:13] op_sel_hi:[0,1,1]\n"
            "v_pk_fma_f32 v[14:15], v[158:159], v[190:191], v[14:15] op_sel_hi:[0,1,1]\n"
            "v_pk_add_f32 v[168:169], v[168:169], 1.0 op_sel_hi:[1,0]\n"
            "v_pk_add_f32 v[170:171], v[170:171], 1.0 op_sel_hi:[1,0]\n"
            "v_pk_add_f32 v[172:173], v[172:173], 1.0 op_sel_hi:[1,0]\n"
            "v_pk_add_f32 v[174:175], v[174:175], 1.0 op_sel_hi:[1,0]\n"
            "v_rcp_f32 v168, v168\n"
            "v_rcp_f32 v169, v169\n"
            "v_mfma_f32_4x4x4_16b_bf16 v[176:179], v[208:209], v[80:81], 0 cbsz:2 abid:3\n"
            "v_rcp_f32 v170, v170\n"
            "v_mfma_f32_4x4x4_16b_bf16 v[180:183], v[208:209], v[82:83], 0 cbsz:2 abid:3\n"
            "v_rcp_f32 v171, v171\n"
            "v_mfma_f32_4x4x4_16b_bf16 v[184:187], v[208:209], v[84:85], 0 cbsz:2 abid:3\n"
            "v_rcp_f32 v172, v172\n"
            "v_mfma_f32_4x4x4_16b_bf16 v[188:191], v[208:209], v[86:87], 0 cbsz:2 abid:3\n"
            "v_rcp_f32 v173, v173\n"
            "v_rcp_f32 v174, v174\n"
            "ds_read_b128 v[124:127], v228 offset:18432\n"
            "ds_read_b128 v[192:195], v228 offset:19456\n"
            "ds_read_b128 v[128:131], v228 offset:20480\n"
            "ds_read_b128 v[132:135], v228 offset:21504\n"
            "ds_read_b128 v[80:83], v228 offset:22528\n"
            "ds_read_b128 v[84:87], v228 offset:23552\n"
            "v_rcp_f32 v175, v175\n"
            "v_pk_fma_f32 v[0:1], v[146:147], v[176:177], v[0:1] op_sel:[1,0,0]\n"
            "v_pk_fma_f32 v[2:3], v[146:147], v[178:179], v[2:3] op_sel:[1,0,0]\n"
            "v_pk_fma_f32 v[4:5], v[150:151], v[180:181], v[4:5] op_sel:[1,0,0]\n"
            "v_pk_fma_f32 v[6:7], v[150:151], v[182:183], v[6:7] op_sel:[1,0,0]\n"
            "v_pk_fma_f32 v[8:9], v[154:155], v[184:185], v[8:9] op_sel:[1,0,0]\n"
            "v_pk_fma_f32 v[10:11], v[154:155], v[186:187], v[10:11] op_sel:[1,0,0]\n"
            "v_pk_fma_f32 v[12:13], v[158:159], v[188:189], v[12:13] op_sel:[1,0,0]\n"
            "v_pk_fma_f32 v[14:15], v[158:159], v[190:191], v[14:15] op_sel:[1,0,0]\n"
            "v_pk_mul_f32 v[168:169], v[168:169], v[136:137]\n"
            "v_pk_mul_f32 v[170:171], v[170:171], v[138:139]\n"
            "v_pk_mul_f32 v[172:173], v[172:173], v[140:141]\n"
            "v_pk_mul_f32 v[174:175], v[174:175], v[142:143]\n"
            "s_cmp_lg_u32 s73, s90\n"
            "s_cbranch_scc1 Llk_%=\n"
            "Llkr_%=:\n"
            "s_waitcnt vmcnt(8)\n"
            "v_mfma_f32_4x4x4_16b_bf16 v[176:179], v[208:209], v[40:41], 0 cbsz:2\n"
            "v_mfma_f32_4x4x4_16b_bf16 v[180:183], v[208:209], v[42:43], 0 cbsz:2\n"
            "v_mfma_f32_4x4x4_16b_bf16 v[184:187], v[208:209], v[44:45], 0 cbsz:2\n"
            "v_mfma_f32_4x4x4_16b_bf16 v[188:191], v[208:209], v[46:47], 0 cbsz:2\n"
            "ds_read_b128 v[136:139], v228 offset:24576\n"
            "ds_read_b128 v[140:143], v228 offset:25600\n"
            "ds_read_b128 v[144:147], v228 offset:26624\n"
            "ds_read_b128 v[148:151], v228 offset:27648\n"
            "ds_read_b128 v[152:155], v228 offset:28672\n"
            "ds_read_b128 v[156:159], v228 offset:29696\n"
            "ds_read_b128 v[40:43], v228 offset:30720\n"
            "ds_read_b128 v[44:47], v228 offset:31744\n"
            "s_nop 1\n"
            "v_pk_fma_f32 v[16:17], v[160:161], v[176:177], v[16:17] op_sel_hi:[0,1,1]\n"
            "v_pk_fma_f32 v[18:19], v[160:161], v[178:179], v[18:19] op_sel_hi:[0,1,1]\n"
            "v_pk_fma_f32 v[20:21], v[164:165], v[180:181], v[20:21] op_sel_hi:[0,1,1]\n"
            "v_pk_fma_f32 v[22:23], v[164:165], v[182:183], v[22:23] op_sel_hi:[0,1,1]\n"
            "v_pk_fma_f32 v[24:25], v[168:169], v[184:185], v[24:25] op_sel_hi:[0,1,1]\n"
            "v_pk_fma_f32 v[26:27], v[168:169], v[186:187], v[26:27] op_sel_hi:[0,1,1]\n"
            "v_pk_fma_f32 v[28:29], v[172:173], v[188:189], v[28:29] op_sel_hi:[0,1,1]\n"
            "v_pk_fma_f32 v[30:31], v[172:173], v[190:191], v[30:31] op_sel_hi:[0,1,1]\n"
            "s_cmp_le_u32 s74, 1\n"
            "s_cbranch_scc1 Ltail_%=\n"
            "v_mfma_f32_4x4x4_16b_bf16 v[176:179], v[208:209], v[56:57], 0 cbsz:2 abid:1\n"
            "v_mfma_f32_4x4x4_16b_bf16 v[180:183], v[208:209], v[58:59], 0 cbsz:2 abid:1\n"
            "v_mfma_f32_4x4x4_16b_bf16 v[184:187], v[208:209], v[60:61], 0 cbsz:2 abid:1\n"
            "v_mfma_f32_4x4x4_16b_bf16 v[188:191], v[208:209], v[62:63], 0 cbsz:2 abid:1\n"
            "s_nop 1\n"
            "v_pk_fma_f32 v[16:17], v[160:161], v[176:177], v[16:17] op_sel:[1,0,0]\n"
            "v_pk_fma_f32 v[18:19], v[160:161], v[178:179], v[18:19] op_sel:[1,0,0]\n"
            "v_pk_fma_f32 v[20:21], v[164:165], v[180:181], v[20:21] op_sel:[1,0,0]\n"
            "v_pk_fma_f32 v[22:23], v[164:165], v[182:183], v[22:23] op_sel:[1,0,0]\n"
            "v_pk_fma_f32 v[24:25], v[168:169], v[184:185], v[24:25] op_sel:[1,0,0]\n"
            "v_pk_fma_f32 v[26:27], v[168:169], v[186:187], v[26:27] op_sel:[1,0,0]\n"
            "v_pk_fma_f32 v[28:29], v[172:173], v[188:189], v[28:29] op_sel:[1,0,0]\n"
            "v_pk_fma_f32 v[30:31], v[172:173], v[190:191], v[30:31] op_sel:[1,0,0]\n"
            "s_cmp_le_u32 s74, 2\n"
            "s_cbranch_scc1 Ltail_%=\n"
            "s_waitcnt vmcnt(4)\n"
            "v_mfma_f32_4x4x4_16b_bf16 v[176:179], v[208:209], v[72:73], 0 cbsz:2 abid:2\n"
            "v_mfma_f32_4x4x4_16b_bf16 v[180:183], v[208:209], v[74:75], 0 cbsz:2 abid:2\n"
            "v_mfma_f32_4x4x4_16b_bf16 v[184:187], v[208:209], v[76:77], 0 cbsz:2 abid:2\n"
            "v_mfma_f32_4x4x4_16b_bf16 v[188:191], v[208:209], v[78:79], 0 cbsz:2 abid:2\n"
            "s_nop 1\n"
            "v_pk_fma_f32 v[16:17], v[162:163], v[176:177], v[16:17] op_sel_hi:[0,1,1]\n"
            "v_pk_fma_f32 v[18:19], v[162:163], v[178:179], v[18:19] op_sel_hi:[0,1,1]\n"
            "v_pk_fma_f32 v[20:21], v[166:167], v[180:181], v[20:21] op_sel_hi:[0,1,1]\n"
            "v_pk_fma_f32 v[22:23], v[166:167], v[182:183], v[22:23] op_sel_hi:[0,1,1]\n"
            "v_pk_fma_f32 v[24:25], v[170:171], v[184:185], v[24:25] op_sel_hi:[0,1,1]\n"
            "v_pk_fma_f32 v[26:27], v[170:171], v[186:187], v[26:27] op_sel_hi:[0,1,1]\n"
            "v_pk_fma_f32 v[28:29], v[174:175], v[188:189], v[28:29] op_sel_hi:[0,1,1]\n"
            "v_pk_fma_f32 v[30:31], v[174:175], v[190:191], v[30:31] op_sel_hi:[0,1,1]\n"
            "s_cmp_le_u32 s74, 3\n"
            "s_cbranch_scc1 Ltail_%=\n"
            "v_mfma_f32_4x4x4_16b_bf16 v[176:179], v[208:209], v[88:89], 0 cbsz:2 abid:3\n"
            "v_mfma_f32_4x4x4_16b_bf16 v[180:183], v[208:209], v[90:91], 0 cbsz:2 abid:3\n"
            "v_mfma_f32_4x4x4_16b_bf16 v[184:187], v[208:209], v[92:93], 0 cbsz:2 abid:3\n"
            "v_mfma_f32_4x4x4_16b_bf16 v[188:191], v[208:209], v[94:95], 0 cbsz:2 abid:3\n"
            "s_nop 1\n"
            "v_pk_fma_f32 v[16:17], v[162:163], v[176:177], v[16:17] op_sel:[1,0,0]\n"
            "v_pk_fma_f32 v[18:19], v[162:163], v[178:179], v[18:19] op_sel:[1,0,0]\n"
            "v_pk_fma_f32 v[20:21], v[166:167], v[180:181], v[20:21] op_sel:[1,0,0]\n"
            "v_pk_fma_f32 v[22:23], v[166:167], v[182:183], v[22:23] op_sel:[1,0,0]\n"
            "v_pk_fma_f32 v[24:25], v[170:171], v[184:185], v[24:25] op_sel:[1,0,0]\n"
            "v_pk_fma_f32 v[26:27], v[170:171], v[186:187], v[26:27] op_sel:[1,0,0]\n"
            "v_pk_fma_f32 v[28:29], v[174:175], v[188:189], v[28:29] op_sel:[1,0,0]\n"
            "v_pk_fma_f32 v[30:31], v[174:175], v[190:191], v[30:31] op_sel:[1,0,0]\n"
            "Ltail_%=:\n"
            "s_setprio 3\n"
            "s_cmp_lg_u32 s73, s90\n"
            "s_cbranch_scc1 Lspin_%=\n"
            "Lgo_%=:\n"
            "s_waitcnt vmcnt(0)\n"
            "v_mfma_f32_4x4x4_16b_bf16 v[0:3], v[224:225], v[32:33], v[0:3]\n"
            "v_mfma_f32_4x4x4_16b_bf16 v[4:7], v[224:225], v[34:35], v[4:7]\n"
            "v_mfma_f32_4x4x4_16b_bf16 v[8:11], v[224:225], v[36:37], v[8:11]\n"
            "v_mfma_f32_4x4x4_16b_bf16 v[12:15], v[224:225], v[38:39], v[12:15]\n"
            "v_mfma_f32_4x4x4_16b_bf16 v[16:19], v[224:225], v[48:49], v[16:19]\n"
            "v_mfma_f32_4x4x4_16b_bf16 v[20:23], v[224:225], v[50:51], v[20:23]\n"
            "v_mfma_f32_4x4x4_16b_bf16 v[24:27], v[224:225], v[52:53], v[24:27]\n"
            "v_mfma_f32_4x4x4_16b_bf16 v[28:31], v[224:225], v[54:55], v[28:31]\n"
            "v_cvt_pk_bf16_f32 v176, v0, v4\n"
            "v_cvt_pk_bf16_f32 v177, v8, v12\n"
            "ds_write_b64 v226, v[176:177] offset:0\n"
            "v_cvt_pk_bf16_f32 v178, v1, v5\n"
            "v_cvt_pk_bf16_f32 v179, v9, v13\n"
            "ds_write_b64 v226, v[178:179] offset:272\n"
            "v_cvt_pk_bf16_f32 v180, v2, v6\n"
            "v_cvt_pk_bf16_f32 v181, v10, v14\n"
            "ds_write_b64 v226, v[180:181] offset:544\n"
            "v_cvt_pk_bf16_f32 v182, v3, v7\n"
            "v_cvt_pk_bf16_f32 v183, v11, v15\n"
            "ds_write_b64 v226, v[182:183] offset:816\n"
            "ds_read_b128 v[176:179], v227 offset:0\n"
            "ds_read_b128 v[180:183], v227 offset:64\n"
            "v_cvt_pk_bf16_f32 v184, v16, v20\n"
            "v_cvt_pk_bf16_f32 v185, v24, v28\n"
            "ds_write_b64 v226, v[184:185] offset:128\n"
            "v_cvt_pk_bf16_f32 v186, v17, v21\n"
            "v_cvt_pk_bf16_f32 v187, v25, v29\n"
            "ds_write_b64 v226, v[186:187] offset:400\n"
            "v_cvt_pk_bf16_f32 v188, v18, v22\n"
            "v_cvt_pk_bf16_f32 v189, v26, v30\n"
            "ds_write_b64 v226, v[188:189] offset:672\n"
            "v_cvt_pk_bf16_f32 v190, v19, v23\n"
            "v_cvt_pk_bf16_f32 v191, v27, v31\n"
            "ds_write_b64 v226, v[190:191] offset:944\n"
            "ds_read_b128 v[184:187], v227 offset:128\n"
            "ds_read_b128 v[188:191], v227 offset:192\n"
            "s_waitcnt lgkmcnt(7)\n"
            "v_mfma_f32_16x16x32_bf16 v[0:3], v[176:179], v[236:239], 0\n"
            "v_mfma_f32_16x16x32_bf16 v[4:7], v[176:179], v[252:255], 0\n"
            "s_waitcnt lgkmcnt(6)\n"
            "v_mfma_f32_16x16x32_bf16 v[0:3], v[180:183], v[240:243], v[0:3]\n"
            "v_mfma_f32_16x16x32_bf16 v[4:7], v[180:183], v[96:99], v[4:7]\n"
            "buffer_load_dwordx4 v[72:75], v218, s[20:23], s63 offen offset:0 sc1\n"
            "v_mfma_f32_16x16x32_bf16 v[8:11], v[176:179], v[108:111], 0\n"
            "v_mfma_f32_16x16x32_bf16 v[12:15], v[176:179], v[200:203], 0\n"
            "v_mfma_f32_16x16x32_bf16 v[8:11], v[180:183], v[112:115], v[8:11]\n"
            "v_mfma_f32_16x16x32_bf16 v[12:15], v[180:183], v[204:207], v[12:15]\n"
            "buffer_load_dwordx4 v[76:79], v218, s[20:23], s63 offen offset:256 sc1\n"
            "v_mfma_f32_16x16x32_bf16 v[16:19], v[176:179], v[64:67], 0\n"
            "v_mfma_f32_16x16x32_bf16 v[20:23], v[176:179], v[128:131], 0\n"
            "v_mfma_f32_16x16x32_bf16 v[16:19], v[180:183], v[68:71], v[16:19]\n"
            "v_mfma_f32_16x16x32_bf16 v[20:23], v[180:183], v[132:135], v[20:23]\n"
            "buffer_load_dwordx4 v[88:91], v218, s[20:23], s63 offen offset:512 sc1\n"
            "v_mfma_f32_16x16x32_bf16 v[24:27], v[176:179], v[136:139], 0\n"
            "v_mfma_f32_16x16x32_bf16 v[28:31], v[176:179], v[152:155], 0\n"
            "v_mfma_f32_16x16x32_bf16 v[24:27], v[180:183], v[140:143], v[24:27]\n"
            "v_mfma_f32_16x16x32_bf16 v[28:31], v[180:183], v[156:159], v[28:31]\n"
            "buffer_load_dwordx4 v[92:95], v218, s[20:23], s63 offen offset:768 sc1\n"
            "s_waitcnt lgkmcnt(1)\n"
            "v_mfma_f32_16x16x32_bf16 v[0:3], v[184:187], v[244:247], v[0:3]\n"
            "v_mfma_f32_16x16x32_bf16 v[4:7], v[184:187], v[100:103], v[4:7]\n"
            "s_waitcnt lgkmcnt(0)\n"
            "v_mfma_f32_16x16x32_bf16 v[0:3], v[188:191], v[248:251], v[0:3]\n"
            "v_mfma_f32_16x16x32_bf16 v[4:7], v[188:191], v[104:107], v[4:7]\n"
            "v_mfma_f32_16x16x32_bf16 v[8:11], v[184:187], v[116:119], v[8:11]\n"
            "v_mfma_f32_16x16x32_bf16 v[12:15], v[184:187], v[232:235], v[12:15]\n"
            "v_mfma_f32_16x16x32_bf16 v[8:11], v[188:191], v[196:199], v[8:11]\n"
            "v_mfma_f32_16x16x32_bf16 v[12:15], v[188:191], v[120:123], v[12:15]\n"
            "v_mfma_f32_16x16x32_bf16 v[16:19], v[184:187], v[124:127], v[16:19]\n"
            "v_mfma_f32_16x16x32_bf16 v[20:23], v[184:187], v[80:83], v[20:23]\n"
            "v_mfma_f32_16x16x32_bf16 v[16:19], v[188:191], v[192:195], v[16:19]\n"
            "v_mfma_f32_16x16x32_bf16 v[20:23], v[188:191], v[84:87], v[20:23]\n"
            "v_cvt_pk_bf16_f32 v0, v0, v1\n"
            "v_cvt_pk_bf16_f32 v1, v2, v3\n"
            "v_cvt_pk_bf16_f32 v2, v4, v5\n"
            "v_cvt_pk_bf16_f32 v3, v6, v7\n"
            "buffer_store_dwordx4 v[0:3], v217, s[32:35], 0 offen offset:0 sc1\n"
            "v_mfma_f32_16x16x32_bf16 v[24:27], v[184:187], v[144:147], v[24:27]\n"
            "v_mfma_f32_16x16x32_bf16 v[28:31], v[184:187], v[40:43], v[28:31]\n"
            "v_mfma_f32_16x16x32_bf16 v[24:27], v[188:191], v[148:151], v[24:27]\n"
            "v_mfma_f32_16x16x32_bf16 v[28:31], v[188:191], v[44:47], v[28:31]\n"
            "v_cvt_pk_bf16_f32 v8, v8, v9\n"
            "v_cvt_pk_bf16_f32 v9, v10, v11\n"
            "v_cvt_pk_bf16_f32 v10, v12, v13\n"
            "v_cvt_pk_bf16_f32 v11, v14, v15\n"
            "buffer_store_dwordx4 v[8:11], v217, s[32:35], 0 offen offset:256 sc1\n"
            "v_cvt_pk_bf16_f32 v16, v16, v17\n"
            "v_cvt_pk_bf16_f32 v17, v18, v19\n"
            "v_cvt_pk_bf16_f32 v18, v20, v21\n"
            "v_cvt_pk_bf16_f32 v19, v22, v23\n"
            "buffer_store_dwordx4 v[16:19], v217, s[32:35], 0 offen offset:512 sc1\n"
            "v_cvt_pk_bf16_f32 v24, v24, v25\n"
            "v_cvt_pk_bf16_f32 v25, v26, v27\n"
            "v_cvt_pk_bf16_f32 v26, v28, v29\n"
            "v_cvt_pk_bf16_f32 v27, v30, v31\n"
            "buffer_store_dwordx4 v[24:27], v217, s[32:35], 0 offen offset:768 sc1\n"
            "s_cmp_lt_u32 s3, 6\n"
            "s_cbranch_scc0 Ltwo_%=\n"
            "s_endpgm\n"
            "Ltwo_%=:\n"
            "ds_read_b128 v[32:35], v231 offset:0\n"
            "ds_read_b128 v[36:39], v231 offset:1024\n"
            "ds_read_b128 v[48:51], v231 offset:2048\n"
            "ds_read_b128 v[52:55], v231 offset:3072\n"
            "s_waitcnt vmcnt(4)\n"
            "v_mfma_f32_4x4x4_16b_bf16 v[0:3], v[224:225], v[72:73], 0\n"
            "v_mfma_f32_4x4x4_16b_bf16 v[4:7], v[224:225], v[74:75], 0\n"
            "v_mfma_f32_4x4x4_16b_bf16 v[8:11], v[224:225], v[76:77], 0\n"
            "v_mfma_f32_4x4x4_16b_bf16 v[12:15], v[224:225], v[78:79], 0\n"
            "v_mfma_f32_4x4x4_16b_bf16 v[16:19], v[224:225], v[88:89], 0\n"
            "v_mfma_f32_4x4x4_16b_bf16 v[20:23], v[224:225], v[90:91], 0\n"
            "v_mfma_f32_4x4x4_16b_bf16 v[24:27], v[224:225], v[92:93], 0\n"
            "v_mfma_f32_4x4x4_16b_bf16 v[28:31], v[224:225], v[94:95], 0\n"
            "s_waitcnt lgkmcnt(0)\n"
            "v_mfma_f32_4x4x4_16b_bf16 v[0:3], v[224:225], v[32:33], v[0:3]\n"
            "v_mfma_f32_4x4x4_16b_bf16 v[4:7], v[224:225], v[34:35], v[4:7]\n"
            "v_mfma_f32_4x4x4_16b_bf16 v[8:11], v[224:225], v[36:37], v[8:11]\n"
            "v_mfma_f32_4x4x4_16b_bf16 v[12:15], v[224:225], v[38:39], v[12:15]\n"
            "v_mfma_f32_4x4x4_16b_bf16 v[16:19], v[224:225], v[48:49], v[16:19]\n"
            "v_mfma_f32_4x4x4_16b_bf16 v[20:23], v[224:225], v[50:51], v[20:23]\n"
            "v_mfma_f32_4x4x4_16b_bf16 v[24:27], v[224:225], v[52:53], v[24:27]\n"
            "v_mfma_f32_4x4x4_16b_bf16 v[28:31], v[224:225], v[54:55], v[28:31]\n"
            "v_cvt_pk_bf16_f32 v176, v0, v4\n"
            "v_cvt_pk_bf16_f32 v177, v8, v12\n"
            "ds_write_b64 v226, v[176:177] offset:0\n"
            "v_cvt_pk_bf16_f32 v178, v1, v5\n"
            "v_cvt_pk_bf16_f32 v179, v9, v13\n"
            "ds_write_b64 v226, v[178:179] offset:272\n"
            "v_cvt_pk_bf16_f32 v180, v2, v6\n"
            "v_cvt_pk_bf16_f32 v181, v10, v14\n"
            "ds_write_b64 v226, v[180:181] offset:544\n"
            "v_cvt_pk_bf16_f32 v182, v3, v7\n"
            "v_cvt_pk_bf16_f32 v183, v11, v15\n"
            "ds_write_b64 v226, v[182:183] offset:816\n"
            "ds_read_b128 v[176:179], v227 offset:0\n"
            "ds_read_b128 v[180:183], v227 offset:64\n"
            "v_cvt_pk_bf16_f32 v184, v16, v20\n"
            "v_cvt_pk_bf16_f32 v185, v24, v28\n"
            "ds_write_b64 v226, v[184:185] offset:128\n"
            "v_cvt_pk_bf16_f32 v186, v17, v21\n"
            "v_cvt_pk_bf16_f32 v187, v25, v29\n"
            "ds_write_b64 v226, v[186:187] offset:400\n"
            "v_cvt_pk_bf16_f32 v188, v18, v22\n"
            "v_cvt_pk_bf16_f32 v189, v26, v30\n"
            "ds_write_b64 v226, v[188:189] offset:672\n"
            "v_cvt_pk_bf16_f32 v190, v19, v23\n"
            "v_cvt_pk_bf16_f32 v191, v27, v31\n"
            "ds_write_b64 v226, v[190:191] offset:944\n"
            "ds_read_b128 v[184:187], v227 offset:128\n"
            "ds_read_b128 v[188:191], v227 offset:192\n"
            "s_waitcnt lgkmcnt(7)\n"
            "v_mfma_f32_16x16x32_bf16 v[0:3], v[176:179], v[236:239], 0\n"
            "v_mfma_f32_16x16x32_bf16 v[4:7], v[176:179], v[252:255], 0\n"
            "s_waitcnt lgkmcnt(6)\n"
            "v_mfma_f32_16x16x32_bf16 v[0:3], v[180:183], v[240:243], v[0:3]\n"
            "v_mfma_f32_16x16x32_bf16 v[4:7], v[180:183], v[96:99], v[4:7]\n"
            "v_mfma_f32_16x16x32_bf16 v[8:11], v[176:179], v[108:111], 0\n"
            "v_mfma_f32_16x16x32_bf16 v[12:15], v[176:179], v[200:203], 0\n"
            "v_mfma_f32_16x16x32_bf16 v[8:11], v[180:183], v[112:115], v[8:11]\n"
            "v_mfma_f32_16x16x32_bf16 v[12:15], v[180:183], v[204:207], v[12:15]\n"
            "v_mfma_f32_16x16x32_bf16 v[16:19], v[176:179], v[64:67], 0\n"
            "v_mfma_f32_16x16x32_bf16 v[20:23], v[176:179], v[128:131], 0\n"
            "v_mfma_f32_16x16x32_bf16 v[16:19], v[180:183], v[68:71], v[16:19]\n"
            "v_mfma_f32_16x16x32_bf16 v[20:23], v[180:183], v[132:135], v[20:23]\n"
            "v_mfma_f32_16x16x32_bf16 v[24:27], v[176:179], v[136:139], 0\n"
            "v_mfma_f32_16x16x32_bf16 v[28:31], v[176:179], v[152:155], 0\n"
            "v_mfma_f32_16x16x32_bf16 v[24:27], v[180:183], v[140:143], v[24:27]\n"
            "v_mfma_f32_16x16x32_bf16 v[28:31], v[180:183], v[156:159], v[28:31]\n"
            "s_waitcnt lgkmcnt(1)\n"
            "v_mfma_f32_16x16x32_bf16 v[0:3], v[184:187], v[244:247], v[0:3]\n"
            "v_mfma_f32_16x16x32_bf16 v[4:7], v[184:187], v[100:103], v[4:7]\n"
            "s_waitcnt lgkmcnt(0)\n"
            "v_mfma_f32_16x16x32_bf16 v[0:3], v[188:191], v[248:251], v[0:3]\n"
            "v_mfma_f32_16x16x32_bf16 v[4:7], v[188:191], v[104:107], v[4:7]\n"
            "v_mfma_f32_16x16x32_bf16 v[8:11], v[184:187], v[116:119], v[8:11]\n"
            "v_mfma_f32_16x16x32_bf16 v[12:15], v[184:187], v[232:235], v[12:15]\n"
            "v_mfma_f32_16x16x32_bf16 v[8:11], v[188:191], v[196:199], v[8:11]\n"
            "v_mfma_f32_16x16x32_bf16 v[12:15], v[188:191], v[120:123], v[12:15]\n"
            "v_mfma_f32_16x16x32_bf16 v[16:19], v[184:187], v[124:127], v[16:19]\n"
            "v_mfma_f32_16x16x32_bf16 v[20:23], v[184:187], v[80:83], v[20:23]\n"
            "v_mfma_f32_16x16x32_bf16 v[16:19], v[188:191], v[192:195], v[16:19]\n"
            "v_mfma_f32_16x16x32_bf16 v[20:23], v[188:191], v[84:87], v[20:23]\n"
            "v_cvt_pk_bf16_f32 v0, v0, v1\n"
            "v_cvt_pk_bf16_f32 v1, v2, v3\n"
            "v_cvt_pk_bf16_f32 v2, v4, v5\n"
            "v_cvt_pk_bf16_f32 v3, v6, v7\n"
            "buffer_store_dwordx4 v[0:3], v218, s[32:35], 0 offen offset:0 sc1\n"
            "v_mfma_f32_16x16x32_bf16 v[24:27], v[184:187], v[144:147], v[24:27]\n"
            "v_mfma_f32_16x16x32_bf16 v[28:31], v[184:187], v[40:43], v[28:31]\n"
            "v_mfma_f32_16x16x32_bf16 v[24:27], v[188:191], v[148:151], v[24:27]\n"
            "v_mfma_f32_16x16x32_bf16 v[28:31], v[188:191], v[44:47], v[28:31]\n"
            "v_cvt_pk_bf16_f32 v8, v8, v9\n"
            "v_cvt_pk_bf16_f32 v9, v10, v11\n"
            "v_cvt_pk_bf16_f32 v10, v12, v13\n"
            "v_cvt_pk_bf16_f32 v11, v14, v15\n"
            "buffer_store_dwordx4 v[8:11], v218, s[32:35], 0 offen offset:256 sc1\n"
            "v_cvt_pk_bf16_f32 v16, v16, v17\n"
            "v_cvt_pk_bf16_f32 v17, v18, v19\n"
            "v_cvt_pk_bf16_f32 v18, v20, v21\n"
            "v_cvt_pk_bf16_f32 v19, v22, v23\n"
            "buffer_store_dwordx4 v[16:19], v218, s[32:35], 0 offen offset:512 sc1\n"
            "v_cvt_pk_bf16_f32 v24, v24, v25\n"
            "v_cvt_pk_bf16_f32 v25, v26, v27\n"
            "v_cvt_pk_bf16_f32 v26, v28, v29\n"
            "v_cvt_pk_bf16_f32 v27, v30, v31\n"
            "buffer_store_dwordx4 v[24:27], v218, s[32:35], 0 offen offset:768 sc1\n"
            "Lend_%=:\n"
            "s_endpgm\n"
            "s_mov_b32 s68, 0\n"
            "s_branch Lout_%=\n"
            "Lpoll2_%=:\n"
            "s_sub_u32 s99, s99, 1\n"
            "s_cmp_lt_i32 s99, 0\n"
            "s_cbranch_scc1 Lpoll3_%=\n"
            "s_sleep 2\n"
            "buffer_load_dwordx2 v[142:143], v180, s[20:23], 0 offen sc1\n"
            "s_branch Lpoll_%=\n"
            "Lover_%=:\n"
            "s_mov_b32 s37, 0\n"
            "v_mov_b32 v144, 255\n"
            "ds_write_b32 v219, v144\n"
            "s_branch Lover2_%=\n"
            "Llng_%=:\n"
            "s_nop 1\n"
            "s_mov_b32 s38, 0\n"
            "v_max_u32_dpp v74, v74, v74 row_bcast:15 row_mask:0xa bank_mask:0xf\n"
            "s_nop 1\n"
            "v_max_u32_dpp v74, v74, v74 row_bcast:31 row_mask:0xc bank_mask:0xf\n"
            "v_max_u32 v74, s38, v74\n"
            "s_nop 0\n"
            "v_readlane_b32 s38, v74, 63\n"
            "v_max_u32_dpp v75, v75, v75 row_bcast:15 row_mask:0xa bank_mask:0xf\n"
            "s_nop 1\n"
            "v_max_u32_dpp v75, v75, v75 row_bcast:31 row_mask:0xc bank_mask:0xf\n"
            "v_max_u32 v75, s38, v75\n"
            "s_nop 0\n"
            "v_readlane_b32 s38, v75, 63\n"
            "v_max_u32_dpp v76, v76, v76 row_bcast:15 row_mask:0xa bank_mask:0xf\n"
            "s_nop 1\n"
            "v_max_u32_dpp v76, v76, v76 row_bcast:31 row_mask:0xc bank_mask:0xf\n"
            "v_max_u32 v76, s38, v76\n"
            "s_nop 0\n"
            "v_readlane_b32 s38, v76, 63\n"
            "v_max_u32_dpp v77, v77, v77 row_bcast:15 row_mask:0xa bank_mask:0xf\n"
            "s_nop 1\n"
            "v_max_u32_dpp v77, v77, v77 row_bcast:31 row_mask:0xc bank_mask:0xf\n"
            "v_max_u32 v77, s38, v77\n"
            "s_nop 0\n"
            "v_readlane_b32 s38, v77, 63\n"
            "v_max_u32_dpp v78, v78, v78 row_bcast:15 row_mask:0xa bank_mask:0xf\n"
            "s_nop 1\n"
            "v_max_u32_dpp v78, v78, v78 row_bcast:31 row_mask:0xc bank_mask:0xf\n"
            "v_max_u32 v78, s38, v78\n"
            "s_nop 0\n"
            "v_readlane_b32 s38, v78, 63\n"
            "v_max_u32_dpp v79, v79, v79 row_bcast:15 row_mask:0xa bank_mask:0xf\n"
            "s_nop 1\n"
            "v_max_u32_dpp v79, v79, v79 row_bcast:31 row_mask:0xc bank_mask:0xf\n"
            "v_max_u32 v79, s38, v79\n"
            "s_nop 0\n"
            "v_readlane_b32 s38, v79, 63\n"
            "v_max_u32_dpp v80, v80, v80 row_bcast:15 row_mask:0xa bank_mask:0xf\n"
            "s_nop 1\n"
            "v_max_u32_dpp v80, v80, v80 row_bcast:31 row_mask:0xc bank_mask:0xf\n"
            "v_max_u32 v80, s38, v80\n"
            "s_nop 0\n"
            "v_readlane_b32 s38, v80, 63\n"
            "s_branch Llng2_%=\n"
            "Lre0_%=:\n"
            "s_sub_u32 s99, s99, 1\n"
            "s_cmp_lt_i32 s99, 0\n"
            "s_cbranch_scc1 Lok0_%=\n"
            "s_sleep 1\n"
            "buffer_load_dwordx4 v[34:37], v81, s[20:23], s15 offen offset:0 sc1\n"
            "s_waitcnt vmcnt(0)\n"
            "s_branch Lchk0_%=\n"
            "Lre1_%=:\n"
            "s_sub_u32 s99, s99, 1\n"
            "s_cmp_lt_i32 s99, 0\n"
            "s_cbranch_scc1 Lok1_%=\n"
            "s_sleep 1\n"
            "buffer_load_dwordx4 v[40:43], v82, s[20:23], s15 offen offset:1024 sc1\n"
            "s_waitcnt vmcnt(0)\n"
            "s_branch Lchk1_%=\n"
            "Lre2_%=:\n"
            "s_sub_u32 s99, s99, 1\n"
            "s_cmp_lt_i32 s99, 0\n"
            "s_cbranch_scc1 Lok2_%=\n"
            "s_sleep 1\n"
            "buffer_load_dwordx4 v[46:49], v83, s[20:23], s15 offen offset:2048 sc1\n"
            "s_waitcnt vmcnt(0)\n"
            "s_branch Lchk2_%=\n"
            "Lre3_%=:\n"
            "s_sub_u32 s99, s99, 1\n"
            "s_cmp_lt_i32 s99, 0\n"
            "s_cbranch_scc1 Lok3_%=\n"
            "s_sleep 1\n"
            "buffer_load_dwordx4 v[52:55], v84, s[20:23], s15 offen offset:3072 sc1\n"
            "s_waitcnt vmcnt(0)\n"
            "s_branch Lchk3_%=\n"
            "Lre4_%=:\n"
            "s_sub_u32 s99, s99, 1\n"
            "s_cmp_lt_i32 s99, 0\n"
            "s_cbranch_scc1 Lok4_%=\n"
            "s_sleep 1\n"
            "buffer_load_dwordx4 v[58:61], v85, s[20:23], s16 offen offset:0 sc1\n"
            "s_waitcnt vmcnt(0)\n"
            "s_branch Lchk4_%=\n"
            "Lre5_%=:\n"
            "s_sub_u32 s99, s99, 1\n"
            "s_cmp_lt_i32 s99, 0\n"
            "s_cbranch_scc1 Lok5_%=\n"
            "s_sleep 1\n"
            "buffer_load_dwordx4 v[64:67], v86, s[20:23], s16 offen offset:1024 sc1\n"
            "s_waitcnt vmcnt(0)\n"
            "s_branch Lchk5_%=\n"
            "Lre6_%=:\n"
            "s_sub_u32 s99, s99, 1\n"
            "s_cmp_lt_i32 s99, 0\n"
            "s_cbranch_scc1 Lok6_%=\n"
            "s_sleep 1\n"
            "buffer_load_dwordx4 v[70:73], v87, s[20:23], s16 offen offset:2048 sc1\n"
            "s_waitcnt vmcnt(0)\n"
            "s_branch Lchk6_%=\n"
            "Llong_%=:\n"
            "v_and_b32 v243, 31, v243\n"
            "v_readfirstlane_b32 s86, v247\n"
            "v_add_u32 v243, 64, v243\n"
            "s_nop 0\n"
            "v_lshl_add_u32 v247, v243, 3, s86\n"
            "v_mov_b32 v136, s64\n"
            "v_cmpx_le_u32 vcc, s69, v243\n"
            "v_mov_b32 v136, s81\n"
            "v_cmpx_le_u32 vcc, s78, v243\n"
            "v_mov_b32 v136, s82\n"
            "v_cmpx_le_u32 vcc, s79, v243\n"
            "v_mov_b32 v136, s83\n"
            "v_cmpx_le_u32 vcc, s80, v243\n"
            "v_mov_b32 v136, s84\n"
            "s_mov_b64 exec, -1\n"
            "v_and_b32 v137, 0xffff, v136\n"
            "ds_read_b128 v[140:143], v137\n"
            "v_ashrrev_i32 v136, 16, v136\n"
            "v_sub_u32 v136, v243, v136\n"
            "v_lshl_or_b32 v137, v136, 16, v138\n"
            "v_lshlrev_b32 v136, 4, v136\n"
            "s_waitcnt lgkmcnt(0)\n"
            "v_cmp_gt_u32 vcc, v140, v137\n"
            "v_cmp_gt_u32 s[70:71], v141, v137\n"
            "v_cmp_gt_u32 s[86:87], v142, v137\n"
            "v_cmp_gt_u32 s[88:89], v143, v137\n"
            "v_add_u32 v140, v140, v136\n"
            "v_add_u32 v141, v141, v136\n"
            "v_add_u32 v142, v142, v136\n"
            "v_add_u32 v143, v143, v136\n"
            "v_cndmask_b32 v140, v251, v140, vcc\n"
            "v_cndmask_b32 v141, v251, v141, s[70:71]\n"
            "v_cndmask_b32 v142, v251, v142, s[86:87]\n"
            "v_cndmask_b32 v143, v251, v143, s[88:89]\n"
            "v_perm_b32 v140, v141, v140, s68\n"
            "v_perm_b32 v141, v143, v142, s68\n"
            "ds_write_b64 v247, v[140:141]\n"
            "s_branch Llong2_%=\n"
            "Lslow_%=:\n"
            "s_waitcnt vmcnt(0) lgkmcnt(0)\n"
            "s_mov_b32 s68, -1\n"
            "Lout_%=:\n"
            : "+{v0}"(tid), "={s68}"(slow), "={s90}"(tag), "={s3}"(wq)
            : "{s[0:1]}"(ka), "{s2}"(bx)
            : "memory", "vcc", "scc", "v1", "v2", "v3", "v4", "v5", "v6", "v7", "v8", "v9", "v10", "v11", "v12", "v13", "v14", "v15", "v16", "v17", "v18", "v19", "v20", "v21", "v22", "v23", "v24", "v25", "v26", "v27", "v28", "v29", "v30", "v31", "v32", "v33", "v34", "v35", "v36", "v37", "v38", "v39", "v40", "v41", "v42", "v43", "v44", "v45", "v46", "v47", "v48", "v49", "v50", "v51", "v52", "v53", "v54", "v55", "v56", "v57", "v58", "v59", "v60", "v61", "v62", "v63", "v64", "v65", "v66", "v67", "v68", "v69", "v70", "v71", "v72", "v73", "v74", "v75", "v76", "v77", "v78", "v79", "v80", "v81", "v82", "v83", "v84", "v85", "v86", "v87", "v88", "v89", "v90", "v91", "v92", "v93", "v94", "v95", "v96", "v97", "v98", "v99", "v100", "v101", "v102", "v103", "v104", "v105", "v106", "v107", "v108", "v109", "v110", "v111", "v112", "v113", "v114", "v115", "v116", "v117", "v118", "v119", "v120", "v121", "v122", "v123", "v124", "v125", "v126", "v127", "v128", "v129", "v130", "v131", "v132", "v133", "v134", "v135", "v136", "v137", "v138", "v139", "v140", "v141", "v142", "v143", "v144", "v145", "v146", "v147", "v148", "v149", "v150", "v151", "v152", "v153", "v154", "v155", "v156", "v157", "v158", "v159", "v160", "v161", "v162", "v163", "v164", "v165", "v166", "v167", "v168", "v169", "v170", "v171", "v172", "v173", "v174", "v175", "v176", "v177", "v178", "v179", "v180", "v181", "v182", "v183", "v184", "v185", "v186", "v187", "v188", "v189", "v190", "v191", "v192", "v193", "v194", "v195", "v196", "v197", "v198", "v199", "v200", "v201", "v202", "v203", "v204", "v205", "v206", "v207", "v208", "v209", "v210", "v211", "v212", "v213", "v214", "v215", "v216", "v217", "v218", "v219", "v220", "v221", "v222", "v223", "v224", "v225", "v226", "v227", "v228", "v229", "v230", "v231", "v232", "v233", "v234", "v235", "v236", "v237", "v238", "v239", "v240", "v241", "v242", "v243", "v244", "v245", "v246", "v247", "v248", "v249", "v250", "v251", "v252", "v253", "v254", "v255", "s4", "s5", "s6", "s7", "s8", "s9", "s10", "s11", "s12", "s13", "s14", "s15", "s16", "s17", "s18", "s19", "s20", "s21", "s22", "s23", "s24", "s25", "s26", "s27", "s28", "s29", "s30", "s31", "s33", "s34", "s35", "s36", "s37", "s38", "s39", "s40", "s41", "s42", "s43", "s44", "s45", "s46", "s47", "s48", "s49", "s50", "s51", "s52", "s53", "s54", "s55", "s56", "s57", "s58", "s59", "s60", "s61", "s62", "s63", "s64", "s65", "s66", "s67", "s69", "s70", "s71", "s72", "s73", "s74", "s75", "s76", "s77", "s78", "s79", "s80", "s81", "s82", "s83", "s84", "s85", "s86", "s87", "s88", "s89", "s91", "s92", "s93", "s94", "s95", "s96", "s97", "s98", "s99");
    const int w = __builtin_amdgcn_readfirstlane((int)wq);
    const unsigned lds0 = (unsigned)(uintptr_t)(__attribute__((address_space(3))) unsigned char*)smem;
    asm volatile("" : "+s"(ka));
    unsigned char* ws = (unsigned char*)ka[6];
    if (__builtin_amdgcn_readfirstlane((int)slow) == -1) {
        const int lane = lane_opaque();
        tag = (unsigned)__builtin_amdgcn_readfirstlane((int)tag);
        const int* dst = (const int*)ka[0]; const int* src = (const int*)ka[2];
        const unsigned short *Y = (const unsigned short*)ka[4], *ef = (const unsigned short*)ka[8], *x = (const unsigned short*)ka[10], *w_rad = (const unsigned short*)ka[14],
                             *cg = (const unsigned short*)ka[16], *w_mix = (const unsigned short*)ka[18];
        unsigned short* out = (unsigned short*)ka[12];
        const int N = (int)ka[20], E = (int)(ka[20] >> 32), nh = (int)ka[23];
        const int half = (bx >> 2) & 1, b = (bx & 3) * 32 + (bx >> 3), sS = 40 * (1 - half), sK = 40 * half;
        const int node0 = (b * N + 127) >> 7, npb = (((b + 1) * N + 127) >> 7) - node0;
        const __amdgpu_buffer_rsrc_t rsw = __builtin_amdgcn_make_buffer_rsrc((void*)ws, 0, W_END, 0x00020000);
        lds_wait(lds0 + SY_C3, 4);
        const __amdgpu_buffer_rsrc_t rso = __builtin_amdgcn_make_buffer_rsrc((void*)out, 0, N * 1024, 0x00020000);
#pragma unroll 1
        for (int it = 0; it < 10; ++it) {
            const int nl = (it < 5 ? sS + 8 * it : sK + 8 * (it - 5)) + w, node = node0 + nl;
            const bool nv = nl < npb;
            const unsigned ro = nv ? (unsigned)node * 1024u + (unsigned)lane * 16u : OOB_OFF;
            float a8[8] = {0.f, 0.f, 0.f, 0.f, 0.f, 0.f, 0.f, 0.f};
#pragma unroll 1
            for (int e0 = 0; e0 < (nv ? E : 0); e0 += 64) {
                const int ec = min(e0 + lane, E - 1);
                u64 mm = __ballot(e0 + lane < E && dst[ec] == node && (src[ec] >= nh) == (half != 0));
#pragma unroll 1
                while (mm) {
                    const int e = e0 + __builtin_ctzll(mm);
                    mm &= mm - 1;
                    const unsigned short* fe = (const unsigned short*)ef + (size_t)e * DE_DIM;
                    const unsigned short* ye = (const unsigned short*)Y + (size_t)e * M_DIM;
                    const unsigned short* xp = x + (size_t)src[e] * (C_DIM * M_DIM) + lane * 8;
                    float z0 = 0.f, z1 = 0.f;
#pragma unroll 1
                    for (int f = 0; f < DE_DIM; ++f) { z0 += bf2f(fe[f]) * bf2f(w_rad[f * C_DIM + 2 * lane]); z1 += bf2f(fe[f]) * bf2f(w_rad[f * C_DIM + 2 * lane + 1]); }
                    const float r0 = z0 / (1.f + __expf(-z0)), r1 = z1 / (1.f + __expf(-z1));
#pragma unroll 1
                    for (int i = 0; i < 4; ++i) {
                        const float x0 = bf2f(xp[i]) * r0, x1 = bf2f(xp[4 + i]) * r1;
#pragma unroll
                        for (int o = 0; o < 4; ++o) {
                            float wv4 = 0.f;
#pragma unroll
                            for (int f = 0; f < 4; ++f) wv4 += bf2f(cg[(i * 4 + f) * 4 + o]) * bf2f(ye[f]);
                            a8[o] += x0 * wv4; a8[4 + o] += x1 * wv4;
                        }
                    }
                }
            }
            if (it >= 5) {
                const u32x4 pv = __builtin_amdgcn_raw_buffer_load_b128(rsw, ro, W_AGG, SC1);
#pragma unroll
                for (int i = 0; i < 4; ++i) { a8[2 * i] += __uint_as_float(pv[i] << 16); a8[2 * i + 1] += __uint_as_float(pv[i] & 0xffff0000u); }
            }
            const u32x4 rv = (u32x4){pack2bf(a8[0], a8[1]), pack2bf(a8[2], a8[3]), pack2bf(a8[4], a8[5]), pack2bf(a8[6], a8[7])};
            if (it < 5) {
                __builtin_amdgcn_raw_buffer_store_b128(rv, rsw, ro, W_AGG, SC1);
                if (it == 4) {
                    asm volatile("s_waitcnt vmcnt(0)" ::: "memory");
                    if (lds_arrive_rtn(lds0 + 4) == 7u) flag_store(ws + W_FLG + bx * 128, tag);
                    poll_tag(ws + W_FLG + (bx ^ 4) * 128, tag);
                }
            } else {
                *(u32x4*)(smem + L_LST + w * 1024 + lane * 16) = rv;
#pragma unroll
                for (int i = 0; i < 8; ++i) a8[i] = 0.f;
#pragma unroll 1
                for (int c = 0; c < C_DIM; ++c) {
                    const unsigned short* ap = (const unsigned short*)(smem + L_LST + w * 1024 + c * 8);
                    const float w0 = bf2f(w_mix[c * C_DIM + 2 * lane]), w1 = bf2f(w_mix[c * C_DIM + 2 * lane + 1]);
#pragma unroll
                    for (int m = 0; m < 4; ++m) { a8[m] += bf2f(ap[m]) * w0; a8[4 + m] += bf2f(ap[m]) * w1; }
                }
                __builtin_amdgcn_raw_buffer_store_b128((u32x4){pack2bf(a8[0], a8[1]), pack2bf(a8[2], a8[3]), pack2bf(a8[4], a8[5]), pack2bf(a8[6], a8[7])}, rso, ro, 0, 0);
            }
        }
    }
}

__global__ void kf_zero(float* __restrict__ agg, size_t n) {
    for (size_t i = (size_t)blockIdx.x * blockDim.x + threadIdx.x; i < n; i += (size_t)gridDim.x * blockDim.x) agg[i] = 0.f;
}
__global__ void kf_edges(const unsigned short* __restrict__ x, const int* __restrict__ src, const int* __restrict__ dst,
                         const unsigned short* __restrict__ Y, const unsigned short* __restrict__ ef,
                         const unsigned short* __restrict__ w_rad, const unsigned short* __restrict__ cg,
                         float* __restrict__ agg, int E) {
    const int ch = threadIdx.x;
    for (int e = blockIdx.x; e < E; e += gridDim.x) {
        float z = 0.f;
        for (int f = 0; f < DE_DIM; ++f) z += bf2f(ef[(size_t)e * DE_DIM + f]) * bf2f(w_rad[f * C_DIM + ch]);
        const float r = z / (1.f + __expf(-z));
        float a4[4] = {0.f, 0.f, 0.f, 0.f};
        for (int ii = 0; ii < 4; ++ii) {
            const float xv = bf2f(x[((size_t)src[e] * C_DIM + ch) * M_DIM + ii]) * r;
            for (int o = 0; o < 4; ++o) {
                float wv = 0.f;
                for (int f = 0; f < 4; ++f) wv += bf2f(cg[(ii * 4 + f) * 4 + o]) * bf2f(Y[(size_t)e * 4 + f]);
                a4[o] += xv * wv;
            }
        }
        for (int o = 0; o < 4; ++o) atomicAdd(&agg[((size_t)dst[e] * C_DIM + ch) * M_DIM + o], a4[o]);
    }
}
__global__ void kf_mix(const float* __restrict__ agg, const unsigned short* __restrict__ w_mix, unsigned short* __restrict__ out, int N) {
    const int d = threadIdx.x;
    for (int n = blockIdx.x; n < N; n += gridDim.x) {
        float a4[4] = {0.f, 0.f, 0.f, 0.f};
        for (int c = 0; c < C_DIM; ++c) {
            const float wv = bf2f(w_mix[c * C_DIM + d]);
            for (int m = 0; m < 4; ++m) a4[m] += bf2f(f2bf(agg[((size_t)n * C_DIM + c) * M_DIM + m])) * wv;
        }
        for (int m = 0; m < 4; ++m) out[((size_t)n * C_DIM + d) * M_DIM + m] = f2bf(a4[m]);
    }
}

extern "C" void kernel_launch(void* const* d_in, const int* in_sizes, int n_in,
                              void* d_out, int out_size, void* d_ws, size_t ws_size,
                              hipStream_t stream) {
    const unsigned short* x = (const unsigned short*)d_in[0];
    const int* src = (const int*)d_in[1];
    const int* dst = (const int*)d_in[2];
    const unsigned short* Y = (const unsigned short*)d_in[3];
    const unsigned short* ef = (const unsigned short*)d_in[4];
    const unsigned short* w_rad = (const unsigned short*)d_in[5];
    const unsigned short* cg = (const unsigned short*)d_in[6];
    const unsigned short* w_mix = (const unsigned short*)d_in[7];
    unsigned short* out = (unsigned short*)d_out;
    const int N = in_sizes[0] / (C_DIM * M_DIM);
    const int E = in_sizes[1];
    const int epb = (E + 255) / 256;
    const int nh = (N + 1) / 2;
    u64 hi = (u64)(uintptr_t)d_out | (u64)(uintptr_t)d_ws;
    for (int i = 0; i < 8; ++i) hi |= (u64)(uintptr_t)d_in[i];
    const bool fast = (hi >> 48) == 0 && N >= 256 && N <= NB_MAX * NPB_MAX && E > 0 && epb <= EPB_MAX && ws_size >= (size_t)W_END;
    if (!fast) {
        float* agg = (float*)d_ws;
        if (N <= 0) return;
        kf_zero<<<1024, 256, 0, stream>>>(agg, (size_t)N * C_DIM * M_DIM);
        if (E > 0) kf_edges<<<4096, C_DIM, 0, stream>>>(x, src, dst, Y, ef, w_rad, cg, agg, E);
        kf_mix<<<2048, C_DIM, 0, stream>>>(agg, w_mix, out, N);
        return;
    }
    int k = 0;
    while ((N << k) <= 32768) ++k;
    const unsigned magic = (unsigned)(((1ull << (39 - k)) + N - 1) / N);
    const unsigned F = 0x00020000u;
    k_fused<<<256, 512, 0, stream>>>(dst, E * 4u, F, src, E * 4u, F, Y, E * 8u, F, d_ws, W_END, F, ef, E * 32u, F, x, N * 1024u, F, out, N * 1024u, F, w_rad, DE_DIM * C_DIM * 2u, F,
                                     cg, 128u, F, w_mix, C_DIM * C_DIM * 2u, F, N, E, epb, magic, k, nh * 1024u, nh, 0u);
}
```

```cpp
#include <hip/hip_runtime.h>
#include <hip/hip_bf16.h>
#include <stdint.h>

typedef __attribute__((ext_vector_type(4))) unsigned u32x4;
typedef unsigned long long u64;

#define C_DIM 128
#define M_DIM 4
#define DE_DIM 16
#define NPB_MAX 80
#define NB_MAX 128
#define EPB_MAX 1280
#define OOB_OFF 0x80000000u
#define SC1 16
#define SPIN_MAX (1u << 22)
#define W_FLG 4096
#define W_AGG 5804032
#define W_END 16289792
#define L_LST 5120
#define L_TOTAL 156672
#define SY_C3 56

static __device__ __forceinline__ int lane_opaque() {
    int l;
    asm volatile("v_mbcnt_lo_u32_b32 %0, -1, 0\n\tv_mbcnt_hi_u32_b32 %0, -1, %0" : "=v"(l));
    return l;
}

static __device__ __forceinline__ float bf2f(unsigned short v) { return __uint_as_float(((unsigned)v) << 16); }
typedef __bf16 bf16v2 __attribute__((ext_vector_type(2)));
static __device__ __forceinline__ unsigned pack2bf(float lo, float hi) {
    bf16v2 r = (bf16v2){(__bf16)lo, (__bf16)hi};
    return __builtin_bit_cast(unsigned, r);
}
static __device__ __forceinline__ unsigned short f2bf(float v) { return (unsigned short)(pack2bf(v, 0.f) & 0xffffu); }
static __device__ __forceinline__ void flag_store(const void* p, unsigned v) {
    unsigned t0, t1; u64 sv;
    asm volatile("v_mov_b32 %0, 0\n\tv_mov_b32 %1, %4\n\ts_mov_b64 %2, exec\n\ts_mov_b64 exec, 1\n\t"
                 "global_store_dword %0, %1, %3 sc1\n\ts_mov_b64 exec, %2" : "=&v"(t0), "=&v"(t1), "=&s"(sv) : "s"(p), "s"(v) : "memory");
}
static __device__ __forceinline__ unsigned lds_arrive_rtn(unsigned a) {
    unsigned t0, t1; u64 sv;
    asm volatile("v_mov_b32 %0, %3\n\tv_mov_b32 %1, 1\n\ts_mov_b64 %2, exec\n\ts_mov_b64 exec, 1\n\t"
                 "ds_add_rtn_u32 %1, %0, %1\n\ts_mov_b64 exec, %2\n\ts_waitcnt lgkmcnt(0)" : "=&v"(t0), "=&v"(t1), "=&s"(sv) : "s"(a) : "memory");
    return (unsigned)__builtin_amdgcn_readfirstlane((int)t1);
}
template <int SLEEP = 0> static __device__ __forceinline__ void lds_wait(unsigned a, unsigned n) {
    for (;;) {
        unsigned v;
        asm volatile("ds_read_b32 %0, %1\n\ts_waitcnt lgkmcnt(0)" : "=v"(v) : "v"(a) : "memory");
        if ((unsigned)__builtin_amdgcn_readfirstlane((int)v) >= n) break;
        if (SLEEP) __builtin_amdgcn_s_sleep(SLEEP);
    }
}
static __device__ __forceinline__ unsigned ld_agent(const void* p) { return __hip_atomic_load((const unsigned*)p, __ATOMIC_RELAXED, __HIP_MEMORY_SCOPE_AGENT); }
static __device__ __forceinline__ void poll_tag(const void* p, unsigned tag) {
#pragma unroll 1
    for (unsigned spin = 0; spin < SPIN_MAX; ++spin) {
        if (__all(ld_agent(p) == tag)) break;
        __builtin_amdgcn_s_sleep(2);
    }
    __builtin_amdgcn_fence(__ATOMIC_ACQUIRE, "wavefront");
}

#define BUF(t, n) t n, unsigned n##_bytes, unsigned n##_fmt
__global__ __launch_bounds__(512, 2) void k_fused(
    BUF(const void*, dst_), BUF(const void*, src_), BUF(const void*, Y_), BUF(void*, ws_), BUF(const void*, ef_), BUF(const void*, x_), BUF(void*, out_), BUF(const void*, w_rad_),
    BUF(const void*, cg_), BUF(const void*, w_mix_), unsigned N_, unsigned E_, unsigned epb_, unsigned magic_, unsigned shift_, unsigned nhb_, unsigned nh_, unsigned zero_)
{
    __shared__ __attribute__((aligned(256))) unsigned char smem[L_TOTAL];
    const __attribute__((address_space(4))) u64* ka = (const __attribute__((address_space(4))) u64*)__builtin_amdgcn_kernarg_segment_ptr();
    const int bx = blockIdx.x;
    unsigned slow, tag, wq, tid = threadIdx.x;
    asm volatile(
            "s_load_dwordx16 s[8:23], s[0:1], 0\n"
            "s_load_dwordx16 s[40:55], s[0:1], 128\n"
            "s_load_dwordx16 s[24:39], s[0:1], 64\n"
            "s_getpc_b64 s[4:5]\n"
            "v_readfirstlane_b32 s3, v0\n"
            "v_and_b32 v243, 63, v0\n"
            "s_setprio 3\n"
            "s_lshr_b32 s3, s3, 6\n"
            "v_mul_u32_u24 v54, 3, v243\n"
            "s_cmp_lg_u32 s3, 4\n"
            "s_cbranch_scc1 Lncp_%=\n"
            "s_and_b32 s4, s4, 0xffffff80\n"
            "s_movk_i32 s6, 17408\n"
            "s_mov_b32 s7, 0x20000\n"
            "s_lshr_b32 s68, s2, 3\n"
            "s_lshl_b32 s68, s68, 7\n"
            "v_lshlrev_b32 v81, 12, v243\n"
            "buffer_load_dword v53, v81, s[4:7], s68 offen\n"
            "Lncp_%=:\n"
            "s_mul_i32 s68, s3, 162\n"
            "v_cmp_gt_u32 vcc, 54, v243\n"
            "v_add_u32 v54, s68, v54\n"
            "v_mov_b32 v82, 0x1000000\n"
            "v_cndmask_b32 v54, v82, v54, vcc\n"
            "v_mov_b32 v219, 0\n"
            "s_waitcnt lgkmcnt(0)\n"
            "s_mul_i32 s95, s2, s50\n"
            "v_add_lshl_u32 v81, v54, s95, 2\n"
            "buffer_load_dwordx3 v[32:34], v81, s[8:11], 0 offen nt\n"
            "buffer_load_dwordx3 v[36:38], v81, s[12:15], 0 offen nt\n"
            "v_lshlrev_b32 v82, 1, v81\n"
            "buffer_load_dword v40, off, s[20:23], 0\n"
            "buffer_load_dwordx4 v[42:45], v82, s[16:19], 0 offen nt\n"
            "buffer_load_dwordx2 v[46:47], v82, s[16:19], 0 offen offset:16 nt\n"
            "v_lshlrev_b32 v193, 7, v243\n"
            "s_lshl_b32 s54, s95, 5\n"
            "s_bfe_u32 s8, s2, 0x10002\n"
            "s_and_b32 s9, s2, 3\n"
            "s_lshl_b32 s9, s9, 5\n"
            "s_lshr_b32 s68, s2, 3\n"
            "s_add_u32 s9, s9, s68\n"
            "s_mul_i32 s55, s8, s53\n"
            "s_lshl_b32 s69, s9, 13\n"
            "s_add_u32 s69, s69, s55\n"
            "s_cmp_lt_u32 s3, 4\n"
            "s_cbranch_scc1 Lx0_%=\n"
            "v_lshlrev_b32 v81, 4, v0\n"
            "v_and_b32 v81, 0xe00, v81\n"
            "v_and_b32 v82, 31, v0\n"
            "v_lshl_or_b32 v81, v82, 3, v81\n"
            "buffer_load_dwordx2 v[48:49], v81, s[36:39], 0 offen\n"
            "buffer_load_dwordx2 v[50:51], v81, s[36:39], 0 offen offset:256\n"
            "s_cmp_eq_u32 s3, 4\n"
            "s_cbranch_scc1 Lx4_%=\n"
            "s_sub_u32 s68, 7, s3\n"
            "s_cmp_lt_u32 s68, 3\n"
            "s_cselect_b32 s68, s68, 0x20000\n"
            "s_lshl_b32 s68, s68, 13\n"
            "s_add_u32 s68, s68, s54\n"
            "buffer_load_dword v52, v193, s[24:27], s68 offen\n"
            "s_branch Lx1_%=\n"
            "Lx4_%=:\n"
            "v_lshlrev_b32 v81, 2, v243\n"
            "buffer_load_dword v52, v81, s[40:43], 0 offen\n"
            "s_branch Lx1_%=\n"
            "Lx0_%=:\n"
            "s_add_u32 s68, s3, 3\n"
            "s_cmp_lt_u32 s68, 3\n"
            "s_cselect_b32 s68, s68, 0x20000\n"
            "s_lshl_b32 s68, s68, 13\n"
            "s_add_u32 s68, s68, s54\n"
            "buffer_load_dword v52, v193, s[24:27], s68 offen\n"
            "s_and_b32 s68, s9, 31\n"
            "s_lshl_b32 s68, s68, 7\n"
            "s_cmp_eq_u32 s3, 3\n"
            "s_cselect_b32 s68, s68, 0x40000000\n"
            "v_lshlrev_b32 v81, 5, v193\n"
            "buffer_load_dword v48, v81, s[44:47], s68 offen\n"
            "buffer_load_dword v49, off, s[20:23], 0\n"
            "Lx1_%=:\n"
            "v_mov_b64 v[82:83], 0\n"
            "v_mov_b64 v[84:85], 0\n"
            "v_lshlrev_b32 v81, 4, v0\n"
            "s_cmp_lt_u32 s3, 4\n"
            "s_cselect_b32 s68, 0, 72704\n"
            "v_add_u32 v86, s68, v81\n"
            "ds_write_b128 v86, v[82:85]\n"
            "s_cselect_b32 s68, 80896, 0\n"
            "v_add_u32 v86, s68, v81\n"
            "ds_write_b128 v86, v[82:85]\n"
            "v_and_b32 v196, 15, v0\n"
            "v_mov_b32 v209, 1\n"
            "s_cmp_lg_u32 s3, 0\n"
            "s_cbranch_scc1 Lnk_%=\n"
            "v_lshlrev_b32 v81, 2, v243\n"
            "v_sub_u32 v82, 127, v243\n"
            "ds_write_b32 v81, v82 offset:96\n"
            "v_lshlrev_b32 v81, 2, v196\n"
            "v_sub_u32 v82, 63, v196\n"
            "ds_write_b32 v81, v82 offset:352\n"
            "v_bfrev_b32 v82, 1\n"
            "ds_write2_b32 v219, v82, v82 offset0:20 offset1:21\n"
            "Lnk_%=:\n"
            "s_sub_i32 s96, s49, s95\n"
            "s_min_i32 s96, s96, s50\n"
            "s_max_i32 s96, s96, 0\n"
            "s_and_b32 s12, s3, 3\n"
            "s_lshl_b32 s12, s12, 3\n"
            "s_lshl_b32 s68, 1, s12\n"
            "v_mov_b32 v80, s68\n"
            "s_sub_u32 s68, s68, 1\n"
            "s_and_b32 s68, s68, 0x10101010\n"
            "s_cmp_lt_u32 s3, 4\n"
            "s_cselect_b32 s13, s68, 0x10101010\n"
            "s_cselect_b32 s14, 0, s68\n"
            "s_mov_b32 s39, 0x10101010\n"
            "v_cmp_gt_u32 s[72:73], s96, v54\n"
            "v_lshlrev_b32 v77, 10, v54\n"
            "v_add_u32 v81, 1, v54\n"
            "v_cmp_gt_u32 s[74:75], s96, v81\n"
            "v_lshlrev_b32 v78, 10, v81\n"
            "v_add_u32 v81, 2, v54\n"
            "v_cmp_gt_u32 s[76:77], s96, v81\n"
            "v_lshlrev_b32 v79, 10, v81\n"
            "s_mul_i32 s10, s9, s48\n"
            "s_add_u32 s10, s10, 127\n"
            "s_add_u32 s11, s10, s48\n"
            "s_lshr_b32 s10, s10, 7\n"
            "s_lshr_b32 s11, s11, 7\n"
            "s_sub_u32 s11, s11, s10\n"
            "v_lshlrev_b32 v192, 4, v243\n"
            "v_lshlrev_b32 v194, 3, v243\n"
            "v_lshrrev_b32 v195, 4, v243\n"
            "v_lshlrev_b32 v214, 4, v196\n"
            "v_and_b32 v249, 3, v0\n"
            "v_lshlrev_b32 v250, 4, v249\n"
            "v_mov_b32 v251, 80\n"
            "v_lshrrev_b32 v83, 2, v196\n"
            "s_lshl_b32 s68, s3, 9\n"
            "s_add_u32 s68, s68, 62464\n"
            "v_add_u32 v212, s68, v194\n"
            "v_lshl_add_u32 v213, v195, 7, s68\n"
            "v_lshl_add_u32 v213, v249, 5, v213\n"
            "v_lshl_add_u32 v213, v83, 3, v213\n"
            "v_mov_b32 v224, 0x3f80\n"
            "v_mov_b32 v225, 0\n"
            "v_lshlrev_b64 v[224:225], v250, v[224:225]\n"
            "s_mul_i32 s68, s3, 4352\n"
            "s_sub_u32 s38, s3, 3\n"
            "s_max_i32 s38, s38, 0\n"
            "s_lshl_b32 s38, s38, 12\n"
            "s_add_u32 s68, s68, s38\n"
            "s_add_u32 s68, s68, 72704\n"
            "v_mov_b32 v81, s68\n"
            "v_add_u32 v231, s68, v192\n"
            "v_add_u32 v239, 0xfffff000, v231\n"
            "v_mov_b32 v236, v231\n"
            "s_add_u32 s38, s3, 3\n"
            "s_and_b32 s38, s38, 3\n"
            "s_lshl2_add_u32 s41, s38, 16\n"
            "s_mul_i32 s38, s38, 8448\n"
            "s_add_u32 s38, s38, 90112\n"
            "v_add_u32 v252, s38, v192\n"
            "s_movk_i32 s38, 272\n"
            "v_mad_u32_u24 v227, v196, s38, v81\n"
            "v_lshl_add_u32 v227, v195, 4, v227\n"
            "s_movk_i32 s38, 1088\n"
            "v_mad_u32_u24 v226, v195, s38, v81\n"
            "v_lshl_add_u32 v226, v196, 3, v226\n"
            "v_add_u32 v228, 123904, v192\n"
            "v_and_b32 v253, 1, v195\n"
            "v_lshl_add_u32 v253, v196, 1, v253\n"
            "v_lshlrev_b32 v253, 2, v253\n"
            "v_and_b32 v229, 1, v0\n"
            "v_lshlrev_b32 v229, 4, v229\n"
            "v_lshlrev_b32 v81, 25, v0\n"
            "v_and_b32 v81, 2.0, v81\n"
            "v_or_b32 v229, v229, v81\n"
            "v_bfe_u32 v206, v0, 1, 2\n"
            "v_lshlrev_b32 v207, 4, v206\n"
            "v_bfe_u32 v83, v0, 3, 2\n"
            "s_mul_i32 s68, s3, 768\n"
            "s_add_u32 s68, s68, 66560\n"
            "v_add_u32 v247, s68, v194\n"
            "v_lshl_add_u32 v81, v249, 3, s68\n"
            "v_lshl_add_u32 v237, v195, 1, v81\n"
            "v_add_u32 v237, 32, v237\n"
            "v_lshl_add_u32 v81, v206, 3, s68\n"
            "v_lshl_add_u32 v238, v83, 1, v81\n"
            "v_add_u32 v238, 64, v238\n"
            "v_and_b32 v81, 7, v0\n"
            "v_lshlrev_b32 v82, 2, v81\n"
            "s_mov_b32 s68, 1982153488\n"
            "v_bfe_u32 v240, s68, v82, 4\n"
            "v_lshlrev_b32 v240, 4, v240\n"
            "v_add_u32 v240, 3328, v240\n"
            "s_mov_b32 s68, 2205745445\n"
            "v_bfe_u32 v241, s68, v82, 4\n"
            "v_lshlrev_b32 v241, 4, v241\n"
            "v_add_u32 v241, 3072, v241\n"
            "s_mov_b32 s68, 2305097727\n"
            "v_bfe_u32 v242, s68, v82, 4\n"
            "v_lshlrev_b32 v242, 4, v242\n"
            "v_add_u32 v242, 3072, v242\n"
            "v_cmp_lt_u32 vcc, 5, v81\n"
            "v_add_u32 v82, 256, v242\n"
            "s_nop 0\n"
            "v_cndmask_b32 v242, v242, v82, vcc\n"
            "v_cmp_lt_u32 vcc, 3, v81\n"
            "s_nop 1\n"
            "v_cndmask_b32 v242, 8, v242, vcc\n"
            "s_mov_b32 s68, 1998716928\n"
            "v_lshlrev_b32 v82, 2, v81\n"
            "v_bfe_u32 v208, s68, v82, 4\n"
            "s_add_u32 s38, s3, 1\n"
            "s_and_b32 s38, s38, 3\n"
            "s_add_u32 s38, s38, 4\n"
            "v_readlane_b32 s64, v240, s3\n"
            "v_readlane_b32 s65, v241, s3\n"
            "v_readlane_b32 s66, v242, s3\n"
            "v_readlane_b32 s40, v242, s38\n"
            "v_lshl_add_u32 v244, v195, 2, s64\n"
            "v_lshl_add_u32 v245, v195, 2, s65\n"
            "v_lshl_add_u32 v246, v195, 2, s66\n"
            "v_lshl_add_u32 v248, v83, 2, s64\n"
            "s_add_u32 s64, s64, 512\n"
            "s_add_u32 s65, s65, 512\n"
            "s_add_u32 s66, s66, 512\n"
            "s_add_u32 s40, s40, 512\n"
            "s_lshl_b32 s68, s2, 7\n"
            "s_add_u32 s38, s68, 4096\n"
            "s_add_u32 s56, s20, s38\n"
            "s_addc_u32 s57, s21, 0\n"
            "s_xor_b32 s38, s38, 512\n"
            "s_add_u32 s58, s20, s38\n"
            "s_addc_u32 s59, s21, 0\n"
            "s_cmp_lt_u32 s3, 6\n"
            "s_mov_b32 s60, 78\n"
            "s_cmov_b32 s60, 78\n"
            "s_mov_b32 s61, 4294961748\n"
            "s_cmov_b32 s61, 4294961748\n"
            "s_movk_i32 s62, 0x3862\n"
            "s_cmov_b32 s62, 0x9062\n"
            "s_cmp_lt_u32 s3, 4\n"
            "s_cmov_b32 s60, 590\n"
            "s_cmov_b32 s61, 4294612224\n"
            "s_cmovk_i32 s62, 0x82\n"
            "s_mov_b32 s63, 5804032\n"
            "s_and_b32 s68, s9, 127\n"
            "s_lshl3_add_u32 s68, s68, s3\n"
            "s_mul_i32 s68, s68, 512\n"
            "s_add_u32 s67, s68, s55\n"
            "s_add_u32 s67, s67, 3145728\n"
            "s_lshr_b32 s38, s3, 2\n"
            "s_lshl_b32 s38, s38, 10\n"
            "s_lshl_b32 s15, s3, 9\n"
            "s_add_u32 s15, s15, 25600\n"
            "v_add_u32 v94, s15, v194\n"
            "s_lshr_b32 s68, s38, 1\n"
            "s_sub_u32 s15, s15, s68\n"
            "s_and_b32 s68, s3, 1\n"
            "s_lshl1_add_u32 s68, s2, s68\n"
            "s_lshl_b32 s68, s68, 10\n"
            "s_add_u32 s36, s68, 36864\n"
            "v_mov_b32 v93, 335549440\n"
            "v_lshlrev_b32 v95, 4, v0\n"
            "s_mul_i32 s68, s2, 20480\n"
            "s_add_u32 s68, s68, 557056\n"
            "s_add_u32 s4, s20, s68\n"
            "s_addc_u32 s5, s21, 0\n"
            "s_lshl_b32 s6, s96, 4\n"
            "s_add_u32 s6, s6, 4223\n"
            "s_and_b32 s6, s6, 0xffffff80\n"
            "s_mov_b32 s7, 0x20000\n"
            "v_mov_b64 v[0:1], 0\n"
            "v_mov_b64 v[2:3], 0\n"
            "v_mov_b64 v[4:5], 0\n"
            "v_mov_b64 v[6:7], 0\n"
            "v_mov_b64 v[8:9], 0\n"
            "v_mov_b64 v[10:11], 0\n"
            "v_mov_b64 v[12:13], 0\n"
            "v_mov_b64 v[14:15], 0\n"
            "v_mov_b64 v[16:17], 0\n"
            "v_mov_b64 v[18:19], 0\n"
            "v_mov_b64 v[20:21], 0\n"
            "v_mov_b64 v[22:23], 0\n"
            "v_mov_b64 v[24:25], 0\n"
            "v_mov_b64 v[26:27], 0\n"
            "v_mov_b64 v[28:29], 0\n"
            "v_mov_b64 v[30:31], 0\n"
            "s_waitcnt lgkmcnt(0)\n"
            "s_barrier\n"
            "s_waitcnt vmcnt(6)\n"
            "v_lshlrev_b32 v81, s52, v32\n"
            "v_lshlrev_b32 v84, s52, v36\n"
            "v_lshlrev_b32 v82, s52, v33\n"
            "v_lshlrev_b32 v85, s52, v37\n"
            "v_lshlrev_b32 v83, s52, v34\n"
            "v_lshlrev_b32 v86, s52, v38\n"
            "v_mul_hi_u32_u24 v81, s51, v81\n"
            "v_mul_hi_u32_u24 v84, s51, v84\n"
            "v_lshl_add_u32 v68, v81, 2, s38\n"
            "v_and_b32 v81, 64, v84\n"
            "v_lshl_add_u32 v68, v81, 3, v68\n"
            "s_mov_b64 exec, s[72:73]\n"
            "ds_add_rtn_u32 v71, v68, v80 offset:3072\n"
            "s_mov_b64 exec, -1\n"
            "v_mul_hi_u32_u24 v82, s51, v82\n"
            "v_mul_hi_u32_u24 v85, s51, v85\n"
            "v_lshl_add_u32 v69, v82, 2, s38\n"
            "v_and_b32 v82, 64, v85\n"
            "v_lshl_add_u32 v69, v82, 3, v69\n"
            "s_mov_b64 exec, s[74:75]\n"
            "ds_add_rtn_u32 v72, v69, v80 offset:3072\n"
            "s_mov_b64 exec, -1\n"
            "v_mul_hi_u32_u24 v83, s51, v83\n"
            "v_mul_hi_u32_u24 v86, s51, v86\n"
            "v_lshl_add_u32 v70, v83, 2, s38\n"
            "v_and_b32 v83, 64, v86\n"
            "v_lshl_add_u32 v70, v83, 3, v70\n"
            "s_mov_b64 exec, s[76:77]\n"
            "ds_add_rtn_u32 v73, v70, v80 offset:3072\n"
            "s_mov_b64 exec, -1\n"
            "v_bfe_u32 v84, v84, 3, 3\n"
            "v_and_b32 v81, 127, v32\n"
            "v_lshl_or_b32 v81, v81, 3, v84\n"
            "v_bfe_u32 v85, v85, 3, 3\n"
            "v_and_b32 v82, 127, v33\n"
            "v_lshl_or_b32 v82, v82, 3, v85\n"
            "v_bfe_u32 v86, v86, 3, 3\n"
            "v_and_b32 v83, 127, v34\n"
            "v_lshl_or_b32 v83, v83, 3, v86\n"
            "s_waitcnt vmcnt(5)\n"
            "v_readfirstlane_b32 s90, v40\n"
            "s_mul_i32 s90, s90, 1664525\n"
            "s_add_u32 s90, s90, 1013904223\n"
            "s_lshl_b32 s93, s90, 14\n"
            "s_and_b32 s94, s90, 0xffe00000\n"
            "s_waitcnt lgkmcnt(0)\n"
            "s_barrier\n"
            "ds_read_b128 v[128:131], v192 offset:3072\n"
            "ds_read_b128 v[132:135], v192 offset:4096\n"
            "v_or3_b32 v59, v81, v77, s94\n"
            "v_or_b32 v57, s93, v36\n"
            "v_lshrrev_b32 v74, 1, v68\n"
            "v_add_u32 v74, s15, v74\n"
            "v_bfe_u32 v71, v71, s12, 8\n"
            "v_or3_b32 v63, v82, v78, s94\n"
            "v_or_b32 v61, s93, v37\n"
            "v_lshrrev_b32 v75, 1, v69\n"
            "v_add_u32 v75, s15, v75\n"
            "v_bfe_u32 v72, v72, s12, 8\n"
            "v_or3_b32 v67, v83, v79, s94\n"
            "v_or_b32 v65, s93, v38\n"
            "v_lshrrev_b32 v76, 1, v70\n"
            "v_add_u32 v76, s15, v76\n"
            "v_bfe_u32 v73, v73, s12, 8\n"
            "s_waitcnt lgkmcnt(1)\n"
            "v_dot4_u32_u8 v136, v128, s39, 0\n"
            "v_dot4_u32_u8 v137, v129, s39, 0\n"
            "v_dot4_u32_u8 v138, v130, s39, 0\n"
            "v_dot4_u32_u8 v139, v131, s39, 0\n"
            "s_waitcnt lgkmcnt(0)\n"
            "v_dot4_u32_u8 v136, v132, s39, v136\n"
            "v_dot4_u32_u8 v137, v133, s39, v137\n"
            "v_dot4_u32_u8 v138, v134, s39, v138\n"
            "v_dot4_u32_u8 v139, v135, s39, v139\n"
            "v_dot4_u32_u8 v140, v128, s13, 0\n"
            "v_dot4_u32_u8 v141, v129, s13, v136\n"
            "v_dot4_u32_u8 v142, v130, s13, 0\n"
            "v_dot4_u32_u8 v143, v131, s13, v138\n"
            "v_add_u32 v87, v136, v137\n"
            "v_add3_u32 v88, v87, v138, v139\n"
            "v_dot4_u32_u8 v140, v132, s14, v140\n"
            "v_dot4_u32_u8 v141, v133, s14, v141\n"
            "s_mov_b32 s68, 0x10001\n"
            "v_add_u32_dpp v89, v88, v88 row_shr:1 row_mask:0xf bank_mask:0xf bound_ctrl:1\n"
            "v_dot4_u32_u8 v142, v134, s14, v142\n"
            "v_dot4_u32_u8 v143, v135, s14, v143\n"
            "v_add_u32_dpp v89, v89, v89 row_shr:2 row_mask:0xf bank_mask:0xf bound_ctrl:1\n"
            "v_lshl_add_u32 v140, v141, 16, v140\n"
            "v_mad_u32_u24 v90, v87, s68, v93\n"
            "v_add_u32_dpp v89, v89, v89 row_shr:4 row_mask:0xf bank_mask:0xf bound_ctrl:1\n"
            "v_add_u32 v140, v140, v93\n"
            "v_lshl_add_u32 v141, v143, 16, v142\n"
            "v_add_u32_dpp v89, v89, v89 row_shr:8 row_mask:0xf bank_mask:0xf bound_ctrl:1\n"
            "v_add_u32 v141, v141, v90\n"
            "s_and_b32 s38, s3, 6\n"
            "v_add_u32_dpp v89, v89, v89 row_bcast:15 row_mask:0xa bank_mask:0xf\n"
            "s_cmp_lg_u32 s38, 4\n"
            "s_nop 0\n"
            "v_add_u32_dpp v89, v89, v89 row_bcast:31 row_mask:0xc bank_mask:0xf\n"
            "v_sub_u32 v89, v89, v88\n"
            "v_mad_u32_u24 v140, v89, s68, v140\n"
            "v_mad_u32_u24 v141, v89, s68, v141\n"
            "ds_write_b64 v94, v[140:141]\n"
            "ds_read_u16 v74, v74\n"
            "ds_read_u16 v75, v75\n"
            "ds_read_u16 v76, v76\n"
            "s_cbranch_scc1 Lnt_%=\n"
            "s_bitcmp1_b32 s3, 0\n"
            "s_cbranch_scc0 Lt0_%=\n"
            "v_add_u32 v89, v89, v87\n"
            "v_mov_b64 v[136:137], v[138:139]\n"
            "Lt0_%=:\n"
            "v_lshl_add_u32 v128, v136, 16, v89\n"
            "v_add_u32 v89, v89, v136\n"
            "v_lshl_add_u32 v130, v137, 16, v89\n"
            "v_mov_b32 v129, s90\n"
            "v_mov_b32 v131, s90\n"
            "buffer_store_dwordx4 v[128:131], v192, s[20:23], s36 offen sc1\n"
            "s_waitcnt vmcnt(4)\n"
            "s_branch Lt1_%=\n"
            "Lnt_%=:\n"
            "s_waitcnt vmcnt(3)\n"
            "Lt1_%=:\n"
            "v_mov_b32 v56, v42\n"
            "v_mov_b32 v58, v43\n"
            "v_mov_b32 v60, v44\n"
            "v_mov_b32 v62, v45\n"
            "v_mov_b32 v64, v46\n"
            "v_mov_b32 v66, v47\n"
            "s_waitcnt lgkmcnt(0)\n"
            "v_lshl_add_u32 v74, v71, 4, v74\n"
            "v_lshl_add_u32 v75, v72, 4, v75\n"
            "v_lshl_add_u32 v76, v73, 4, v76\n"
            "s_mov_b64 exec, s[72:73]\n"
            "ds_write_b128 v74, v[56:59]\n"
            "s_mov_b64 exec, s[74:75]\n"
            "ds_write_b128 v75, v[60:63]\n"
            "s_mov_b64 exec, s[76:77]\n"
            "ds_write_b128 v76, v[64:67]\n"
            "s_mov_b64 exec, -1\n"
            "s_cmp_lt_u32 s3, 4\n"
            "s_waitcnt lgkmcnt(0)\n"
            "s_barrier\n"
            "s_cbranch_scc1 Lcol_%=\n"
            "ds_read_b128 v[56:59], v95 offset:1024\n"
            "ds_read_b128 v[60:63], v95 offset:5120\n"
            "ds_read_b128 v[64:67], v95 offset:9216\n"
            "ds_read_b128 v[68:71], v95 offset:13312\n"
            "ds_read_b128 v[72:75], v95 offset:17408\n"
            "s_movk_i32 s72, 4096\n"
            "s_movk_i32 s73, 8192\n"
            "s_movk_i32 s74, 12288\n"
            "s_movk_i32 s75, 16384\n"
            "s_waitcnt lgkmcnt(4)\n"
            "buffer_store_dwordx4 v[56:59], v95, s[4:7], 0 offen sc1\n"
            "s_waitcnt lgkmcnt(3)\n"
            "buffer_store_dwordx4 v[60:63], v95, s[4:7], s72 offen sc1\n"
            "s_waitcnt lgkmcnt(2)\n"
            "buffer_store_dwordx4 v[64:67], v95, s[4:7], s73 offen sc1\n"
            "s_waitcnt lgkmcnt(1)\n"
            "buffer_store_dwordx4 v[68:71], v95, s[4:7], s74 offen sc1\n"
            "s_waitcnt lgkmcnt(0)\n"
            "buffer_store_dwordx4 v[72:75], v95, s[4:7], s75 offen sc1\n"
            "s_setprio 2\n"
            "s_and_b32 s68, s3, 2\n"
            "s_lshl_b32 s68, s68, 13\n"
            "s_and_b32 s76, s3, 1\n"
            "s_lshl_b32 s76, s76, 12\n"
            "s_add_u32 s68, s68, s76\n"
            "v_lshl_add_u32 v81, v195, 10, s68\n"
            "v_add_u32 v81, v81, v214\n"
            "buffer_load_dwordx4 v[128:131], v81, s[44:47], 0 offen offset:0\n"
            "buffer_load_dwordx4 v[132:135], v81, s[44:47], 0 offen offset:256\n"
            "buffer_load_dwordx4 v[136:139], v81, s[44:47], s73 offen offset:0\n"
            "buffer_load_dwordx4 v[140:143], v81, s[44:47], s73 offen offset:256\n"
            "buffer_load_dwordx4 v[144:147], v81, s[44:47], 0 offen offset:512\n"
            "buffer_load_dwordx4 v[148:151], v81, s[44:47], 0 offen offset:768\n"
            "buffer_load_dwordx4 v[152:155], v81, s[44:47], s73 offen offset:512\n"
            "buffer_load_dwordx4 v[156:159], v81, s[44:47], s73 offen offset:768\n"
            "v_lshlrev_b32 v81, 4, v95\n"
            "v_and_b32 v81, 0x1800, v81\n"
            "v_lshrrev_b32 v82, 3, v95\n"
            "v_and_b32 v82, 0x100, v82\n"
            "v_lshlrev_b32 v83, 1, v95\n"
            "v_and_b32 v83, 0xe0, v83\n"
            "v_lshrrev_b32 v84, 7, v95\n"
            "v_and_b32 v84, 12, v84\n"
            "v_or3_b32 v81, v81, v82, v83\n"
            "v_add_u32 v84, 115456, v84\n"
            "v_add_u32 v81, v81, v84\n"
            "s_mov_b32 s78, 0xbfb8aa3b\n"
            "s_waitcnt vmcnt(14)\n"
            "v_lshlrev_b32 v160, 16, v48\n"
            "v_lshlrev_b32 v161, 16, v50\n"
            "v_mul_f32 v160, s78, v160\n"
            "v_mul_f32 v161, s78, v161\n"
            "v_cvt_pk_bf16_f32 v164, v160, v161\n"
            "v_lshlrev_b32 v162, 16, v164\n"
            "v_and_b32 v163, 0xffff0000, v164\n"
            "v_sub_f32 v160, v160, v162\n"
            "v_sub_f32 v161, v161, v163\n"
            "v_cvt_pk_bf16_f32 v165, v160, v161\n"
            "ds_write_b32 v81, v164 offset:0\n"
            "ds_write_b32 v81, v165 offset:512\n"
            "v_and_b32 v160, 0xffff0000, v48\n"
            "v_and_b32 v161, 0xffff0000, v50\n"
            "v_mul_f32 v160, s78, v160\n"
            "v_mul_f32 v161, s78, v161\n"
            "v_cvt_pk_bf16_f32 v166, v160, v161\n"
            "v_lshlrev_b32 v162, 16, v166\n"
            "v_and_b32 v163, 0xffff0000, v166\n"
            "v_sub_f32 v160, v160, v162\n"
            "v_sub_f32 v161, v161, v163\n"
            "v_cvt_pk_bf16_f32 v167, v160, v161\n"
            "ds_write_b32 v81, v166 offset:1024\n"
            "ds_write_b32 v81, v167 offset:1536\n"
            "v_lshlrev_b32 v160, 16, v49\n"
            "v_lshlrev_b32 v161, 16, v51\n"
            "v_mul_f32 v160, s78, v160\n"
            "v_mul_f32 v161, s78, v161\n"
            "v_cvt_pk_bf16_f32 v168, v160, v161\n"
            "v_lshlrev_b32 v162, 16, v168\n"
            "v_and_b32 v163, 0xffff0000, v168\n"
            "v_sub_f32 v160, v160, v162\n"
            "v_sub_f32 v161, v161, v163\n"
            "v_cvt_pk_bf16_f32 v169, v160, v161\n"
            "ds_write_b32 v81, v168 offset:16\n"
            "ds_write_b32 v81, v169 offset:528\n"
            "v_and_b32 v160, 0xffff0000, v49\n"
            "v_and_b32 v161, 0xffff0000, v51\n"
            "v_mul_f32 v160, s78, v160\n"
            "v_mul_f32 v161, s78, v161\n"
            "v_cvt_pk_bf16_f32 v170, v160, v161\n"
            "v_lshlrev_b32 v162, 16, v170\n"
            "v_and_b32 v163, 0xffff0000, v170\n"
            "v_sub_f32 v160, v160, v162\n"
            "v_sub_f32 v161, v161, v163\n"
            "v_cvt_pk_bf16_f32 v171, v160, v161\n"
            "ds_write_b32 v81, v170 offset:1040\n"
            "ds_write_b32 v81, v171 offset:1552\n"
            "s_cmp_lg_u32 s3, 4\n"
            "s_cbranch_scc1 Lncg_%=\n"
            "v_lshlrev_b32 v81, 2, v243\n"
            "s_waitcnt vmcnt(14)\n"
            "ds_write_b32 v81, v52 offset:62464\n"
            "v_lshrrev_b32 v81, 1, v243\n"
            "v_and_b32 v81, 6, v81\n"
            "v_lshl_add_u32 v81, v249, 5, v81\n"
            "ds_read_u16 v160, v81 offset:62464\n"
            "ds_read_u16 v161, v81 offset:62472\n"
            "ds_read_u16 v162, v81 offset:62480\n"
            "ds_read_u16 v163, v81 offset:62488\n"
            "s_mov_b32 s78, 0xbf317218\n"
            "s_waitcnt lgkmcnt(0)\n"
            "v_lshlrev_b32 v160, 16, v160\n"
            "v_lshlrev_b32 v161, 16, v161\n"
            "v_lshlrev_b32 v162, 16, v162\n"
            "v_lshlrev_b32 v163, 16, v163\n"
            "v_mul_f32 v160, s78, v160\n"
            "v_mul_f32 v161, s78, v161\n"
            "v_mul_f32 v162, s78, v162\n"
            "v_mul_f32 v163, s78, v163\n"
            "v_cvt_pk_bf16_f32 v168, v160, v161\n"
            "v_cvt_pk_bf16_f32 v169, v162, v163\n"
            "v_lshlrev_b32 v164, 16, v168\n"
            "v_and_b32 v165, 0xffff0000, v168\n"
            "v_lshlrev_b32 v166, 16, v169\n"
            "v_and_b32 v167, 0xffff0000, v169\n"
            "v_sub_f32 v160, v160, v164\n"
            "v_sub_f32 v161, v161, v165\n"
            "v_sub_f32 v162, v162, v166\n"
            "v_sub_f32 v163, v163, v167\n"
            "v_cvt_pk_bf16_f32 v170, v160, v161\n"
            "v_cvt_pk_bf16_f32 v171, v162, v163\n"
            "ds_write_b128 v192, v[168:171] offset:61440\n"
            "Lncg_%=:\n"
            "s_mov_b64 exec, 1\n"
            "ds_add_u32 v219, v209 offset:64\n"
            "s_mov_b64 exec, -1\n"
            "s_cmp_lg_u32 s3, 7\n"
            "s_cbranch_scc1 Lt2_%=\n"
            "s_sleep 16\n"
            "s_add_u32 s68, s69, 0\n"
            "buffer_load_dword v230, v193, s[28:31], s68 offen\n"
            "Lt2_%=:\n"
            "s_mul_i32 s68, s3, 27\n"
            "s_sub_u32 s68, s68, 108\n"
            "v_lshrrev_b32 v81, 1, v243\n"
            "v_add_u32 v81, s68, v81\n"
            "v_min_u32 v81, 79, v81\n"
            "v_lshlrev_b32 v180, 2, v81\n"
            "v_cmp_lt_u32 vcc, 39, v81\n"
            "v_and_b32 v82, 1, v243\n"
            "v_mul_u32_u24 v181, 80, v82\n"
            "v_add_u32 v82, 160, v181\n"
            "v_cndmask_b32 v181, v181, v82, vcc\n"
            "v_cndmask_b32 v82, 1, 0, vcc\n"
            "v_xor_b32 v82, s8, v82\n"
            "v_lshlrev_b32 v182, 8, v82\n"
            "v_mul_u32_u24 v183, 704, v81\n"
            "v_add_u32 v183, 5120, v183\n"
            "v_cmp_gt_u32 vcc, s11, v81\n"
            "v_add_lshl_u32 v184, v81, s10, 10\n"
            "v_bfrev_b32 v82, 1\n"
            "v_cndmask_b32 v184, v82, v184, vcc\n"
            "s_branch Lhpa_%=\n"
            "Lhp_%=:\n"
            "s_sleep 1\n"
            "Lhpa_%=:\n"
            "ds_read_b32 v220, v219 offset:64\n"
            "s_waitcnt lgkmcnt(0)\n"
            "v_readfirstlane_b32 s68, v220\n"
            "s_cmp_lt_u32 s68, 4\n"
            "s_cbranch_scc1 Lhp_%=\n"
            "v_add_u32 v81, 115456, v192\n"
            "ds_read_b128 v[96:99], v81 offset:0\n"
            "ds_read_b128 v[100:103], v81 offset:1024\n"
            "ds_read_b128 v[104:107], v81 offset:2048\n"
            "ds_read_b128 v[108:111], v81 offset:3072\n"
            "ds_read_b128 v[112:115], v81 offset:4096\n"
            "ds_read_b128 v[116:119], v81 offset:5120\n"
            "ds_read_b128 v[120:123], v81 offset:6144\n"
            "ds_read_b128 v[124:127], v81 offset:7168\n"
            "ds_read_b128 v[200:203], v192 offset:61440\n"
            "v_lshrrev_b32 v82, 2, v196\n"
            "s_lshl_b32 s68, s3, 10\n"
            "s_add_u32 s68, s68, 119808\n"
            "v_lshl_add_u32 v82, v82, 13, s68\n"
            "v_lshl_add_u32 v82, v195, 8, v82\n"
            "v_lshl_add_u32 v82, v249, 6, v82\n"
            "s_mov_b32 s78, 0x05040100\n"
            "s_mov_b32 s79, 0x07060302\n"
            "s_waitcnt vmcnt(1)\n"
            "s_cmp_eq_u32 s3, 7\n"
            "s_cbranch_scc1 Lt3_%=\n"
            "s_waitcnt vmcnt(0)\n"
            "Lt3_%=:\n"
            "v_perm_b32 v160, v132, v128, s78\n"
            "v_perm_b32 v161, v140, v136, s78\n"
            "v_perm_b32 v162, v148, v144, s78\n"
            "v_perm_b32 v163, v156, v152, s78\n"
            "ds_write_b128 v82, v[160:163] offset:0\n"
            "v_perm_b32 v164, v132, v128, s79\n"
            "v_perm_b32 v165, v140, v136, s79\n"
            "v_perm_b32 v166, v148, v144, s79\n"
            "v_perm_b32 v167, v156, v152, s79\n"
            "ds_write_b128 v82, v[164:167] offset:4096\n"
            "v_perm_b32 v168, v133, v129, s78\n"
            "v_perm_b32 v169, v141, v137, s78\n"
            "v_perm_b32 v170, v149, v145, s78\n"
            "v_perm_b32 v171, v157, v153, s78\n"
            "ds_write_b128 v82, v[168:171] offset:16\n"
            "v_perm_b32 v172, v133, v129, s79\n"
            "v_perm_b32 v173, v141, v137, s79\n"
            "v_perm_b32 v174, v149, v145, s79\n"
            "v_perm_b32 v175, v157, v153, s79\n"
            "ds_write_b128 v82, v[172:175] offset:4112\n"
            "v_perm_b32 v160, v134, v130, s78\n"
            "v_perm_b32 v161, v142, v138, s78\n"
            "v_perm_b32 v162, v150, v146, s78\n"
            "v_perm_b32 v163, v158, v154, s78\n"
            "ds_write_b128 v82, v[160:163] offset:32\n"
            "v_perm_b32 v164, v134, v130, s79\n"
            "v_perm_b32 v165, v142, v138, s79\n"
            "v_perm_b32 v166, v150, v146, s79\n"
            "v_perm_b32 v167, v158, v154, s79\n"
            "ds_write_b128 v82, v[164:167] offset:4128\n"
            "v_perm_b32 v168, v135, v131, s78\n"
            "v_perm_b32 v169, v143, v139, s78\n"
            "v_perm_b32 v170, v151, v147, s78\n"
            "v_perm_b32 v171, v159, v155, s78\n"
            "ds_write_b128 v82, v[168:171] offset:48\n"
            "v_perm_b32 v172, v135, v131, s79\n"
            "v_perm_b32 v173, v143, v139, s79\n"
            "v_perm_b32 v174, v151, v147, s79\n"
            "v_perm_b32 v175, v159, v155, s79\n"
            "ds_write_b128 v82, v[172:175] offset:4144\n"
            "s_cmp_lg_u32 s3, 7\n"
            "s_cbranch_scc1 Lsort_%=\n"
            "s_mov_b64 exec, 1\n"
            "ds_add_u32 v219, v209 offset:48\n"
            "s_mov_b64 exec, -1\n"
            "Lri_%=:\n"
            "ds_read_b32 v220, v219 offset:60\n"
            "s_waitcnt lgkmcnt(0)\n"
            "v_readfirstlane_b32 s68, v220\n"
            "s_cmp_lt_u32 s68, 4\n"
            "s_cbranch_scc1 Lri_%=\n"
            "s_add_u32 s68, s54, 24576\n"
            "s_nop 1\n"
            "buffer_load_dword v230, v193, s[24:27], s68 offen\n"
            "s_add_u32 s68, s54, 32768\n"
            "buffer_load_dword v230, v193, s[24:27], s68 offen\n"
            "s_and_b32 s68, s9, 31\n"
            "s_add_u32 s68, s68, 0\n"
            "s_lshl_b32 s68, s68, 13\n"
            "s_add_u32 s68, s68, s55\n"
            "buffer_load_dword v230, v193, s[28:31], s68 offen\n"
            "s_and_b32 s68, s9, 31\n"
            "s_add_u32 s68, s68, 32\n"
            "s_lshl_b32 s68, s68, 13\n"
            "s_add_u32 s68, s68, s55\n"
            "buffer_load_dword v230, v193, s[28:31], s68 offen\n"
            "s_add_u32 s68, s69, 1048576\n"
            "buffer_load_dword v230, v193, s[28:31], s68 offen\n"
            "s_add_u32 s68, s69, 2097152\n"
            "buffer_load_dword v230, v193, s[28:31], s68 offen\n"
            "s_branch Lw7a_%=\n"
            "Lw7_%=:\n"
            "s_sleep 1\n"
            "Lw7a_%=:\n"
            "ds_read_b32 v220, v219 offset:44\n"
            "s_waitcnt lgkmcnt(0)\n"
            "v_readfirstlane_b32 s68, v220\n"
            "s_cmp_lt_u32 s68, 4\n"
            "s_cbranch_scc1 Lw7_%=\n"
            "s_setprio 3\n"
            "s_branch Lmain_%=\n"
            "Lsort_%=:\n"
            "s_branch Lc3a_%=\n"
            "Lc3_%=:\n"
            "s_sleep 1\n"
            "Lc3a_%=:\n"
            "ds_read_b32 v220, v219 offset:44\n"
            "s_waitcnt lgkmcnt(0)\n"
            "v_readfirstlane_b32 s68, v220\n"
            "s_cmp_lt_u32 s68, 3\n"
            "s_cbranch_scc1 Lc3_%=\n"
            "s_setprio 3\n"
            "Lc4_%=:\n"
            "ds_read_b32 v220, v219 offset:44\n"
            "s_waitcnt lgkmcnt(0)\n"
            "v_readfirstlane_b32 s68, v220\n"
            "s_cmp_lt_u32 s68, 4\n"
            "s_cbranch_scc1 Lc4_%=\n"
            "ds_read_b32 v160, v180 offset:96\n"
            "ds_read_b128 v[128:131], v181 offset:96\n"
            "ds_read_b128 v[132:135], v181 offset:112\n"
            "ds_read_b128 v[136:139], v181 offset:128\n"
            "ds_read_b128 v[140:143], v181 offset:144\n"
            "ds_read_b128 v[144:147], v181 offset:160\n"
            "s_waitcnt lgkmcnt(4)\n"
            "v_sub_u32 v128, v160, v128\n"
            "v_sub_u32 v129, v160, v129\n"
            "v_sub_u32 v130, v160, v130\n"
            "v_sub_u32 v131, v160, v131\n"
            "v_lshrrev_b32 v161, 31, v128\n"
            "v_lshrrev_b32 v162, 31, v129\n"
            "v_alignbit_b32 v161, v161, v130, 31\n"
            "v_alignbit_b32 v162, v162, v131, 31\n"
            "v_lshrrev_b32 v163, 14, v160\n"
            "v_bfe_u32 v166, v160, 7, 7\n"
            "v_sub_u32 v166, v163, v166\n"
            "s_waitcnt lgkmcnt(3)\n"
            "v_sub_u32 v132, v160, v132\n"
            "v_sub_u32 v133, v160, v133\n"
            "v_sub_u32 v134, v160, v134\n"
            "v_sub_u32 v135, v160, v135\n"
            "v_alignbit_b32 v161, v161, v132, 31\n"
            "v_alignbit_b32 v162, v162, v133, 31\n"
            "v_alignbit_b32 v161, v161, v134, 31\n"
            "v_alignbit_b32 v162, v162, v135, 31\n"
            "v_cmp_lt_u32 vcc, 44, v163\n"
            "v_max_u32 v164, 8, v163\n"
            "s_waitcnt lgkmcnt(2)\n"
            "v_sub_u32 v136, v160, v136\n"
            "v_sub_u32 v137, v160, v137\n"
            "v_sub_u32 v138, v160, v138\n"
            "v_sub_u32 v139, v160, v139\n"
            "v_alignbit_b32 v161, v161, v136, 31\n"
            "v_alignbit_b32 v162, v162, v137, 31\n"
            "v_alignbit_b32 v161, v161, v138, 31\n"
            "v_alignbit_b32 v162, v162, v139, 31\n"
            "v_mov_b32 v165, 255\n"
            "v_lshl_or_b32 v183, v163, 16, v183\n"
            "v_lshl_or_b32 v183, v166, 24, v183\n"
            "s_waitcnt lgkmcnt(1)\n"
            "v_sub_u32 v140, v160, v140\n"
            "v_sub_u32 v141, v160, v141\n"
            "v_sub_u32 v142, v160, v142\n"
            "v_sub_u32 v143, v160, v143\n"
            "v_alignbit_b32 v161, v161, v140, 31\n"
            "v_alignbit_b32 v162, v162, v141, 31\n"
            "v_alignbit_b32 v161, v161, v142, 31\n"
            "v_alignbit_b32 v162, v162, v143, 31\n"
            "v_cndmask_b32 v164, v164, v165, vcc\n"
            "s_waitcnt lgkmcnt(0)\n"
            "v_sub_u32 v144, v160, v144\n"
            "v_sub_u32 v145, v160, v145\n"
            "v_sub_u32 v146, v160, v146\n"
            "v_sub_u32 v147, v160, v147\n"
            "v_alignbit_b32 v161, v161, v144, 31\n"
            "v_alignbit_b32 v162, v162, v145, 31\n"
            "v_alignbit_b32 v161, v161, v146, 31\n"
            "v_alignbit_b32 v162, v162, v147, 31\n"
            "v_bcnt_u32_b32 v161, v161, 0\n"
            "v_bcnt_u32_b32 v161, v162, v161\n"
            "s_nop 1\n"
            "v_add_u32_dpp v161, v161, v161 quad_perm:[1,0,3,2] row_mask:0xf bank_mask:0xf\n"
            "v_lshl_add_u32 v182, v161, 2, v182\n"
            "ds_write2st64_b32 v182, v183, v164 offset0:14 offset1:12\n"
            "ds_write_b32 v182, v184 offset:4096\n"
            "s_mov_b64 exec, 1\n"
            "ds_add_u32 v219, v209 offset:48\n"
            "s_mov_b64 exec, -1\n"
            "s_lshl3_add_u32 s68, s2, s3\n"
            "s_cmp_lg_u32 s68, 4\n"
            "s_cbranch_scc1 Lmain_%=\n"
            "v_mov_b32 v160, s90\n"
            "s_mov_b64 exec, 1\n"
            "buffer_store_dword v160, off, s[20:23], 0 sc1\n"
            "s_mov_b64 exec, -1\n"
            "s_branch Lmain_%=\n"
            "Lcol_%=:\n"
            "s_lshl_b32 s38, s8, 7\n"
            "s_add_u32 s38, s38, s9\n"
            "s_bfe_u32 s68, s38, 0x10001\n"
            "s_lshl_b32 s68, s68, 10\n"
            "s_lshr_b32 s72, s38, 2\n"
            "s_lshl4_add_u32 s68, s72, s68\n"
            "s_and_b32 s72, s38, 1\n"
            "s_lshl3_add_u32 s68, s72, s68\n"
            "s_add_u32 s68, s68, 36864\n"
            "v_lshl_add_u32 v180, v95, 7, s68\n"
            "s_sleep 10\n"
            "buffer_load_dwordx2 v[142:143], v180, s[20:23], 0 offen sc1\n"
            "s_mul_i32 s17, s3, 1792\n"
            "s_add_u32 s17, s17, 76800\n"
            "v_lshl_add_u32 v181, v243, 2, s17\n"
            "s_mul_i32 s15, s3, 1310720\n"
            "s_add_u32 s15, s15, 561152\n"
            "s_add_u32 s16, s15, 4096\n"
            "s_mul_i32 s18, s3, 1280\n"
            "s_add_u32 s18, s18, 84992\n"
            "s_lshl_b32 s19, s10, 3\n"
            "s_lshl_b32 s97, s50, 5\n"
            "s_mul_i32 s98, s97, s3\n"
            "s_lshl_b32 s98, s98, 6\n"
            "v_mul_u32_u24 v182, 704, v243\n"
            "v_add_u32 v182, 5120, v182\n"
            "v_add_u32 v184, 704, v182\n"
            "v_mul_u32_u24 v183, 704, v196\n"
            "v_add_u32 v183, 50176, v183\n"
            "v_add_u32 v185, 704, v183\n"
            "v_add_u32 v187, s18, v192\n"
            "v_add_u32 v188, s18, v214\n"
            "v_lshlrev_b32 v198, 5, v243\n"
            "v_lshlrev_b32 v199, 5, v196\n"
            "s_mov_b32 s68, 16797696\n"
            "v_mul_lo_u32 v189, v243, s68\n"
            "v_mov_b32 v190, 0x4000\n"
            "v_mov_b32 v191, 2.0\n"
            "v_mov_b32 v186, s98\n"
            "v_mov_b32 v197, v80\n"
            "s_branch Lcpa_%=\n"
            "Lcp_%=:\n"
            "s_sleep 1\n"
            "Lcpa_%=:\n"
            "ds_read_b32 v220, v219 offset:64\n"
            "s_waitcnt lgkmcnt(0)\n"
            "v_readfirstlane_b32 s68, v220\n"
            "s_cmp_lt_u32 s68, 4\n"
            "s_cbranch_scc1 Lcp_%=\n"
            "v_add_u32 v81, 115456, v192\n"
            "ds_read_b128 v[96:99], v81 offset:0\n"
            "ds_read_b128 v[100:103], v81 offset:1024\n"
            "ds_read_b128 v[104:107], v81 offset:2048\n"
            "ds_read_b128 v[108:111], v81 offset:3072\n"
            "ds_read_b128 v[112:115], v81 offset:4096\n"
            "ds_read_b128 v[116:119], v81 offset:5120\n"
            "ds_read_b128 v[120:123], v81 offset:6144\n"
            "ds_read_b128 v[124:127], v81 offset:7168\n"
            "ds_read_b128 v[200:203], v192 offset:61440\n"
            "s_mov_b32 s99, 0x400000\n"
            "Lpoll_%=:\n"
            "s_waitcnt vmcnt(0)\n"
            "v_cmp_ne_u32 vcc, s90, v143\n"
            "s_cbranch_vccnz Lpoll2_%=\n"
            "Lpoll3_%=:\n"
            "v_lshrrev_b32 v144, 18, v142\n"
            "v_and_b32 v146, 0xffff, v142\n"
            "v_cmp_ne_u32 s[86:87], 0, v144\n"
            "v_add_u32_dpp v145, v144, v144 row_shr:1 row_mask:0xf bank_mask:0xf bound_ctrl:1\n"
            "v_add_u32 v146, v146, v189\n"
            "v_cmp_lt_u32 s[88:89], 64, v144\n"
            "v_add_u32_dpp v145, v145, v145 row_shr:2 row_mask:0xf bank_mask:0xf bound_ctrl:1\n"
            "v_lshl_add_u32 v146, v144, 2, v146\n"
            "v_sub_u32 v147, s17, v144\n"
            "v_add_u32_dpp v145, v145, v145 row_shr:4 row_mask:0xf bank_mask:0xf bound_ctrl:1\n"
            "s_add_u32 s38, s17, -4\n"
            "s_nop 0\n"
            "v_add_u32_dpp v145, v145, v145 row_shr:8 row_mask:0xf bank_mask:0xf bound_ctrl:1\n"
            "s_nop 1\n"
            "v_add_u32_dpp v145, v145, v145 row_bcast:15 row_mask:0xa bank_mask:0xf\n"
            "s_nop 1\n"
            "v_add_u32_dpp v145, v145, v145 row_bcast:31 row_mask:0xc bank_mask:0xf\n"
            "v_cmp_ge_u32 vcc, 1792, v145\n"
            "v_readlane_b32 s37, v145, 63\n"
            "v_add_u32 v147, v147, v145\n"
            "v_add_u32 v148, s38, v145\n"
            "v_lshlrev_b32 v145, 2, v145\n"
            "v_and_b32 v148, -64, v148\n"
            "v_sub_u32 v146, v146, v145\n"
            "v_max_u32 v148, v148, v147\n"
            "s_and_b64 exec, vcc, s[86:87]\n"
            "ds_write_b32 v147, v146\n"
            "ds_write_b32 v148, v146\n"
            "s_mov_b64 exec, -1\n"
            "ds_read2st64_b32 v[74:75], v181 offset0:0 offset1:1\n"
            "ds_read2st64_b32 v[76:77], v181 offset0:2 offset1:3\n"
            "ds_read2st64_b32 v[78:79], v181 offset0:4 offset1:5\n"
            "ds_read_b32 v80, v181 offset:1536\n"
            "s_cmp_gt_u32 s37, 1792\n"
            "s_cbranch_scc1 Lover_%=\n"
            "Lover2_%=:\n"
            "s_lshr_b32 s37, s37, 2\n"
            "v_cmp_gt_i32 s[72:73], s37, v243\n"
            "s_sub_i32 s38, s37, 64\n"
            "v_cmp_gt_i32 s[74:75], s38, v243\n"
            "s_sub_i32 s38, s37, 128\n"
            "v_cmp_gt_i32 s[76:77], s38, v243\n"
            "s_sub_i32 s38, s37, 192\n"
            "v_cmp_gt_i32 s[78:79], s38, v243\n"
            "s_sub_i32 s38, s37, 256\n"
            "v_cmp_gt_i32 s[80:81], s38, v243\n"
            "s_sub_i32 s38, s37, 320\n"
            "v_cmp_gt_i32 s[82:83], s38, v243\n"
            "s_sub_i32 s38, s37, 384\n"
            "v_cmp_gt_i32 s[84:85], s38, v243\n"
            "v_cndmask_b32 v81, v191, v192, s[72:73]\n"
            "v_cndmask_b32 v82, v191, v192, s[74:75]\n"
            "v_cndmask_b32 v83, v191, v192, s[76:77]\n"
            "v_cndmask_b32 v84, v191, v192, s[78:79]\n"
            "v_cndmask_b32 v85, v191, v192, s[80:81]\n"
            "v_cndmask_b32 v86, v191, v192, s[82:83]\n"
            "v_cndmask_b32 v87, v191, v192, s[84:85]\n"
            "s_cmp_lg_u64 s[88:89], 0\n"
            "s_waitcnt lgkmcnt(0)\n"
            "v_max_u32_dpp v74, v74, v74 row_shr:1 row_mask:0xf bank_mask:0xf bound_ctrl:1\n"
            "v_max_u32_dpp v75, v75, v75 row_shr:1 row_mask:0xf bank_mask:0xf bound_ctrl:1\n"
            "v_max_u32_dpp v76, v76, v76 row_shr:1 row_mask:0xf bank_mask:0xf bound_ctrl:1\n"
            "v_max_u32_dpp v77, v77, v77 row_shr:1 row_mask:0xf bank_mask:0xf bound_ctrl:1\n"
            "v_max_u32_dpp v78, v78, v78 row_shr:1 row_mask:0xf bank_mask:0xf bound_ctrl:1\n"
            "v_max_u32_dpp v79, v79, v79 row_shr:1 row_mask:0xf bank_mask:0xf bound_ctrl:1\n"
            "v_max_u32_dpp v80, v80, v80 row_shr:1 row_mask:0xf bank_mask:0xf bound_ctrl:1\n"
            "v_max_u32_dpp v74, v74, v74 row_shr:2 row_mask:0xf bank_mask:0xf bound_ctrl:1\n"
            "v_max_u32_dpp v75, v75, v75 row_shr:2 row_mask:0xf bank_mask:0xf bound_ctrl:1\n"
            "v_max_u32_dpp v76, v76, v76 row_shr:2 row_mask:0xf bank_mask:0xf bound_ctrl:1\n"
            "v_max_u32_dpp v77, v77, v77 row_shr:2 row_mask:0xf bank_mask:0xf bound_ctrl:1\n"
            "v_max_u32_dpp v78, v78, v78 row_shr:2 row_mask:0xf bank_mask:0xf bound_ctrl:1\n"
            "v_max_u32_dpp v79, v79, v79 row_shr:2 row_mask:0xf bank_mask:0xf bound_ctrl:1\n"
            "v_max_u32_dpp v80, v80, v80 row_shr:2 row_mask:0xf bank_mask:0xf bound_ctrl:1\n"
            "v_max_u32_dpp v74, v74, v74 row_shr:4 row_mask:0xf bank_mask:0xf bound_ctrl:1\n"
            "v_max_u32_dpp v75, v75, v75 row_shr:4 row_mask:0xf bank_mask:0xf bound_ctrl:1\n"
            "v_max_u32_dpp v76, v76, v76 row_shr:4 row_mask:0xf bank_mask:0xf bound_ctrl:1\n"
            "v_max_u32_dpp v77, v77, v77 row_shr:4 row_mask:0xf bank_mask:0xf bound_ctrl:1\n"
            "v_max_u32_dpp v78, v78, v78 row_shr:4 row_mask:0xf bank_mask:0xf bound_ctrl:1\n"
            "v_max_u32_dpp v79, v79, v79 row_shr:4 row_mask:0xf bank_mask:0xf bound_ctrl:1\n"
            "v_max_u32_dpp v80, v80, v80 row_shr:4 row_mask:0xf bank_mask:0xf bound_ctrl:1\n"
            "v_max_u32_dpp v74, v74, v74 row_shr:8 row_mask:0xf bank_mask:0xf bound_ctrl:1\n"
            "v_max_u32_dpp v75, v75, v75 row_shr:8 row_mask:0xf bank_mask:0xf bound_ctrl:1\n"
            "v_max_u32_dpp v76, v76, v76 row_shr:8 row_mask:0xf bank_mask:0xf bound_ctrl:1\n"
            "v_max_u32_dpp v77, v77, v77 row_shr:8 row_mask:0xf bank_mask:0xf bound_ctrl:1\n"
            "v_max_u32_dpp v78, v78, v78 row_shr:8 row_mask:0xf bank_mask:0xf bound_ctrl:1\n"
            "v_max_u32_dpp v79, v79, v79 row_shr:8 row_mask:0xf bank_mask:0xf bound_ctrl:1\n"
            "v_max_u32_dpp v80, v80, v80 row_shr:8 row_mask:0xf bank_mask:0xf bound_ctrl:1\n"
            "s_cbranch_scc1 Llng_%=\n"
            "Llng2_%=:\n"
            "v_mad_u32_u24 v81, v74, 1, v81\n"
            "buffer_load_dwordx4 v[34:37], v81, s[20:23], s15 offen offset:0 sc1\n"
            "v_mad_u32_u24 v82, v75, 1, v82\n"
            "buffer_load_dwordx4 v[40:43], v82, s[20:23], s15 offen offset:1024 sc1\n"
            "v_mad_u32_u24 v83, v76, 1, v83\n"
            "buffer_load_dwordx4 v[46:49], v83, s[20:23], s15 offen offset:2048 sc1\n"
            "v_mad_u32_u24 v84, v77, 1, v84\n"
            "buffer_load_dwordx4 v[52:55], v84, s[20:23], s15 offen offset:3072 sc1\n"
            "v_mad_u32_u24 v85, v78, 1, v85\n"
            "buffer_load_dwordx4 v[58:61], v85, s[20:23], s16 offen offset:0 sc1\n"
            "v_mad_u32_u24 v86, v79, 1, v86\n"
            "buffer_load_dwordx4 v[64:67], v86, s[20:23], s16 offen offset:1024 sc1\n"
            "v_mad_u32_u24 v87, v80, 1, v87\n"
            "buffer_load_dwordx4 v[70:73], v87, s[20:23], s16 offen offset:2048 sc1\n"
            "s_mov_b64 exec, 1\n"
            "ds_add_u32 v219, v209 offset:60\n"
            "s_mov_b64 exec, -1\n"
            "v_lshrrev_b32 v88, 24, v74\n"
            "v_mad_u32_u24 v88, v88, s97, v186\n"
            "v_lshrrev_b32 v89, 24, v75\n"
            "v_mad_u32_u24 v89, v89, s97, v186\n"
            "v_lshrrev_b32 v90, 24, v76\n"
            "v_mad_u32_u24 v90, v90, s97, v186\n"
            "v_lshrrev_b32 v91, 24, v77\n"
            "v_mad_u32_u24 v91, v91, s97, v186\n"
            "v_lshrrev_b32 v92, 24, v78\n"
            "v_mad_u32_u24 v92, v92, s97, v186\n"
            "v_lshrrev_b32 v93, 24, v79\n"
            "v_mad_u32_u24 v93, v93, s97, v186\n"
            "v_lshrrev_b32 v94, 24, v80\n"
            "v_mad_u32_u24 v94, v94, s97, v186\n"
            "s_movk_i32 s38, 0x3fff\n"
            "s_mov_b32 s36, 0x1fffff\n"
            "s_waitcnt vmcnt(6)\n"
            "Lchk0_%=:\n"
            "v_xor_b32 v35, s93, v35\n"
            "v_xor_b32 v37, s94, v37\n"
            "v_cmp_lt_u32 vcc, s38, v35\n"
            "v_cmp_lt_u32 s[86:87], s36, v37\n"
            "v_subrev_u32 v135, s19, v37\n"
            "v_and_b32 v135, 0x3ff, v135\n"
            "s_or_b64 vcc, vcc, s[86:87]\n"
            "s_and_b64 vcc, vcc, s[72:73]\n"
            "s_cbranch_scc1 Lre0_%=\n"
            "Lok0_%=:\n"
            "v_lshlrev_b32 v144, 2, v135\n"
            "v_lshrrev_b32 v145, 1, v135\n"
            "v_and_b32 v145, 0x1fc, v145\n"
            "v_and_b32 v146, 4, v135\n"
            "v_lshl_or_b32 v146, v146, 5, v190\n"
            "s_mov_b64 exec, s[72:73]\n"
            "ds_add_rtn_u32 v128, v144, v197 offset:512\n"
            "ds_add_u32 v145, v146 offset:96\n"
            "s_mov_b64 exec, -1\n"
            "s_waitcnt vmcnt(5)\n"
            "Lchk1_%=:\n"
            "v_xor_b32 v41, s93, v41\n"
            "v_xor_b32 v43, s94, v43\n"
            "v_cmp_lt_u32 vcc, s38, v41\n"
            "v_cmp_lt_u32 s[86:87], s36, v43\n"
            "v_subrev_u32 v136, s19, v43\n"
            "v_and_b32 v136, 0x3ff, v136\n"
            "s_or_b64 vcc, vcc, s[86:87]\n"
            "s_and_b64 vcc, vcc, s[74:75]\n"
            "s_cbranch_scc1 Lre1_%=\n"
            "Lok1_%=:\n"
            "v_lshlrev_b32 v144, 2, v136\n"
            "v_lshrrev_b32 v145, 1, v136\n"
            "v_and_b32 v145, 0x1fc, v145\n"
            "v_and_b32 v146, 4, v136\n"
            "v_lshl_or_b32 v146, v146, 5, v190\n"
            "s_mov_b64 exec, s[74:75]\n"
            "ds_add_rtn_u32 v129, v144, v197 offset:512\n"
            "ds_add_u32 v145, v146 offset:96\n"
            "s_mov_b64 exec, -1\n"
            "s_waitcnt vmcnt(4)\n"
            "Lchk2_%=:\n"
            "v_xor_b32 v47, s93, v47\n"
            "v_xor_b32 v49, s94, v49\n"
            "v_cmp_lt_u32 vcc, s38, v47\n"
            "v_cmp_lt_u32 s[86:87], s36, v49\n"
            "v_subrev_u32 v137, s19, v49\n"
            "v_and_b32 v137, 0x3ff, v137\n"
            "s_or_b64 vcc, vcc, s[86:87]\n"
            "s_and_b64 vcc, vcc, s[76:77]\n"
            "s_cbranch_scc1 Lre2_%=\n"
            "Lok2_%=:\n"
            "v_lshlrev_b32 v144, 2, v137\n"
            "v_lshrrev_b32 v145, 1, v137\n"
            "v_and_b32 v145, 0x1fc, v145\n"
            "v_and_b32 v146, 4, v137\n"
            "v_lshl_or_b32 v146, v146, 5, v190\n"
            "s_mov_b64 exec, s[76:77]\n"
            "ds_add_rtn_u32 v130, v144, v197 offset:512\n"
            "ds_add_u32 v145, v146 offset:96\n"
            "s_mov_b64 exec, -1\n"
            "s_waitcnt vmcnt(3)\n"
            "Lchk3_%=:\n"
            "v_xor_b32 v53, s93, v53\n"
            "v_xor_b32 v55, s94, v55\n"
            "v_cmp_lt_u32 vcc, s38, v53\n"
            "v_cmp_lt_u32 s[86:87], s36, v55\n"
            "v_subrev_u32 v138, s19, v55\n"
            "v_and_b32 v138, 0x3ff, v138\n"
            "s_or_b64 vcc, vcc, s[86:87]\n"
            "s_and_b64 vcc, vcc, s[78:79]\n"
            "s_cbranch_scc1 Lre3_%=\n"
            "Lok3_%=:\n"
            "v_lshlrev_b32 v144, 2, v138\n"
            "v_lshrrev_b32 v145, 1, v138\n"
            "v_and_b32 v145, 0x1fc, v145\n"
            "v_and_b32 v146, 4, v138\n"
            "v_lshl_or_b32 v146, v146, 5, v190\n"
            "s_mov_b64 exec, s[78:79]\n"
            "ds_add_rtn_u32 v131, v144, v197 offset:512\n"
            "ds_add_u32 v145, v146 offset:96\n"
            "s_mov_b64 exec, -1\n"
            "s_waitcnt vmcnt(2)\n"
            "Lchk4_%=:\n"
            "v_xor_b32 v59, s93, v59\n"
            "v_xor_b32 v61, s94, v61\n"
            "v_cmp_lt_u32 vcc, s38, v59\n"
            "v_cmp_lt_u32 s[86:87], s36, v61\n"
            "v_subrev_u32 v139, s19, v61\n"
            "v_and_b32 v139, 0x3ff, v139\n"
            "s_or_b64 vcc, vcc, s[86:87]\n"
            "s_and_b64 vcc, vcc, s[80:81]\n"
            "s_cbranch_scc1 Lre4_%=\n"
            "Lok4_%=:\n"
            "v_lshlrev_b32 v144, 2, v139\n"
            "v_lshrrev_b32 v145, 1, v139\n"
            "v_and_b32 v145, 0x1fc, v145\n"
            "v_and_b32 v146, 4, v139\n"
            "v_lshl_or_b32 v146, v146, 5, v190\n"
            "s_mov_b64 exec, s[80:81]\n"
            "ds_add_rtn_u32 v132, v144, v197 offset:512\n"
            "ds_add_u32 v145, v146 offset:96\n"
            "s_mov_b64 exec, -1\n"
            "s_waitcnt vmcnt(1)\n"
            "Lchk5_%=:\n"
            "v_xor_b32 v65, s93, v65\n"
            "v_xor_b32 v67, s94, v67\n"
            "v_cmp_lt_u32 vcc, s38, v65\n"
            "v_cmp_lt_u32 s[86:87], s36, v67\n"
            "v_subrev_u32 v140, s19, v67\n"
            "v_and_b32 v140, 0x3ff, v140\n"
            "s_or_b64 vcc, vcc, s[86:87]\n"
            "s_and_b64 vcc, vcc, s[82:83]\n"
            "s_cbranch_scc1 Lre5_%=\n"
            "Lok5_%=:\n"
            "v_lshlrev_b32 v144, 2, v140\n"
            "v_lshrrev_b32 v145, 1, v140\n"
            "v_and_b32 v145, 0x1fc, v145\n"
            "v_and_b32 v146, 4, v140\n"
            "v_lshl_or_b32 v146, v146, 5, v190\n"
            "s_mov_b64 exec, s[82:83]\n"
            "ds_add_rtn_u32 v133, v144, v197 offset:512\n"
            "ds_add_u32 v145, v146 offset:96\n"
            "s_mov_b64 exec, -1\n"
            "s_cmp_eq_u64 s[84:85], 0\n"
            "s_cbranch_scc1 Lr7_%=\n"
            "s_waitcnt vmcnt(0)\n"
            "Lchk6_%=:\n"
            "v_xor_b32 v71, s93, v71\n"
            "v_xor_b32 v73, s94, v73\n"
            "v_cmp_lt_u32 vcc, s38, v71\n"
            "v_cmp_lt_u32 s[86:87], s36, v73\n"
            "v_subrev_u32 v141, s19, v73\n"
            "v_and_b32 v141, 0x3ff, v141\n"
            "s_or_b64 vcc, vcc, s[86:87]\n"
            "s_and_b64 vcc, vcc, s[84:85]\n"
            "s_cbranch_scc1 Lre6_%=\n"
            "Lok6_%=:\n"
            "v_lshlrev_b32 v144, 2, v141\n"
            "v_lshrrev_b32 v145, 1, v141\n"
            "v_and_b32 v145, 0x1fc, v145\n"
            "v_and_b32 v146, 4, v141\n"
            "v_lshl_or_b32 v146, v146, 5, v190\n"
            "s_mov_b64 exec, s[84:85]\n"
            "ds_add_rtn_u32 v134, v144, v197 offset:512\n"
            "ds_add_u32 v145, v146 offset:96\n"
            "s_mov_b64 exec, -1\n"
            "Lr7_%=:\n"
            "s_mov_b64 exec, 1\n"
            "ds_add_u32 v219, v209 offset:44\n"
            "s_mov_b64 exec, -1\n"
            "v_lshlrev_b32 v32, 10, v35\n"
            "v_lshrrev_b32 v33, 10, v37\n"
            "v_mov_b32 v35, v36\n"
            "v_lshl_add_u32 v33, v33, 5, v88\n"
            "v_lshl_add_u32 v135, v135, 1, s18\n"
            "v_lshlrev_b32 v38, 10, v41\n"
            "v_lshrrev_b32 v39, 10, v43\n"
            "v_mov_b32 v41, v42\n"
            "v_lshl_add_u32 v39, v39, 5, v89\n"
            "v_lshl_add_u32 v136, v136, 1, s18\n"
            "v_lshlrev_b32 v44, 10, v47\n"
            "v_lshrrev_b32 v45, 10, v49\n"
            "v_mov_b32 v47, v48\n"
            "v_lshl_add_u32 v45, v45, 5, v90\n"
            "v_lshl_add_u32 v137, v137, 1, s18\n"
            "v_lshlrev_b32 v50, 10, v53\n"
            "v_lshrrev_b32 v51, 10, v55\n"
            "v_mov_b32 v53, v54\n"
            "v_lshl_add_u32 v51, v51, 5, v91\n"
            "v_lshl_add_u32 v138, v138, 1, s18\n"
            "v_lshlrev_b32 v56, 10, v59\n"
            "v_lshrrev_b32 v57, 10, v61\n"
            "v_mov_b32 v59, v60\n"
            "v_lshl_add_u32 v57, v57, 5, v92\n"
            "v_lshl_add_u32 v139, v139, 1, s18\n"
            "v_lshlrev_b32 v62, 10, v65\n"
            "v_lshrrev_b32 v63, 10, v67\n"
            "v_mov_b32 v65, v66\n"
            "v_lshl_add_u32 v63, v63, 5, v93\n"
            "v_lshl_add_u32 v140, v140, 1, s18\n"
            "v_lshlrev_b32 v68, 10, v71\n"
            "v_lshrrev_b32 v69, 10, v73\n"
            "v_mov_b32 v71, v72\n"
            "v_lshl_add_u32 v69, v69, 5, v94\n"
            "v_lshl_add_u32 v141, v141, 1, s18\n"
            "Lcc_%=:\n"
            "ds_read_b32 v220, v219 offset:44\n"
            "s_waitcnt lgkmcnt(0)\n"
            "v_readfirstlane_b32 s68, v220\n"
            "s_cmp_lt_u32 s68, 4\n"
            "s_cbranch_scc1 Lcc_%=\n"
            "ds_read_b128 v[144:147], v198 offset:512\n"
            "ds_read_b128 v[148:151], v198 offset:528\n"
            "ds_read_b128 v[152:155], v199 offset:2560\n"
            "ds_read_b128 v[156:159], v199 offset:2576\n"
            "v_bfe_u32 v128, v128, s12, 8\n"
            "v_bfe_u32 v129, v129, s12, 8\n"
            "v_bfe_u32 v130, v130, s12, 8\n"
            "v_bfe_u32 v131, v131, s12, 8\n"
            "v_bfe_u32 v132, v132, s12, 8\n"
            "v_bfe_u32 v133, v133, s12, 8\n"
            "v_bfe_u32 v134, v134, s12, 8\n"
            "s_waitcnt lgkmcnt(3)\n"
            "v_dot4_u32_u8 v160, v144, s13, v182\n"
            "v_dot4_u32_u8 v182, v144, s39, v182\n"
            "v_dot4_u32_u8 v161, v145, s13, v182\n"
            "v_dot4_u32_u8 v182, v145, s39, v182\n"
            "v_dot4_u32_u8 v162, v146, s13, v182\n"
            "v_dot4_u32_u8 v182, v146, s39, v182\n"
            "v_dot4_u32_u8 v163, v147, s13, v182\n"
            "v_dot4_u32_u8 v182, v147, s39, v182\n"
            "s_waitcnt lgkmcnt(2)\n"
            "v_dot4_u32_u8 v164, v148, s13, v182\n"
            "v_dot4_u32_u8 v182, v148, s39, v182\n"
            "v_dot4_u32_u8 v165, v149, s13, v182\n"
            "v_dot4_u32_u8 v182, v149, s39, v182\n"
            "v_dot4_u32_u8 v166, v150, s13, v182\n"
            "v_dot4_u32_u8 v182, v150, s39, v182\n"
            "v_dot4_u32_u8 v167, v151, s13, v182\n"
            "v_dot4_u32_u8 v182, v151, s39, v182\n"
            "s_waitcnt lgkmcnt(0)\n"
            "v_dot4_u32_u8 v168, v152, s13, v183\n"
            "v_dot4_u32_u8 v183, v152, s39, v183\n"
            "v_dot4_u32_u8 v169, v153, s13, v183\n"
            "v_dot4_u32_u8 v183, v153, s39, v183\n"
            "v_lshl_or_b32 v160, v161, 16, v160\n"
            "v_lshl_or_b32 v161, v163, 16, v162\n"
            "v_lshl_or_b32 v162, v165, 16, v164\n"
            "v_lshl_or_b32 v163, v167, 16, v166\n"
            "ds_write_b128 v187, v[160:163]\n"
            "v_dot4_u32_u8 v170, v154, s13, v183\n"
            "v_dot4_u32_u8 v183, v154, s39, v183\n"
            "v_dot4_u32_u8 v171, v155, s13, v183\n"
            "v_dot4_u32_u8 v183, v155, s39, v183\n"
            "v_dot4_u32_u8 v172, v156, s13, v183\n"
            "v_dot4_u32_u8 v183, v156, s39, v183\n"
            "v_dot4_u32_u8 v173, v157, s13, v183\n"
            "v_dot4_u32_u8 v183, v157, s39, v183\n"
            "v_dot4_u32_u8 v174, v158, s13, v183\n"
            "v_dot4_u32_u8 v183, v158, s39, v183\n"
            "v_dot4_u32_u8 v175, v159, s13, v183\n"
            "v_dot4_u32_u8 v183, v159, s39, v183\n"
            "v_cmp_gt_u32 vcc, v182, v184\n"
            "v_lshl_or_b32 v168, v169, 16, v168\n"
            "v_lshl_or_b32 v169, v171, 16, v170\n"
            "v_lshl_or_b32 v170, v173, 16, v172\n"
            "v_lshl_or_b32 v171, v175, 16, v174\n"
            "ds_write_b128 v188, v[168:171] offset:1024\n"
            "ds_read_u16 v135, v135\n"
            "ds_read_u16 v136, v136\n"
            "ds_read_u16 v137, v137\n"
            "ds_read_u16 v138, v138\n"
            "ds_read_u16 v139, v139\n"
            "ds_read_u16 v140, v140\n"
            "ds_read_u16 v141, v141\n"
            "v_cmp_gt_u32 s[86:87], v183, v185\n"
            "s_or_b64 vcc, vcc, s[86:87]\n"
            "s_cbranch_scc1 Lbig_%=\n"
            "s_waitcnt lgkmcnt(6)\n"
            "v_lshl_add_u32 v135, v128, 4, v135\n"
            "s_mov_b64 exec, s[72:73]\n"
            "ds_write_b128 v135, v[32:35]\n"
            "s_waitcnt lgkmcnt(6)\n"
            "v_lshl_add_u32 v136, v129, 4, v136\n"
            "s_mov_b64 exec, s[74:75]\n"
            "ds_write_b128 v136, v[38:41]\n"
            "s_waitcnt lgkmcnt(6)\n"
            "v_lshl_add_u32 v137, v130, 4, v137\n"
            "s_mov_b64 exec, s[76:77]\n"
            "ds_write_b128 v137, v[44:47]\n"
            "s_waitcnt lgkmcnt(6)\n"
            "v_lshl_add_u32 v138, v131, 4, v138\n"
            "s_mov_b64 exec, s[78:79]\n"
            "ds_write_b128 v138, v[50:53]\n"
            "s_waitcnt lgkmcnt(6)\n"
            "v_lshl_add_u32 v139, v132, 4, v139\n"
            "s_mov_b64 exec, s[80:81]\n"
            "ds_write_b128 v139, v[56:59]\n"
            "s_waitcnt lgkmcnt(6)\n"
            "v_lshl_add_u32 v140, v133, 4, v140\n"
            "s_mov_b64 exec, s[82:83]\n"
            "ds_write_b128 v140, v[62:65]\n"
            "s_waitcnt lgkmcnt(6)\n"
            "v_lshl_add_u32 v141, v134, 4, v141\n"
            "s_mov_b64 exec, s[84:85]\n"
            "ds_write_b128 v141, v[68:71]\n"
            "Lbig_%=:\n"
            "s_mov_b64 exec, 1\n"
            "ds_add_u32 v219, v209 offset:56\n"
            "s_mov_b64 exec, -1\n"
            "Lmain_%=:\n"
            "Ls_%=:\n"
            "ds_read_b32 v220, v219 offset:48\n"
            "s_waitcnt lgkmcnt(0)\n"
            "v_readfirstlane_b32 s68, v220\n"
            "s_cmp_lt_u32 s68, 4\n"
            "s_cbranch_scc1 Ls_%=\n"
            "v_mov_b32 v140, s64\n"
            "ds_read_b32 v128, v240\n"
            "ds_read_b32 v129, v241\n"
            "ds_read_b32 v130, v242\n"
            "ds_read_b32 v131, v219\n"
            "ds_read_b32 v138, v248 offset:512\n"
            "ds_read_b32 v139, v244 offset:512\n"
            "ds_read_u8 v140, v140 offset:2\n"
            "ds_read_b32 v217, v244 offset:1024\n"
            "ds_read_b32 v215, v245 offset:1024\n"
            "ds_read_b32 v218, v246 offset:1024\n"
            "s_waitcnt lgkmcnt(6)\n"
            "v_max_u32 v128, 12, v128\n"
            "v_mov_b32_dpp v134, v130 quad_perm:[1,2,3,0] row_mask:0xf bank_mask:0xf\n"
            "v_add_u32 v132, v128, v129\n"
            "v_sub_u32 v133, 0, v132\n"
            "v_lshl_add_u32 v135, v132, 1, v208\n"
            "v_and_b32 v133, 3, v133\n"
            "v_add_lshl_u32 v133, v132, v133, 1\n"
            "v_add3_u32 v135, v135, v134, v130\n"
            "v_readlane_b32 s73, v128, s3\n"
            "s_nop 0\n"
            "v_add_u32_dpp v135, v133, v135 row_shr:4 row_mask:0xf bank_mask:0xf bound_ctrl:1\n"
            "v_readlane_b32 s75, v129, s3\n"
            "v_readlane_b32 s80, v130, s3\n"
            "v_sub_u32_dpp v133, v135, v135 quad_perm:[1,2,3,0] row_mask:0xf bank_mask:0xf\n"
            "v_mov_b32_dpp v136, v135 quad_perm:[3,0,1,2] row_mask:0xf bank_mask:0xf\n"
            "v_lshl_add_u32 v137, v134, 3, 8\n"
            "v_mad_i32_i24 v133, v133, 3, v137\n"
            "v_sub_u32_dpp v136, v135, v136 quad_perm:[2,3,0,1] row_mask:0xf bank_mask:0xf\n"
            "v_add_u32 v134, -4, v134\n"
            "v_add_u32 v133, v133, v136\n"
            "v_ashrrev_i32 v133, 4, v133\n"
            "v_max_i32 v133, 4, v133\n"
            "v_min_i32 v133, v133, v134\n"
            "v_max_i32 v133, 0, v133\n"
            "s_add_i32 s69, s73, -1\n"
            "s_lshr_b32 s69, s69, 1\n"
            "v_mov_b32_dpp v134, v133 quad_perm:[3,0,1,2] row_mask:0xf bank_mask:0xf\n"
            "v_readlane_b32 s77, v133, s3\n"
            "v_sub_u32 v134, v130, v134\n"
            "v_add3_u32 v132, v132, v133, v131\n"
            "v_readlane_b32 s76, v134, s3\n"
            "v_add_u32 v132, v132, v134\n"
            "v_cmp_lt_u32 vcc, 72, v132\n"
            "v_readlane_b32 s73, v132, s3\n"
            "s_cbranch_vccnz Lslow_%=\n"
            "s_sub_u32 s43, 0, s73\n"
            "s_and_b32 s43, s43, 3\n"
            "s_min_u32 s43, s43, 3\n"
            "s_add_u32 s73, s73, s43\n"
            "s_sub_u32 s81, 8, s43\n"
            "s_max_i32 s69, s69, s81\n"
            "s_waitcnt lgkmcnt(3)\n"
            "v_max_u32 v140, 8, v140\n"
            "v_add_u32 v140, s43, v140\n"
            "v_sub_u32_sdwa v134, v140, v138 dst_sel:DWORD dst_unused:UNUSED_PAD src0_sel:DWORD src1_sel:BYTE_2\n"
            "v_sub_u32 v134, v206, v134\n"
            "v_add_u32 v136, 4, v134\n"
            "v_cmp_lt_u32_sdwa vcc, v134, v138 src0_sel:DWORD src1_sel:BYTE_2\n"
            "v_cmp_lt_u32_sdwa s[86:87], v136, v138 src0_sel:DWORD src1_sel:BYTE_2\n"
            "v_and_b32 v138, 0xffff, v138\n"
            "v_lshl_add_u32 v138, v134, 4, v138\n"
            "v_sub_u32_sdwa v135, v140, v139 dst_sel:DWORD dst_unused:UNUSED_PAD src0_sel:DWORD src1_sel:BYTE_2\n"
            "v_sub_u32 v135, v249, v135\n"
            "v_add_u32 v136, 4, v135\n"
            "v_cmp_lt_u32_sdwa s[70:71], v135, v139 src0_sel:DWORD src1_sel:BYTE_2\n"
            "v_cmp_lt_u32_sdwa s[88:89], v136, v139 src0_sel:DWORD src1_sel:BYTE_2\n"
            "v_and_b32 v139, 0xffff, v139\n"
            "v_lshl_add_u32 v139, v135, 4, v139\n"
            "v_cndmask_b32 v232, v251, v138, vcc\n"
            "v_cndmask_b32 v221, v251, v139, s[70:71]\n"
            "v_add_u32 v138, 64, v138\n"
            "v_add_u32 v139, 64, v139\n"
            "v_cndmask_b32 v233, v251, v138, s[86:87]\n"
            "v_cndmask_b32 v210, v251, v139, s[88:89]\n"
            "Lw_%=:\n"
            "ds_read_b32 v220, v219 offset:56\n"
            "s_waitcnt lgkmcnt(0)\n"
            "v_readfirstlane_b32 s68, v220\n"
            "s_cmp_lt_u32 s68, 4\n"
            "s_cbranch_scc1 Lw_%=\n"
            "ds_read_b32 v232, v232 offset:4\n"
            "ds_read_b128 v[196:199], v221\n"
            "ds_read_b32 v233, v233 offset:4\n"
            "s_waitcnt lgkmcnt(2)\n"
            "v_add_u32 v232, v232, v229\n"
            "buffer_load_dwordx4 v[160:163], v232, s[24:27], 0 offen\n"
            "s_waitcnt lgkmcnt(0)\n"
            "v_add_u32 v233, v233, v229\n"
            "v_add_u32_dpp v220, v196, v214 quad_perm:[0,0,0,0] row_mask:0xf bank_mask:0xf\n"
            "v_add_u32_dpp v221, v196, v214 quad_perm:[1,1,1,1] row_mask:0xf bank_mask:0xf\n"
            "v_add_u32_dpp v222, v196, v214 quad_perm:[2,2,2,2] row_mask:0xf bank_mask:0xf\n"
            "v_add_u32_dpp v223, v196, v214 quad_perm:[3,3,3,3] row_mask:0xf bank_mask:0xf\n"
            "buffer_load_dwordx4 v[32:35], v220, s[28:31], 0 offen\n"
            "buffer_load_dwordx4 v[36:39], v220, s[28:31], 0 offen offset:256\n"
            "v_mfma_f32_4x4x4_16b_bf16 v[204:207], v[202:203], v[198:199], 0\n"
            "buffer_load_dwordx4 v[48:51], v221, s[28:31], 0 offen\n"
            "buffer_load_dwordx4 v[52:55], v221, s[28:31], 0 offen offset:256\n"
            "buffer_load_dwordx4 v[232:235], v233, s[24:27], 0 offen\n"
            "buffer_load_dwordx4 v[64:67], v222, s[28:31], 0 offen\n"
            "buffer_load_dwordx4 v[68:71], v222, s[28:31], 0 offen offset:256\n"
            "v_mfma_f32_4x4x4_16b_bf16 v[204:207], v[200:201], v[198:199], v[204:207]\n"
            "buffer_load_dwordx4 v[80:83], v223, s[28:31], 0 offen\n"
            "buffer_load_dwordx4 v[84:87], v223, s[28:31], 0 offen offset:256\n"
            "buffer_load_dwordx4 v[40:43], v220, s[28:31], 0 offen offset:512\n"
            "buffer_load_dwordx4 v[44:47], v220, s[28:31], 0 offen offset:768\n"
            "buffer_load_dwordx4 v[56:59], v221, s[28:31], 0 offen offset:512\n"
            "buffer_load_dwordx4 v[60:63], v221, s[28:31], 0 offen offset:768\n"
            "buffer_load_dwordx4 v[72:75], v222, s[28:31], 0 offen offset:512\n"
            "buffer_load_dwordx4 v[76:79], v222, s[28:31], 0 offen offset:768\n"
            "buffer_load_dwordx4 v[88:91], v223, s[28:31], 0 offen offset:512\n"
            "buffer_load_dwordx4 v[92:95], v223, s[28:31], 0 offen offset:768\n"
            "s_add_u32 s78, s69, s77\n"
            "s_add_u32 s79, s78, s75\n"
            "s_sub_u32 s83, s79, s80\n"
            "s_add_u32 s83, s83, s76\n"
            "s_add_u32 s80, s79, s76\n"
            "s_sub_u32 s84, s80, s69\n"
            "s_pack_ll_b32_b16 s81, s40, s69\n"
            "s_pack_ll_b32_b16 s82, s65, s78\n"
            "s_pack_ll_b32_b16 s83, s66, s83\n"
            "s_pack_ll_b32_b16 s84, s64, s84\n"
            "v_subrev_u32 v128, s43, v243\n"
            "v_mov_b32 v136, s64\n"
            "v_cmpx_le_i32 vcc, s69, v128\n"
            "v_mov_b32 v136, s81\n"
            "v_cmpx_le_i32 vcc, s78, v128\n"
            "v_mov_b32 v136, s82\n"
            "v_cmpx_le_i32 vcc, s79, v128\n"
            "v_mov_b32 v136, s83\n"
            "v_cmpx_le_i32 vcc, s80, v128\n"
            "v_mov_b32 v136, s84\n"
            "s_mov_b64 exec, -1\n"
            "v_and_b32 v137, 0xffff, v136\n"
            "ds_read_b128 v[140:143], v137\n"
            "v_ashrrev_i32 v136, 16, v136\n"
            "v_sub_u32 v136, v128, v136\n"
            "v_cvt_pk_bf16_f32 v204, v204, v205\n"
            "v_cvt_pk_bf16_f32 v205, v206, v207\n"
            "ds_write_b64 v212, v[204:205]\n"
            "ds_read_b128 v[196:199], v210\n"
            "ds_read_b64 v[208:209], v213\n"
            "s_add_u32 s73, s73, 3\n"
            "s_and_b32 s74, s73, 3\n"
            "s_add_u32 s74, s74, 1\n"
            "s_lshr_b32 s73, s73, 2\n"
            "s_mov_b32 s68, 0x05040100\n"
            "v_add_u32 v217, v217, v214\n"
            "v_add3_u32 v215, v215, v214, s63\n"
            "v_add_u32 v218, v218, v214\n"
            "v_not_b32 v223, s90\n"
            "s_waitcnt lgkmcnt(3)\n"
            "v_cmp_eq_u32 vcc, s64, v137\n"
            "v_mov_b32 v132, 60\n"
            "s_nop 0\n"
            "v_cndmask_b32 v132, 0, v132, vcc\n"
            "v_bfe_u32 v137, v140, 16, 8\n"
            "v_max_u32 v137, 8, v137\n"
            "v_lshrrev_b32 v138, 1, v137\n"
            "v_add_u32 v138, v138, v132\n"
            "v_sub_u32_sdwa v132, v138, v140 dst_sel:DWORD dst_unused:UNUSED_PAD src0_sel:DWORD src1_sel:BYTE_3\n"
            "v_sub_u32_sdwa v133, v137, v140 dst_sel:DWORD dst_unused:UNUSED_PAD src0_sel:DWORD src1_sel:BYTE_2\n"
            "v_med3_i32 v132, v132, 0, v133\n"
            "v_sub_u32 v132, v136, v132\n"
            "v_cmp_lt_u32_sdwa vcc, v132, v140 src0_sel:DWORD src1_sel:BYTE_2\n"
            "v_lshl_add_u32 v140, v132, 4, v140\n"
            "v_sub_u32_sdwa v132, v138, v141 dst_sel:DWORD dst_unused:UNUSED_PAD src0_sel:DWORD src1_sel:BYTE_3\n"
            "v_sub_u32_sdwa v133, v137, v141 dst_sel:DWORD dst_unused:UNUSED_PAD src0_sel:DWORD src1_sel:BYTE_2\n"
            "v_med3_i32 v132, v132, 0, v133\n"
            "v_sub_u32 v132, v136, v132\n"
            "v_cmp_lt_u32_sdwa s[70:71], v132, v141 src0_sel:DWORD src1_sel:BYTE_2\n"
            "v_lshl_add_u32 v141, v132, 4, v141\n"
            "v_sub_u32_sdwa v132, v138, v142 dst_sel:DWORD dst_unused:UNUSED_PAD src0_sel:DWORD src1_sel:BYTE_3\n"
            "v_sub_u32_sdwa v133, v137, v142 dst_sel:DWORD dst_unused:UNUSED_PAD src0_sel:DWORD src1_sel:BYTE_2\n"
            "v_med3_i32 v132, v132, 0, v133\n"
            "v_sub_u32 v132, v136, v132\n"
            "v_cmp_lt_u32_sdwa s[86:87], v132, v142 src0_sel:DWORD src1_sel:BYTE_2\n"
            "v_lshl_add_u32 v142, v132, 4, v142\n"
            "v_sub_u32_sdwa v132, v138, v143 dst_sel:DWORD dst_unused:UNUSED_PAD src0_sel:DWORD src1_sel:BYTE_3\n"
            "v_sub_u32_sdwa v133, v137, v143 dst_sel:DWORD dst_unused:UNUSED_PAD src0_sel:DWORD src1_sel:BYTE_2\n"
            "v_med3_i32 v132, v132, 0, v133\n"
            "v_sub_u32 v132, v136, v132\n"
            "v_cmp_lt_u32_sdwa s[88:89], v132, v143 src0_sel:DWORD src1_sel:BYTE_2\n"
            "v_lshl_add_u32 v143, v132, 4, v143\n"
            "v_cndmask_b32 v140, v251, v140, vcc\n"
            "v_cndmask_b32 v141, v251, v141, s[70:71]\n"
            "v_cndmask_b32 v142, v251, v142, s[86:87]\n"
            "v_cndmask_b32 v143, v251, v143, s[88:89]\n"
            "v_perm_b32 v140, v141, v140, s68\n"
            "v_perm_b32 v141, v143, v142, s68\n"
            "ds_write_b64 v247, v[140:141]\n"
            "ds_read_u16 v211, v238\n"
            "s_cmp_gt_u32 s73, 13\n"
            "s_cbranch_scc1 Llong_%=\n"
            "Llong2_%=:\n"
            "s_add_u32 s69, s69, s43\n"
            "v_add_u32 v216, s63, v218\n"
            "s_lshl_b32 s72, s75, 8\n"
            "s_lshl_b32 s76, s76, 16\n"
            "s_add_u32 s72, s72, s76\n"
            "s_add_u32 s72, s72, s77\n"
            "s_add_u32 s72, s72, 0xfefbfbfc\n"
            "s_add_u32 s75, s75, 0xfefc\n"
            "s_cmp_lt_u32 s3, 4\n"
            "s_cselect_b32 s72, s75, s72\n"
            "s_cmp_lt_u32 s3, 6\n"
            "s_cselect_b32 s75, 1, 0\n"
            "v_lshl_or_b32 v218, s75, 31, v218\n"
            "s_mov_b32 s85, 15\n"
            "s_mov_b32 s42, 0\n"
            "s_waitcnt vmcnt(17)\n"
            "ds_bpermute_b32 v192, v253, v160\n"
            "ds_bpermute_b32 v193, v253, v161\n"
            "ds_bpermute_b32 v194, v253, v162\n"
            "ds_bpermute_b32 v195, v253, v163\n"
            "s_waitcnt lgkmcnt(0)\n"
            "v_mfma_f32_16x16x32_bf16 v[176:179], v[192:195], v[96:99], 0\n"
            "v_mfma_f32_16x16x32_bf16 v[180:183], v[192:195], v[100:103], 0\n"
            "v_mfma_f32_16x16x32_bf16 v[184:187], v[192:195], v[104:107], 0\n"
            "v_mfma_f32_16x16x32_bf16 v[188:191], v[192:195], v[108:111], 0\n"
            "v_mfma_f32_16x16x32_bf16 v[128:131], v[192:195], v[112:115], 0\n"
            "v_mfma_f32_16x16x32_bf16 v[132:135], v[192:195], v[116:119], 0\n"
            "s_nop 2\n"
            "v_exp_f32 v144, v176\n"
            "v_exp_f32 v145, v177\n"
            "v_exp_f32 v146, v178\n"
            "v_exp_f32 v147, v179\n"
            "v_exp_f32 v148, v180\n"
            "v_exp_f32 v149, v181\n"
            "v_exp_f32 v150, v182\n"
            "v_exp_f32 v151, v183\n"
            "v_exp_f32 v152, v184\n"
            "v_exp_f32 v153, v185\n"
            "v_exp_f32 v154, v186\n"
            "v_exp_f32 v155, v187\n"
            "v_exp_f32 v156, v188\n"
            "v_exp_f32 v157, v189\n"
            "v_exp_f32 v158, v190\n"
            "v_exp_f32 v159, v191\n"
            "v_pk_add_f32 v[144:145], v[144:145], 1.0 op_sel_hi:[1,0]\n"
            "v_pk_add_f32 v[146:147], v[146:147], 1.0 op_sel_hi:[1,0]\n"
            "v_pk_add_f32 v[148:149], v[148:149], 1.0 op_sel_hi:[1,0]\n"
            "v_pk_add_f32 v[150:151], v[150:151], 1.0 op_sel_hi:[1,0]\n"
            "v_pk_add_f32 v[152:153], v[152:153], 1.0 op_sel_hi:[1,0]\n"
            "v_pk_add_f32 v[154:155], v[154:155], 1.0 op_sel_hi:[1,0]\n"
            "v_pk_add_f32 v[156:157], v[156:157], 1.0 op_sel_hi:[1,0]\n"
            "v_pk_add_f32 v[158:159], v[158:159], 1.0 op_sel_hi:[1,0]\n"
            "v_rcp_f32 v144, v144\n"
            "v_rcp_f32 v145, v145\n"
            "v_rcp_f32 v146, v146\n"
            "v_rcp_f32 v147, v147\n"
            "v_rcp_f32 v148, v148\n"
            "v_rcp_f32 v149, v149\n"
            "v_rcp_f32 v150, v150\n"
            "v_rcp_f32 v151, v151\n"
            "v_rcp_f32 v152, v152\n"
            "v_rcp_f32 v153, v153\n"
            "v_rcp_f32 v154, v154\n"
            "v_rcp_f32 v155, v155\n"
            "v_rcp_f32 v156, v156\n"
            "v_rcp_f32 v157, v157\n"
            "v_rcp_f32 v158, v158\n"
            "v_rcp_f32 v159, v159\n"
            "v_pk_mul_f32 v[144:145], v[144:145], v[176:177]\n"
            "v_pk_mul_f32 v[146:147], v[146:147], v[178:179]\n"
            "v_pk_mul_f32 v[148:149], v[148:149], v[180:181]\n"
            "v_pk_mul_f32 v[150:151], v[150:151], v[182:183]\n"
            "v_pk_mul_f32 v[152:153], v[152:153], v[184:185]\n"
            "v_pk_mul_f32 v[154:155], v[154:155], v[186:187]\n"
            "v_pk_mul_f32 v[156:157], v[156:157], v[188:189]\n"
            "v_pk_mul_f32 v[158:159], v[158:159], v[190:191]\n"
            "s_branch Ldisp_%=\n"
            "Lfast_%=:\n"
            "v_mfma_f32_16x16x32_bf16 v[136:139], v[192:195], v[120:123], 0\n"
            "v_exp_f32 v160, v128\n"
            "v_add_u32 v237, 32, v237\n"
            "v_exp_f32 v161, v129\n"
            "s_waitcnt vmcnt(12) lgkmcnt(0)\n"
            "v_mfma_f32_4x4x4_16b_bf16 v[176:179], v[208:209], v[32:33], 0 cbsz:2\n"
            "v_exp_f32 v162, v130\n"
            "v_mfma_f32_4x4x4_16b_bf16 v[180:183], v[208:209], v[34:35], 0 cbsz:2\n"
            "v_exp_f32 v163, v131\n"
            "v_mfma_f32_4x4x4_16b_bf16 v[184:187], v[208:209], v[36:37], 0 cbsz:2\n"
            "v_exp_f32 v164, v132\n"
            "v_mfma_f32_4x4x4_16b_bf16 v[188:191], v[208:209], v[38:39], 0 cbsz:2\n"
            "v_exp_f32 v165, v133\n"
            "v_add_u32_dpp v219, v196, v214 quad_perm:[0,0,0,0] row_mask:0xf bank_mask:0xf\n"
            "v_mfma_f32_4x4x4_16b_bf16 v[204:207], v[202:203], v[198:199], 0\n"
            "v_exp_f32 v166, v134\n"
            "buffer_load_dwordx4 v[32:35], v219, s[28:31], 0 offen\n"
            "buffer_load_dwordx4 v[36:39], v219, s[28:31], 0 offen offset:256\n"
            "v_exp_f32 v167, v135\n"
            "v_pk_fma_f32 v[0:1], v[144:145], v[176:177], v[0:1] op_sel_hi:[0,1,1]\n"
            "v_pk_fma_f32 v[2:3], v[144:145], v[178:179], v[2:3] op_sel_hi:[0,1,1]\n"
            "v_pk_fma_f32 v[4:5], v[148:149], v[180:181], v[4:5] op_sel_hi:[0,1,1]\n"
            "v_pk_fma_f32 v[6:7], v[148:149], v[182:183], v[6:7] op_sel_hi:[0,1,1]\n"
            "v_pk_fma_f32 v[8:9], v[152:153], v[184:185], v[8:9] op_sel_hi:[0,1,1]\n"
            "v_pk_fma_f32 v[10:11], v[152:153], v[186:187], v[10:11] op_sel_hi:[0,1,1]\n"
            "v_pk_fma_f32 v[12:13], v[156:157], v[188:189], v[12:13] op_sel_hi:[0,1,1]\n"
            "v_pk_fma_f32 v[14:15], v[156:157], v[190:191], v[14:15] op_sel_hi:[0,1,1]\n"
            "v_pk_add_f32 v[160:161], v[160:161], 1.0 op_sel_hi:[1,0]\n"
            "v_pk_add_f32 v[162:163], v[162:163], 1.0 op_sel_hi:[1,0]\n"
            "v_pk_add_f32 v[164:165], v[164:165], 1.0 op_sel_hi:[1,0]\n"
            "v_pk_add_f32 v[166:167], v[166:167], 1.0 op_sel_hi:[1,0]\n"
            "v_mfma_f32_16x16x32_bf16 v[140:143], v[192:195], v[124:127], 0\n"
            "ds_bpermute_b32 v192, v253, v232\n"
            "ds_bpermute_b32 v193, v253, v233\n"
            "ds_bpermute_b32 v194, v253, v234\n"
            "ds_bpermute_b32 v195, v253, v235\n"
            "ds_read_b32 v230, v211 offset:4\n"
            "ds_read_u16 v210, v237\n"
            "v_rcp_f32 v160, v160\n"
            "v_rcp_f32 v161, v161\n"
            "v_mfma_f32_4x4x4_16b_bf16 v[176:179], v[208:209], v[48:49], 0 cbsz:2 abid:1\n"
            "v_rcp_f32 v162, v162\n"
            "v_mfma_f32_4x4x4_16b_bf16 v[180:183], v[208:209], v[50:51], 0 cbsz:2 abid:1\n"
            "v_rcp_f32 v163, v163\n"
            "v_mfma_f32_4x4x4_16b_bf16 v[184:187], v[208:209], v[52:53], 0 cbsz:2 abid:1\n"
            "v_rcp_f32 v164, v164\n"
            "v_mfma_f32_4x4x4_16b_bf16 v[188:191], v[208:209], v[54:55], 0 cbsz:2 abid:1\n"
            "v_rcp_f32 v165, v165\n"
            "v_add_u32_dpp v220, v196, v214 quad_perm:[1,1,1,1] row_mask:0xf bank_mask:0xf\n"
            "v_mfma_f32_4x4x4_16b_bf16 v[204:207], v[200:201], v[198:199], v[204:207]\n"
            "v_rcp_f32 v166, v166\n"
            "buffer_load_dwordx4 v[48:51], v220, s[28:31], 0 offen\n"
            "buffer_load_dwordx4 v[52:55], v220, s[28:31], 0 offen offset:256\n"
            "v_rcp_f32 v167, v167\n"
            "v_pk_fma_f32 v[0:1], v[144:145], v[176:177], v[0:1] op_sel:[1,0,0]\n"
            "v_pk_fma_f32 v[2:3], v[144:145], v[178:179], v[2:3] op_sel:[1,0,0]\n"
            "v_pk_fma_f32 v[4:5], v[148:149], v[180:181], v[4:5] op_sel:[1,0,0]\n"
            "v_pk_fma_f32 v[6:7], v[148:149], v[182:183], v[6:7] op_sel:[1,0,0]\n"
            "v_pk_fma_f32 v[8:9], v[152:153], v[184:185], v[8:9] op_sel:[1,0,0]\n"
            "v_pk_fma_f32 v[10:11], v[152:153], v[186:187], v[10:11] op_sel:[1,0,0]\n"
            "v_pk_fma_f32 v[12:13], v[156:157], v[188:189], v[12:13] op_sel:[1,0,0]\n"
            "v_pk_fma_f32 v[14:15], v[156:157], v[190:191], v[14:15] op_sel:[1,0,0]\n"
            "v_pk_mul_f32 v[160:161], v[160:161], v[128:129]\n"
            "v_pk_mul_f32 v[162:163], v[162:163], v[130:131]\n"
            "v_pk_mul_f32 v[164:165], v[164:165], v[132:133]\n"
            "v_pk_mul_f32 v[166:167], v[166:167], v[134:135]\n"
            "s_waitcnt vmcnt(12) lgkmcnt(0)\n"
            "v_add_u32 v230, v230, v229\n"
            "buffer_load_dwordx4 v[232:235], v230, s[24:27], 0 offen\n"
            "s_bitcmp0_b32 s85, 0\n"
            "s_cbranch_scc1 Lnpf02_%=\n"
            "s_buffer_load_dwordx2 s[86:87], s[28:31], s67 offset:60\n"
            "s_buffer_load_dwordx2 s[86:87], s[28:31], s67 offset:188\n"
            "s_buffer_load_dwordx2 s[86:87], s[28:31], s67 offset:316\n"
            "s_buffer_load_dwordx2 s[86:87], s[28:31], s67 offset:444\n"
            "s_add_u32 s67, s67, 524288\n"
            "Lnpf02_%=:\n"
            "s_lshr_b32 s85, s85, 1\n"
            "v_mfma_f32_16x16x32_bf16 v[128:131], v[192:195], v[96:99], 0\n"
            "v_exp_f32 v168, v136\n"
            "v_exp_f32 v169, v137\n"
            "v_mfma_f32_4x4x4_16b_bf16 v[176:179], v[208:209], v[64:65], 0 cbsz:2 abid:2\n"
            "v_exp_f32 v170, v138\n"
            "v_mfma_f32_4x4x4_16b_bf16 v[180:183], v[208:209], v[66:67], 0 cbsz:2 abid:2\n"
            "v_exp_f32 v171, v139\n"
            "v_mfma_f32_4x4x4_16b_bf16 v[184:187], v[208:209], v[68:69], 0 cbsz:2 abid:2\n"
            "v_exp_f32 v172, v140\n"
            "v_mfma_f32_4x4x4_16b_bf16 v[188:191], v[208:209], v[70:71], 0 cbsz:2 abid:2\n"
            "v_exp_f32 v173, v141\n"
            "v_add_u32_dpp v221, v196, v214 quad_perm:[2,2,2,2] row_mask:0xf bank_mask:0xf\n"
            "v_exp_f32 v174, v142\n"
            "buffer_load_dwordx4 v[64:67], v221, s[28:31], 0 offen\n"
            "buffer_load_dwordx4 v[68:71], v221, s[28:31], 0 offen offset:256\n"
            "v_exp_f32 v175, v143\n"
            "v_pk_fma_f32 v[0:1], v[146:147], v[176:177], v[0:1] op_sel_hi:[0,1,1]\n"
            "v_pk_fma_f32 v[2:3], v[146:147], v[178:179], v[2:3] op_sel_hi:[0,1,1]\n"
            "v_pk_fma_f32 v[4:5], v[150:151], v[180:181], v[4:5] op_sel_hi:[0,1,1]\n"
            "v_pk_fma_f32 v[6:7], v[150:151], v[182:183], v[6:7] op_sel_hi:[0,1,1]\n"
            "v_pk_fma_f32 v[8:9], v[154:155], v[184:185], v[8:9] op_sel_hi:[0,1,1]\n"
            "v_pk_fma_f32 v[10:11], v[154:155], v[186:187], v[10:11] op_sel_hi:[0,1,1]\n"
            "v_pk_fma_f32 v[12:13], v[158:159], v[188:189], v[12:13] op_sel_hi:[0,1,1]\n"
            "v_pk_fma_f32 v[14:15], v[158:159], v[190:191], v[14:15] op_sel_hi:[0,1,1]\n"
            "v_pk_add_f32 v[168:169], v[168:169], 1.0 op_sel_hi:[1,0]\n"
            "v_pk_add_f32 v[170:171], v[170:171], 1.0 op_sel_hi:[1,0]\n"
            "v_pk_add_f32 v[172:173], v[172:173], 1.0 op_sel_hi:[1,0]\n"
            "v_pk_add_f32 v[174:175], v[174:175], 1.0 op_sel_hi:[1,0]\n"
            "v_cvt_pk_bf16_f32 v204, v204, v205\n"
            "v_cvt_pk_bf16_f32 v205, v206, v207\n"
            "ds_write_b64 v212, v[204:205]\n"
            "v_mfma_f32_16x16x32_bf16 v[132:135], v[192:195], v[100:103], 0\n"
            "v_rcp_f32 v168, v168\n"
            "v_rcp_f32 v169, v169\n"
            "v_mfma_f32_4x4x4_16b_bf16 v[176:179], v[208:209], v[80:81], 0 cbsz:2 abid:3\n"
            "v_rcp_f32 v170, v170\n"
            "v_mfma_f32_4x4x4_16b_bf16 v[180:183], v[208:209], v[82:83], 0 cbsz:2 abid:3\n"
            "v_rcp_f32 v171, v171\n"
            "v_mfma_f32_4x4x4_16b_bf16 v[184:187], v[208:209], v[84:85], 0 cbsz:2 abid:3\n"
            "v_rcp_f32 v172, v172\n"
            "v_mfma_f32_4x4x4_16b_bf16 v[188:191], v[208:209], v[86:87], 0 cbsz:2 abid:3\n"
            "v_rcp_f32 v173, v173\n"
            "v_add_u32_dpp v222, v196, v214 quad_perm:[3,3,3,3] row_mask:0xf bank_mask:0xf\n"
            "v_rcp_f32 v174, v174\n"
            "buffer_load_dwordx4 v[80:83], v222, s[28:31], 0 offen\n"
            "buffer_load_dwordx4 v[84:87], v222, s[28:31], 0 offen offset:256\n"
            "v_rcp_f32 v175, v175\n"
            "v_pk_fma_f32 v[0:1], v[146:147], v[176:177], v[0:1] op_sel:[1,0,0]\n"
            "v_pk_fma_f32 v[2:3], v[146:147], v[178:179], v[2:3] op_sel:[1,0,0]\n"
            "v_pk_fma_f32 v[4:5], v[150:151], v[180:181], v[4:5] op_sel:[1,0,0]\n"
            "v_pk_fma_f32 v[6:7], v[150:151], v[182:183], v[6:7] op_sel:[1,0,0]\n"
            "v_pk_fma_f32 v[8:9], v[154:155], v[184:185], v[8:9] op_sel:[1,0,0]\n"
            "v_pk_fma_f32 v[10:11], v[154:155], v[186:187], v[10:11] op_sel:[1,0,0]\n"
            "v_pk_fma_f32 v[12:13], v[158:159], v[188:189], v[12:13] op_sel:[1,0,0]\n"
            "v_pk_fma_f32 v[14:15], v[158:159], v[190:191], v[14:15] op_sel:[1,0,0]\n"
            "v_pk_mul_f32 v[168:169], v[168:169], v[136:137]\n"
            "v_pk_mul_f32 v[170:171], v[170:171], v[138:139]\n"
            "v_pk_mul_f32 v[172:173], v[172:173], v[140:141]\n"
            "v_pk_mul_f32 v[174:175], v[174:175], v[142:143]\n"
            "v_add_u32 v238, 32, v238\n"
            "ds_read_b128 v[196:199], v210\n"
            "ds_read_u16 v211, v238\n"
            "v_mfma_f32_16x16x32_bf16 v[136:139], v[192:195], v[104:107], 0\n"
            "v_exp_f32 v144, v128\n"
            "v_exp_f32 v145, v129\n"
            "s_waitcnt vmcnt(13)\n"
            "v_mfma_f32_4x4x4_16b_bf16 v[176:179], v[208:209], v[40:41], 0 cbsz:2\n"
            "v_exp_f32 v146, v130\n"
            "v_mfma_f32_4x4x4_16b_bf16 v[180:183], v[208:209], v[42:43], 0 cbsz:2\n"
            "v_exp_f32 v147, v131\n"
            "v_mfma_f32_4x4x4_16b_bf16 v[184:187], v[208:209], v[44:45], 0 cbsz:2\n"
            "v_exp_f32 v148, v132\n"
            "v_mfma_f32_4x4x4_16b_bf16 v[188:191], v[208:209], v[46:47], 0 cbsz:2\n"
            "v_exp_f32 v149, v133\n"
            "v_exp_f32 v150, v134\n"
            "buffer_load_dwordx4 v[40:43], v219, s[28:31], 0 offen offset:512\n"
            "buffer_load_dwordx4 v[44:47], v219, s[28:31], 0 offen offset:768\n"
            "v_exp_f32 v151, v135\n"
            "v_pk_fma_f32 v[16:17], v[160:161], v[176:177], v[16:17] op_sel_hi:[0,1,1]\n"
            "v_pk_fma_f32 v[18:19], v[160:161], v[178:179], v[18:19] op_sel_hi:[0,1,1]\n"
            "v_pk_fma_f32 v[20:21], v[164:165], v[180:181], v[20:21] op_sel_hi:[0,1,1]\n"
            "v_pk_fma_f32 v[22:23], v[164:165], v[182:183], v[22:23] op_sel_hi:[0,1,1]\n"
            "v_pk_fma_f32 v[24:25], v[168:169], v[184:185], v[24:25] op_sel_hi:[0,1,1]\n"
            "v_pk_fma_f32 v[26:27], v[168:169], v[186:187], v[26:27] op_sel_hi:[0,1,1]\n"
            "v_pk_fma_f32 v[28:29], v[172:173], v[188:189], v[28:29] op_sel_hi:[0,1,1]\n"
            "v_pk_fma_f32 v[30:31], v[172:173], v[190:191], v[30:31] op_sel_hi:[0,1,1]\n"
            "v_pk_add_f32 v[144:145], v[144:145], 1.0 op_sel_hi:[1,0]\n"
            "v_pk_add_f32 v[146:147], v[146:147], 1.0 op_sel_hi:[1,0]\n"
            "v_pk_add_f32 v[148:149], v[148:149], 1.0 op_sel_hi:[1,0]\n"
            "v_pk_add_f32 v[150:151], v[150:151], 1.0 op_sel_hi:[1,0]\n"
            "v_mfma_f32_16x16x32_bf16 v[140:143], v[192:195], v[108:111], 0\n"
            "v_rcp_f32 v144, v144\n"
            "v_rcp_f32 v145, v145\n"
            "v_mfma_f32_4x4x4_16b_bf16 v[176:179], v[208:209], v[56:57], 0 cbsz:2 abid:1\n"
            "v_rcp_f32 v146, v146\n"
            "v_mfma_f32_4x4x4_16b_bf16 v[180:183], v[208:209], v[58:59], 0 cbsz:2 abid:1\n"
            "v_rcp_f32 v147, v147\n"
            "v_mfma_f32_4x4x4_16b_bf16 v[184:187], v[208:209], v[60:61], 0 cbsz:2 abid:1\n"
            "v_rcp_f32 v148, v148\n"
            "v_mfma_f32_4x4x4_16b_bf16 v[188:191], v[208:209], v[62:63], 0 cbsz:2 abid:1\n"
            "v_rcp_f32 v149, v149\n"
            "v_rcp_f32 v150, v150\n"
            "buffer_load_dwordx4 v[56:59], v220, s[28:31], 0 offen offset:512\n"
            "buffer_load_dwordx4 v[60:63], v220, s[28:31], 0 offen offset:768\n"
            "v_rcp_f32 v151, v151\n"
            "v_pk_fma_f32 v[16:17], v[160:161], v[176:177], v[16:17] op_sel:[1,0,0]\n"
            "v_pk_fma_f32 v[18:19], v[160:161], v[178:179], v[18:19] op_sel:[1,0,0]\n"
            "v_pk_fma_f32 v[20:21], v[164:165], v[180:181], v[20:21] op_sel:[1,0,0]\n"
            "v_pk_fma_f32 v[22:23], v[164:165], v[182:183], v[22:23] op_sel:[1,0,0]\n"
            "v_pk_fma_f32 v[24:25], v[168:169], v[184:185], v[24:25] op_sel:[1,0,0]\n"
            "v_pk_fma_f32 v[26:27], v[168:169], v[186:187], v[26:27] op_sel:[1,0,0]\n"
            "v_pk_fma_f32 v[28:29], v[172:173], v[188:189], v[28:29] op_sel:[1,0,0]\n"
            "v_pk_fma_f32 v[30:31], v[172:173], v[190:191], v[30:31] op_sel:[1,0,0]\n"
            "v_pk_mul_f32 v[144:145], v[144:145], v[128:129]\n"
            "v_pk_mul_f32 v[146:147], v[146:147], v[130:131]\n"
            "v_pk_mul_f32 v[148:149], v[148:149], v[132:133]\n"
            "v_pk_mul_f32 v[150:151], v[150:151], v[134:135]\n"
            "v_mfma_f32_16x16x32_bf16 v[128:131], v[192:195], v[112:115], 0\n"
            "v_exp_f32 v152, v136\n"
            "v_exp_f32 v153, v137\n"
            "s_waitcnt vmcnt(13)\n"
            "v_mfma_f32_4x4x4_16b_bf16 v[176:179], v[208:209], v[72:73], 0 cbsz:2 abid:2\n"
            "v_exp_f32 v154, v138\n"
            "v_mfma_f32_4x4x4_16b_bf16 v[180:183], v[208:209], v[74:75], 0 cbsz:2 abid:2\n"
            "v_exp_f32 v155, v139\n"
            "v_mfma_f32_4x4x4_16b_bf16 v[184:187], v[208:209], v[76:77], 0 cbsz:2 abid:2\n"
            "v_exp_f32 v156, v140\n"
            "v_mfma_f32_4x4x4_16b_bf16 v[188:191], v[208:209], v[78:79], 0 cbsz:2 abid:2\n"
            "v_exp_f32 v157, v141\n"
            "v_exp_f32 v158, v142\n"
            "buffer_load_dwordx4 v[72:75], v221, s[28:31], 0 offen offset:512\n"
            "buffer_load_dwordx4 v[76:79], v221, s[28:31], 0 offen offset:768\n"
            "v_exp_f32 v159, v143\n"
            "v_pk_fma_f32 v[16:17], v[162:163], v[176:177], v[16:17] op_sel_hi:[0,1,1]\n"
            "v_pk_fma_f32 v[18:19], v[162:163], v[178:179], v[18:19] op_sel_hi:[0,1,1]\n"
            "v_pk_fma_f32 v[20:21], v[166:167], v[180:181], v[20:21] op_sel_hi:[0,1,1]\n"
            "v_pk_fma_f32 v[22:23], v[166:167], v[182:183], v[22:23] op_sel_hi:[0,1,1]\n"
            "v_pk_fma_f32 v[24:25], v[170:171], v[184:185], v[24:25] op_sel_hi:[0,1,1]\n"
            "v_pk_fma_f32 v[26:27], v[170:171], v[186:187], v[26:27] op_sel_hi:[0,1,1]\n"
            "v_pk_fma_f32 v[28:29], v[174:175], v[188:189], v[28:29] op_sel_hi:[0,1,1]\n"
            "v_pk_fma_f32 v[30:31], v[174:175], v[190:191], v[30:31] op_sel_hi:[0,1,1]\n"
            "v_pk_add_f32 v[152:153], v[152:153], 1.0 op_sel_hi:[1,0]\n"
            "v_pk_add_f32 v[154:155], v[154:155], 1.0 op_sel_hi:[1,0]\n"
            "v_pk_add_f32 v[156:157], v[156:157], 1.0 op_sel_hi:[1,0]\n"
            "v_pk_add_f32 v[158:159], v[158:159], 1.0 op_sel_hi:[1,0]\n"
            "v_mfma_f32_16x16x32_bf16 v[132:135], v[192:195], v[116:119], 0\n"
            "v_rcp_f32 v152, v152\n"
            "v_rcp_f32 v153, v153\n"
            "v_mfma_f32_4x4x4_16b_bf16 v[176:179], v[208:209], v[88:89], 0 cbsz:2 abid:3\n"
            "v_rcp_f32 v154, v154\n"
            "v_mfma_f32_4x4x4_16b_bf16 v[180:183], v[208:209], v[90:91], 0 cbsz:2 abid:3\n"
            "v_rcp_f32 v155, v155\n"
            "v_mfma_f32_4x4x4_16b_bf16 v[184:187], v[208:209], v[92:93], 0 cbsz:2 abid:3\n"
            "v_rcp_f32 v156, v156\n"
            "v_mfma_f32_4x4x4_16b_bf16 v[188:191], v[208:209], v[94:95], 0 cbsz:2 abid:3\n"
            "ds_read_b64 v[208:209], v213\n"
            "v_rcp_f32 v157, v157\n"
            "v_rcp_f32 v158, v158\n"
            "buffer_load_dwordx4 v[88:91], v222, s[28:31], 0 offen offset:512\n"
            "buffer_load_dwordx4 v[92:95], v222, s[28:31], 0 offen offset:768\n"
            "v_rcp_f32 v159, v159\n"
            "v_pk_fma_f32 v[16:17], v[162:163], v[176:177], v[16:17] op_sel:[1,0,0]\n"
            "v_pk_fma_f32 v[18:19], v[162:163], v[178:179], v[18:19] op_sel:[1,0,0]\n"
            "v_pk_fma_f32 v[20:21], v[166:167], v[180:181], v[20:21] op_sel:[1,0,0]\n"
            "v_pk_fma_f32 v[22:23], v[166:167], v[182:183], v[22:23] op_sel:[1,0,0]\n"
            "v_pk_fma_f32 v[24:25], v[170:171], v[184:185], v[24:25] op_sel:[1,0,0]\n"
            "v_pk_fma_f32 v[26:27], v[170:171], v[186:187], v[26:27] op_sel:[1,0,0]\n"
            "v_pk_fma_f32 v[28:29], v[174:175], v[188:189], v[28:29] op_sel:[1,0,0]\n"
            "v_pk_fma_f32 v[30:31], v[174:175], v[190:191], v[30:31] op_sel:[1,0,0]\n"
            "v_pk_mul_f32 v[152:153], v[152:153], v[136:137]\n"
            "v_pk_mul_f32 v[154:155], v[154:155], v[138:139]\n"
            "v_pk_mul_f32 v[156:157], v[156:157], v[140:141]\n"
            "v_pk_mul_f32 v[158:159], v[158:159], v[142:143]\n"
            "s_sub_u32 s69, s69, 4\n"
            "Lnext_%=:\n"
            "s_sub_u32 s73, s73, 1\n"
            "Ldisp_%=:\n"
            "s_bitcmp1_b32 s60, s73\n"
            "s_cbranch_scc1 Lprio_%=\n"
            "Ld2_%=:\n"
            "s_cmp_gt_u32 s69, 4\n"
            "s_cbranch_scc1 Lfast_%=\n"
            "v_mfma_f32_16x16x32_bf16 v[136:139], v[192:195], v[120:123], 0\n"
            "v_exp_f32 v160, v128\n"
            "v_add_u32 v237, 32, v237\n"
            "v_exp_f32 v161, v129\n"
            "s_waitcnt vmcnt(12) lgkmcnt(0)\n"
            "v_mfma_f32_4x4x4_16b_bf16 v[176:179], v[208:209], v[32:33], 0 cbsz:2\n"
            "v_exp_f32 v162, v130\n"
            "v_mfma_f32_4x4x4_16b_bf16 v[180:183], v[208:209], v[34:35], 0 cbsz:2\n"
            "v_exp_f32 v163, v131\n"
            "v_mfma_f32_4x4x4_16b_bf16 v[184:187], v[208:209], v[36:37], 0 cbsz:2\n"
            "v_exp_f32 v164, v132\n"
            "v_mfma_f32_4x4x4_16b_bf16 v[188:191], v[208:209], v[38:39], 0 cbsz:2\n"
            "v_exp_f32 v165, v133\n"
            "v_add_u32_dpp v219, v196, v214 quad_perm:[0,0,0,0] row_mask:0xf bank_mask:0xf\n"
            "v_mfma_f32_4x4x4_16b_bf16 v[204:207], v[202:203], v[198:199], 0\n"
            "v_exp_f32 v166, v134\n"
            "buffer_load_dwordx4 v[32:35], v219, s[28:31], 0 offen\n"
            "buffer_load_dwordx4 v[36:39], v219, s[28:31], 0 offen offset:256\n"
            "v_exp_f32 v167, v135\n"
            "v_pk_fma_f32 v[0:1], v[144:145], v[176:177], v[0:1] op_sel_hi:[0,1,1]\n"
            "v_pk_fma_f32 v[2:3], v[144:145], v[178:179], v[2:3] op_sel_hi:[0,1,1]\n"
            "v_pk_fma_f32 v[4:5], v[148:149], v[180:181], v[4:5] op_sel_hi:[0,1,1]\n"
            "v_pk_fma_f32 v[6:7], v[148:149], v[182:183], v[6:7] op_sel_hi:[0,1,1]\n"
            "v_pk_fma_f32 v[8:9], v[152:153], v[184:185], v[8:9] op_sel_hi:[0,1,1]\n"
            "v_pk_fma_f32 v[10:11], v[152:153], v[186:187], v[10:11] op_sel_hi:[0,1,1]\n"
            "v_pk_fma_f32 v[12:13], v[156:157], v[188:189], v[12:13] op_sel_hi:[0,1,1]\n"
            "v_pk_fma_f32 v[14:15], v[156:157], v[190:191], v[14:15] op_sel_hi:[0,1,1]\n"
            "v_pk_add_f32 v[160:161], v[160:161], 1.0 op_sel_hi:[1,0]\n"
            "v_pk_add_f32 v[162:163], v[162:163], 1.0 op_sel_hi:[1,0]\n"
            "v_pk_add_f32 v[164:165], v[164:165], 1.0 op_sel_hi:[1,0]\n"
            "v_pk_add_f32 v[166:167], v[166:167], 1.0 op_sel_hi:[1,0]\n"
            "s_cmp_eq_u32 s69, 1\n"
            "s_cbranch_scc1 Lf00_%=\n"
            "Lr00_%=:\n"
            "v_mfma_f32_16x16x32_bf16 v[140:143], v[192:195], v[124:127], 0\n"
            "ds_bpermute_b32 v192, v253, v232\n"
            "ds_bpermute_b32 v193, v253, v233\n"
            "ds_bpermute_b32 v194, v253, v234\n"
            "ds_bpermute_b32 v195, v253, v235\n"
            "ds_read_b32 v230, v211 offset:4\n"
            "ds_read_u16 v210, v237\n"
            "v_rcp_f32 v160, v160\n"
            "v_rcp_f32 v161, v161\n"
            "v_mfma_f32_4x4x4_16b_bf16 v[176:179], v[208:209], v[48:49], 0 cbsz:2 abid:1\n"
            "v_rcp_f32 v162, v162\n"
            "v_mfma_f32_4x4x4_16b_bf16 v[180:183], v[208:209], v[50:51], 0 cbsz:2 abid:1\n"
            "v_rcp_f32 v163, v163\n"
            "v_mfma_f32_4x4x4_16b_bf16 v[184:187], v[208:209], v[52:53], 0 cbsz:2 abid:1\n"
            "v_rcp_f32 v164, v164\n"
            "v_mfma_f32_4x4x4_16b_bf16 v[188:191], v[208:209], v[54:55], 0 cbsz:2 abid:1\n"
            "v_rcp_f32 v165, v165\n"
            "v_add_u32_dpp v220, v196, v214 quad_perm:[1,1,1,1] row_mask:0xf bank_mask:0xf\n"
            "v_mfma_f32_4x4x4_16b_bf16 v[204:207], v[200:201], v[198:199], v[204:207]\n"
            "v_rcp_f32 v166, v166\n"
            "buffer_load_dwordx4 v[48:51], v220, s[28:31], 0 offen\n"
            "buffer_load_dwordx4 v[52:55], v220, s[28:31], 0 offen offset:256\n"
            "v_rcp_f32 v167, v167\n"
            "v_pk_fma_f32 v[0:1], v[144:145], v[176:177], v[0:1] op_sel:[1,0,0]\n"
            "v_pk_fma_f32 v[2:3], v[144:145], v[178:179], v[2:3] op_sel:[1,0,0]\n"
            "v_pk_fma_f32 v[4:5], v[148:149], v[180:181], v[4:5] op_sel:[1,0,0]\n"
            "v_pk_fma_f32 v[6:7], v[148:149], v[182:183], v[6:7] op_sel:[1,0,0]\n"
            "v_pk_fma_f32 v[8:9], v[152:153], v[184:185], v[8:9] op_sel:[1,0,0]\n"
            "v_pk_fma_f32 v[10:11], v[152:153], v[186:187], v[10:11] op_sel:[1,0,0]\n"
            "v_pk_fma_f32 v[12:13], v[156:157], v[188:189], v[12:13] op_sel:[1,0,0]\n"
            "v_pk_fma_f32 v[14:15], v[156:157], v[190:191], v[14:15] op_sel:[1,0,0]\n"
            "v_pk_mul_f32 v[160:161], v[160:161], v[128:129]\n"
            "v_pk_mul_f32 v[162:163], v[162:163], v[130:131]\n"
            "v_pk_mul_f32 v[164:165], v[164:165], v[132:133]\n"
            "v_pk_mul_f32 v[166:167], v[166:167], v[134:135]\n"
            "s_cmp_eq_u32 s69, 2\n"
            "s_cbranch_scc1 Lf01_%=\n"
            "Lr01_%=:\n"
            "s_waitcnt vmcnt(12) lgkmcnt(0)\n"
            "v_add_u32 v230, v230, v229\n"
            "buffer_load_dwordx4 v[232:235], v230, s[24:27], 0 offen\n"
            "s_bitcmp0_b32 s85, 0\n"
            "s_cbranch_scc1 Lnps02_%=\n"
            "s_buffer_load_dwordx2 s[86:87], s[28:31], s67 offset:60\n"
            "s_buffer_load_dwordx2 s[86:87], s[28:31], s67 offset:188\n"
            "s_buffer_load_dwordx2 s[86:87], s[28:31], s67 offset:316\n"
            "s_buffer_load_dwordx2 s[86:87], s[28:31], s67 offset:444\n"
            "s_add_u32 s67, s67, 524288\n"
            "Lnps02_%=:\n"
            "s_lshr_b32 s85, s85, 1\n"
            "v_mfma_f32_16x16x32_bf16 v[128:131], v[192:195], v[96:99], 0\n"
            "v_exp_f32 v168, v136\n"
            "v_exp_f32 v169, v137\n"
            "v_mfma_f32_4x4x4_16b_bf16 v[176:179], v[208:209], v[64:65], 0 cbsz:2 abid:2\n"
            "v_exp_f32 v170, v138\n"
            "v_mfma_f32_4x4x4_16b_bf16 v[180:183], v[208:209], v[66:67], 0 cbsz:2 abid:2\n"
            "v_exp_f32 v171, v139\n"
            "v_mfma_f32_4x4x4_16b_bf16 v[184:187], v[208:209], v[68:69], 0 cbsz:2 abid:2\n"
            "v_exp_f32 v172, v140\n"
            "v_mfma_f32_4x4x4_16b_bf16 v[188:191], v[208:209], v[70:71], 0 cbsz:2 abid:2\n"
            "v_exp_f32 v173, v141\n"
            "v_add_u32_dpp v221, v196, v214 quad_perm:[2,2,2,2] row_mask:0xf bank_mask:0xf\n"
            "v_exp_f32 v174, v142\n"
            "buffer_load_dwordx4 v[64:67], v221, s[28:31], 0 offen\n"
            "buffer_load_dwordx4 v[68:71], v221, s[28:31], 0 offen offset:256\n"
            "v_exp_f32 v175, v143\n"
            "v_pk_fma_f32 v[0:1], v[146:147], v[176:177], v[0:1] op_sel_hi:[0,1,1]\n"
            "v_pk_fma_f32 v[2:3], v[146:147], v[178:179], v[2:3] op_sel_hi:[0,1,1]\n"
            "v_pk_fma_f32 v[4:5], v[150:151], v[180:181], v[4:5] op_sel_hi:[0,1,1]\n"
            "v_pk_fma_f32 v[6:7], v[150:151], v[182:183], v[6:7] op_sel_hi:[0,1,1]\n"
            "v_pk_fma_f32 v[8:9], v[154:155], v[184:185], v[8:9] op_sel_hi:[0,1,1]\n"
            "v_pk_fma_f32 v[10:11], v[154:155], v[186:187], v[10:11] op_sel_hi:[0,1,1]\n"
            "v_pk_fma_f32 v[12:13], v[158:159], v[188:189], v[12:13] op_sel_hi:[0,1,1]\n"
            "v_pk_fma_f32 v[14:15], v[158:159], v[190:191], v[14:15] op_sel_hi:[0,1,1]\n"
            "v_pk_add_f32 v[168:169], v[168:169], 1.0 op_sel_hi:[1,0]\n"
            "v_pk_add_f32 v[170:171], v[170:171], 1.0 op_sel_hi:[1,0]\n"
            "v_pk_add_f32 v[172:173], v[172:173], 1.0 op_sel_hi:[1,0]\n"
            "v_pk_add_f32 v[174:175], v[174:175], 1.0 op_sel_hi:[1,0]\n"
            "v_cvt_pk_bf16_f32 v204, v204, v205\n"
            "v_cvt_pk_bf16_f32 v205, v206, v207\n"
            "ds_write_b64 v212, v[204:205]\n"
            "s_cmp_eq_u32 s69, 3\n"
            "s_cbranch_scc1 Lf02_%=\n"
            "Lr02_%=:\n"
            "v_mfma_f32_16x16x32_bf16 v[132:135], v[192:195], v[100:103], 0\n"
            "v_rcp_f32 v168, v168\n"
            "v_rcp_f32 v169, v169\n"
            "v_mfma_f32_4x4x4_16b_bf16 v[176:179], v[208:209], v[80:81], 0 cbsz:2 abid:3\n"
            "v_rcp_f32 v170, v170\n"
            "v_mfma_f32_4x4x4_16b_bf16 v[180:183], v[208:209], v[82:83], 0 cbsz:2 abid:3\n"
            "v_rcp_f32 v171, v171\n"
            "v_mfma_f32_4x4x4_16b_bf16 v[184:187], v[208:209], v[84:85], 0 cbsz:2 abid:3\n"
            "v_rcp_f32 v172, v172\n"
            "v_mfma_f32_4x4x4_16b_bf16 v[188:191], v[208:209], v[86:87], 0 cbsz:2 abid:3\n"
            "v_rcp_f32 v173, v173\n"
            "v_add_u32_dpp v222, v196, v214 quad_perm:[3,3,3,3] row_mask:0xf bank_mask:0xf\n"
            "v_rcp_f32 v174, v174\n"
            "buffer_load_dwordx4 v[80:83], v222, s[28:31], 0 offen\n"
            "buffer_load_dwordx4 v[84:87], v222, s[28:31], 0 offen offset:256\n"
            "v_rcp_f32 v175, v175\n"
            "v_pk_fma_f32 v[0:1], v[146:147], v[176:177], v[0:1] op_sel:[1,0,0]\n"
            "v_pk_fma_f32 v[2:3], v[146:147], v[178:179], v[2:3] op_sel:[1,0,0]\n"
            "v_pk_fma_f32 v[4:5], v[150:151], v[180:181], v[4:5] op_sel:[1,0,0]\n"
            "v_pk_fma_f32 v[6:7], v[150:151], v[182:183], v[6:7] op_sel:[1,0,0]\n"
            "v_pk_fma_f32 v[8:9], v[154:155], v[184:185], v[8:9] op_sel:[1,0,0]\n"
            "v_pk_fma_f32 v[10:11], v[154:155], v[186:187], v[10:11] op_sel:[1,0,0]\n"
            "v_pk_fma_f32 v[12:13], v[158:159], v[188:189], v[12:13] op_sel:[1,0,0]\n"
            "v_pk_fma_f32 v[14:15], v[158:159], v[190:191], v[14:15] op_sel:[1,0,0]\n"
            "v_pk_mul_f32 v[168:169], v[168:169], v[136:137]\n"
            "v_pk_mul_f32 v[170:171], v[170:171], v[138:139]\n"
            "v_pk_mul_f32 v[172:173], v[172:173], v[140:141]\n"
            "v_pk_mul_f32 v[174:175], v[174:175], v[142:143]\n"
            "s_cmp_eq_u32 s69, 4\n"
            "s_cbranch_scc1 Lf03_%=\n"
            "Lr03_%=:\n"
            "v_add_u32 v238, 32, v238\n"
            "ds_read_b128 v[196:199], v210\n"
            "ds_read_u16 v211, v238\n"
            "v_mfma_f32_16x16x32_bf16 v[136:139], v[192:195], v[104:107], 0\n"
            "v_exp_f32 v144, v128\n"
            "v_exp_f32 v145, v129\n"
            "s_waitcnt vmcnt(13)\n"
            "v_mfma_f32_4x4x4_16b_bf16 v[176:179], v[208:209], v[40:41], 0 cbsz:2\n"
            "v_exp_f32 v146, v130\n"
            "v_mfma_f32_4x4x4_16b_bf16 v[180:183], v[208:209], v[42:43], 0 cbsz:2\n"
            "v_exp_f32 v147, v131\n"
            "v_mfma_f32_4x4x4_16b_bf16 v[184:187], v[208:209], v[44:45], 0 cbsz:2\n"
            "v_exp_f32 v148, v132\n"
            "v_mfma_f32_4x4x4_16b_bf16 v[188:191], v[208:209], v[46:47], 0 cbsz:2\n"
            "v_exp_f32 v149, v133\n"
            "v_exp_f32 v150, v134\n"
            "buffer_load_dwordx4 v[40:43], v219, s[28:31], 0 offen offset:512\n"
            "buffer_load_dwordx4 v[44:47], v219, s[28:31], 0 offen offset:768\n"
            "v_exp_f32 v151, v135\n"
            "v_pk_fma_f32 v[16:17], v[160:161], v[176:177], v[16:17] op_sel_hi:[0,1,1]\n"
            "v_pk_fma_f32 v[18:19], v[160:161], v[178:179], v[18:19] op_sel_hi:[0,1,1]\n"
            "v_pk_fma_f32 v[20:21], v[164:165], v[180:181], v[20:21] op_sel_hi:[0,1,1]\n"
            "v_pk_fma_f32 v[22:23], v[164:165], v[182:183], v[22:23] op_sel_hi:[0,1,1]\n"
            "v_pk_fma_f32 v[24:25], v[168:169], v[184:185], v[24:25] op_sel_hi:[0,1,1]\n"
            "v_pk_fma_f32 v[26:27], v[168:169], v[186:187], v[26:27] op_sel_hi:[0,1,1]\n"
            "v_pk_fma_f32 v[28:29], v[172:173], v[188:189], v[28:29] op_sel_hi:[0,1,1]\n"
            "v_pk_fma_f32 v[30:31], v[172:173], v[190:191], v[30:31] op_sel_hi:[0,1,1]\n"
            "v_pk_add_f32 v[144:145], v[144:145], 1.0 op_sel_hi:[1,0]\n"
            "v_pk_add_f32 v[146:147], v[146:147], 1.0 op_sel_hi:[1,0]\n"
            "v_pk_add_f32 v[148:149], v[148:149], 1.0 op_sel_hi:[1,0]\n"
            "v_pk_add_f32 v[150:151], v[150:151], 1.0 op_sel_hi:[1,0]\n"
            "s_cmp_eq_u32 s69, 1\n"
            "s_cbranch_scc1 Lf10_%=\n"
            "Lr10_%=:\n"
            "v_mfma_f32_16x16x32_bf16 v[140:143], v[192:195], v[108:111], 0\n"
            "v_rcp_f32 v144, v144\n"
            "v_rcp_f32 v145, v145\n"
            "v_mfma_f32_4x4x4_16b_bf16 v[176:179], v[208:209], v[56:57], 0 cbsz:2 abid:1\n"
            "v_rcp_f32 v146, v146\n"
            "v_mfma_f32_4x4x4_16b_bf16 v[180:183], v[208:209], v[58:59], 0 cbsz:2 abid:1\n"
            "v_rcp_f32 v147, v147\n"
            "v_mfma_f32_4x4x4_16b_bf16 v[184:187], v[208:209], v[60:61], 0 cbsz:2 abid:1\n"
            "v_rcp_f32 v148, v148\n"
            "v_mfma_f32_4x4x4_16b_bf16 v[188:191], v[208:209], v[62:63], 0 cbsz:2 abid:1\n"
            "v_rcp_f32 v149, v149\n"
            "v_rcp_f32 v150, v150\n"
            "buffer_load_dwordx4 v[56:59], v220, s[28:31], 0 offen offset:512\n"
            "buffer_load_dwordx4 v[60:63], v220, s[28:31], 0 offen offset:768\n"
            "v_rcp_f32 v151, v151\n"
            "v_pk_fma_f32 v[16:17], v[160:161], v[176:177], v[16:17] op_sel:[1,0,0]\n"
            "v_pk_fma_f32 v[18:19], v[160:161], v[178:179], v[18:19] op_sel:[1,0,0]\n"
            "v_pk_fma_f32 v[20:21], v[164:165], v[180:181], v[20:21] op_sel:[1,0,0]\n"
            "v_pk_fma_f32 v[22:23], v[164:165], v[182:183], v[22:23] op_sel:[1,0,0]\n"
            "v_pk_fma_f32 v[24:25], v[168:169], v[184:185], v[24:25] op_sel:[1,0,0]\n"
            "v_pk_fma_f32 v[26:27], v[168:169], v[186:187], v[26:27] op_sel:[1,0,0]\n"
            "v_pk_fma_f32 v[28:29], v[172:173], v[188:189], v[28:29] op_sel:[1,0,0]\n"
            "v_pk_fma_f32 v[30:31], v[172:173], v[190:191], v[30:31] op_sel:[1,0,0]\n"
            "v_pk_mul_f32 v[144:145], v[144:145], v[128:129]\n"
            "v_pk_mul_f32 v[146:147], v[146:147], v[130:131]\n"
            "v_pk_mul_f32 v[148:149], v[148:149], v[132:133]\n"
            "v_pk_mul_f32 v[150:151], v[150:151], v[134:135]\n"
            "s_cmp_eq_u32 s69, 2\n"
            "s_cbranch_scc1 Lf11_%=\n"
            "Lr11_%=:\n"
            "v_mfma_f32_16x16x32_bf16 v[128:131], v[192:195], v[112:115], 0\n"
            "v_exp_f32 v152, v136\n"
            "v_exp_f32 v153, v137\n"
            "s_waitcnt vmcnt(13)\n"
            "v_mfma_f32_4x4x4_16b_bf16 v[176:179], v[208:209], v[72:73], 0 cbsz:2 abid:2\n"
            "v_exp_f32 v154, v138\n"
            "v_mfma_f32_4x4x4_16b_bf16 v[180:183], v[208:209], v[74:75], 0 cbsz:2 abid:2\n"
            "v_exp_f32 v155, v139\n"
            "v_mfma_f32_4x4x4_16b_bf16 v[184:187], v[208:209], v[76:77], 0 cbsz:2 abid:2\n"
            "v_exp_f32 v156, v140\n"
            "v_mfma_f32_4x4x4_16b_bf16 v[188:191], v[208:209], v[78:79], 0 cbsz:2 abid:2\n"
            "v_exp_f32 v157, v141\n"
            "v_exp_f32 v158, v142\n"
            "buffer_load_dwordx4 v[72:75], v221, s[28:31], 0 offen offset:512\n"
            "buffer_load_dwordx4 v[76:79], v221, s[28:31], 0 offen offset:768\n"
            "v_exp_f32 v159, v143\n"
            "v_pk_fma_f32 v[16:17], v[162:163], v[176:177], v[16:17] op_sel_hi:[0,1,1]\n"
            "v_pk_fma_f32 v[18:19], v[162:163], v[178:179], v[18:19] op_sel_hi:[0,1,1]\n"
            "v_pk_fma_f32 v[20:21], v[166:167], v[180:181], v[20:21] op_sel_hi:[0,1,1]\n"
            "v_pk_fma_f32 v[22:23], v[166:167], v[182:183], v[22:23] op_sel_hi:[0,1,1]\n"
            "v_pk_fma_f32 v[24:25], v[170:171], v[184:185], v[24:25] op_sel_hi:[0,1,1]\n"
            "v_pk_fma_f32 v[26:27], v[170:171], v[186:187], v[26:27] op_sel_hi:[0,1,1]\n"
            "v_pk_fma_f32 v[28:29], v[174:175], v[188:189], v[28:29] op_sel_hi:[0,1,1]\n"
            "v_pk_fma_f32 v[30:31], v[174:175], v[190:191], v[30:31] op_sel_hi:[0,1,1]\n"
            "v_pk_add_f32 v[152:153], v[152:153], 1.0 op_sel_hi:[1,0]\n"
            "v_pk_add_f32 v[154:155], v[154:155], 1.0 op_sel_hi:[1,0]\n"
            "v_pk_add_f32 v[156:157], v[156:157], 1.0 op_sel_hi:[1,0]\n"
            "v_pk_add_f32 v[158:159], v[158:159], 1.0 op_sel_hi:[1,0]\n"
            "s_cmp_eq_u32 s69, 3\n"
            "s_cbranch_scc1 Lf12_%=\n"
            "Lr12_%=:\n"
            "v_mfma_f32_16x16x32_bf16 v[132:135], v[192:195], v[116:119], 0\n"
            "v_rcp_f32 v152, v152\n"
            "v_rcp_f32 v153, v153\n"
            "v_mfma_f32_4x4x4_16b_bf16 v[176:179], v[208:209], v[88:89], 0 cbsz:2 abid:3\n"
            "v_rcp_f32 v154, v154\n"
            "v_mfma_f32_4x4x4_16b_bf16 v[180:183], v[208:209], v[90:91], 0 cbsz:2 abid:3\n"
            "v_rcp_f32 v155, v155\n"
            "v_mfma_f32_4x4x4_16b_bf16 v[184:187], v[208:209], v[92:93], 0 cbsz:2 abid:3\n"
            "v_rcp_f32 v156, v156\n"
            "v_mfma_f32_4x4x4_16b_bf16 v[188:191], v[208:209], v[94:95], 0 cbsz:2 abid:3\n"
            "ds_read_b64 v[208:209], v213\n"
            "v_rcp_f32 v157, v157\n"
            "v_rcp_f32 v158, v158\n"
            "buffer_load_dwordx4 v[88:91], v222, s[28:31], 0 offen offset:512\n"
            "buffer_load_dwordx4 v[92:95], v222, s[28:31], 0 offen offset:768\n"
            "v_rcp_f32 v159, v159\n"
            "v_pk_fma_f32 v[16:17], v[162:163], v[176:177], v[16:17] op_sel:[1,0,0]\n"
            "v_pk_fma_f32 v[18:19], v[162:163], v[178:179], v[18:19] op_sel:[1,0,0]\n"
            "v_pk_fma_f32 v[20:21], v[166:167], v[180:181], v[20:21] op_sel:[1,0,0]\n"
            "v_pk_fma_f32 v[22:23], v[166:167], v[182:183], v[22:23] op_sel:[1,0,0]\n"
            "v_pk_fma_f32 v[24:25], v[170:171], v[184:185], v[24:25] op_sel:[1,0,0]\n"
            "v_pk_fma_f32 v[26:27], v[170:171], v[186:187], v[26:27] op_sel:[1,0,0]\n"
            "v_pk_fma_f32 v[28:29], v[174:175], v[188:189], v[28:29] op_sel:[1,0,0]\n"
            "v_pk_fma_f32 v[30:31], v[174:175], v[190:191], v[30:31] op_sel:[1,0,0]\n"
            "v_pk_mul_f32 v[152:153], v[152:153], v[136:137]\n"
            "v_pk_mul_f32 v[154:155], v[154:155], v[138:139]\n"
            "v_pk_mul_f32 v[156:157], v[156:157], v[140:141]\n"
            "v_pk_mul_f32 v[158:159], v[158:159], v[142:143]\n"
            "s_cmp_eq_u32 s69, 4\n"
            "s_cbranch_scc1 Lf13_%=\n"
            "Lr13_%=:\n"
            "s_and_b32 s68, s72, 0xff\n"
            "s_add_u32 s69, s69, s68\n"
            "s_lshr_b32 s72, s72, 8\n"
            "v_mov_b32 v231, v239\n"
            "v_mov_b32 v239, v252\n"
            "s_and_b32 s68, s62, 14\n"
            "s_lshr_b32 s62, s62, 4\n"
            "s_cmp_lg_u32 s68, 0\n"
            "s_cbranch_scc1 Lb1_%=\n"
            "v_mov_b32 v215, v216\n"
            "s_branch Lnext_%=\n"
            "Lb1_%=:\n"
            "s_cmp_lg_u32 s68, 6\n"
            "s_cbranch_scc1 Lb2_%=\n"
            "s_lshl_b32 s68, s3, 2\n"
            "v_mov_b32 v176, s68\n"
            "ds_write_b32 v176, v176\n"
            "s_branch Lnext_%=\n"
            "Lb2_%=:\n"
            "s_cmp_lg_u32 s68, 8\n"
            "s_cbranch_scc1 Lnext_%=\n"
            "s_waitcnt vmcnt(0)\n"
            "v_mov_b32 v176, 4\n"
            "v_mov_b32 v177, 1\n"
            "s_mov_b64 exec, 1\n"
            "ds_add_rtn_u32 v177, v176, v177\n"
            "s_waitcnt lgkmcnt(0)\n"
            "v_readfirstlane_b32 s68, v177\n"
            "s_mov_b64 exec, 0xff\n"
            "v_mov_b32 v176, s90\n"
            "v_mov_b32 v177, s90\n"
            "v_mov_b32 v178, s90\n"
            "v_mov_b32 v179, s90\n"
            "s_cmp_lg_u32 s68, 7\n"
            "s_cbranch_scc1 Lpub2_%=\n"
            "global_store_dwordx4 v214, v[176:179], s[56:57] sc1\n"
            "Lpub2_%=:\n"
            "s_mov_b64 exec, -1\n"
            "s_branch Lnext_%=\n"
            "Lprio_%=:\n"
            "s_cmp_eq_u32 s73, 1\n"
            "s_cbranch_scc1 Llast_%=\n"
            "s_lshl_b32 s68, s73, 1\n"
            "s_lshr_b32 s68, s61, s68\n"
            "s_bitcmp1_b32 s68, 1\n"
            "s_cbranch_scc1 Lq2_%=\n"
            "s_bitcmp1_b32 s68, 0\n"
            "s_setprio 0\n"
            "s_cbranch_scc0 Lq9_%=\n"
            "s_setprio 1\n"
            "s_branch Lq9_%=\n"
            "Lq2_%=:\n"
            "s_bitcmp1_b32 s68, 0\n"
            "s_setprio 2\n"
            "s_cbranch_scc0 Lq9_%=\n"
            "s_setprio 3\n"
            "Lq9_%=:\n"
            "s_cmp_lg_u32 s73, 2\n"
            "s_cbranch_scc1 Ld2_%=\n"
            "v_mov_b32 v223, 0\n"
            "global_load_dword v223, v223, s[58:59] sc1\n"
            "s_branch Ld2_%=\n"
            "Lf00_%=:\n"
            "s_call_b64 s[70:71], Lflush0_%=\n"
            "s_branch Lr00_%=\n"
            "Lf01_%=:\n"
            "s_call_b64 s[70:71], Lflush0_%=\n"
            "s_branch Lr01_%=\n"
            "Lf02_%=:\n"
            "s_call_b64 s[70:71], Lflush0_%=\n"
            "s_branch Lr02_%=\n"
            "Lf03_%=:\n"
            "s_call_b64 s[70:71], Lflush0_%=\n"
            "s_branch Lr03_%=\n"
            "Lflush0_%=:\n"
            "s_bitcmp0_b32 s62, 0\n"
            "s_cbranch_scc1 Lfa0_%=\n"
            "Lfw0_%=:\n"
            "v_mov_b32 v184, s41\n"
            "ds_read_b32 v184, v184\n"
            "ds_read_b128 v[176:179], v231 offset:0\n"
            "ds_read_b128 v[180:183], v231 offset:1024\n"
            "s_waitcnt lgkmcnt(0)\n"
            "v_readfirstlane_b32 s68, v184\n"
            "s_cmp_eq_u32 s68, 0\n"
            "s_cbranch_scc1 Lfw0_%=\n"
            "v_mfma_f32_4x4x4_16b_bf16 v[0:3], v[224:225], v[176:177], v[0:3]\n"
            "v_mfma_f32_4x4x4_16b_bf16 v[4:7], v[224:225], v[178:179], v[4:7]\n"
            "v_mfma_f32_4x4x4_16b_bf16 v[8:11], v[224:225], v[180:181], v[8:11]\n"
            "v_mfma_f32_4x4x4_16b_bf16 v[12:15], v[224:225], v[182:183], v[12:15]\n"
            "s_nop 1\n"
            "Lfa0_%=:\n"
            "s_cmp_lg_u32 s72, 0xff\n"
            "s_cbranch_scc1 Lfb0_%=\n"
            "ds_read_b128 v[184:187], v236 offset:0\n"
            "ds_read_b128 v[188:191], v236 offset:1024\n"
            "Lfb0_%=:\n"
            "v_cvt_pk_bf16_f32 v176, v0, v1\n"
            "v_cvt_pk_bf16_f32 v177, v2, v3\n"
            "v_cvt_pk_bf16_f32 v178, v4, v5\n"
            "v_cvt_pk_bf16_f32 v179, v6, v7\n"
            "v_cvt_pk_bf16_f32 v180, v8, v9\n"
            "v_cvt_pk_bf16_f32 v181, v10, v11\n"
            "v_cvt_pk_bf16_f32 v182, v12, v13\n"
            "v_cvt_pk_bf16_f32 v183, v14, v15\n"
            "s_bitcmp1_b32 s62, 1\n"
            "s_cbranch_scc1 Lpark0_%=\n"
            "buffer_store_dwordx4 v[176:179], v215, s[20:23], 0 offen offset:0 sc1\n"
            "buffer_store_dwordx4 v[180:183], v215, s[20:23], 0 offen offset:256 sc1\n"
            "s_branch Lzero0_%=\n"
            "Lpark0_%=:\n"
            "ds_write_b128 v231, v[176:179] offset:0\n"
            "ds_write_b128 v231, v[180:183] offset:1024\n"
            "Lzero0_%=:\n"
            "s_cmp_lg_u32 s72, 0xff\n"
            "s_cbranch_scc1 Lzz0_%=\n"
            "s_waitcnt lgkmcnt(0)\n"
            "v_mfma_f32_4x4x4_16b_bf16 v[0:3], v[224:225], v[184:185], 0\n"
            "v_mfma_f32_4x4x4_16b_bf16 v[4:7], v[224:225], v[186:187], 0\n"
            "v_mfma_f32_4x4x4_16b_bf16 v[8:11], v[224:225], v[188:189], 0\n"
            "v_mfma_f32_4x4x4_16b_bf16 v[12:15], v[224:225], v[190:191], 0\n"
            "s_setpc_b64 s[70:71]\n"
            "Lzz0_%=:\n"
            "v_mov_b64 v[0:1], 0\n"
            "v_mov_b64 v[2:3], 0\n"
            "v_mov_b64 v[4:5], 0\n"
            "v_mov_b64 v[6:7], 0\n"
            "v_mov_b64 v[8:9], 0\n"
            "v_mov_b64 v[10:11], 0\n"
            "v_mov_b64 v[12:13], 0\n"
            "v_mov_b64 v[14:15], 0\n"
            "s_setpc_b64 s[70:71]\n"
            "Lf10_%=:\n"
            "s_call_b64 s[70:71], Lflush1_%=\n"
            "s_branch Lr10_%=\n"
            "Lf11_%=:\n"
            "s_call_b64 s[70:71], Lflush1_%=\n"
            "s_branch Lr11_%=\n"
            "Lf12_%=:\n"
            "s_call_b64 s[70:71], Lflush1_%=\n"
            "s_branch Lr12_%=\n"
            "Lf13_%=:\n"
            "s_call_b64 s[70:71], Lflush1_%=\n"
            "s_branch Lr13_%=\n"
            "Lflush1_%=:\n"
            "s_bitcmp0_b32 s62, 0\n"
            "s_cbranch_scc1 Lfa1_%=\n"
            "Lfw1_%=:\n"
            "v_mov_b32 v184, s41\n"
            "ds_read_b32 v184, v184\n"
            "ds_read_b128 v[176:179], v231 offset:2048\n"
            "ds_read_b128 v[180:183], v231 offset:3072\n"
            "s_waitcnt lgkmcnt(0)\n"
            "v_readfirstlane_b32 s68, v184\n"
            "s_cmp_eq_u32 s68, 0\n"
            "s_cbranch_scc1 Lfw1_%=\n"
            "v_mfma_f32_4x4x4_16b_bf16 v[16:19], v[224:225], v[176:177], v[16:19]\n"
            "v_mfma_f32_4x4x4_16b_bf16 v[20:23], v[224:225], v[178:179], v[20:23]\n"
            "v_mfma_f32_4x4x4_16b_bf16 v[24:27], v[224:225], v[180:181], v[24:27]\n"
            "v_mfma_f32_4x4x4_16b_bf16 v[28:31], v[224:225], v[182:183], v[28:31]\n"
            "s_nop 1\n"
            "Lfa1_%=:\n"
            "s_cmp_lg_u32 s72, 0xff\n"
            "s_cbranch_scc1 Lfb1_%=\n"
            "ds_read_b128 v[184:187], v236 offset:2048\n"
            "ds_read_b128 v[188:191], v236 offset:3072\n"
            "Lfb1_%=:\n"
            "v_cvt_pk_bf16_f32 v176, v16, v17\n"
            "v_cvt_pk_bf16_f32 v177, v18, v19\n"
            "v_cvt_pk_bf16_f32 v178, v20, v21\n"
            "v_cvt_pk_bf16_f32 v179, v22, v23\n"
            "v_cvt_pk_bf16_f32 v180, v24, v25\n"
            "v_cvt_pk_bf16_f32 v181, v26, v27\n"
            "v_cvt_pk_bf16_f32 v182, v28, v29\n"
            "v_cvt_pk_bf16_f32 v183, v30, v31\n"
            "s_bitcmp1_b32 s62, 1\n"
            "s_cbranch_scc1 Lpark1_%=\n"
            "buffer_store_dwordx4 v[176:179], v215, s[20:23], 0 offen offset:512 sc1\n"
            "buffer_store_dwordx4 v[180:183], v215, s[20:23], 0 offen offset:768 sc1\n"
            "s_branch Lzero1_%=\n"
            "Lpark1_%=:\n"
            "ds_write_b128 v231, v[176:179] offset:2048\n"
            "ds_write_b128 v231, v[180:183] offset:3072\n"
            "Lzero1_%=:\n"
            "s_cmp_lg_u32 s72, 0xff\n"
            "s_cbranch_scc1 Lzz1_%=\n"
            "s_waitcnt lgkmcnt(0)\n"
            "v_mfma_f32_4x4x4_16b_bf16 v[16:19], v[224:225], v[184:185], 0\n"
            "v_mfma_f32_4x4x4_16b_bf16 v[20:23], v[224:225], v[186:187], 0\n"
            "v_mfma_f32_4x4x4_16b_bf16 v[24:27], v[224:225], v[188:189], 0\n"
            "v_mfma_f32_4x4x4_16b_bf16 v[28:31], v[224:225], v[190:191], 0\n"
            "s_setpc_b64 s[70:71]\n"
            "Lzz1_%=:\n"
            "v_mov_b64 v[16:17], 0\n"
            "v_mov_b64 v[18:19], 0\n"
            "v_mov_b64 v[20:21], 0\n"
            "v_mov_b64 v[22:23], 0\n"
            "v_mov_b64 v[24:25], 0\n"
            "v_mov_b64 v[26:27], 0\n"
            "v_mov_b64 v[28:29], 0\n"
            "v_mov_b64 v[30:31], 0\n"
            "s_setpc_b64 s[70:71]\n"
            "Llk_%=:\n"
            "s_waitcnt vmcnt(4)\n"
            "v_readfirstlane_b32 s73, v223\n"
            "s_cmp_lg_u32 s73, s90\n"
            "s_cbranch_scc1 Llk2_%=\n"
            "buffer_load_dwordx4 v[32:35], v217, s[20:23], s63 offen offset:0 sc1\n"
            "buffer_load_dwordx4 v[36:39], v217, s[20:23], s63 offen offset:256 sc1\n"
            "buffer_load_dwordx4 v[48:51], v217, s[20:23], s63 offen offset:512 sc1\n"
            "buffer_load_dwordx4 v[52:55], v217, s[20:23], s63 offen offset:768 sc1\n"
            "s_branch Llkr_%=\n"
            "Llk2_%=:\n"
            "v_mov_b32 v223, 0\n"
            "global_load_dword v223, v223, s[58:59] sc1\n"
            "s_branch Llkr_%=\n"
            "Lspin_%=:\n"
            "s_waitcnt vmcnt(0)\n"
            "s_mov_b32 s72, 0x400000\n"
            "Lsp1_%=:\n"
            "v_readfirstlane_b32 s73, v223\n"
            "s_sub_u32 s72, s72, 1\n"
            "s_cmp_eq_u32 s72, 0\n"
            "s_cbranch_scc1 Lsp2_%=\n"
            "s_cmp_eq_u32 s73, s90\n"
            "s_cbranch_scc1 Lsp2_%=\n"
            "s_sleep 1\n"
            "v_mov_b32 v223, 0\n"
            "global_load_dword v223, v223, s[58:59] sc1\n"
            "s_waitcnt vmcnt(0)\n"
            "s_branch Lsp1_%=\n"
            "Lsp2_%=:\n"
            "buffer_load_dwordx4 v[32:35], v217, s[20:23], s63 offen offset:0 sc1\n"
            "buffer_load_dwordx4 v[36:39], v217, s[20:23], s63 offen offset:256 sc1\n"
            "buffer_load_dwordx4 v[48:51], v217, s[20:23], s63 offen offset:512 sc1\n"
            "buffer_load_dwordx4 v[52:55], v217, s[20:23], s63 offen offset:768 sc1\n"
            "s_branch Lgo_%=\n"
            "Llast_%=:\n"
            "s_waitcnt vmcnt(16)\n"
            "v_readfirstlane_b32 s73, v223\n"
            "v_mov_b32 v222, v217\n"
            "s_cmp_eq_u32 s73, s90\n"
            "s_cbranch_scc1 Lp4_%=\n"
            "v_bfrev_b32 v222, 1\n"
            "v_mov_b32 v223, 0\n"
            "global_load_dword v223, v223, s[58:59] sc1\n"
            "Lp4_%=:\n"
            "v_mfma_f32_16x16x32_bf16 v[136:139], v[192:195], v[120:123], 0\n"
            "v_exp_f32 v160, v128\n"
            "v_exp_f32 v161, v129\n"
            "s_waitcnt vmcnt(13) lgkmcnt(0)\n"
            "v_mfma_f32_4x4x4_16b_bf16 v[176:179], v[208:209], v[32:33], 0 cbsz:2\n"
            "v_exp_f32 v162, v130\n"
            "v_mfma_f32_4x4x4_16b_bf16 v[180:183], v[208:209], v[34:35], 0 cbsz:2\n"
            "v_exp_f32 v163, v131\n"
            "v_mfma_f32_4x4x4_16b_bf16 v[184:187], v[208:209], v[36:37], 0 cbsz:2\n"
            "v_exp_f32 v164, v132\n"
            "v_mfma_f32_4x4x4_16b_bf16 v[188:191], v[208:209], v[38:39], 0 cbsz:2\n"
            "v_exp_f32 v165, v133\n"
            "v_exp_f32 v166, v134\n"
            "buffer_load_dwordx4 v[32:35], v222, s[20:23], s63 offen offset:0 sc1\n"
            "buffer_load_dwordx4 v[36:39], v222, s[20:23], s63 offen offset:256 sc1\n"
            "ds_read_b128 v[236:239], v228 offset:0\n"
            "ds_read_b128 v[240:243], v228 offset:1024\n"
            "ds_read_b128 v[244:247], v228 offset:2048\n"
            "ds_read_b128 v[248:251], v228 offset:3072\n"
            "ds_read_b128 v[252:255], v228 offset:4096\n"
            "ds_read_b128 v[96:99], v228 offset:5120\n"
            "v_exp_f32 v167, v135\n"
            "v_pk_fma_f32 v[0:1], v[144:145], v[176:177], v[0:1] op_sel_hi:[0,1,1]\n"
            "v_pk_fma_f32 v[2:3], v[144:145], v[178:179], v[2:3] op_sel_hi:[0,1,1]\n"
            "v_pk_fma_f32 v[4:5], v[148:149], v[180:181], v[4:5] op_sel_hi:[0,1,1]\n"
            "v_pk_fma_f32 v[6:7], v[148:149], v[182:183], v[6:7] op_sel_hi:[0,1,1]\n"
            "v_pk_fma_f32 v[8:9], v[152:153], v[184:185], v[8:9] op_sel_hi:[0,1,1]\n"
            "v_pk_fma_f32 v[10:11], v[152:153], v[186:187], v[10:11] op_sel_hi:[0,1,1]\n"
            "v_pk_fma_f32 v[12:13], v[156:157], v[188:189], v[12:13] op_sel_hi:[0,1,1]\n"
            "v_pk_fma_f32 v[14:15], v[156:157], v[190:191], v[14:15] op_sel_hi:[0,1,1]\n"
            "v_pk_add_f32 v[160:161], v[160:161], 1.0 op_sel_hi:[1,0]\n"
            "v_pk_add_f32 v[162:163], v[162:163], 1.0 op_sel_hi:[1,0]\n"
            "v_pk_add_f32 v[164:165], v[164:165], 1.0 op_sel_hi:[1,0]\n"
            "v_pk_add_f32 v[166:167], v[166:167], 1.0 op_sel_hi:[1,0]\n"
            "v_mfma_f32_16x16x32_bf16 v[140:143], v[192:195], v[124:127], 0\n"
            "v_rcp_f32 v160, v160\n"
            "v_rcp_f32 v161, v161\n"
            "v_mfma_f32_4x4x4_16b_bf16 v[176:179], v[208:209], v[48:49], 0 cbsz:2 abid:1\n"
            "v_rcp_f32 v162, v162\n"
            "v_mfma_f32_4x4x4_16b_bf16 v[180:183], v[208:209], v[50:51], 0 cbsz:2 abid:1\n"
            "v_rcp_f32 v163, v163\n"
            "v_mfma_f32_4x4x4_16b_bf16 v[184:187], v[208:209], v[52:53], 0 cbsz:2 abid:1\n"
            "v_rcp_f32 v164, v164\n"
            "v_mfma_f32_4x4x4_16b_bf16 v[188:191], v[208:209], v[54:55], 0 cbsz:2 abid:1\n"
            "v_rcp_f32 v165, v165\n"
            "v_rcp_f32 v166, v166\n"
            "buffer_load_dwordx4 v[48:51], v222, s[20:23], s63 offen offset:512 sc1\n"
            "buffer_load_dwordx4 v[52:55], v222, s[20:23], s63 offen offset:768 sc1\n"
            "ds_read_b128 v[100:103], v228 offset:6144\n"
            "ds_read_b128 v[104:107], v228 offset:7168\n"
            "ds_read_b128 v[108:111], v228 offset:8192\n"
            "ds_read_b128 v[112:115], v228 offset:9216\n"
            "ds_read_b128 v[116:119], v228 offset:10240\n"
            "ds_read_b128 v[196:199], v228 offset:11264\n"
            "v_rcp_f32 v167, v167\n"
            "v_pk_fma_f32 v[0:1], v[144:145], v[176:177], v[0:1] op_sel:[1,0,0]\n"
            "v_pk_fma_f32 v[2:3], v[144:145], v[178:179], v[2:3] op_sel:[1,0,0]\n"
            "v_pk_fma_f32 v[4:5], v[148:149], v[180:181], v[4:5] op_sel:[1,0,0]\n"
            "v_pk_fma_f32 v[6:7], v[148:149], v[182:183], v[6:7] op_sel:[1,0,0]\n"
            "v_pk_fma_f32 v[8:9], v[152:153], v[184:185], v[8:9] op_sel:[1,0,0]\n"
            "v_pk_fma_f32 v[10:11], v[152:153], v[186:187], v[10:11] op_sel:[1,0,0]\n"
            "v_pk_fma_f32 v[12:13], v[156:157], v[188:189], v[12:13] op_sel:[1,0,0]\n"
            "v_pk_fma_f32 v[14:15], v[156:157], v[190:191], v[14:15] op_sel:[1,0,0]\n"
            "v_pk_mul_f32 v[160:161], v[160:161], v[128:129]\n"
            "v_pk_mul_f32 v[162:163], v[162:163], v[130:131]\n"
            "v_pk_mul_f32 v[164:165], v[164:165], v[132:133]\n"
            "v_pk_mul_f32 v[166:167], v[166:167], v[134:135]\n"
            "v_exp_f32 v168, v136\n"
            "v_exp_f32 v169, v137\n"
            "s_waitcnt vmcnt(12)\n"
            "v_mfma_f32_4x4x4_16b_bf16 v[176:179], v[208:209], v[64:65], 0 cbsz:2 abid:2\n"
            "v_exp_f32 v170, v138\n"
            "v_mfma_f32_4x4x4_16b_bf16 v[180:183], v[208:209], v[66:67], 0 cbsz:2 abid:2\n"
            "v_exp_f32 v171, v139\n"
            "v_mfma_f32_4x4x4_16b_bf16 v[184:187], v[208:209], v[68:69], 0 cbsz:2 abid:2\n"
            "v_exp_f32 v172, v140\n"
            "v_mfma_f32_4x4x4_16b_bf16 v[188:191], v[208:209], v[70:71], 0 cbsz:2 abid:2\n"
            "v_exp_f32 v173, v141\n"
            "v_exp_f32 v174, v142\n"
            "ds_read_b128 v[200:203], v228 offset:12288\n"
            "ds_read_b128 v[204:207], v228 offset:13312\n"
            "ds_read_b128 v[232:235], v228 offset:14336\n"
            "ds_read_b128 v[120:123], v228 offset:15360\n"
            "ds_read_b128 v[64:67], v228 offset:16384\n"
            "ds_read_b128 v[68:71], v228 offset:17408\n"
            "v_exp_f32 v175, v143\n"
            "v_pk_fma_f32 v[0:1], v[146:147], v[176:177], v[0:1] op_sel_hi:[0,1,1]\n"
            "v_pk_fma_f32 v[2:3], v[146:147], v[178:179], v[2:3] op_sel_hi:[0,1,1]\n"
            "v_pk_fma_f32 v[4:5], v[150:151], v[180:181], v[4:5] op_sel_hi:[0,1,1]\n"
            "v_pk_fma_f32 v[6:7], v[150:151], v[182:183], v[6:7] op_sel_hi:[0,1,1]\n"
            "v_pk_fma_f32 v[8:9], v[154:155], v[184:185], v[8:9] op_sel_hi:[0,1,1]\n"
            "v_pk_fma_f32 v[10:11], v[154:155], v[186:187], v[10:11] op_sel_hi:[0,1,1]\n"
            "v_pk_fma_f32 v[12:13], v[158:159], v[188:189], v[12:13] op_sel_hi:[0,1,1]\n"
            "v_pk_fma_f32 v[14:15], v[158:159], v[190:191], v[14:15] op_sel_hi:[0,1,1]\n"
            "v_pk_add_f32 v[168:169], v[168:169], 1.0 op_sel_hi:[1,0]\n"
            "v_pk_add_f32 v[170:171], v[170:171], 1.0 op_sel_hi:[1,0]\n"
            "v_pk_add_f32 v[172:173], v[172:173], 1.0 op_sel_hi:[1,0]\n"
            "v_pk_add_f32 v[174:175], v[174:175], 1.0 op_sel_hi:[1,0]\n"
            "v_rcp_f32 v168, v168\n"
            "v_rcp_f32 v169, v169\n"
            "v_mfma_f32_4x4x4_16b_bf16 v[176:179], v[208:209], v[80:81], 0 cbsz:2 abid:3\n"
            "v_rcp_f32 v170, v170\n"
            "v_mfma_f32_4x4x4_16b_bf16 v[180:183], v[208:209], v[82:83], 0 cbsz:2 abid:3\n"
            "v_rcp_f32 v171, v171\n"
            "v_mfma_f32_4x4x4_16b_bf16 v[184:187], v[208:209], v[84:85], 0 cbsz:2 abid:3\n"
            "v_rcp_f32 v172, v172\n"
            "v_mfma_f32_4x4x4_16b_bf16 v[188:191], v[208:209], v[86:87], 0 cbsz:2 abid:3\n"
            "v_rcp_f32 v173, v173\n"
            "v_rcp_f32 v174, v174\n"
            "ds_read_b128 v[124:127], v228 offset:18432\n"
            "ds_read_b128 v[192:195], v228 offset:19456\n"
            "ds_read_b128 v[128:131], v228 offset:20480\n"
            "ds_read_b128 v[132:135], v228 offset:21504\n"
            "ds_read_b128 v[80:83], v228 offset:22528\n"
            "ds_read_b128 v[84:87], v228 offset:23552\n"
            "v_rcp_f32 v175, v175\n"
            "v_pk_fma_f32 v[0:1], v[146:147], v[176:177], v[0:1] op_sel:[1,0,0]\n"
            "v_pk_fma_f32 v[2:3], v[146:147], v[178:179], v[2:3] op_sel:[1,0,0]\n"
            "v_pk_fma_f32 v[4:5], v[150:151], v[180:181], v[4:5] op_sel:[1,0,0]\n"
            "v_pk_fma_f32 v[6:7], v[150:151], v[182:183], v[6:7] op_sel:[1,0,0]\n"
            "v_pk_fma_f32 v[8:9], v[154:155], v[184:185], v[8:9] op_sel:[1,0,0]\n"
            "v_pk_fma_f32 v[10:11], v[154:155], v[186:187], v[10:11] op_sel:[1,0,0]\n"
            "v_pk_fma_f32 v[12:13], v[158:159], v[188:189], v[12:13] op_sel:[1,0,0]\n"
            "v_pk_fma_f32 v[14:15], v[158:159], v[190:191], v[14:15] op_sel:[1,0,0]\n"
            "v_pk_mul_f32 v[168:169], v[168:169], v[136:137]\n"
            "v_pk_mul_f32 v[170:171], v[170:171], v[138:139]\n"
            "v_pk_mul_f32 v[172:173], v[172:173], v[140:141]\n"
            "v_pk_mul_f32 v[174:175], v[174:175], v[142:143]\n"
            "s_cmp_lg_u32 s73, s90\n"
            "s_cbranch_scc1 Llk_%=\n"
            "Llkr_%=:\n"
            "s_waitcnt vmcnt(8)\n"
            "v_mfma_f32_4x4x4_16b_bf16 v[176:179], v[208:209], v[40:41], 0 cbsz:2\n"
            "v_mfma_f32_4x4x4_16b_bf16 v[180:183], v[208:209], v[42:43], 0 cbsz:2\n"
            "v_mfma_f32_4x4x4_16b_bf16 v[184:187], v[208:209], v[44:45], 0 cbsz:2\n"
            "v_mfma_f32_4x4x4_16b_bf16 v[188:191], v[208:209], v[46:47], 0 cbsz:2\n"
            "ds_read_b128 v[136:139], v228 offset:24576\n"
            "ds_read_b128 v[140:143], v228 offset:25600\n"
            "ds_read_b128 v[144:147], v228 offset:26624\n"
            "ds_read_b128 v[148:151], v228 offset:27648\n"
            "ds_read_b128 v[152:155], v228 offset:28672\n"
            "ds_read_b128 v[156:159], v228 offset:29696\n"
            "ds_read_b128 v[40:43], v228 offset:30720\n"
            "ds_read_b128 v[44:47], v228 offset:31744\n"
            "s_nop 1\n"
            "v_pk_fma_f32 v[16:17], v[160:161], v[176:177], v[16:17] op_sel_hi:[0,1,1]\n"
            "v_pk_fma_f32 v[18:19], v[160:161], v[178:179], v[18:19] op_sel_hi:[0,1,1]\n"
            "v_pk_fma_f32 v[20:21], v[164:165], v[180:181], v[20:21] op_sel_hi:[0,1,1]\n"
            "v_pk_fma_f32 v[22:23], v[164:165], v[182:183], v[22:23] op_sel_hi:[0,1,1]\n"
            "v_pk_fma_f32 v[24:25], v[168:169], v[184:185], v[24:25] op_sel_hi:[0,1,1]\n"
            "v_pk_fma_f32 v[26:27], v[168:169], v[186:187], v[26:27] op_sel_hi:[0,1,1]\n"
            "v_pk_fma_f32 v[28:29], v[172:173], v[188:189], v[28:29] op_sel_hi:[0,1,1]\n"
            "v_pk_fma_f32 v[30:31], v[172:173], v[190:191], v[30:31] op_sel_hi:[0,1,1]\n"
            "s_cmp_le_u32 s74, 1\n"
            "s_cbranch_scc1 Ltail_%=\n"
            "v_mfma_f32_4x4x4_16b_bf16 v[176:179], v[208:209], v[56:57], 0 cbsz:2 abid:1\n"
            "v_mfma_f32_4x4x4_16b_bf16 v[180:183], v[208:209], v[58:59], 0 cbsz:2 abid:1\n"
            "v_mfma_f32_4x4x4_16b_bf16 v[184:187], v[208:209], v[60:61], 0 cbsz:2 abid:1\n"
            "v_mfma_f32_4x4x4_16b_bf16 v[188:191], v[208:209], v[62:63], 0 cbsz:2 abid:1\n"
            "s_nop 1\n"
            "v_pk_fma_f32 v[16:17], v[160:161], v[176:177], v[16:17] op_sel:[1,0,0]\n"
            "v_pk_fma_f32 v[18:19], v[160:161], v[178:179], v[18:19] op_sel:[1,0,0]\n"
            "v_pk_fma_f32 v[20:21], v[164:165], v[180:181], v[20:21] op_sel:[1,0,0]\n"
            "v_pk_fma_f32 v[22:23], v[164:165], v[182:183], v[22:23] op_sel:[1,0,0]\n"
            "v_pk_fma_f32 v[24:25], v[168:169], v[184:185], v[24:25] op_sel:[1,0,0]\n"
            "v_pk_fma_f32 v[26:27], v[168:169], v[186:187], v[26:27] op_sel:[1,0,0]\n"
            "v_pk_fma_f32 v[28:29], v[172:173], v[188:189], v[28:29] op_sel:[1,0,0]\n"
            "v_pk_fma_f32 v[30:31], v[172:173], v[190:191], v[30:31] op_sel:[1,0,0]\n"
            "s_cmp_le_u32 s74, 2\n"
            "s_cbranch_scc1 Ltail_%=\n"
            "s_waitcnt vmcnt(4)\n"
            "v_mfma_f32_4x4x4_16b_bf16 v[176:179], v[208:209], v[72:73], 0 cbsz:2 abid:2\n"
            "v_mfma_f32_4x4x4_16b_bf16 v[180:183], v[208:209], v[74:75], 0 cbsz:2 abid:2\n"
            "v_mfma_f32_4x4x4_16b_bf16 v[184:187], v[208:209], v[76:77], 0 cbsz:2 abid:2\n"
            "v_mfma_f32_4x4x4_16b_bf16 v[188:191], v[208:209], v[78:79], 0 cbsz:2 abid:2\n"
            "s_nop 1\n"
            "v_pk_fma_f32 v[16:17], v[162:163], v[176:177], v[16:17] op_sel_hi:[0,1,1]\n"
            "v_pk_fma_f32 v[18:19], v[162:163], v[178:179], v[18:19] op_sel_hi:[0,1,1]\n"
            "v_pk_fma_f32 v[20:21], v[166:167], v[180:181], v[20:21] op_sel_hi:[0,1,1]\n"
            "v_pk_fma_f32 v[22:23], v[166:167], v[182:183], v[22:23] op_sel_hi:[0,1,1]\n"
            "v_pk_fma_f32 v[24:25], v[170:171], v[184:185], v[24:25] op_sel_hi:[0,1,1]\n"
            "v_pk_fma_f32 v[26:27], v[170:171], v[186:187], v[26:27] op_sel_hi:[0,1,1]\n"
            "v_pk_fma_f32 v[28:29], v[174:175], v[188:189], v[28:29] op_sel_hi:[0,1,1]\n"
            "v_pk_fma_f32 v[30:31], v[174:175], v[190:191], v[30:31] op_sel_hi:[0,1,1]\n"
            "s_cmp_le_u32 s74, 3\n"
            "s_cbranch_scc1 Ltail_%=\n"
            "v_mfma_f32_4x4x4_16b_bf16 v[176:179], v[208:209], v[88:89], 0 cbsz:2 abid:3\n"
            "v_mfma_f32_4x4x4_16b_bf16 v[180:183], v[208:209], v[90:91], 0 cbsz:2 abid:3\n"
            "v_mfma_f32_4x4x4_16b_bf16 v[184:187], v[208:209], v[92:93], 0 cbsz:2 abid:3\n"
            "v_mfma_f32_4x4x4_16b_bf16 v[188:191], v[208:209], v[94:95], 0 cbsz:2 abid:3\n"
            "s_nop 1\n"
            "v_pk_fma_f32 v[16:17], v[162:163], v[176:177], v[16:17] op_sel:[1,0,0]\n"
            "v_pk_fma_f32 v[18:19], v[162:163], v[178:179], v[18:19] op_sel:[1,0,0]\n"
            "v_pk_fma_f32 v[20:21], v[166:167], v[180:181], v[20:21] op_sel:[1,0,0]\n"
            "v_pk_fma_f32 v[22:23], v[166:167], v[182:183], v[22:23] op_sel:[1,0,0]\n"
            "v_pk_fma_f32 v[24:25], v[170:171], v[184:185], v[24:25] op_sel:[1,0,0]\n"
            "v_pk_fma_f32 v[26:27], v[170:171], v[186:187], v[26:27] op_sel:[1,0,0]\n"
            "v_pk_fma_f32 v[28:29], v[174:175], v[188:189], v[28:29] op_sel:[1,0,0]\n"
            "v_pk_fma_f32 v[30:31], v[174:175], v[190:191], v[30:31] op_sel:[1,0,0]\n"
            "Ltail_%=:\n"
            "s_setprio 3\n"
            "s_cmp_lg_u32 s73, s90\n"
            "s_cbranch_scc1 Lspin_%=\n"
            "Lgo_%=:\n"
            "s_waitcnt vmcnt(0)\n"
            "v_mfma_f32_4x4x4_16b_bf16 v[0:3], v[224:225], v[32:33], v[0:3]\n"
            "v_mfma_f32_4x4x4_16b_bf16 v[4:7], v[224:225], v[34:35], v[4:7]\n"
            "v_mfma_f32_4x4x4_16b_bf16 v[8:11], v[224:225], v[36:37], v[8:11]\n"
            "v_mfma_f32_4x4x4_16b_bf16 v[12:15], v[224:225], v[38:39], v[12:15]\n"
            "v_mfma_f32_4x4x4_16b_bf16 v[16:19], v[224:225], v[48:49], v[16:19]\n"
            "v_mfma_f32_4x4x4_16b_bf16 v[20:23], v[224:225], v[50:51], v[20:23]\n"
            "v_mfma_f32_4x4x4_16b_bf16 v[24:27], v[224:225], v[52:53], v[24:27]\n"
            "v_mfma_f32_4x4x4_16b_bf16 v[28:31], v[224:225], v[54:55], v[28:31]\n"
            "v_cvt_pk_bf16_f32 v176, v0, v4\n"
            "v_cvt_pk_bf16_f32 v177, v8, v12\n"
            "ds_write_b64 v226, v[176:177] offset:0\n"
            "v_cvt_pk_bf16_f32 v178, v1, v5\n"
            "v_cvt_pk_bf16_f32 v179, v9, v13\n"
            "ds_write_b64 v226, v[178:179] offset:272\n"
            "v_cvt_pk_bf16_f32 v180, v2, v6\n"
            "v_cvt_pk_bf16_f32 v181, v10, v14\n"
            "ds_write_b64 v226, v[180:181] offset:544\n"
            "v_cvt_pk_bf16_f32 v182, v3, v7\n"
            "v_cvt_pk_bf16_f32 v183, v11, v15\n"
            "ds_write_b64 v226, v[182:183] offset:816\n"
            "ds_read_b128 v[176:179], v227 offset:0\n"
            "ds_read_b128 v[180:183], v227 offset:64\n"
            "v_cvt_pk_bf16_f32 v184, v16, v20\n"
            "v_cvt_pk_bf16_f32 v185, v24, v28\n"
            "ds_write_b64 v226, v[184:185] offset:128\n"
            "v_cvt_pk_bf16_f32 v186, v17, v21\n"
            "v_cvt_pk_bf16_f32 v187, v25, v29\n"
            "ds_write_b64 v226, v[186:187] offset:400\n"
            "v_cvt_pk_bf16_f32 v188, v18, v22\n"
            "v_cvt_pk_bf16_f32 v189, v26, v30\n"
            "ds_write_b64 v226, v[188:189] offset:672\n"
            "v_cvt_pk_bf16_f32 v190, v19, v23\n"
            "v_cvt_pk_bf16_f32 v191, v27, v31\n"
            "ds_write_b64 v226, v[190:191] offset:944\n"
            "ds_read_b128 v[184:187], v227 offset:128\n"
            "ds_read_b128 v[188:191], v227 offset:192\n"
            "s_waitcnt lgkmcnt(7)\n"
            "v_mfma_f32_16x16x32_bf16 v[0:3], v[176:179], v[236:239], 0\n"
            "v_mfma_f32_16x16x32_bf16 v[4:7], v[176:179], v[252:255], 0\n"
            "s_waitcnt lgkmcnt(6)\n"
            "v_mfma_f32_16x16x32_bf16 v[0:3], v[180:183], v[240:243], v[0:3]\n"
            "v_mfma_f32_16x16x32_bf16 v[4:7], v[180:183], v[96:99], v[4:7]\n"
            "buffer_load_dwordx4 v[72:75], v218, s[20:23], s63 offen offset:0 sc1\n"
            "v_mfma_f32_16x16x32_bf16 v[8:11], v[176:179], v[108:111], 0\n"
            "v_mfma_f32_16x16x32_bf16 v[12:15], v[176:179], v[200:203], 0\n"
            "v_mfma_f32_16x16x32_bf16 v[8:11], v[180:183], v[112:115], v[8:11]\n"
            "v_mfma_f32_16x16x32_bf16 v[12:15], v[180:183], v[204:207], v[12:15]\n"
            "buffer_load_dwordx4 v[76:79], v218, s[20:23], s63 offen offset:256 sc1\n"
            "v_mfma_f32_16x16x32_bf16 v[16:19], v[176:179], v[64:67], 0\n"
            "v_mfma_f32_16x16x32_bf16 v[20:23], v[176:179], v[128:131], 0\n"
            "v_mfma_f32_16x16x32_bf16 v[16:19], v[180:183], v[68:71], v[16:19]\n"
            "v_mfma_f32_16x16x32_bf16 v[20:23], v[180:183], v[132:135], v[20:23]\n"
            "buffer_load_dwordx4 v[88:91], v218, s[20:23], s63 offen offset:512 sc1\n"
            "v_mfma_f32_16x16x32_bf16 v[24:27], v[176:179], v[136:139], 0\n"
            "v_mfma_f32_16x16x32_bf16 v[28:31], v[176:179], v[152:155], 0\n"
            "v_mfma_f32_16x16x32_bf16 v[24:27], v[180:183], v[140:143], v[24:27]\n"
            "v_mfma_f32_16x16x32_bf16 v[28:31], v[180:183], v[156:159], v[28:31]\n"
            "buffer_load_dwordx4 v[92:95], v218, s[20:23], s63 offen offset:768 sc1\n"
            "s_waitcnt lgkmcnt(1)\n"
            "v_mfma_f32_16x16x32_bf16 v[0:3], v[184:187], v[244:247], v[0:3]\n"
            "v_mfma_f32_16x16x32_bf16 v[4:7], v[184:187], v[100:103], v[4:7]\n"
            "s_waitcnt lgkmcnt(0)\n"
            "v_mfma_f32_16x16x32_bf16 v[0:3], v[188:191], v[248:251], v[0:3]\n"
            "v_mfma_f32_16x16x32_bf16 v[4:7], v[188:191], v[104:107], v[4:7]\n"
            "v_mfma_f32_16x16x32_bf16 v[8:11], v[184:187], v[116:119], v[8:11]\n"
            "v_mfma_f32_16x16x32_bf16 v[12:15], v[184:187], v[232:235], v[12:15]\n"
            "v_mfma_f32_16x16x32_bf16 v[8:11], v[188:191], v[196:199], v[8:11]\n"
            "v_mfma_f32_16x16x32_bf16 v[12:15], v[188:191], v[120:123], v[12:15]\n"
            "v_mfma_f32_16x16x32_bf16 v[16:19], v[184:187], v[124:127], v[16:19]\n"
            "v_mfma_f32_16x16x32_bf16 v[20:23], v[184:187], v[80:83], v[20:23]\n"
            "v_mfma_f32_16x16x32_bf16 v[16:19], v[188:191], v[192:195], v[16:19]\n"
            "v_mfma_f32_16x16x32_bf16 v[20:23], v[188:191], v[84:87], v[20:23]\n"
            "v_cvt_pk_bf16_f32 v0, v0, v1\n"
            "v_cvt_pk_bf16_f32 v1, v2, v3\n"
            "v_cvt_pk_bf16_f32 v2, v4, v5\n"
            "v_cvt_pk_bf16_f32 v3, v6, v7\n"
            "buffer_store_dwordx4 v[0:3], v217, s[32:35], 0 offen offset:0 sc1\n"
            "v_mfma_f32_16x16x32_bf16 v[24:27], v[184:187], v[144:147], v[24:27]\n"
            "v_mfma_f32_16x16x32_bf16 v[28:31], v[184:187], v[40:43], v[28:31]\n"
            "v_mfma_f32_16x16x32_bf16 v[24:27], v[188:191], v[148:151], v[24:27]\n"
            "v_mfma_f32_16x16x32_bf16 v[28:31], v[188:191], v[44:47], v[28:31]\n"
            "v_cvt_pk_bf16_f32 v8, v8, v9\n"
            "v_cvt_pk_bf16_f32 v9, v10, v11\n"
            "v_cvt_pk_bf16_f32 v10, v12, v13\n"
            "v_cvt_pk_bf16_f32 v11, v14, v15\n"
            "buffer_store_dwordx4 v[8:11], v217, s[32:35], 0 offen offset:256 sc1\n"
            "v_cvt_pk_bf16_f32 v16, v16, v17\n"
            "v_cvt_pk_bf16_f32 v17, v18, v19\n"
            "v_cvt_pk_bf16_f32 v18, v20, v21\n"
            "v_cvt_pk_bf16_f32 v19, v22, v23\n"
            "buffer_store_dwordx4 v[16:19], v217, s[32:35], 0 offen offset:512 sc1\n"
            "v_cvt_pk_bf16_f32 v24, v24, v25\n"
            "v_cvt_pk_bf16_f32 v25, v26, v27\n"
            "v_cvt_pk_bf16_f32 v26, v28, v29\n"
            "v_cvt_pk_bf16_f32 v27, v30, v31\n"
            "buffer_store_dwordx4 v[24:27], v217, s[32:35], 0 offen offset:768 sc1\n"
            "s_cmp_lt_u32 s3, 6\n"
            "s_cbranch_scc0 Ltwo_%=\n"
            "s_endpgm\n"
            "Ltwo_%=:\n"
            "ds_read_b128 v[32:35], v231 offset:0\n"
            "ds_read_b128 v[36:39], v231 offset:1024\n"
            "ds_read_b128 v[48:51], v231 offset:2048\n"
            "ds_read_b128 v[52:55], v231 offset:3072\n"
            "s_waitcnt vmcnt(4)\n"
            "v_mfma_f32_4x4x4_16b_bf16 v[0:3], v[224:225], v[72:73], 0\n"
            "v_mfma_f32_4x4x4_16b_bf16 v[4:7], v[224:225], v[74:75], 0\n"
            "v_mfma_f32_4x4x4_16b_bf16 v[8:11], v[224:225], v[76:77], 0\n"
            "v_mfma_f32_4x4x4_16b_bf16 v[12:15], v[224:225], v[78:79], 0\n"
            "v_mfma_f32_4x4x4_16b_bf16 v[16:19], v[224:225], v[88:89], 0\n"
            "v_mfma_f32_4x4x4_16b_bf16 v[20:23], v[224:225], v[90:91], 0\n"
            "v_mfma_f32_4x4x4_16b_bf16 v[24:27], v[224:225], v[92:93], 0\n"
            "v_mfma_f32_4x4x4_16b_bf16 v[28:31], v[224:225], v[94:95], 0\n"
            "s_waitcnt lgkmcnt(0)\n"
            "v_mfma_f32_4x4x4_16b_bf16 v[0:3], v[224:225], v[32:33], v[0:3]\n"
            "v_mfma_f32_4x4x4_16b_bf16 v[4:7], v[224:225], v[34:35], v[4:7]\n"
            "v_mfma_f32_4x4x4_16b_bf16 v[8:11], v[224:225], v[36:37], v[8:11]\n"
            "v_mfma_f32_4x4x4_16b_bf16 v[12:15], v[224:225], v[38:39], v[12:15]\n"
            "v_mfma_f32_4x4x4_16b_bf16 v[16:19], v[224:225], v[48:49], v[16:19]\n"
            "v_mfma_f32_4x4x4_16b_bf16 v[20:23], v[224:225], v[50:51], v[20:23]\n"
            "v_mfma_f32_4x4x4_16b_bf16 v[24:27], v[224:225], v[52:53], v[24:27]\n"
            "v_mfma_f32_4x4x4_16b_bf16 v[28:31], v[224:225], v[54:55], v[28:31]\n"
            "v_cvt_pk_bf16_f32 v176, v0, v4\n"
            "v_cvt_pk_bf16_f32 v177, v8, v12\n"
            "ds_write_b64 v226, v[176:177] offset:0\n"
            "v_cvt_pk_bf16_f32 v178, v1, v5\n"
            "v_cvt_pk_bf16_f32 v179, v9, v13\n"
            "ds_write_b64 v226, v[178:179] offset:272\n"
            "v_cvt_pk_bf16_f32 v180, v2, v6\n"
            "v_cvt_pk_bf16_f32 v181, v10, v14\n"
            "ds_write_b64 v226, v[180:181] offset:544\n"
            "v_cvt_pk_bf16_f32 v182, v3, v7\n"
            "v_cvt_pk_bf16_f32 v183, v11, v15\n"
            "ds_write_b64 v226, v[182:183] offset:816\n"
            "ds_read_b128 v[176:179], v227 offset:0\n"
            "ds_read_b128 v[180:183], v227 offset:64\n"
            "v_cvt_pk_bf16_f32 v184, v16, v20\n"
            "v_cvt_pk_bf16_f32 v185, v24, v28\n"
            "ds_write_b64 v226, v[184:185] offset:128\n"
            "v_cvt_pk_bf16_f32 v186, v17, v21\n"
            "v_cvt_pk_bf16_f32 v187, v25, v29\n"
            "ds_write_b64 v226, v[186:187] offset:400\n"
            "v_cvt_pk_bf16_f32 v188, v18, v22\n"
            "v_cvt_pk_bf16_f32 v189, v26, v30\n"
            "ds_write_b64 v226, v[188:189] offset:672\n"
            "v_cvt_pk_bf16_f32 v190, v19, v23\n"
            "v_cvt_pk_bf16_f32 v191, v27, v31\n"
            "ds_write_b64 v226, v[190:191] offset:944\n"
            "ds_read_b128 v[184:187], v227 offset:128\n"
            "ds_read_b128 v[188:191], v227 offset:192\n"
            "s_waitcnt lgkmcnt(7)\n"
            "v_mfma_f32_16x16x32_bf16 v[0:3], v[176:179], v[236:239], 0\n"
            "v_mfma_f32_16x16x32_bf16 v[4:7], v[176:179], v[252:255], 0\n"
            "s_waitcnt lgkmcnt(6)\n"
            "v_mfma_f32_16x16x32_bf16 v[0:3], v[180:183], v[240:243], v[0:3]\n"
            "v_mfma_f32_16x16x32_bf16 v[4:7], v[180:183], v[96:99], v[4:7]\n"
            "v_mfma_f32_16x16x32_bf16 v[8:11], v[176:179], v[108:111], 0\n"
            "v_mfma_f32_16x16x32_bf16 v[12:15], v[176:179], v[200:203], 0\n"
            "v_mfma_f32_16x16x32_bf16 v[8:11], v[180:183], v[112:115], v[8:11]\n"
            "v_mfma_f32_16x16x32_bf16 v[12:15], v[180:183], v[204:207], v[12:15]\n"
            "v_mfma_f32_16x16x32_bf16 v[16:19], v[176:179], v[64:67], 0\n"
            "v_mfma_f32_16x16x32_bf16 v[20:23], v[176:179], v[128:131], 0\n"
            "v_mfma_f32_16x16x32_bf16 v[16:19], v[180:183], v[68:71], v[16:19]\n"
            "v_mfma_f32_16x16x32_bf16 v[20:23], v[180:183], v[132:135], v[20:23]\n"
            "v_mfma_f32_16x16x32_bf16 v[24:27], v[176:179], v[136:139], 0\n"
            "v_mfma_f32_16x16x32_bf16 v[28:31], v[176:179], v[152:155], 0\n"
            "v_mfma_f32_16x16x32_bf16 v[24:27], v[180:183], v[140:143], v[24:27]\n"
            "v_mfma_f32_16x16x32_bf16 v[28:31], v[180:183], v[156:159], v[28:31]\n"
            "s_waitcnt lgkmcnt(1)\n"
            "v_mfma_f32_16x16x32_bf16 v[0:3], v[184:187], v[244:247], v[0:3]\n"
            "v_mfma_f32_16x16x32_bf16 v[4:7], v[184:187], v[100:103], v[4:7]\n"
            "s_waitcnt lgkmcnt(0)\n"
            "v_mfma_f32_16x16x32_bf16 v[0:3], v[188:191], v[248:251], v[0:3]\n"
            "v_mfma_f32_16x16x32_bf16 v[4:7], v[188:191], v[104:107], v[4:7]\n"
            "v_mfma_f32_16x16x32_bf16 v[8:11], v[184:187], v[116:119], v[8:11]\n"
            "v_mfma_f32_16x16x32_bf16 v[12:15], v[184:187], v[232:235], v[12:15]\n"
            "v_mfma_f32_16x16x32_bf16 v[8:11], v[188:191], v[196:199], v[8:11]\n"
            "v_mfma_f32_16x16x32_bf16 v[12:15], v[188:191], v[120:123], v[12:15]\n"
            "v_mfma_f32_16x16x32_bf16 v[16:19], v[184:187], v[124:127], v[16:19]\n"
            "v_mfma_f32_16x16x32_bf16 v[20:23], v[184:187], v[80:83], v[20:23]\n"
            "v_mfma_f32_16x16x32_bf16 v[16:19], v[188:191], v[192:195], v[16:19]\n"
            "v_mfma_f32_16x16x32_bf16 v[20:23], v[188:191], v[84:87], v[20:23]\n"
            "v_cvt_pk_bf16_f32 v0, v0, v1\n"
            "v_cvt_pk_bf16_f32 v1, v2, v3\n"
            "v_cvt_pk_bf16_f32 v2, v4, v5\n"
            "v_cvt_pk_bf16_f32 v3, v6, v7\n"
            "buffer_store_dwordx4 v[0:3], v218, s[32:35], 0 offen offset:0 sc1\n"
            "v_mfma_f32_16x16x32_bf16 v[24:27], v[184:187], v[144:147], v[24:27]\n"
            "v_mfma_f32_16x16x32_bf16 v[28:31], v[184:187], v[40:43], v[28:31]\n"
            "v_mfma_f32_16x16x32_bf16 v[24:27], v[188:191], v[148:151], v[24:27]\n"
            "v_mfma_f32_16x16x32_bf16 v[28:31], v[188:191], v[44:47], v[28:31]\n"
            "v_cvt_pk_bf16_f32 v8, v8, v9\n"
            "v_cvt_pk_bf16_f32 v9, v10, v11\n"
            "v_cvt_pk_bf16_f32 v10, v12, v13\n"
            "v_cvt_pk_bf16_f32 v11, v14, v15\n"
            "buffer_store_dwordx4 v[8:11], v218, s[32:35], 0 offen offset:256 sc1\n"
            "v_cvt_pk_bf16_f32 v16, v16, v17\n"
            "v_cvt_pk_bf16_f32 v17, v18, v19\n"
            "v_cvt_pk_bf16_f32 v18, v20, v21\n"
            "v_cvt_pk_bf16_f32 v19, v22, v23\n"
            "buffer_store_dwordx4 v[16:19], v218, s[32:35], 0 offen offset:512 sc1\n"
            "v_cvt_pk_bf16_f32 v24, v24, v25\n"
            "v_cvt_pk_bf16_f32 v25, v26, v27\n"
            "v_cvt_pk_bf16_f32 v26, v28, v29\n"
            "v_cvt_pk_bf16_f32 v27, v30, v31\n"
            "buffer_store_dwordx4 v[24:27], v218, s[32:35], 0 offen offset:768 sc1\n"
            "Lend_%=:\n"
            "s_endpgm\n"
            "s_mov_b32 s68, 0\n"
            "s_branch Lout_%=\n"
            "Lpoll2_%=:\n"
            "s_sub_u32 s99, s99, 1\n"
            "s_cmp_lt_i32 s99, 0\n"
            "s_cbranch_scc1 Lpoll3_%=\n"
            "s_sleep 2\n"
            "buffer_load_dwordx2 v[142:143], v180, s[20:23], 0 offen sc1\n"
            "s_branch Lpoll_%=\n"
            "Lover_%=:\n"
            "s_mov_b32 s37, 0\n"
            "v_mov_b32 v144, 255\n"
            "ds_write_b32 v219, v144\n"
            "s_branch Lover2_%=\n"
            "Llng_%=:\n"
            "s_nop 1\n"
            "s_mov_b32 s38, 0\n"
            "v_max_u32_dpp v74, v74, v74 row_bcast:15 row_mask:0xa bank_mask:0xf\n"
            "s_nop 1\n"
            "v_max_u32_dpp v74, v74, v74 row_bcast:31 row_mask:0xc bank_mask:0xf\n"
            "v_max_u32 v74, s38, v74\n"
            "s_nop 0\n"
            "v_readlane_b32 s38, v74, 63\n"
            "v_max_u32_dpp v75, v75, v75 row_bcast:15 row_mask:0xa bank_mask:0xf\n"
            "s_nop 1\n"
            "v_max_u32_dpp v75, v75, v75 row_bcast:31 row_mask:0xc bank_mask:0xf\n"
            "v_max_u32 v75, s38, v75\n"
            "s_nop 0\n"
            "v_readlane_b32 s38, v75, 63\n"
            "v_max_u32_dpp v76, v76, v76 row_bcast:15 row_mask:0xa bank_mask:0xf\n"
            "s_nop 1\n"
            "v_max_u32_dpp v76, v76, v76 row_bcast:31 row_mask:0xc bank_mask:0xf\n"
            "v_max_u32 v76, s38, v76\n"
            "s_nop 0\n"
            "v_readlane_b32 s38, v76, 63\n"
            "v_max_u32_dpp v77, v77, v77 row_bcast:15 row_mask:0xa bank_mask:0xf\n"
            "s_nop 1\n"
            "v_max_u32_dpp v77, v77, v77 row_bcast:31 row_mask:0xc bank_mask:0xf\n"
            "v_max_u32 v77, s38, v77\n"
            "s_nop 0\n"
            "v_readlane_b32 s38, v77, 63\n"
            "v_max_u32_dpp v78, v78, v78 row_bcast:15 row_mask:0xa bank_mask:0xf\n"
            "s_nop 1\n"
            "v_max_u32_dpp v78, v78, v78 row_bcast:31 row_mask:0xc bank_mask:0xf\n"
            "v_max_u32 v78, s38, v78\n"
            "s_nop 0\n"
            "v_readlane_b32 s38, v78, 63\n"
            "v_max_u32_dpp v79, v79, v79 row_bcast:15 row_mask:0xa bank_mask:0xf\n"
            "s_nop 1\n"
            "v_max_u32_dpp v79, v79, v79 row_bcast:31 row_mask:0xc bank_mask:0xf\n"
            "v_max_u32 v79, s38, v79\n"
            "s_nop 0\n"
            "v_readlane_b32 s38, v79, 63\n"
            "v_max_u32_dpp v80, v80, v80 row_bcast:15 row_mask:0xa bank_mask:0xf\n"
            "s_nop 1\n"
            "v_max_u32_dpp v80, v80, v80 row_bcast:31 row_mask:0xc bank_mask:0xf\n"
            "v_max_u32 v80, s38, v80\n"
            "s_nop 0\n"
            "v_readlane_b32 s38, v80, 63\n"
            "s_branch Llng2_%=\n"
            "Lre0_%=:\n"
            "s_sub_u32 s99, s99, 1\n"
            "s_cmp_lt_i32 s99, 0\n"
            "s_cbranch_scc1 Lok0_%=\n"
            "s_sleep 1\n"
            "buffer_load_dwordx4 v[34:37], v81, s[20:23], s15 offen offset:0 sc1\n"
            "s_waitcnt vmcnt(0)\n"
            "s_branch Lchk0_%=\n"
            "Lre1_%=:\n"
            "s_sub_u32 s99, s99, 1\n"
            "s_cmp_lt_i32 s99, 0\n"
            "s_cbranch_scc1 Lok1_%=\n"
            "s_sleep 1\n"
            "buffer_load_dwordx4 v[40:43], v82, s[20:23], s15 offen offset:1024 sc1\n"
            "s_waitcnt vmcnt(0)\n"
            "s_branch Lchk1_%=\n"
            "Lre2_%=:\n"
            "s_sub_u32 s99, s99, 1\n"
            "s_cmp_lt_i32 s99, 0\n"
            "s_cbranch_scc1 Lok2_%=\n"
            "s_sleep 1\n"
            "buffer_load_dwordx4 v[46:49], v83, s[20:23], s15 offen offset:2048 sc1\n"
            "s_waitcnt vmcnt(0)\n"
            "s_branch Lchk2_%=\n"
            "Lre3_%=:\n"
            "s_sub_u32 s99, s99, 1\n"
            "s_cmp_lt_i32 s99, 0\n"
            "s_cbranch_scc1 Lok3_%=\n"
            "s_sleep 1\n"
            "buffer_load_dwordx4 v[52:55], v84, s[20:23], s15 offen offset:3072 sc1\n"
            "s_waitcnt vmcnt(0)\n"
            "s_branch Lchk3_%=\n"
            "Lre4_%=:\n"
            "s_sub_u32 s99, s99, 1\n"
            "s_cmp_lt_i32 s99, 0\n"
            "s_cbranch_scc1 Lok4_%=\n"
            "s_sleep 1\n"
            "buffer_load_dwordx4 v[58:61], v85, s[20:23], s16 offen offset:0 sc1\n"
            "s_waitcnt vmcnt(0)\n"
            "s_branch Lchk4_%=\n"
            "Lre5_%=:\n"
            "s_sub_u32 s99, s99, 1\n"
            "s_cmp_lt_i32 s99, 0\n"
            "s_cbranch_scc1 Lok5_%=\n"
            "s_sleep 1\n"
            "buffer_load_dwordx4 v[64:67], v86, s[20:23], s16 offen offset:1024 sc1\n"
            "s_waitcnt vmcnt(0)\n"
            "s_branch Lchk5_%=\n"
            "Lre6_%=:\n"
            "s_sub_u32 s99, s99, 1\n"
            "s_cmp_lt_i32 s99, 0\n"
            "s_cbranch_scc1 Lok6_%=\n"
            "s_sleep 1\n"
            "buffer_load_dwordx4 v[70:73], v87, s[20:23], s16 offen offset:2048 sc1\n"
            "s_waitcnt vmcnt(0)\n"
            "s_branch Lchk6_%=\n"
            "Llong_%=:\n"
            "v_and_b32 v243, 31, v243\n"
            "v_readfirstlane_b32 s86, v247\n"
            "v_add_u32 v243, 64, v243\n"
            "s_nop 0\n"
            "v_lshl_add_u32 v247, v243, 3, s86\n"
            "v_subrev_u32 v128, s43, v243\n"
            "v_mov_b32 v136, s64\n"
            "v_cmpx_le_i32 vcc, s69, v128\n"
            "v_mov_b32 v136, s81\n"
            "v_cmpx_le_i32 vcc, s78, v128\n"
            "v_mov_b32 v136, s82\n"
            "v_cmpx_le_i32 vcc, s79, v128\n"
            "v_mov_b32 v136, s83\n"
            "v_cmpx_le_i32 vcc, s80, v128\n"
            "v_mov_b32 v136, s84\n"
            "s_mov_b64 exec, -1\n"
            "v_and_b32 v137, 0xffff, v136\n"
            "ds_read_b128 v[140:143], v137\n"
            "v_ashrrev_i32 v136, 16, v136\n"
            "v_sub_u32 v136, v128, v136\n"
            "s_waitcnt lgkmcnt(0)\n"
            "v_cmp_eq_u32 vcc, s64, v137\n"
            "v_mov_b32 v132, 60\n"
            "s_nop 0\n"
            "v_cndmask_b32 v132, 0, v132, vcc\n"
            "v_bfe_u32 v137, v140, 16, 8\n"
            "v_max_u32 v137, 8, v137\n"
            "v_lshrrev_b32 v138, 1, v137\n"
            "v_add_u32 v138, v138, v132\n"
            "v_sub_u32_sdwa v132, v138, v140 dst_sel:DWORD dst_unused:UNUSED_PAD src0_sel:DWORD src1_sel:BYTE_3\n"
            "v_sub_u32_sdwa v133, v137, v140 dst_sel:DWORD dst_unused:UNUSED_PAD src0_sel:DWORD src1_sel:BYTE_2\n"
            "v_med3_i32 v132, v132, 0, v133\n"
            "v_sub_u32 v132, v136, v132\n"
            "v_cmp_lt_u32_sdwa vcc, v132, v140 src0_sel:DWORD src1_sel:BYTE_2\n"
            "v_lshl_add_u32 v140, v132, 4, v140\n"
            "v_sub_u32_sdwa v132, v138, v141 dst_sel:DWORD dst_unused:UNUSED_PAD src0_sel:DWORD src1_sel:BYTE_3\n"
            "v_sub_u32_sdwa v133, v137, v141 dst_sel:DWORD dst_unused:UNUSED_PAD src0_sel:DWORD src1_sel:BYTE_2\n"
            "v_med3_i32 v132, v132, 0, v133\n"
            "v_sub_u32 v132, v136, v132\n"
            "v_cmp_lt_u32_sdwa s[70:71], v132, v141 src0_sel:DWORD src1_sel:BYTE_2\n"
            "v_lshl_add_u32 v141, v132, 4, v141\n"
            "v_sub_u32_sdwa v132, v138, v142 dst_sel:DWORD dst_unused:UNUSED_PAD src0_sel:DWORD src1_sel:BYTE_3\n"
            "v_sub_u32_sdwa v133, v137, v142 dst_sel:DWORD dst_unused:UNUSED_PAD src0_sel:DWORD src1_sel:BYTE_2\n"
            "v_med3_i32 v132, v132, 0, v133\n"
            "v_sub_u32 v132, v136, v132\n"
            "v_cmp_lt_u32_sdwa s[86:87], v132, v142 src0_sel:DWORD src1_sel:BYTE_2\n"
            "v_lshl_add_u32 v142, v132, 4, v142\n"
            "v_sub_u32_sdwa v132, v138, v143 dst_sel:DWORD dst_unused:UNUSED_PAD src0_sel:DWORD src1_sel:BYTE_3\n"
            "v_sub_u32_sdwa v133, v137, v143 dst_sel:DWORD dst_unused:UNUSED_PAD src0_sel:DWORD src1_sel:BYTE_2\n"
            "v_med3_i32 v132, v132, 0, v133\n"
            "v_sub_u32 v132, v136, v132\n"
            "v_cmp_lt_u32_sdwa s[88:89], v132, v143 src0_sel:DWORD src1_sel:BYTE_2\n"
            "v_lshl_add_u32 v143, v132, 4, v143\n"
            "v_cndmask_b32 v140, v251, v140, vcc\n"
            "v_cndmask_b32 v141, v251, v141, s[70:71]\n"
            "v_cndmask_b32 v142, v251, v142, s[86:87]\n"
            "v_cndmask_b32 v143, v251, v143, s[88:89]\n"
            "v_perm_b32 v140, v141, v140, s68\n"
            "v_perm_b32 v141, v143, v142, s68\n"
            "ds_write_b64 v247, v[140:141]\n"
            "s_branch Llong2_%=\n"
            "Lslow_%=:\n"
            "s_waitcnt vmcnt(0) lgkmcnt(0)\n"
            "s_mov_b32 s68, -1\n"
            "Lout_%=:\n"
            : "+{v0}"(tid), "={s68}"(slow), "={s90}"(tag), "={s3}"(wq)
            : "{s[0:1]}"(ka), "{s2}"(bx)
            : "memory", "vcc", "scc", "v1", "v2", "v3", "v4", "v5", "v6", "v7", "v8", "v9", "v10", "v11", "v12", "v13", "v14", "v15", "v16", "v17", "v18", "v19", "v20", "v21", "v22", "v23", "v24", "v25", "v26", "v27", "v28", "v29", "v30", "v31", "v32", "v33", "v34", "v35", "v36", "v37", "v38", "v39", "v40", "v41", "v42", "v43", "v44", "v45", "v46", "v47", "v48", "v49", "v50", "v51", "v52", "v53", "v54", "v55", "v56", "v57", "v58", "v59", "v60", "v61", "v62", "v63", "v64", "v65", "v66", "v67", "v68", "v69", "v70", "v71", "v72", "v73", "v74", "v75", "v76", "v77", "v78", "v79", "v80", "v81", "v82", "v83", "v84", "v85", "v86", "v87", "v88", "v89", "v90", "v91", "v92", "v93", "v94", "v95", "v96", "v97", "v98", "v99", "v100", "v101", "v102", "v103", "v104", "v105", "v106", "v107", "v108", "v109", "v110", "v111", "v112", "v113", "v114", "v115", "v116", "v117", "v118", "v119", "v120", "v121", "v122", "v123", "v124", "v125", "v126", "v127", "v128", "v129", "v130", "v131", "v132", "v133", "v134", "v135", "v136", "v137", "v138", "v139", "v140", "v141", "v142", "v143", "v144", "v145", "v146", "v147", "v148", "v149", "v150", "v151", "v152", "v153", "v154", "v155", "v156", "v157", "v158", "v159", "v160", "v161", "v162", "v163", "v164", "v165", "v166", "v167", "v168", "v169", "v170", "v171", "v172", "v173", "v174", "v175", "v176", "v177", "v178", "v179", "v180", "v181", "v182", "v183", "v184", "v185", "v186", "v187", "v188", "v189", "v190", "v191", "v192", "v193", "v194", "v195", "v196", "v197", "v198", "v199", "v200", "v201", "v202", "v203", "v204", "v205", "v206", "v207", "v208", "v209", "v210", "v211", "v212", "v213", "v214", "v215", "v216", "v217", "v218", "v219", "v220", "v221", "v222", "v223", "v224", "v225", "v226", "v227", "v228", "v229", "v230", "v231", "v232", "v233", "v234", "v235", "v236", "v237", "v238", "v239", "v240", "v241", "v242", "v243", "v244", "v245", "v246", "v247", "v248", "v249", "v250", "v251", "v252", "v253", "v254", "v255", "s4", "s5", "s6", "s7", "s8", "s9", "s10", "s11", "s12", "s13", "s14", "s15", "s16", "s17", "s18", "s19", "s20", "s21", "s22", "s23", "s24", "s25", "s26", "s27", "s28", "s29", "s30", "s31", "s33", "s34", "s35", "s36", "s37", "s38", "s39", "s40", "s41", "s42", "s43", "s44", "s45", "s46", "s47", "s48", "s49", "s50", "s51", "s52", "s53", "s54", "s55", "s56", "s57", "s58", "s59", "s60", "s61", "s62", "s63", "s64", "s65", "s66", "s67", "s69", "s70", "s71", "s72", "s73", "s74", "s75", "s76", "s77", "s78", "s79", "s80", "s81", "s82", "s83", "s84", "s85", "s86", "s87", "s88", "s89", "s91", "s92", "s93", "s94", "s95", "s96", "s97", "s98", "s99");
    const int w = __builtin_amdgcn_readfirstlane((int)wq);
    const unsigned lds0 = (unsigned)(uintptr_t)(__attribute__((address_space(3))) unsigned char*)smem;
    asm volatile("" : "+s"(ka));
    unsigned char* ws = (unsigned char*)ka[6];
    if (__builtin_amdgcn_readfirstlane((int)slow) == -1) {
        const int lane = lane_opaque();
        tag = (unsigned)__builtin_amdgcn_readfirstlane((int)tag);
        const int* dst = (const int*)ka[0]; const int* src = (const int*)ka[2];
        const unsigned short *Y = (const unsigned short*)ka[4], *ef = (const unsigned short*)ka[8], *x = (const unsigned short*)ka[10], *w_rad = (const unsigned short*)ka[14],
                             *cg = (const unsigned short*)ka[16], *w_mix = (const unsigned short*)ka[18];
        unsigned short* out = (unsigned short*)ka[12];
        const int N = (int)ka[20], E = (int)(ka[20] >> 32), nh = (int)ka[23];
        const int half = (bx >> 2) & 1, b = (bx & 3) * 32 + (bx >> 3), sS = 40 * (1 - half), sK = 40 * half;
        const int node0 = (b * N + 127) >> 7, npb = (((b + 1) * N + 127) >> 7) - node0;
        const __amdgpu_buffer_rsrc_t rsw = __builtin_amdgcn_make_buffer_rsrc((void*)ws, 0, W_END, 0x00020000);
        lds_wait(lds0 + SY_C3, 4);
        const __amdgpu_buffer_rsrc_t rso = __builtin_amdgcn_make_buffer_rsrc((void*)out, 0, N * 1024, 0x00020000);
#pragma unroll 1
        for (int it = 0; it < 10; ++it) {
            const int nl = (it < 5 ? sS + 8 * it : sK + 8 * (it - 5)) + w, node = node0 + nl;
            const bool nv = nl < npb;
            const unsigned ro = nv ? (unsigned)node * 1024u + (unsigned)lane * 16u : OOB_OFF;
            float a8[8] = {0.f, 0.f, 0.f, 0.f, 0.f, 0.f, 0.f, 0.f};
#pragma unroll 1
            for (int e0 = 0; e0 < (nv ? E : 0); e0 += 64) {
                const int ec = min(e0 + lane, E - 1);
                u64 mm = __ballot(e0 + lane < E && dst[ec] == node && (src[ec] >= nh) == (half != 0));
#pragma unroll 1
                while (mm) {
                    const int e = e0 + __builtin_ctzll(mm);
                    mm &= mm - 1;
                    const unsigned short* fe = (const unsigned short*)ef + (size_t)e * DE_DIM;
                    const unsigned short* ye = (const unsigned short*)Y + (size_t)e * M_DIM;
                    const unsigned short* xp = x + (size_t)src[e] * (C_DIM * M_DIM) + lane * 8;
                    float z0 = 0.f, z1 = 0.f;
#pragma unroll 1
                    for (int f = 0; f < DE_DIM; ++f) { z0 += bf2f(fe[f]) * bf2f(w_rad[f * C_DIM + 2 * lane]); z1 += bf2f(fe[f]) * bf2f(w_rad[f * C_DIM + 2 * lane + 1]); }
                    const float r0 = z0 / (1.f + __expf(-z0)), r1 = z1 / (1.f + __expf(-z1));
#pragma unroll 1
                    for (int i = 0; i < 4; ++i) {
                        const float x0 = bf2f(xp[i]) * r0, x1 = bf2f(xp[4 + i]) * r1;
#pragma unroll
                        for (int o = 0; o < 4; ++o) {
                            float wv4 = 0.f;
#pragma unroll
                            for (int f = 0; f < 4; ++f) wv4 += bf2f(cg[(i * 4 + f) * 4 + o]) * bf2f(ye[f]);
                            a8[o] += x0 * wv4; a8[4 + o] += x1 * wv4;
                        }
                    }
                }
            }
            if (it >= 5) {
                const u32x4 pv = __builtin_amdgcn_raw_buffer_load_b128(rsw, ro, W_AGG, SC1);
#pragma unroll
                for (int i = 0; i < 4; ++i) { a8[2 * i] += __uint_as_float(pv[i] << 16); a8[2 * i + 1] += __uint_as_float(pv[i] & 0xffff0000u); }
            }
            const u32x4 rv = (u32x4){pack2bf(a8[0], a8[1]), pack2bf(a8[2], a8[3]), pack2bf(a8[4], a8[5]), pack2bf(a8[6], a8[7])};
            if (it < 5) {
                __builtin_amdgcn_raw_buffer_store_b128(rv, rsw, ro, W_AGG, SC1);
                if (it == 4) {
                    asm volatile("s_waitcnt vmcnt(0)" ::: "memory");
                    if (lds_arrive_rtn(lds0 + 4) == 7u) flag_store(ws + W_FLG + bx * 128, tag);
                    poll_tag(ws + W_FLG + (bx ^ 4) * 128, tag);
                }
            } else {
                *(u32x4*)(smem + L_LST + w * 1024 + lane * 16) = rv;
#pragma unroll
                for (int i = 0; i < 8; ++i) a8[i] = 0.f;
#pragma unroll 1
                for (int c = 0; c < C_DIM; ++c) {
                    const unsigned short* ap = (const unsigned short*)(smem + L_LST + w * 1024 + c * 8);
                    const float w0 = bf2f(w_mix[c * C_DIM + 2 * lane]), w1 = bf2f(w_mix[c * C_DIM + 2 * lane + 1]);
#pragma unroll
                    for (int m = 0; m < 4; ++m) { a8[m] += bf2f(ap[m]) * w0; a8[4 + m] += bf2f(ap[m]) * w1; }
                }
                __builtin_amdgcn_raw_buffer_store_b128((u32x4){pack2bf(a8[0], a8[1]), pack2bf(a8[2], a8[3]), pack2bf(a8[4], a8[5]), pack2bf(a8[6], a8[7])}, rso, ro, 0, 0);
            }
        }
    }
}

__global__ void kf_zero(float* __restrict__ agg, size_t n) {
    for (size_t i = (size_t)blockIdx.x * blockDim.x + threadIdx.x; i < n; i += (size_t)gridDim.x * blockDim.x) agg[i] = 0.f;
}
__global__ void kf_edges(const unsigned short* __restrict__ x, const int* __restrict__ src, const int* __restrict__ dst,
                         const unsigned short* __restrict__ Y, const unsigned short* __restrict__ ef,
                         const unsigned short* __restrict__ w_rad, const unsigned short* __restrict__ cg,
                         float* __restrict__ agg, int E) {
    const int ch = threadIdx.x;
    for (int e = blockIdx.x; e < E; e += gridDim.x) {
        float z = 0.f;
        for (int f = 0; f < DE_DIM; ++f) z += bf2f(ef[(size_t)e * DE_DIM + f]) * bf2f(w_rad[f * C_DIM + ch]);
        const float r = z / (1.f + __expf(-z));
        float a4[4] = {0.f, 0.f, 0.f, 0.f};
        for (int ii = 0; ii < 4; ++ii) {
            const float xv = bf2f(x[((size_t)src[e] * C_DIM + ch) * M_DIM + ii]) * r;
            for (int o = 0; o < 4; ++o) {
                float wv = 0.f;
                for (int f = 0; f < 4; ++f) wv += bf2f(cg[(ii * 4 + f) * 4 + o]) * bf2f(Y[(size_t)e * 4 + f]);
                a4[o] += xv * wv;
            }
        }
        for (int o = 0; o < 4; ++o) atomicAdd(&agg[((size_t)dst[e] * C_DIM + ch) * M_DIM + o], a4[o]);
    }
}
__global__ void kf_mix(const float* __restrict__ agg, const unsigned short* __restrict__ w_mix, unsigned short* __restrict__ out, int N) {
    const int d = threadIdx.x;
    for (int n = blockIdx.x; n < N; n += gridDim.x) {
        float a4[4] = {0.f, 0.f, 0.f, 0.f};
        for (int c = 0; c < C_DIM; ++c) {
            const float wv = bf2f(w_mix[c * C_DIM + d]);
            for (int m = 0; m < 4; ++m) a4[m] += bf2f(f2bf(agg[((size_t)n * C_DIM + c) * M_DIM + m])) * wv;
        }
        for (int m = 0; m < 4; ++m) out[((size_t)n * C_DIM + d) * M_DIM + m] = f2bf(a4[m]);
    }
}

extern "C" void kernel_launch(void* const* d_in, const int* in_sizes, int n_in,
                              void* d_out, int out_size, void* d_ws, size_t ws_size,
                              hipStream_t stream) {
    const unsigned short* x = (const unsigned short*)d_in[0];
    const int* src = (const int*)d_in[1];
    const int* dst = (const int*)d_in[2];
    const unsigned short* Y = (const unsigned short*)d_in[3];
    const unsigned short* ef = (const unsigned short*)d_in[4];
    const unsigned short* w_rad = (const unsigned short*)d_in[5];
    const unsigned short* cg = (const unsigned short*)d_in[6];
    const unsigned short* w_mix = (const unsigned short*)d_in[7];
    unsigned short* out = (unsigned short*)d_out;
    const int N = in_sizes[0] / (C_DIM * M_DIM);
    const int E = in_sizes[1];
    const int epb = (E + 255) / 256;
    const int nh = (N + 1) / 2;
    u64 hi = (u64)(uintptr_t)d_out | (u64)(uintptr_t)d_ws;
    for (int i = 0; i < 8; ++i) hi |= (u64)(uintptr_t)d_in[i];
    const bool fast = (hi >> 48) == 0 && N >= 256 && N <= NB_MAX * NPB_MAX && E > 0 && epb <= EPB_MAX && ws_size >= (size_t)W_END;
    if (!fast) {
        float* agg = (float*)d_ws;
        if (N <= 0) return;
        kf_zero<<<1024, 256, 0, stream>>>(agg, (size_t)N * C_DIM * M_DIM);
        if (E > 0) kf_edges<<<4096, C_DIM, 0, stream>>>(x, src, dst, Y, ef, w_rad, cg, agg, E);
        kf_mix<<<2048, C_DIM, 0, stream>>>(agg, w_mix, out, N);
        return;
    }
    int k = 0;
    while ((N << k) <= 32768) ++k;
    const unsigned magic = (unsigned)(((1ull << (39 - k)) + N - 1) / N);
    const unsigned F = 0x00020000u;
    k_fused<<<256, 512, 0, stream>>>(dst, E * 4u, F, src, E * 4u, F, Y, E * 8u, F, d_ws, W_END, F, ef, E * 32u, F, x, N * 1024u, F, out, N * 1024u, F, w_rad, DE_DIM * C_DIM * 2u, F,
                                     cg, 128u, F, w_mix, C_DIM * C_DIM * 2u, F, N, E, epb, magic, k, nh * 1024u, nh, 0u);
}
```

```cpp
#include <hip/hip_runtime.h>
#include <hip/hip_bf16.h>
#include <stdint.h>

typedef __attribute__((ext_vector_type(4))) unsigned u32x4;
typedef unsigned long long u64;

#define C_DIM 128
#define M_DIM 4
#define DE_DIM 16
#define NPB_MAX 80
#define NB_MAX 128
#define EPB_MAX 1280
#define OOB_OFF 0x80000000u
#define SC1 16
#define SPIN_MAX (1u << 22)
#define W_FLG 4096
#define W_AGG 5804032
#define W_END 16289792
#define L_LST 5120
#define L_TOTAL 156672
#define SY_C3 56

static __device__ __forceinline__ int lane_opaque() {
    int l;
    asm volatile("v_mbcnt_lo_u32_b32 %0, -1, 0\n\tv_mbcnt_hi_u32_b32 %0, -1, %0" : "=v"(l));
    return l;
}

static __device__ __forceinline__ float bf2f(unsigned short v) { return __uint_as_float(((unsigned)v) << 16); }
typedef __bf16 bf16v2 __attribute__((ext_vector_type(2)));
static __device__ __forceinline__ unsigned pack2bf(float lo, float hi) {
    bf16v2 r = (bf16v2){(__bf16)lo, (__bf16)hi};
    return __builtin_bit_cast(unsigned, r);
}
static __device__ __forceinline__ unsigned short f2bf(float v) { return (unsigned short)(pack2bf(v, 0.f) & 0xffffu); }
static __device__ __forceinline__ void flag_store(const void* p, unsigned v) {
    unsigned t0, t1; u64 sv;
    asm volatile("v_mov_b32 %0, 0\n\tv_mov_b32 %1, %4\n\ts_mov_b64 %2, exec\n\ts_mov_b64 exec, 1\n\t"
                 "global_store_dword %0, %1, %3 sc1\n\ts_mov_b64 exec, %2" : "=&v"(t0), "=&v"(t1), "=&s"(sv) : "s"(p), "s"(v) : "memory");
}
static __device__ __forceinline__ unsigned lds_arrive_rtn(unsigned a) {
    unsigned t0, t1; u64 sv;
    asm volatile("v_mov_b32 %0, %3\n\tv_mov_b32 %1, 1\n\ts_mov_b64 %2, exec\n\ts_mov_b64 exec, 1\n\t"
                 "ds_add_rtn_u32 %1, %0, %1\n\ts_mov_b64 exec, %2\n\ts_waitcnt lgkmcnt(0)" : "=&v"(t0), "=&v"(t1), "=&s"(sv) : "s"(a) : "memory");
    return (unsigned)__builtin_amdgcn_readfirstlane((int)t1);
}
template <int SLEEP = 0> static __device__ __forceinline__ void lds_wait(unsigned a, unsigned n) {
    for (;;) {
        unsigned v;
        asm volatile("ds_read_b32 %0, %1\n\ts_waitcnt lgkmcnt(0)" : "=v"(v) : "v"(a) : "memory");
        if ((unsigned)__builtin_amdgcn_readfirstlane((int)v) >= n) break;
        if (SLEEP) __builtin_amdgcn_s_sleep(SLEEP);
    }
}
static __device__ __forceinline__ unsigned ld_agent(const void* p) { return __hip_atomic_load((const unsigned*)p, __ATOMIC_RELAXED, __HIP_MEMORY_SCOPE_AGENT); }
static __device__ __forceinline__ void poll_tag(const void* p, unsigned tag) {
#pragma unroll 1
    for (unsigned spin = 0; spin < SPIN_MAX; ++spin) {
        if (__all(ld_agent(p) == tag)) break;
        __builtin_amdgcn_s_sleep(2);
    }
    __builtin_amdgcn_fence(__ATOMIC_ACQUIRE, "wavefront");
}

#define BUF(t, n) t n, unsigned n##_bytes, unsigned n##_fmt
__global__ __launch_bounds__(512, 2) void k_fused(
    BUF(const void*, dst_), BUF(const void*, src_), BUF(const void*, Y_), BUF(void*, ws_), BUF(const void*, ef_), BUF(const void*, x_), BUF(void*, out_), BUF(const void*, w_rad_),
    BUF(const void*, cg_), BUF(const void*, w_mix_), unsigned N_, unsigned E_, unsigned epb_, unsigned magic_, unsigned shift_, unsigned nhb_, unsigned nh_, unsigned zero_)
{
    __shared__ __attribute__((aligned(256))) unsigned char smem[L_TOTAL];
    const __attribute__((address_space(4))) u64* ka = (const __attribute__((address_space(4))) u64*)__builtin_amdgcn_kernarg_segment_ptr();
    const int bx = blockIdx.x;
    unsigned slow, tag, wq, tid = threadIdx.x;
    asm volatile(
            "s_load_dwordx16 s[8:23], s[0:1], 0\n"
            "s_load_dwordx16 s[40:55], s[0:1], 128\n"
            "s_load_dwordx16 s[24:39], s[0:1], 64\n"
            "s_getpc_b64 s[4:5]\n"
            "v_readfirstlane_b32 s3, v0\n"
            "v_and_b32 v243, 63, v0\n"
            "s_setprio 3\n"
            "s_lshr_b32 s3, s3, 6\n"
            "v_mul_u32_u24 v54, 3, v243\n"
            "s_cmp_lg_u32 s3, 4\n"
            "s_cbranch_scc1 Lncp_%=\n"
            "s_and_b32 s4, s4, 0xffffff80\n"
            "s_movk_i32 s6, 17408\n"
            "s_mov_b32 s7, 0x20000\n"
            "s_lshr_b32 s68, s2, 3\n"
            "s_lshl_b32 s68, s68, 7\n"
            "v_lshlrev_b32 v81, 12, v243\n"
            "buffer_load_dword v53, v81, s[4:7], s68 offen\n"
            "Lncp_%=:\n"
            "s_mul_i32 s68, s3, 162\n"
            "v_cmp_gt_u32 vcc, 54, v243\n"
            "v_add_u32 v54, s68, v54\n"
            "v_mov_b32 v82, 0x1000000\n"
            "v_cndmask_b32 v54, v82, v54, vcc\n"
            "v_mov_b32 v219, 0\n"
            "s_waitcnt lgkmcnt(0)\n"
            "s_mul_i32 s95, s2, s50\n"
            "v_add_lshl_u32 v81, v54, s95, 2\n"
            "buffer_load_dwordx3 v[32:34], v81, s[8:11], 0 offen nt\n"
            "buffer_load_dwordx3 v[36:38], v81, s[12:15], 0 offen nt\n"
            "v_lshlrev_b32 v82, 1, v81\n"
            "buffer_load_dword v40, off, s[20:23], 0\n"
            "buffer_load_dwordx4 v[42:45], v82, s[16:19], 0 offen nt\n"
            "buffer_load_dwordx2 v[46:47], v82, s[16:19], 0 offen offset:16 nt\n"
            "v_lshlrev_b32 v193, 7, v243\n"
            "s_lshl_b32 s54, s95, 5\n"
            "s_bfe_u32 s8, s2, 0x10002\n"
            "s_and_b32 s9, s2, 3\n"
            "s_lshl_b32 s9, s9, 5\n"
            "s_lshr_b32 s68, s2, 3\n"
            "s_add_u32 s9, s9, s68\n"
            "s_mul_i32 s55, s8, s53\n"
            "s_lshl_b32 s69, s9, 13\n"
            "s_add_u32 s69, s69, s55\n"
            "s_cmp_lt_u32 s3, 4\n"
            "s_cbranch_scc1 Lx0_%=\n"
            "v_lshlrev_b32 v81, 4, v0\n"
            "v_and_b32 v81, 0xe00, v81\n"
            "v_and_b32 v82, 31, v0\n"
            "v_lshl_or_b32 v81, v82, 3, v81\n"
            "buffer_load_dwordx2 v[48:49], v81, s[36:39], 0 offen\n"
            "buffer_load_dwordx2 v[50:51], v81, s[36:39], 0 offen offset:256\n"
            "s_cmp_eq_u32 s3, 4\n"
            "s_cbranch_scc1 Lx4_%=\n"
            "s_sub_u32 s68, 7, s3\n"
            "s_cmp_lt_u32 s68, 3\n"
            "s_cselect_b32 s68, s68, 0x20000\n"
            "s_lshl_b32 s68, s68, 13\n"
            "s_add_u32 s68, s68, s54\n"
            "buffer_load_dword v52, v193, s[24:27], s68 offen\n"
            "s_branch Lx1_%=\n"
            "Lx4_%=:\n"
            "v_lshlrev_b32 v81, 2, v243\n"
            "buffer_load_dword v52, v81, s[40:43], 0 offen\n"
            "s_branch Lx1_%=\n"
            "Lx0_%=:\n"
            "s_add_u32 s68, s3, 3\n"
            "s_cmp_lt_u32 s68, 3\n"
            "s_cselect_b32 s68, s68, 0x20000\n"
            "s_lshl_b32 s68, s68, 13\n"
            "s_add_u32 s68, s68, s54\n"
            "buffer_load_dword v52, v193, s[24:27], s68 offen\n"
            "s_and_b32 s68, s9, 31\n"
            "s_lshl_b32 s68, s68, 7\n"
            "s_cmp_eq_u32 s3, 3\n"
            "s_cselect_b32 s68, s68, 0x40000000\n"
            "v_lshlrev_b32 v81, 5, v193\n"
            "buffer_load_dword v48, v81, s[44:47], s68 offen\n"
            "buffer_load_dword v49, off, s[20:23], 0\n"
            "Lx1_%=:\n"
            "v_mov_b64 v[82:83], 0\n"
            "v_mov_b64 v[84:85], 0\n"
            "v_lshlrev_b32 v81, 4, v0\n"
            "s_cmp_lt_u32 s3, 4\n"
            "s_cselect_b32 s68, 0, 72704\n"
            "v_add_u32 v86, s68, v81\n"
            "ds_write_b128 v86, v[82:85]\n"
            "s_cselect_b32 s68, 80896, 0\n"
            "v_add_u32 v86, s68, v81\n"
            "ds_write_b128 v86, v[82:85]\n"
            "v_and_b32 v196, 15, v0\n"
            "v_mov_b32 v209, 1\n"
            "s_cmp_lg_u32 s3, 0\n"
            "s_cbranch_scc1 Lnk_%=\n"
            "v_lshlrev_b32 v81, 2, v243\n"
            "v_sub_u32 v82, 127, v243\n"
            "ds_write_b32 v81, v82 offset:96\n"
            "v_lshlrev_b32 v81, 2, v196\n"
            "v_sub_u32 v82, 63, v196\n"
            "ds_write_b32 v81, v82 offset:352\n"
            "v_bfrev_b32 v82, 1\n"
            "ds_write2_b32 v219, v82, v82 offset0:20 offset1:21\n"
            "Lnk_%=:\n"
            "s_sub_i32 s96, s49, s95\n"
            "s_min_i32 s96, s96, s50\n"
            "s_max_i32 s96, s96, 0\n"
            "s_and_b32 s12, s3, 3\n"
            "s_lshl_b32 s12, s12, 3\n"
            "s_lshl_b32 s68, 1, s12\n"
            "v_mov_b32 v80, s68\n"
            "s_sub_u32 s68, s68, 1\n"
            "s_and_b32 s68, s68, 0x10101010\n"
            "s_cmp_lt_u32 s3, 4\n"
            "s_cselect_b32 s13, s68, 0x10101010\n"
            "s_cselect_b32 s14, 0, s68\n"
            "s_mov_b32 s39, 0x10101010\n"
            "v_cmp_gt_u32 s[72:73], s96, v54\n"
            "v_lshlrev_b32 v77, 10, v54\n"
            "v_add_u32 v81, 1, v54\n"
            "v_cmp_gt_u32 s[74:75], s96, v81\n"
            "v_lshlrev_b32 v78, 10, v81\n"
            "v_add_u32 v81, 2, v54\n"
            "v_cmp_gt_u32 s[76:77], s96, v81\n"
            "v_lshlrev_b32 v79, 10, v81\n"
            "s_mul_i32 s10, s9, s48\n"
            "s_add_u32 s10, s10, 127\n"
            "s_add_u32 s11, s10, s48\n"
            "s_lshr_b32 s10, s10, 7\n"
            "s_lshr_b32 s11, s11, 7\n"
            "s_sub_u32 s11, s11, s10\n"
            "v_lshlrev_b32 v192, 4, v243\n"
            "v_lshlrev_b32 v194, 3, v243\n"
            "v_lshrrev_b32 v195, 4, v243\n"
            "v_lshlrev_b32 v214, 4, v196\n"
            "v_and_b32 v249, 3, v0\n"
            "v_lshlrev_b32 v250, 4, v249\n"
            "v_mov_b32 v251, 80\n"
            "v_lshrrev_b32 v83, 2, v196\n"
            "s_lshl_b32 s68, s3, 9\n"
            "s_add_u32 s68, s68, 62464\n"
            "v_add_u32 v212, s68, v194\n"
            "v_lshl_add_u32 v213, v195, 7, s68\n"
            "v_lshl_add_u32 v213, v249, 5, v213\n"
            "v_lshl_add_u32 v213, v83, 3, v213\n"
            "v_mov_b32 v224, 0x3f80\n"
            "v_mov_b32 v225, 0\n"
            "v_lshlrev_b64 v[224:225], v250, v[224:225]\n"
            "s_mul_i32 s68, s3, 4352\n"
            "s_sub_u32 s38, s3, 3\n"
            "s_max_i32 s38, s38, 0\n"
            "s_lshl_b32 s38, s38, 12\n"
            "s_add_u32 s68, s68, s38\n"
            "s_add_u32 s68, s68, 72704\n"
            "v_mov_b32 v81, s68\n"
            "v_add_u32 v231, s68, v192\n"
            "v_add_u32 v239, 0xfffff000, v231\n"
            "v_mov_b32 v236, v231\n"
            "s_add_u32 s38, s3, 3\n"
            "s_and_b32 s38, s38, 3\n"
            "s_lshl2_add_u32 s41, s38, 16\n"
            "s_mul_i32 s38, s38, 8448\n"
            "s_add_u32 s38, s38, 90112\n"
            "v_add_u32 v252, s38, v192\n"
            "s_movk_i32 s38, 272\n"
            "v_mad_u32_u24 v227, v196, s38, v81\n"
            "v_lshl_add_u32 v227, v195, 4, v227\n"
            "s_movk_i32 s38, 1088\n"
            "v_mad_u32_u24 v226, v195, s38, v81\n"
            "v_lshl_add_u32 v226, v196, 3, v226\n"
            "v_add_u32 v228, 123904, v192\n"
            "v_and_b32 v253, 1, v195\n"
            "v_lshl_add_u32 v253, v196, 1, v253\n"
            "v_lshlrev_b32 v253, 2, v253\n"
            "v_and_b32 v229, 1, v0\n"
            "v_lshlrev_b32 v229, 4, v229\n"
            "v_lshlrev_b32 v81, 25, v0\n"
            "v_and_b32 v81, 2.0, v81\n"
            "v_or_b32 v229, v229, v81\n"
            "v_bfe_u32 v206, v0, 1, 2\n"
            "v_lshlrev_b32 v207, 4, v206\n"
            "v_bfe_u32 v83, v0, 3, 2\n"
            "s_mul_i32 s68, s3, 768\n"
            "s_add_u32 s68, s68, 66560\n"
            "v_add_u32 v247, s68, v194\n"
            "v_lshl_add_u32 v81, v249, 3, s68\n"
            "v_lshl_add_u32 v237, v195, 1, v81\n"
            "v_add_u32 v237, 32, v237\n"
            "v_lshl_add_u32 v81, v206, 3, s68\n"
            "v_lshl_add_u32 v238, v83, 1, v81\n"
            "v_add_u32 v238, 64, v238\n"
            "v_and_b32 v81, 7, v0\n"
            "v_lshlrev_b32 v82, 2, v81\n"
            "s_mov_b32 s68, 1982153488\n"
            "v_bfe_u32 v240, s68, v82, 4\n"
            "v_lshlrev_b32 v240, 4, v240\n"
            "v_add_u32 v240, 3328, v240\n"
            "s_mov_b32 s68, 2205745445\n"
            "v_bfe_u32 v241, s68, v82, 4\n"
            "v_lshlrev_b32 v241, 4, v241\n"
            "v_add_u32 v241, 3072, v241\n"
            "s_mov_b32 s68, 2305097727\n"
            "v_bfe_u32 v242, s68, v82, 4\n"
            "v_lshlrev_b32 v242, 4, v242\n"
            "v_add_u32 v242, 3072, v242\n"
            "v_cmp_lt_u32 vcc, 5, v81\n"
            "v_add_u32 v82, 256, v242\n"
            "s_nop 0\n"
            "v_cndmask_b32 v242, v242, v82, vcc\n"
            "v_cmp_lt_u32 vcc, 3, v81\n"
            "s_nop 1\n"
            "v_cndmask_b32 v242, 8, v242, vcc\n"
            "s_mov_b32 s68, 1998716928\n"
            "v_lshlrev_b32 v82, 2, v81\n"
            "v_bfe_u32 v208, s68, v82, 4\n"
            "s_add_u32 s38, s3, 1\n"
            "s_and_b32 s38, s38, 3\n"
            "s_add_u32 s38, s38, 4\n"
            "v_readlane_b32 s64, v240, s3\n"
            "v_readlane_b32 s65, v241, s3\n"
            "v_readlane_b32 s66, v242, s3\n"
            "v_readlane_b32 s40, v242, s38\n"
            "v_lshl_add_u32 v244, v195, 2, s64\n"
            "v_lshl_add_u32 v245, v195, 2, s65\n"
            "v_lshl_add_u32 v246, v195, 2, s66\n"
            "v_lshl_add_u32 v248, v83, 2, s64\n"
            "s_add_u32 s64, s64, 512\n"
            "s_add_u32 s65, s65, 512\n"
            "s_add_u32 s66, s66, 512\n"
            "s_add_u32 s40, s40, 512\n"
            "s_lshl_b32 s68, s2, 7\n"
            "s_add_u32 s38, s68, 4096\n"
            "s_add_u32 s56, s20, s38\n"
            "s_addc_u32 s57, s21, 0\n"
            "s_xor_b32 s38, s38, 512\n"
            "s_add_u32 s58, s20, s38\n"
            "s_addc_u32 s59, s21, 0\n"
            "s_cmp_lt_u32 s3, 6\n"
            "s_mov_b32 s60, 78\n"
            "s_cmov_b32 s60, 78\n"
            "s_mov_b32 s61, 4294961748\n"
            "s_cmov_b32 s61, 4294961748\n"
            "s_movk_i32 s62, 0x3862\n"
            "s_cmov_b32 s62, 0x9062\n"
            "s_cmp_lt_u32 s3, 4\n"
            "s_cmov_b32 s60, 590\n"
            "s_cmov_b32 s61, 4294612224\n"
            "s_cmovk_i32 s62, 0x82\n"
            "s_mov_b32 s63, 5804032\n"
            "s_and_b32 s68, s9, 127\n"
            "s_lshl3_add_u32 s68, s68, s3\n"
            "s_mul_i32 s68, s68, 512\n"
            "s_add_u32 s67, s68, s55\n"
            "s_add_u32 s67, s67, 3145728\n"
            "s_lshr_b32 s38, s3, 2\n"
            "s_lshl_b32 s38, s38, 10\n"
            "s_lshl_b32 s15, s3, 9\n"
            "s_add_u32 s15, s15, 25600\n"
            "v_add_u32 v94, s15, v194\n"
            "s_lshr_b32 s68, s38, 1\n"
            "s_sub_u32 s15, s15, s68\n"
            "s_and_b32 s68, s3, 1\n"
            "s_lshl1_add_u32 s68, s2, s68\n"
            "s_lshl_b32 s68, s68, 10\n"
            "s_add_u32 s36, s68, 36864\n"
            "v_mov_b32 v93, 335549440\n"
            "v_lshlrev_b32 v95, 4, v0\n"
            "s_mul_i32 s68, s2, 20480\n"
            "s_add_u32 s68, s68, 557056\n"
            "s_add_u32 s4, s20, s68\n"
            "s_addc_u32 s5, s21, 0\n"
            "s_lshl_b32 s6, s96, 4\n"
            "s_add_u32 s6, s6, 4223\n"
            "s_and_b32 s6, s6, 0xffffff80\n"
            "s_mov_b32 s7, 0x20000\n"
            "v_mov_b64 v[0:1], 0\n"
            "v_mov_b64 v[2:3], 0\n"
            "v_mov_b64 v[4:5], 0\n"
            "v_mov_b64 v[6:7], 0\n"
            "v_mov_b64 v[8:9], 0\n"
            "v_mov_b64 v[10:11], 0\n"
            "v_mov_b64 v[12:13], 0\n"
            "v_mov_b64 v[14:15], 0\n"
            "v_mov_b64 v[16:17], 0\n"
            "v_mov_b64 v[18:19], 0\n"
            "v_mov_b64 v[20:21], 0\n"
            "v_mov_b64 v[22:23], 0\n"
            "v_mov_b64 v[24:25], 0\n"
            "v_mov_b64 v[26:27], 0\n"
            "v_mov_b64 v[28:29], 0\n"
            "v_mov_b64 v[30:31], 0\n"
            "s_waitcnt lgkmcnt(0)\n"
            "s_barrier\n"
            "s_waitcnt vmcnt(6)\n"
            "v_lshlrev_b32 v81, s52, v32\n"
            "v_lshlrev_b32 v84, s52, v36\n"
            "v_lshlrev_b32 v82, s52, v33\n"
            "v_lshlrev_b32 v85, s52, v37\n"
            "v_lshlrev_b32 v83, s52, v34\n"
            "v_lshlrev_b32 v86, s52, v38\n"
            "v_mul_hi_u32_u24 v81, s51, v81\n"
            "v_mul_hi_u32_u24 v84, s51, v84\n"
            "v_lshl_add_u32 v68, v81, 2, s38\n"
            "v_and_b32 v81, 64, v84\n"
            "v_lshl_add_u32 v68, v81, 3, v68\n"
            "s_mov_b64 exec, s[72:73]\n"
            "ds_add_rtn_u32 v71, v68, v80 offset:3072\n"
            "s_mov_b64 exec, -1\n"
            "v_mul_hi_u32_u24 v82, s51, v82\n"
            "v_mul_hi_u32_u24 v85, s51, v85\n"
            "v_lshl_add_u32 v69, v82, 2, s38\n"
            "v_and_b32 v82, 64, v85\n"
            "v_lshl_add_u32 v69, v82, 3, v69\n"
            "s_mov_b64 exec, s[74:75]\n"
            "ds_add_rtn_u32 v72, v69, v80 offset:3072\n"
            "s_mov_b64 exec, -1\n"
            "v_mul_hi_u32_u24 v83, s51, v83\n"
            "v_mul_hi_u32_u24 v86, s51, v86\n"
            "v_lshl_add_u32 v70, v83, 2, s38\n"
            "v_and_b32 v83, 64, v86\n"
            "v_lshl_add_u32 v70, v83, 3, v70\n"
            "s_mov_b64 exec, s[76:77]\n"
            "ds_add_rtn_u32 v73, v70, v80 offset:3072\n"
            "s_mov_b64 exec, -1\n"
            "v_bfe_u32 v84, v84, 3, 3\n"
            "v_and_b32 v81, 127, v32\n"
            "v_lshl_or_b32 v81, v81, 3, v84\n"
            "v_bfe_u32 v85, v85, 3, 3\n"
            "v_and_b32 v82, 127, v33\n"
            "v_lshl_or_b32 v82, v82, 3, v85\n"
            "v_bfe_u32 v86, v86, 3, 3\n"
            "v_and_b32 v83, 127, v34\n"
            "v_lshl_or_b32 v83, v83, 3, v86\n"
            "s_waitcnt vmcnt(5)\n"
            "v_readfirstlane_b32 s90, v40\n"
            "s_mul_i32 s90, s90, 1664525\n"
            "s_add_u32 s90, s90, 1013904223\n"
            "s_lshl_b32 s93, s90, 14\n"
            "s_and_b32 s94, s90, 0xffe00000\n"
            "s_waitcnt lgkmcnt(0)\n"
            "s_barrier\n"
            "ds_read_b128 v[128:131], v192 offset:3072\n"
            "ds_read_b128 v[132:135], v192 offset:4096\n"
            "v_or3_b32 v59, v81, v77, s94\n"
            "v_or_b32 v57, s93, v36\n"
            "v_lshrrev_b32 v74, 1, v68\n"
            "v_add_u32 v74, s15, v74\n"
            "v_bfe_u32 v71, v71, s12, 8\n"
            "v_or3_b32 v63, v82, v78, s94\n"
            "v_or_b32 v61, s93, v37\n"
            "v_lshrrev_b32 v75, 1, v69\n"
            "v_add_u32 v75, s15, v75\n"
            "v_bfe_u32 v72, v72, s12, 8\n"
            "v_or3_b32 v67, v83, v79, s94\n"
            "v_or_b32 v65, s93, v38\n"
            "v_lshrrev_b32 v76, 1, v70\n"
            "v_add_u32 v76, s15, v76\n"
            "v_bfe_u32 v73, v73, s12, 8\n"
            "s_waitcnt lgkmcnt(1)\n"
            "v_dot4_u32_u8 v136, v128, s39, 0\n"
            "v_dot4_u32_u8 v137, v129, s39, 0\n"
            "v_dot4_u32_u8 v138, v130, s39, 0\n"
            "v_dot4_u32_u8 v139, v131, s39, 0\n"
            "s_waitcnt lgkmcnt(0)\n"
            "v_dot4_u32_u8 v136, v132, s39, v136\n"
            "v_dot4_u32_u8 v137, v133, s39, v137\n"
            "v_dot4_u32_u8 v138, v134, s39, v138\n"
            "v_dot4_u32_u8 v139, v135, s39, v139\n"
            "v_dot4_u32_u8 v140, v128, s13, 0\n"
            "v_dot4_u32_u8 v141, v129, s13, v136\n"
            "v_dot4_u32_u8 v142, v130, s13, 0\n"
            "v_dot4_u32_u8 v143, v131, s13, v138\n"
            "v_add_u32 v87, v136, v137\n"
            "v_add3_u32 v88, v87, v138, v139\n"
            "v_dot4_u32_u8 v140, v132, s14, v140\n"
            "v_dot4_u32_u8 v141, v133, s14, v141\n"
            "s_mov_b32 s68, 0x10001\n"
            "v_add_u32_dpp v89, v88, v88 row_shr:1 row_mask:0xf bank_mask:0xf bound_ctrl:1\n"
            "v_dot4_u32_u8 v142, v134, s14, v142\n"
            "v_dot4_u32_u8 v143, v135, s14, v143\n"
            "v_add_u32_dpp v89, v89, v89 row_shr:2 row_mask:0xf bank_mask:0xf bound_ctrl:1\n"
            "v_lshl_add_u32 v140, v141, 16, v140\n"
            "v_mad_u32_u24 v90, v87, s68, v93\n"
            "v_add_u32_dpp v89, v89, v89 row_shr:4 row_mask:0xf bank_mask:0xf bound_ctrl:1\n"
            "v_add_u32 v140, v140, v93\n"
            "v_lshl_add_u32 v141, v143, 16, v142\n"
            "v_add_u32_dpp v89, v89, v89 row_shr:8 row_mask:0xf bank_mask:0xf bound_ctrl:1\n"
            "v_add_u32 v141, v141, v90\n"
            "s_and_b32 s38, s3, 6\n"
            "v_add_u32_dpp v89, v89, v89 row_bcast:15 row_mask:0xa bank_mask:0xf\n"
            "s_cmp_lg_u32 s38, 4\n"
            "s_nop 0\n"
            "v_add_u32_dpp v89, v89, v89 row_bcast:31 row_mask:0xc bank_mask:0xf\n"
            "v_sub_u32 v89, v89, v88\n"
            "v_mad_u32_u24 v140, v89, s68, v140\n"
            "v_mad_u32_u24 v141, v89, s68, v141\n"
            "ds_write_b64 v94, v[140:141]\n"
            "ds_read_u16 v74, v74\n"
            "ds_read_u16 v75, v75\n"
            "ds_read_u16 v76, v76\n"
            "s_cbranch_scc1 Lnt_%=\n"
            "s_bitcmp1_b32 s3, 0\n"
            "s_cbranch_scc0 Lt0_%=\n"
            "v_add_u32 v89, v89, v87\n"
            "v_mov_b64 v[136:137], v[138:139]\n"
            "Lt0_%=:\n"
            "v_lshl_add_u32 v128, v136, 16, v89\n"
            "v_add_u32 v89, v89, v136\n"
            "v_lshl_add_u32 v130, v137, 16, v89\n"
            "v_mov_b32 v129, s90\n"
            "v_mov_b32 v131, s90\n"
            "buffer_store_dwordx4 v[128:131], v192, s[20:23], s36 offen sc1\n"
            "s_waitcnt vmcnt(4)\n"
            "s_branch Lt1_%=\n"
            "Lnt_%=:\n"
            "s_waitcnt vmcnt(3)\n"
            "Lt1_%=:\n"
            "v_mov_b32 v56, v42\n"
            "v_mov_b32 v58, v43\n"
            "v_mov_b32 v60, v44\n"
            "v_mov_b32 v62, v45\n"
            "v_mov_b32 v64, v46\n"
            "v_mov_b32 v66, v47\n"
            "s_waitcnt lgkmcnt(0)\n"
            "v_lshl_add_u32 v74, v71, 4, v74\n"
            "v_lshl_add_u32 v75, v72, 4, v75\n"
            "v_lshl_add_u32 v76, v73, 4, v76\n"
            "s_mov_b64 exec, s[72:73]\n"
            "ds_write_b128 v74, v[56:59]\n"
            "s_mov_b64 exec, s[74:75]\n"
            "ds_write_b128 v75, v[60:63]\n"
            "s_mov_b64 exec, s[76:77]\n"
            "ds_write_b128 v76, v[64:67]\n"
            "s_mov_b64 exec, -1\n"
            "s_cmp_lt_u32 s3, 4\n"
            "s_waitcnt lgkmcnt(0)\n"
            "s_barrier\n"
            "s_cbranch_scc1 Lcol_%=\n"
            "ds_read_b128 v[56:59], v95 offset:1024\n"
            "ds_read_b128 v[60:63], v95 offset:5120\n"
            "ds_read_b128 v[64:67], v95 offset:9216\n"
            "ds_read_b128 v[68:71], v95 offset:13312\n"
            "ds_read_b128 v[72:75], v95 offset:17408\n"
            "s_movk_i32 s72, 4096\n"
            "s_movk_i32 s73, 8192\n"
            "s_movk_i32 s74, 12288\n"
            "s_movk_i32 s75, 16384\n"
            "s_waitcnt lgkmcnt(4)\n"
            "buffer_store_dwordx4 v[56:59], v95, s[4:7], 0 offen sc1\n"
            "s_waitcnt lgkmcnt(3)\n"
            "buffer_store_dwordx4 v[60:63], v95, s[4:7], s72 offen sc1\n"
            "s_waitcnt lgkmcnt(2)\n"
            "buffer_store_dwordx4 v[64:67], v95, s[4:7], s73 offen sc1\n"
            "s_waitcnt lgkmcnt(1)\n"
            "buffer_store_dwordx4 v[68:71], v95, s[4:7], s74 offen sc1\n"
            "s_waitcnt lgkmcnt(0)\n"
            "buffer_store_dwordx4 v[72:75], v95, s[4:7], s75 offen sc1\n"
            "s_setprio 2\n"
            "s_and_b32 s68, s3, 2\n"
            "s_lshl_b32 s68, s68, 13\n"
            "s_and_b32 s76, s3, 1\n"
            "s_lshl_b32 s76, s76, 12\n"
            "s_add_u32 s68, s68, s76\n"
            "v_lshl_add_u32 v81, v195, 10, s68\n"
            "v_add_u32 v81, v81, v214\n"
            "buffer_load_dwordx4 v[128:131], v81, s[44:47], 0 offen offset:0\n"
            "buffer_load_dwordx4 v[132:135], v81, s[44:47], 0 offen offset:256\n"
            "buffer_load_dwordx4 v[136:139], v81, s[44:47], s73 offen offset:0\n"
            "buffer_load_dwordx4 v[140:143], v81, s[44:47], s73 offen offset:256\n"
            "buffer_load_dwordx4 v[144:147], v81, s[44:47], 0 offen offset:512\n"
            "buffer_load_dwordx4 v[148:151], v81, s[44:47], 0 offen offset:768\n"
            "buffer_load_dwordx4 v[152:155], v81, s[44:47], s73 offen offset:512\n"
            "buffer_load_dwordx4 v[156:159], v81, s[44:47], s73 offen offset:768\n"
            "v_lshlrev_b32 v81, 4, v95\n"
            "v_and_b32 v81, 0x1800, v81\n"
            "v_lshrrev_b32 v82, 3, v95\n"
            "v_and_b32 v82, 0x100, v82\n"
            "v_lshlrev_b32 v83, 1, v95\n"
            "v_and_b32 v83, 0xe0, v83\n"
            "v_lshrrev_b32 v84, 7, v95\n"
            "v_and_b32 v84, 12, v84\n"
            "v_or3_b32 v81, v81, v82, v83\n"
            "v_add_u32 v84, 115456, v84\n"
            "v_add_u32 v81, v81, v84\n"
            "s_mov_b32 s78, 0xbfb8aa3b\n"
            "s_waitcnt vmcnt(14)\n"
            "v_lshlrev_b32 v160, 16, v48\n"
            "v_lshlrev_b32 v161, 16, v50\n"
            "v_mul_f32 v160, s78, v160\n"
            "v_mul_f32 v161, s78, v161\n"
            "v_cvt_pk_bf16_f32 v164, v160, v161\n"
            "v_lshlrev_b32 v162, 16, v164\n"
            "v_and_b32 v163, 0xffff0000, v164\n"
            "v_sub_f32 v160, v160, v162\n"
            "v_sub_f32 v161, v161, v163\n"
            "v_cvt_pk_bf16_f32 v165, v160, v161\n"
            "ds_write_b32 v81, v164 offset:0\n"
            "ds_write_b32 v81, v165 offset:512\n"
            "v_and_b32 v160, 0xffff0000, v48\n"
            "v_and_b32 v161, 0xffff0000, v50\n"
            "v_mul_f32 v160, s78, v160\n"
            "v_mul_f32 v161, s78, v161\n"
            "v_cvt_pk_bf16_f32 v166, v160, v161\n"
            "v_lshlrev_b32 v162, 16, v166\n"
            "v_and_b32 v163, 0xffff0000, v166\n"
            "v_sub_f32 v160, v160, v162\n"
            "v_sub_f32 v161, v161, v163\n"
            "v_cvt_pk_bf16_f32 v167, v160, v161\n"
            "ds_write_b32 v81, v166 offset:1024\n"
            "ds_write_b32 v81, v167 offset:1536\n"
            "v_lshlrev_b32 v160, 16, v49\n"
            "v_lshlrev_b32 v161, 16, v51\n"
            "v_mul_f32 v160, s78, v160\n"
            "v_mul_f32 v161, s78, v161\n"
            "v_cvt_pk_bf16_f32 v168, v160, v161\n"
            "v_lshlrev_b32 v162, 16, v168\n"
            "v_and_b32 v163, 0xffff0000, v168\n"
            "v_sub_f32 v160, v160, v162\n"
            "v_sub_f32 v161, v161, v163\n"
            "v_cvt_pk_bf16_f32 v169, v160, v161\n"
            "ds_write_b32 v81, v168 offset:16\n"
            "ds_write_b32 v81, v169 offset:528\n"
            "v_and_b32 v160, 0xffff0000, v49\n"
            "v_and_b32 v161, 0xffff0000, v51\n"
            "v_mul_f32 v160, s78, v160\n"
            "v_mul_f32 v161, s78, v161\n"
            "v_cvt_pk_bf16_f32 v170, v160, v161\n"
            "v_lshlrev_b32 v162, 16, v170\n"
            "v_and_b32 v163, 0xffff0000, v170\n"
            "v_sub_f32 v160, v160, v162\n"
            "v_sub_f32 v161, v161, v163\n"
            "v_cvt_pk_bf16_f32 v171, v160, v161\n"
            "ds_write_b32 v81, v170 offset:1040\n"
            "ds_write_b32 v81, v171 offset:1552\n"
            "s_cmp_lg_u32 s3, 4\n"
            "s_cbranch_scc1 Lncg_%=\n"
            "v_lshlrev_b32 v81, 2, v243\n"
            "s_waitcnt vmcnt(14)\n"
            "ds_write_b32 v81, v52 offset:62464\n"
            "v_lshrrev_b32 v81, 1, v243\n"
            "v_and_b32 v81, 6, v81\n"
            "v_lshl_add_u32 v81, v249, 5, v81\n"
            "ds_read_u16 v160, v81 offset:62464\n"
            "ds_read_u16 v161, v81 offset:62472\n"
            "ds_read_u16 v162, v81 offset:62480\n"
            "ds_read_u16 v163, v81 offset:62488\n"
            "s_mov_b32 s78, 0xbf317218\n"
            "s_waitcnt lgkmcnt(0)\n"
            "v_lshlrev_b32 v160, 16, v160\n"
            "v_lshlrev_b32 v161, 16, v161\n"
            "v_lshlrev_b32 v162, 16, v162\n"
            "v_lshlrev_b32 v163, 16, v163\n"
            "v_mul_f32 v160, s78, v160\n"
            "v_mul_f32 v161, s78, v161\n"
            "v_mul_f32 v162, s78, v162\n"
            "v_mul_f32 v163, s78, v163\n"
            "v_cvt_pk_bf16_f32 v168, v160, v161\n"
            "v_cvt_pk_bf16_f32 v169, v162, v163\n"
            "v_lshlrev_b32 v164, 16, v168\n"
            "v_and_b32 v165, 0xffff0000, v168\n"
            "v_lshlrev_b32 v166, 16, v169\n"
            "v_and_b32 v167, 0xffff0000, v169\n"
            "v_sub_f32 v160, v160, v164\n"
            "v_sub_f32 v161, v161, v165\n"
            "v_sub_f32 v162, v162, v166\n"
            "v_sub_f32 v163, v163, v167\n"
            "v_cvt_pk_bf16_f32 v170, v160, v161\n"
            "v_cvt_pk_bf16_f32 v171, v162, v163\n"
            "ds_write_b128 v192, v[168:171] offset:61440\n"
            "Lncg_%=:\n"
            "s_mov_b64 exec, 1\n"
            "ds_add_u32 v219, v209 offset:64\n"
            "s_mov_b64 exec, -1\n"
            "s_cmp_lg_u32 s3, 7\n"
            "s_cbranch_scc1 Lt2_%=\n"
            "s_sleep 16\n"
            "s_add_u32 s68, s69, 0\n"
            "buffer_load_dword v230, v193, s[28:31], s68 offen\n"
            "Lt2_%=:\n"
            "s_mul_i32 s68, s3, 27\n"
            "s_sub_u32 s68, s68, 108\n"
            "v_lshrrev_b32 v81, 1, v243\n"
            "v_add_u32 v81, s68, v81\n"
            "v_min_u32 v81, 79, v81\n"
            "v_lshlrev_b32 v180, 2, v81\n"
            "v_cmp_lt_u32 vcc, 39, v81\n"
            "v_and_b32 v82, 1, v243\n"
            "v_mul_u32_u24 v181, 80, v82\n"
            "v_add_u32 v82, 160, v181\n"
            "v_cndmask_b32 v181, v181, v82, vcc\n"
            "v_cndmask_b32 v82, 1, 0, vcc\n"
            "v_xor_b32 v82, s8, v82\n"
            "v_lshlrev_b32 v182, 8, v82\n"
            "v_mul_u32_u24 v183, 704, v81\n"
            "v_add_u32 v183, 5120, v183\n"
            "v_cmp_gt_u32 vcc, s11, v81\n"
            "v_add_lshl_u32 v184, v81, s10, 10\n"
            "v_bfrev_b32 v82, 1\n"
            "v_cndmask_b32 v184, v82, v184, vcc\n"
            "s_branch Lhpa_%=\n"
            "Lhp_%=:\n"
            "s_sleep 1\n"
            "Lhpa_%=:\n"
            "ds_read_b32 v220, v219 offset:64\n"
            "s_waitcnt lgkmcnt(0)\n"
            "v_readfirstlane_b32 s68, v220\n"
            "s_cmp_lt_u32 s68, 4\n"
            "s_cbranch_scc1 Lhp_%=\n"
            "v_add_u32 v81, 115456, v192\n"
            "ds_read_b128 v[96:99], v81 offset:0\n"
            "ds_read_b128 v[100:103], v81 offset:1024\n"
            "ds_read_b128 v[104:107], v81 offset:2048\n"
            "ds_read_b128 v[108:111], v81 offset:3072\n"
            "ds_read_b128 v[112:115], v81 offset:4096\n"
            "ds_read_b128 v[116:119], v81 offset:5120\n"
            "ds_read_b128 v[120:123], v81 offset:6144\n"
            "ds_read_b128 v[124:127], v81 offset:7168\n"
            "ds_read_b128 v[200:203], v192 offset:61440\n"
            "v_lshrrev_b32 v82, 2, v196\n"
            "s_lshl_b32 s68, s3, 10\n"
            "s_add_u32 s68, s68, 119808\n"
            "v_lshl_add_u32 v82, v82, 13, s68\n"
            "v_lshl_add_u32 v82, v195, 8, v82\n"
            "v_lshl_add_u32 v82, v249, 6, v82\n"
            "s_mov_b32 s78, 0x05040100\n"
            "s_mov_b32 s79, 0x07060302\n"
            "s_waitcnt vmcnt(1)\n"
            "s_cmp_eq_u32 s3, 7\n"
            "s_cbranch_scc1 Lt3_%=\n"
            "s_waitcnt vmcnt(0)\n"
            "Lt3_%=:\n"
            "v_perm_b32 v160, v132, v128, s78\n"
            "v_perm_b32 v161, v140, v136, s78\n"
            "v_perm_b32 v162, v148, v144, s78\n"
            "v_perm_b32 v163, v156, v152, s78\n"
            "ds_write_b128 v82, v[160:163] offset:0\n"
            "v_perm_b32 v164, v132, v128, s79\n"
            "v_perm_b32 v165, v140, v136, s79\n"
            "v_perm_b32 v166, v148, v144, s79\n"
            "v_perm_b32 v167, v156, v152, s79\n"
            "ds_write_b128 v82, v[164:167] offset:4096\n"
            "v_perm_b32 v168, v133, v129, s78\n"
            "v_perm_b32 v169, v141, v137, s78\n"
            "v_perm_b32 v170, v149, v145, s78\n"
            "v_perm_b32 v171, v157, v153, s78\n"
            "ds_write_b128 v82, v[168:171] offset:16\n"
            "v_perm_b32 v172, v133, v129, s79\n"
            "v_perm_b32 v173, v141, v137, s79\n"
            "v_perm_b32 v174, v149, v145, s79\n"
            "v_perm_b32 v175, v157, v153, s79\n"
            "ds_write_b128 v82, v[172:175] offset:4112\n"
            "v_perm_b32 v160, v134, v130, s78\n"
            "v_perm_b32 v161, v142, v138, s78\n"
            "v_perm_b32 v162, v150, v146, s78\n"
            "v_perm_b32 v163, v158, v154, s78\n"
            "ds_write_b128 v82, v[160:163] offset:32\n"
            "v_perm_b32 v164, v134, v130, s79\n"
            "v_perm_b32 v165, v142, v138, s79\n"
            "v_perm_b32 v166, v150, v146, s79\n"
            "v_perm_b32 v167, v158, v154, s79\n"
            "ds_write_b128 v82, v[164:167] offset:4128\n"
            "v_perm_b32 v168, v135, v131, s78\n"
            "v_perm_b32 v169, v143, v139, s78\n"
            "v_perm_b32 v170, v151, v147, s78\n"
            "v_perm_b32 v171, v159, v155, s78\n"
            "ds_write_b128 v82, v[168:171] offset:48\n"
            "v_perm_b32 v172, v135, v131, s79\n"
            "v_perm_b32 v173, v143, v139, s79\n"
            "v_perm_b32 v174, v151, v147, s79\n"
            "v_perm_b32 v175, v159, v155, s79\n"
            "ds_write_b128 v82, v[172:175] offset:4144\n"
            "s_cmp_lg_u32 s3, 7\n"
            "s_cbranch_scc1 Lsort_%=\n"
            "s_mov_b64 exec, 1\n"
            "ds_add_u32 v219, v209 offset:48\n"
            "s_mov_b64 exec, -1\n"
            "Lri_%=:\n"
            "ds_read_b32 v220, v219 offset:60\n"
            "s_waitcnt lgkmcnt(0)\n"
            "v_readfirstlane_b32 s68, v220\n"
            "s_cmp_lt_u32 s68, 4\n"
            "s_cbranch_scc1 Lri_%=\n"
            "s_add_u32 s68, s54, 24576\n"
            "s_nop 1\n"
            "buffer_load_dword v230, v193, s[24:27], s68 offen\n"
            "s_add_u32 s68, s54, 32768\n"
            "buffer_load_dword v230, v193, s[24:27], s68 offen\n"
            "s_and_b32 s68, s9, 31\n"
            "s_add_u32 s68, s68, 0\n"
            "s_lshl_b32 s68, s68, 13\n"
            "s_add_u32 s68, s68, s55\n"
            "buffer_load_dword v230, v193, s[28:31], s68 offen\n"
            "s_and_b32 s68, s9, 31\n"
            "s_add_u32 s68, s68, 32\n"
            "s_lshl_b32 s68, s68, 13\n"
            "s_add_u32 s68, s68, s55\n"
            "buffer_load_dword v230, v193, s[28:31], s68 offen\n"
            "s_add_u32 s68, s69, 1048576\n"
            "buffer_load_dword v230, v193, s[28:31], s68 offen\n"
            "s_add_u32 s68, s69, 2097152\n"
            "buffer_load_dword v230, v193, s[28:31], s68 offen\n"
            "s_branch Lw7a_%=\n"
            "Lw7_%=:\n"
            "s_sleep 1\n"
            "Lw7a_%=:\n"
            "ds_read_b32 v220, v219 offset:44\n"
            "s_waitcnt lgkmcnt(0)\n"
            "v_readfirstlane_b32 s68, v220\n"
            "s_cmp_lt_u32 s68, 4\n"
            "s_cbranch_scc1 Lw7_%=\n"
            "s_setprio 3\n"
            "s_branch Lmain_%=\n"
            "Lsort_%=:\n"
            "s_branch Lc3a_%=\n"
            "Lc3_%=:\n"
            "s_sleep 1\n"
            "Lc3a_%=:\n"
            "ds_read_b32 v220, v219 offset:44\n"
            "s_waitcnt lgkmcnt(0)\n"
            "v_readfirstlane_b32 s68, v220\n"
            "s_cmp_lt_u32 s68, 3\n"
            "s_cbranch_scc1 Lc3_%=\n"
            "s_setprio 3\n"
            "Lc4_%=:\n"
            "ds_read_b32 v220, v219 offset:44\n"
            "s_waitcnt lgkmcnt(0)\n"
            "v_readfirstlane_b32 s68, v220\n"
            "s_cmp_lt_u32 s68, 4\n"
            "s_cbranch_scc1 Lc4_%=\n"
            "ds_read_b32 v160, v180 offset:96\n"
            "ds_read_b128 v[128:131], v181 offset:96\n"
            "ds_read_b128 v[132:135], v181 offset:112\n"
            "ds_read_b128 v[136:139], v181 offset:128\n"
            "ds_read_b128 v[140:143], v181 offset:144\n"
            "ds_read_b128 v[144:147], v181 offset:160\n"
            "s_waitcnt lgkmcnt(4)\n"
            "v_sub_u32 v128, v160, v128\n"
            "v_sub_u32 v129, v160, v129\n"
            "v_sub_u32 v130, v160, v130\n"
            "v_sub_u32 v131, v160, v131\n"
            "v_lshrrev_b32 v161, 31, v128\n"
            "v_lshrrev_b32 v162, 31, v129\n"
            "v_alignbit_b32 v161, v161, v130, 31\n"
            "v_alignbit_b32 v162, v162, v131, 31\n"
            "v_lshrrev_b32 v163, 14, v160\n"
            "v_bfe_u32 v166, v160, 7, 7\n"
            "v_sub_u32 v166, v163, v166\n"
            "s_waitcnt lgkmcnt(3)\n"
            "v_sub_u32 v132, v160, v132\n"
            "v_sub_u32 v133, v160, v133\n"
            "v_sub_u32 v134, v160, v134\n"
            "v_sub_u32 v135, v160, v135\n"
            "v_alignbit_b32 v161, v161, v132, 31\n"
            "v_alignbit_b32 v162, v162, v133, 31\n"
            "v_alignbit_b32 v161, v161, v134, 31\n"
            "v_alignbit_b32 v162, v162, v135, 31\n"
            "v_cmp_lt_u32 vcc, 44, v163\n"
            "v_max_u32 v164, 8, v163\n"
            "s_waitcnt lgkmcnt(2)\n"
            "v_sub_u32 v136, v160, v136\n"
            "v_sub_u32 v137, v160, v137\n"
            "v_sub_u32 v138, v160, v138\n"
            "v_sub_u32 v139, v160, v139\n"
            "v_alignbit_b32 v161, v161, v136, 31\n"
            "v_alignbit_b32 v162, v162, v137, 31\n"
            "v_alignbit_b32 v161, v161, v138, 31\n"
            "v_alignbit_b32 v162, v162, v139, 31\n"
            "v_mov_b32 v165, 255\n"
            "v_lshl_or_b32 v183, v163, 16, v183\n"
            "v_lshl_or_b32 v183, v166, 24, v183\n"
            "s_waitcnt lgkmcnt(1)\n"
            "v_sub_u32 v140, v160, v140\n"
            "v_sub_u32 v141, v160, v141\n"
            "v_sub_u32 v142, v160, v142\n"
            "v_sub_u32 v143, v160, v143\n"
            "v_alignbit_b32 v161, v161, v140, 31\n"
            "v_alignbit_b32 v162, v162, v141, 31\n"
            "v_alignbit_b32 v161, v161, v142, 31\n"
            "v_alignbit_b32 v162, v162, v143, 31\n"
            "v_cndmask_b32 v164, v164, v165, vcc\n"
            "s_waitcnt lgkmcnt(0)\n"
            "v_sub_u32 v144, v160, v144\n"
            "v_sub_u32 v145, v160, v145\n"
            "v_sub_u32 v146, v160, v146\n"
            "v_sub_u32 v147, v160, v147\n"
            "v_alignbit_b32 v161, v161, v144, 31\n"
            "v_alignbit_b32 v162, v162, v145, 31\n"
            "v_alignbit_b32 v161, v161, v146, 31\n"
            "v_alignbit_b32 v162, v162, v147, 31\n"
            "v_bcnt_u32_b32 v161, v161, 0\n"
            "v_bcnt_u32_b32 v161, v162, v161\n"
            "s_nop 1\n"
            "v_add_u32_dpp v161, v161, v161 quad_perm:[1,0,3,2] row_mask:0xf bank_mask:0xf\n"
            "v_lshl_add_u32 v182, v161, 2, v182\n"
            "ds_write2st64_b32 v182, v183, v164 offset0:14 offset1:12\n"
            "ds_write_b32 v182, v184 offset:4096\n"
            "s_mov_b64 exec, 1\n"
            "ds_add_u32 v219, v209 offset:48\n"
            "s_mov_b64 exec, -1\n"
            "s_lshl3_add_u32 s68, s2, s3\n"
            "s_cmp_lg_u32 s68, 4\n"
            "s_cbranch_scc1 Lmain_%=\n"
            "v_mov_b32 v160, s90\n"
            "s_mov_b64 exec, 1\n"
            "buffer_store_dword v160, off, s[20:23], 0 sc1\n"
            "s_mov_b64 exec, -1\n"
            "s_branch Lmain_%=\n"
            "Lcol_%=:\n"
            "s_lshl_b32 s38, s8, 7\n"
            "s_add_u32 s38, s38, s9\n"
            "s_bfe_u32 s68, s38, 0x10001\n"
            "s_lshl_b32 s68, s68, 10\n"
            "s_lshr_b32 s72, s38, 2\n"
            "s_lshl4_add_u32 s68, s72, s68\n"
            "s_and_b32 s72, s38, 1\n"
            "s_lshl3_add_u32 s68, s72, s68\n"
            "s_add_u32 s68, s68, 36864\n"
            "v_lshl_add_u32 v180, v95, 7, s68\n"
            "s_sleep 10\n"
            "buffer_load_dwordx2 v[142:143], v180, s[20:23], 0 offen sc1\n"
            "s_mul_i32 s17, s3, 1792\n"
            "s_add_u32 s17, s17, 76800\n"
            "v_lshl_add_u32 v181, v243, 2, s17\n"
            "s_mul_i32 s15, s3, 1310720\n"
            "s_add_u32 s15, s15, 561152\n"
            "s_add_u32 s16, s15, 4096\n"
            "s_mul_i32 s18, s3, 1280\n"
            "s_add_u32 s18, s18, 84992\n"
            "s_lshl_b32 s19, s10, 3\n"
            "s_lshl_b32 s97, s50, 5\n"
            "s_mul_i32 s98, s97, s3\n"
            "s_lshl_b32 s98, s98, 6\n"
            "v_mul_u32_u24 v182, 704, v243\n"
            "v_add_u32 v182, 5120, v182\n"
            "v_add_u32 v184, 704, v182\n"
            "v_mul_u32_u24 v183, 704, v196\n"
            "v_add_u32 v183, 50176, v183\n"
            "v_add_u32 v185, 704, v183\n"
            "v_add_u32 v187, s18, v192\n"
            "v_add_u32 v188, s18, v214\n"
            "v_lshlrev_b32 v198, 5, v243\n"
            "v_lshlrev_b32 v199, 5, v196\n"
            "s_mov_b32 s68, 16797696\n"
            "v_mul_lo_u32 v189, v243, s68\n"
            "v_mov_b32 v190, 0x4000\n"
            "v_mov_b32 v191, 2.0\n"
            "v_mov_b32 v186, s98\n"
            "v_mov_b32 v197, v80\n"
            "s_branch Lcpa_%=\n"
            "Lcp_%=:\n"
            "s_sleep 1\n"
            "Lcpa_%=:\n"
            "ds_read_b32 v220, v219 offset:64\n"
            "s_waitcnt lgkmcnt(0)\n"
            "v_readfirstlane_b32 s68, v220\n"
            "s_cmp_lt_u32 s68, 4\n"
            "s_cbranch_scc1 Lcp_%=\n"
            "v_add_u32 v81, 115456, v192\n"
            "ds_read_b128 v[96:99], v81 offset:0\n"
            "ds_read_b128 v[100:103], v81 offset:1024\n"
            "ds_read_b128 v[104:107], v81 offset:2048\n"
            "ds_read_b128 v[108:111], v81 offset:3072\n"
            "ds_read_b128 v[112:115], v81 offset:4096\n"
            "ds_read_b128 v[116:119], v81 offset:5120\n"
            "ds_read_b128 v[120:123], v81 offset:6144\n"
            "ds_read_b128 v[124:127], v81 offset:7168\n"
            "ds_read_b128 v[200:203], v192 offset:61440\n"
            "s_mov_b32 s99, 0x400000\n"
            "Lpoll_%=:\n"
            "s_waitcnt vmcnt(0)\n"
            "v_cmp_ne_u32 vcc, s90, v143\n"
            "s_cbranch_vccnz Lpoll2_%=\n"
            "Lpoll3_%=:\n"
            "v_lshrrev_b32 v144, 18, v142\n"
            "v_and_b32 v146, 0xffff, v142\n"
            "v_cmp_ne_u32 s[86:87], 0, v144\n"
            "v_add_u32_dpp v145, v144, v144 row_shr:1 row_mask:0xf bank_mask:0xf bound_ctrl:1\n"
            "v_add_u32 v146, v146, v189\n"
            "v_cmp_lt_u32 s[88:89], 64, v144\n"
            "v_add_u32_dpp v145, v145, v145 row_shr:2 row_mask:0xf bank_mask:0xf bound_ctrl:1\n"
            "v_lshl_add_u32 v146, v144, 2, v146\n"
            "v_sub_u32 v147, s17, v144\n"
            "v_add_u32_dpp v145, v145, v145 row_shr:4 row_mask:0xf bank_mask:0xf bound_ctrl:1\n"
            "s_add_u32 s38, s17, -4\n"
            "s_nop 0\n"
            "v_add_u32_dpp v145, v145, v145 row_shr:8 row_mask:0xf bank_mask:0xf bound_ctrl:1\n"
            "s_nop 1\n"
            "v_add_u32_dpp v145, v145, v145 row_bcast:15 row_mask:0xa bank_mask:0xf\n"
            "s_nop 1\n"
            "v_add_u32_dpp v145, v145, v145 row_bcast:31 row_mask:0xc bank_mask:0xf\n"
            "v_cmp_ge_u32 vcc, 1792, v145\n"
            "v_readlane_b32 s37, v145, 63\n"
            "v_add_u32 v147, v147, v145\n"
            "v_add_u32 v148, s38, v145\n"
            "v_lshlrev_b32 v145, 2, v145\n"
            "v_and_b32 v148, -64, v148\n"
            "v_sub_u32 v146, v146, v145\n"
            "v_max_u32 v148, v148, v147\n"
            "s_and_b64 exec, vcc, s[86:87]\n"
            "ds_write_b32 v147, v146\n"
            "ds_write_b32 v148, v146\n"
            "s_mov_b64 exec, -1\n"
            "ds_read2st64_b32 v[74:75], v181 offset0:0 offset1:1\n"
            "ds_read2st64_b32 v[76:77], v181 offset0:2 offset1:3\n"
            "ds_read2st64_b32 v[78:79], v181 offset0:4 offset1:5\n"
            "ds_read_b32 v80, v181 offset:1536\n"
            "s_cmp_gt_u32 s37, 1792\n"
            "s_cbranch_scc1 Lover_%=\n"
            "Lover2_%=:\n"
            "s_lshr_b32 s37, s37, 2\n"
            "v_cmp_gt_i32 s[72:73], s37, v243\n"
            "s_sub_i32 s38, s37, 64\n"
            "v_cmp_gt_i32 s[74:75], s38, v243\n"
            "s_sub_i32 s38, s37, 128\n"
            "v_cmp_gt_i32 s[76:77], s38, v243\n"
            "s_sub_i32 s38, s37, 192\n"
            "v_cmp_gt_i32 s[78:79], s38, v243\n"
            "s_sub_i32 s38, s37, 256\n"
            "v_cmp_gt_i32 s[80:81], s38, v243\n"
            "s_sub_i32 s38, s37, 320\n"
            "v_cmp_gt_i32 s[82:83], s38, v243\n"
            "s_sub_i32 s38, s37, 384\n"
            "v_cmp_gt_i32 s[84:85], s38, v243\n"
            "v_cndmask_b32 v81, v191, v192, s[72:73]\n"
            "v_cndmask_b32 v82, v191, v192, s[74:75]\n"
            "v_cndmask_b32 v83, v191, v192, s[76:77]\n"
            "v_cndmask_b32 v84, v191, v192, s[78:79]\n"
            "v_cndmask_b32 v85, v191, v192, s[80:81]\n"
            "v_cndmask_b32 v86, v191, v192, s[82:83]\n"
            "v_cndmask_b32 v87, v191, v192, s[84:85]\n"
            "s_cmp_lg_u64 s[88:89], 0\n"
            "s_waitcnt lgkmcnt(0)\n"
            "v_max_u32_dpp v74, v74, v74 row_shr:1 row_mask:0xf bank_mask:0xf bound_ctrl:1\n"
            "v_max_u32_dpp v75, v75, v75 row_shr:1 row_mask:0xf bank_mask:0xf bound_ctrl:1\n"
            "v_max_u32_dpp v76, v76, v76 row_shr:1 row_mask:0xf bank_mask:0xf bound_ctrl:1\n"
            "v_max_u32_dpp v77, v77, v77 row_shr:1 row_mask:0xf bank_mask:0xf bound_ctrl:1\n"
            "v_max_u32_dpp v78, v78, v78 row_shr:1 row_mask:0xf bank_mask:0xf bound_ctrl:1\n"
            "v_max_u32_dpp v79, v79, v79 row_shr:1 row_mask:0xf bank_mask:0xf bound_ctrl:1\n"
            "v_max_u32_dpp v80, v80, v80 row_shr:1 row_mask:0xf bank_mask:0xf bound_ctrl:1\n"
            "v_max_u32_dpp v74, v74, v74 row_shr:2 row_mask:0xf bank_mask:0xf bound_ctrl:1\n"
            "v_max_u32_dpp v75, v75, v75 row_shr:2 row_mask:0xf bank_mask:0xf bound_ctrl:1\n"
            "v_max_u32_dpp v76, v76, v76 row_shr:2 row_mask:0xf bank_mask:0xf bound_ctrl:1\n"
            "v_max_u32_dpp v77, v77, v77 row_shr:2 row_mask:0xf bank_mask:0xf bound_ctrl:1\n"
            "v_max_u32_dpp v78, v78, v78 row_shr:2 row_mask:0xf bank_mask:0xf bound_ctrl:1\n"
            "v_max_u32_dpp v79, v79, v79 row_shr:2 row_mask:0xf bank_mask:0xf bound_ctrl:1\n"
            "v_max_u32_dpp v80, v80, v80 row_shr:2 row_mask:0xf bank_mask:0xf bound_ctrl:1\n"
            "v_max_u32_dpp v74, v74, v74 row_shr:4 row_mask:0xf bank_mask:0xf bound_ctrl:1\n"
            "v_max_u32_dpp v75, v75, v75 row_shr:4 row_mask:0xf bank_mask:0xf bound_ctrl:1\n"
            "v_max_u32_dpp v76, v76, v76 row_shr:4 row_mask:0xf bank_mask:0xf bound_ctrl:1\n"
            "v_max_u32_dpp v77, v77, v77 row_shr:4 row_mask:0xf bank_mask:0xf bound_ctrl:1\n"
            "v_max_u32_dpp v78, v78, v78 row_shr:4 row_mask:0xf bank_mask:0xf bound_ctrl:1\n"
            "v_max_u32_dpp v79, v79, v79 row_shr:4 row_mask:0xf bank_mask:0xf bound_ctrl:1\n"
            "v_max_u32_dpp v80, v80, v80 row_shr:4 row_mask:0xf bank_mask:0xf bound_ctrl:1\n"
            "v_max_u32_dpp v74, v74, v74 row_shr:8 row_mask:0xf bank_mask:0xf bound_ctrl:1\n"
            "v_max_u32_dpp v75, v75, v75 row_shr:8 row_mask:0xf bank_mask:0xf bound_ctrl:1\n"
            "v_max_u32_dpp v76, v76, v76 row_shr:8 row_mask:0xf bank_mask:0xf bound_ctrl:1\n"
            "v_max_u32_dpp v77, v77, v77 row_shr:8 row_mask:0xf bank_mask:0xf bound_ctrl:1\n"
            "v_max_u32_dpp v78, v78, v78 row_shr:8 row_mask:0xf bank_mask:0xf bound_ctrl:1\n"
            "v_max_u32_dpp v79, v79, v79 row_shr:8 row_mask:0xf bank_mask:0xf bound_ctrl:1\n"
            "v_max_u32_dpp v80, v80, v80 row_shr:8 row_mask:0xf bank_mask:0xf bound_ctrl:1\n"
            "s_cbranch_scc1 Llng_%=\n"
            "Llng2_%=:\n"
            "v_mad_u32_u24 v81, v74, 1, v81\n"
            "buffer_load_dwordx4 v[34:37], v81, s[20:23], s15 offen offset:0 sc1\n"
            "v_mad_u32_u24 v82, v75, 1, v82\n"
            "buffer_load_dwordx4 v[40:43], v82, s[20:23], s15 offen offset:1024 sc1\n"
            "v_mad_u32_u24 v83, v76, 1, v83\n"
            "buffer_load_dwordx4 v[46:49], v83, s[20:23], s15 offen offset:2048 sc1\n"
            "v_mad_u32_u24 v84, v77, 1, v84\n"
            "buffer_load_dwordx4 v[52:55], v84, s[20:23], s15 offen offset:3072 sc1\n"
            "v_mad_u32_u24 v85, v78, 1, v85\n"
            "buffer_load_dwordx4 v[58:61], v85, s[20:23], s16 offen offset:0 sc1\n"
            "v_mad_u32_u24 v86, v79, 1, v86\n"
            "buffer_load_dwordx4 v[64:67], v86, s[20:23], s16 offen offset:1024 sc1\n"
            "v_mad_u32_u24 v87, v80, 1, v87\n"
            "buffer_load_dwordx4 v[70:73], v87, s[20:23], s16 offen offset:2048 sc1\n"
            "s_mov_b64 exec, 1\n"
            "ds_add_u32 v219, v209 offset:60\n"
            "s_mov_b64 exec, -1\n"
            "v_lshrrev_b32 v88, 24, v74\n"
            "v_mad_u32_u24 v88, v88, s97, v186\n"
            "v_lshrrev_b32 v89, 24, v75\n"
            "v_mad_u32_u24 v89, v89, s97, v186\n"
            "v_lshrrev_b32 v90, 24, v76\n"
            "v_mad_u32_u24 v90, v90, s97, v186\n"
            "v_lshrrev_b32 v91, 24, v77\n"
            "v_mad_u32_u24 v91, v91, s97, v186\n"
            "v_lshrrev_b32 v92, 24, v78\n"
            "v_mad_u32_u24 v92, v92, s97, v186\n"
            "v_lshrrev_b32 v93, 24, v79\n"
            "v_mad_u32_u24 v93, v93, s97, v186\n"
            "v_lshrrev_b32 v94, 24, v80\n"
            "v_mad_u32_u24 v94, v94, s97, v186\n"
            "s_movk_i32 s38, 0x3fff\n"
            "s_mov_b32 s36, 0x1fffff\n"
            "s_waitcnt vmcnt(6)\n"
            "Lchk0_%=:\n"
            "v_xor_b32 v35, s93, v35\n"
            "v_xor_b32 v37, s94, v37\n"
            "v_cmp_lt_u32 vcc, s38, v35\n"
            "v_cmp_lt_u32 s[86:87], s36, v37\n"
            "v_subrev_u32 v135, s19, v37\n"
            "v_and_b32 v135, 0x3ff, v135\n"
            "s_or_b64 vcc, vcc, s[86:87]\n"
            "s_and_b64 vcc, vcc, s[72:73]\n"
            "s_cbranch_scc1 Lre0_%=\n"
            "Lok0_%=:\n"
            "v_lshlrev_b32 v144, 2, v135\n"
            "v_lshrrev_b32 v145, 1, v135\n"
            "v_and_b32 v145, 0x1fc, v145\n"
            "v_and_b32 v146, 4, v135\n"
            "v_lshl_or_b32 v146, v146, 5, v190\n"
            "s_mov_b64 exec, s[72:73]\n"
            "ds_add_rtn_u32 v128, v144, v197 offset:512\n"
            "ds_add_u32 v145, v146 offset:96\n"
            "s_mov_b64 exec, -1\n"
            "s_waitcnt vmcnt(5)\n"
            "Lchk1_%=:\n"
            "v_xor_b32 v41, s93, v41\n"
            "v_xor_b32 v43, s94, v43\n"
            "v_cmp_lt_u32 vcc, s38, v41\n"
            "v_cmp_lt_u32 s[86:87], s36, v43\n"
            "v_subrev_u32 v136, s19, v43\n"
            "v_and_b32 v136, 0x3ff, v136\n"
            "s_or_b64 vcc, vcc, s[86:87]\n"
            "s_and_b64 vcc, vcc, s[74:75]\n"
            "s_cbranch_scc1 Lre1_%=\n"
            "Lok1_%=:\n"
            "v_lshlrev_b32 v144, 2, v136\n"
            "v_lshrrev_b32 v145, 1, v136\n"
            "v_and_b32 v145, 0x1fc, v145\n"
            "v_and_b32 v146, 4, v136\n"
            "v_lshl_or_b32 v146, v146, 5, v190\n"
            "s_mov_b64 exec, s[74:75]\n"
            "ds_add_rtn_u32 v129, v144, v197 offset:512\n"
            "ds_add_u32 v145, v146 offset:96\n"
            "s_mov_b64 exec, -1\n"
            "s_waitcnt vmcnt(4)\n"
            "Lchk2_%=:\n"
            "v_xor_b32 v47, s93, v47\n"
            "v_xor_b32 v49, s94, v49\n"
            "v_cmp_lt_u32 vcc, s38, v47\n"
            "v_cmp_lt_u32 s[86:87], s36, v49\n"
            "v_subrev_u32 v137, s19, v49\n"
            "v_and_b32 v137, 0x3ff, v137\n"
            "s_or_b64 vcc, vcc, s[86:87]\n"
            "s_and_b64 vcc, vcc, s[76:77]\n"
            "s_cbranch_scc1 Lre2_%=\n"
            "Lok2_%=:\n"
            "v_lshlrev_b32 v144, 2, v137\n"
            "v_lshrrev_b32 v145, 1, v137\n"
            "v_and_b32 v145, 0x1fc, v145\n"
            "v_and_b32 v146, 4, v137\n"
            "v_lshl_or_b32 v146, v146, 5, v190\n"
            "s_mov_b64 exec, s[76:77]\n"
            "ds_add_rtn_u32 v130, v144, v197 offset:512\n"
            "ds_add_u32 v145, v146 offset:96\n"
            "s_mov_b64 exec, -1\n"
            "s_waitcnt vmcnt(3)\n"
            "Lchk3_%=:\n"
            "v_xor_b32 v53, s93, v53\n"
            "v_xor_b32 v55, s94, v55\n"
            "v_cmp_lt_u32 vcc, s38, v53\n"
            "v_cmp_lt_u32 s[86:87], s36, v55\n"
            "v_subrev_u32 v138, s19, v55\n"
            "v_and_b32 v138, 0x3ff, v138\n"
            "s_or_b64 vcc, vcc, s[86:87]\n"
            "s_and_b64 vcc, vcc, s[78:79]\n"
            "s_cbranch_scc1 Lre3_%=\n"
            "Lok3_%=:\n"
            "v_lshlrev_b32 v144, 2, v138\n"
            "v_lshrrev_b32 v145, 1, v138\n"
            "v_and_b32 v145, 0x1fc, v145\n"
            "v_and_b32 v146, 4, v138\n"
            "v_lshl_or_b32 v146, v146, 5, v190\n"
            "s_mov_b64 exec, s[78:79]\n"
            "ds_add_rtn_u32 v131, v144, v197 offset:512\n"
            "ds_add_u32 v145, v146 offset:96\n"
            "s_mov_b64 exec, -1\n"
            "s_waitcnt vmcnt(2)\n"
            "Lchk4_%=:\n"
            "v_xor_b32 v59, s93, v59\n"
            "v_xor_b32 v61, s94, v61\n"
            "v_cmp_lt_u32 vcc, s38, v59\n"
            "v_cmp_lt_u32 s[86:87], s36, v61\n"
            "v_subrev_u32 v139, s19, v61\n"
            "v_and_b32 v139, 0x3ff, v139\n"
            "s_or_b64 vcc, vcc, s[86:87]\n"
            "s_and_b64 vcc, vcc, s[80:81]\n"
            "s_cbranch_scc1 Lre4_%=\n"
            "Lok4_%=:\n"
            "v_lshlrev_b32 v144, 2, v139\n"
            "v_lshrrev_b32 v145, 1, v139\n"
            "v_and_b32 v145, 0x1fc, v145\n"
            "v_and_b32 v146, 4, v139\n"
            "v_lshl_or_b32 v146, v146, 5, v190\n"
            "s_mov_b64 exec, s[80:81]\n"
            "ds_add_rtn_u32 v132, v144, v197 offset:512\n"
            "ds_add_u32 v145, v146 offset:96\n"
            "s_mov_b64 exec, -1\n"
            "s_waitcnt vmcnt(1)\n"
            "Lchk5_%=:\n"
            "v_xor_b32 v65, s93, v65\n"
            "v_xor_b32 v67, s94, v67\n"
            "v_cmp_lt_u32 vcc, s38, v65\n"
            "v_cmp_lt_u32 s[86:87], s36, v67\n"
            "v_subrev_u32 v140, s19, v67\n"
            "v_and_b32 v140, 0x3ff, v140\n"
            "s_or_b64 vcc, vcc, s[86:87]\n"
            "s_and_b64 vcc, vcc, s[82:83]\n"
            "s_cbranch_scc1 Lre5_%=\n"
            "Lok5_%=:\n"
            "v_lshlrev_b32 v144, 2, v140\n"
            "v_lshrrev_b32 v145, 1, v140\n"
            "v_and_b32 v145, 0x1fc, v145\n"
            "v_and_b32 v146, 4, v140\n"
            "v_lshl_or_b32 v146, v146, 5, v190\n"
            "s_mov_b64 exec, s[82:83]\n"
            "ds_add_rtn_u32 v133, v144, v197 offset:512\n"
            "ds_add_u32 v145, v146 offset:96\n"
            "s_mov_b64 exec, -1\n"
            "s_cmp_eq_u64 s[84:85], 0\n"
            "s_cbranch_scc1 Lr7_%=\n"
            "s_waitcnt vmcnt(0)\n"
            "Lchk6_%=:\n"
            "v_xor_b32 v71, s93, v71\n"
            "v_xor_b32 v73, s94, v73\n"
            "v_cmp_lt_u32 vcc, s38, v71\n"
            "v_cmp_lt_u32 s[86:87], s36, v73\n"
            "v_subrev_u32 v141, s19, v73\n"
            "v_and_b32 v141, 0x3ff, v141\n"
            "s_or_b64 vcc, vcc, s[86:87]\n"
            "s_and_b64 vcc, vcc, s[84:85]\n"
            "s_cbranch_scc1 Lre6_%=\n"
            "Lok6_%=:\n"
            "v_lshlrev_b32 v144, 2, v141\n"
            "v_lshrrev_b32 v145, 1, v141\n"
            "v_and_b32 v145, 0x1fc, v145\n"
            "v_and_b32 v146, 4, v141\n"
            "v_lshl_or_b32 v146, v146, 5, v190\n"
            "s_mov_b64 exec, s[84:85]\n"
            "ds_add_rtn_u32 v134, v144, v197 offset:512\n"
            "ds_add_u32 v145, v146 offset:96\n"
            "s_mov_b64 exec, -1\n"
            "Lr7_%=:\n"
            "s_mov_b64 exec, 1\n"
            "ds_add_u32 v219, v209 offset:44\n"
            "s_mov_b64 exec, -1\n"
            "v_lshlrev_b32 v32, 10, v35\n"
            "v_lshrrev_b32 v33, 10, v37\n"
            "v_mov_b32 v35, v36\n"
            "v_lshl_add_u32 v33, v33, 5, v88\n"
            "v_lshl_add_u32 v135, v135, 1, s18\n"
            "v_lshlrev_b32 v38, 10, v41\n"
            "v_lshrrev_b32 v39, 10, v43\n"
            "v_mov_b32 v41, v42\n"
            "v_lshl_add_u32 v39, v39, 5, v89\n"
            "v_lshl_add_u32 v136, v136, 1, s18\n"
            "v_lshlrev_b32 v44, 10, v47\n"
            "v_lshrrev_b32 v45, 10, v49\n"
            "v_mov_b32 v47, v48\n"
            "v_lshl_add_u32 v45, v45, 5, v90\n"
            "v_lshl_add_u32 v137, v137, 1, s18\n"
            "v_lshlrev_b32 v50, 10, v53\n"
            "v_lshrrev_b32 v51, 10, v55\n"
            "v_mov_b32 v53, v54\n"
            "v_lshl_add_u32 v51, v51, 5, v91\n"
            "v_lshl_add_u32 v138, v138, 1, s18\n"
            "v_lshlrev_b32 v56, 10, v59\n"
            "v_lshrrev_b32 v57, 10, v61\n"
            "v_mov_b32 v59, v60\n"
            "v_lshl_add_u32 v57, v57, 5, v92\n"
            "v_lshl_add_u32 v139, v139, 1, s18\n"
            "v_lshlrev_b32 v62, 10, v65\n"
            "v_lshrrev_b32 v63, 10, v67\n"
            "v_mov_b32 v65, v66\n"
            "v_lshl_add_u32 v63, v63, 5, v93\n"
            "v_lshl_add_u32 v140, v140, 1, s18\n"
            "v_lshlrev_b32 v68, 10, v71\n"
            "v_lshrrev_b32 v69, 10, v73\n"
            "v_mov_b32 v71, v72\n"
            "v_lshl_add_u32 v69, v69, 5, v94\n"
            "v_lshl_add_u32 v141, v141, 1, s18\n"
            "Lcc_%=:\n"
            "ds_read_b32 v220, v219 offset:44\n"
            "s_waitcnt lgkmcnt(0)\n"
            "v_readfirstlane_b32 s68, v220\n"
            "s_cmp_lt_u32 s68, 4\n"
            "s_cbranch_scc1 Lcc_%=\n"
            "ds_read_b128 v[144:147], v198 offset:512\n"
            "ds_read_b128 v[148:151], v198 offset:528\n"
            "ds_read_b128 v[152:155], v199 offset:2560\n"
            "ds_read_b128 v[156:159], v199 offset:2576\n"
            "v_bfe_u32 v128, v128, s12, 8\n"
            "v_bfe_u32 v129, v129, s12, 8\n"
            "v_bfe_u32 v130, v130, s12, 8\n"
            "v_bfe_u32 v131, v131, s12, 8\n"
            "v_bfe_u32 v132, v132, s12, 8\n"
            "v_bfe_u32 v133, v133, s12, 8\n"
            "v_bfe_u32 v134, v134, s12, 8\n"
            "s_waitcnt lgkmcnt(3)\n"
            "v_dot4_u32_u8 v160, v144, s13, v182\n"
            "v_dot4_u32_u8 v182, v144, s39, v182\n"
            "v_dot4_u32_u8 v161, v145, s13, v182\n"
            "v_dot4_u32_u8 v182, v145, s39, v182\n"
            "v_dot4_u32_u8 v162, v146, s13, v182\n"
            "v_dot4_u32_u8 v182, v146, s39, v182\n"
            "v_dot4_u32_u8 v163, v147, s13, v182\n"
            "v_dot4_u32_u8 v182, v147, s39, v182\n"
            "s_waitcnt lgkmcnt(2)\n"
            "v_dot4_u32_u8 v164, v148, s13, v182\n"
            "v_dot4_u32_u8 v182, v148, s39, v182\n"
            "v_dot4_u32_u8 v165, v149, s13, v182\n"
            "v_dot4_u32_u8 v182, v149, s39, v182\n"
            "v_dot4_u32_u8 v166, v150, s13, v182\n"
            "v_dot4_u32_u8 v182, v150, s39, v182\n"
            "v_dot4_u32_u8 v167, v151, s13, v182\n"
            "v_dot4_u32_u8 v182, v151, s39, v182\n"
            "s_waitcnt lgkmcnt(0)\n"
            "v_dot4_u32_u8 v168, v152, s13, v183\n"
            "v_dot4_u32_u8 v183, v152, s39, v183\n"
            "v_dot4_u32_u8 v169, v153, s13, v183\n"
            "v_dot4_u32_u8 v183, v153, s39, v183\n"
            "v_lshl_or_b32 v160, v161, 16, v160\n"
            "v_lshl_or_b32 v161, v163, 16, v162\n"
            "v_lshl_or_b32 v162, v165, 16, v164\n"
            "v_lshl_or_b32 v163, v167, 16, v166\n"
            "ds_write_b128 v187, v[160:163]\n"
            "v_dot4_u32_u8 v170, v154, s13, v183\n"
            "v_dot4_u32_u8 v183, v154, s39, v183\n"
            "v_dot4_u32_u8 v171, v155, s13, v183\n"
            "v_dot4_u32_u8 v183, v155, s39, v183\n"
            "v_dot4_u32_u8 v172, v156, s13, v183\n"
            "v_dot4_u32_u8 v183, v156, s39, v183\n"
            "v_dot4_u32_u8 v173, v157, s13, v183\n"
            "v_dot4_u32_u8 v183, v157, s39, v183\n"
            "v_dot4_u32_u8 v174, v158, s13, v183\n"
            "v_dot4_u32_u8 v183, v158, s39, v183\n"
            "v_dot4_u32_u8 v175, v159, s13, v183\n"
            "v_dot4_u32_u8 v183, v159, s39, v183\n"
            "v_cmp_gt_u32 vcc, v182, v184\n"
            "v_lshl_or_b32 v168, v169, 16, v168\n"
            "v_lshl_or_b32 v169, v171, 16, v170\n"
            "v_lshl_or_b32 v170, v173, 16, v172\n"
            "v_lshl_or_b32 v171, v175, 16, v174\n"
            "ds_write_b128 v188, v[168:171] offset:1024\n"
            "ds_read_u16 v135, v135\n"
            "ds_read_u16 v136, v136\n"
            "ds_read_u16 v137, v137\n"
            "ds_read_u16 v138, v138\n"
            "ds_read_u16 v139, v139\n"
            "ds_read_u16 v140, v140\n"
            "ds_read_u16 v141, v141\n"
            "v_cmp_gt_u32 s[86:87], v183, v185\n"
            "s_or_b64 vcc, vcc, s[86:87]\n"
            "s_cbranch_scc1 Lbig_%=\n"
            "s_waitcnt lgkmcnt(6)\n"
            "v_lshl_add_u32 v135, v128, 4, v135\n"
            "s_mov_b64 exec, s[72:73]\n"
            "ds_write_b128 v135, v[32:35]\n"
            "s_waitcnt lgkmcnt(6)\n"
            "v_lshl_add_u32 v136, v129, 4, v136\n"
            "s_mov_b64 exec, s[74:75]\n"
            "ds_write_b128 v136, v[38:41]\n"
            "s_waitcnt lgkmcnt(6)\n"
            "v_lshl_add_u32 v137, v130, 4, v137\n"
            "s_mov_b64 exec, s[76:77]\n"
            "ds_write_b128 v137, v[44:47]\n"
            "s_waitcnt lgkmcnt(6)\n"
            "v_lshl_add_u32 v138, v131, 4, v138\n"
            "s_mov_b64 exec, s[78:79]\n"
            "ds_write_b128 v138, v[50:53]\n"
            "s_waitcnt lgkmcnt(6)\n"
            "v_lshl_add_u32 v139, v132, 4, v139\n"
            "s_mov_b64 exec, s[80:81]\n"
            "ds_write_b128 v139, v[56:59]\n"
            "s_waitcnt lgkmcnt(6)\n"
            "v_lshl_add_u32 v140, v133, 4, v140\n"
            "s_mov_b64 exec, s[82:83]\n"
            "ds_write_b128 v140, v[62:65]\n"
            "s_waitcnt lgkmcnt(6)\n"
            "v_lshl_add_u32 v141, v134, 4, v141\n"
            "s_mov_b64 exec, s[84:85]\n"
            "ds_write_b128 v141, v[68:71]\n"
            "Lbig_%=:\n"
            "s_mov_b64 exec, 1\n"
            "ds_add_u32 v219, v209 offset:56\n"
            "s_mov_b64 exec, -1\n"
            "Lmain_%=:\n"
            "Ls_%=:\n"
            "ds_read_b32 v220, v219 offset:48\n"
            "s_waitcnt lgkmcnt(0)\n"
            "v_readfirstlane_b32 s68, v220\n"
            "s_cmp_lt_u32 s68, 4\n"
            "s_cbranch_scc1 Ls_%=\n"
            "v_mov_b32 v140, s64\n"
            "ds_read_b32 v128, v240\n"
            "ds_read_b32 v129, v241\n"
            "ds_read_b32 v130, v242\n"
            "ds_read_b32 v131, v219\n"
            "ds_read_b32 v138, v248 offset:512\n"
            "ds_read_b32 v139, v244 offset:512\n"
            "ds_read_u8 v140, v140 offset:2\n"
            "ds_read_b32 v217, v244 offset:1024\n"
            "ds_read_b32 v215, v245 offset:1024\n"
            "ds_read_b32 v218, v246 offset:1024\n"
            "s_waitcnt lgkmcnt(6)\n"
            "v_max_u32 v128, 12, v128\n"
            "v_mov_b32_dpp v134, v130 quad_perm:[1,2,3,0] row_mask:0xf bank_mask:0xf\n"
            "v_add_u32 v132, v128, v129\n"
            "v_sub_u32 v133, 0, v132\n"
            "v_lshl_add_u32 v135, v132, 1, v208\n"
            "v_and_b32 v133, 3, v133\n"
            "v_add_lshl_u32 v133, v132, v133, 1\n"
            "v_add3_u32 v135, v135, v134, v130\n"
            "v_readlane_b32 s73, v128, s3\n"
            "s_nop 0\n"
            "v_add_u32_dpp v135, v133, v135 row_shr:4 row_mask:0xf bank_mask:0xf bound_ctrl:1\n"
            "v_readlane_b32 s75, v129, s3\n"
            "v_readlane_b32 s80, v130, s3\n"
            "v_sub_u32_dpp v133, v135, v135 quad_perm:[1,2,3,0] row_mask:0xf bank_mask:0xf\n"
            "v_mov_b32_dpp v136, v135 quad_perm:[3,0,1,2] row_mask:0xf bank_mask:0xf\n"
            "v_lshl_add_u32 v137, v134, 3, 8\n"
            "v_mad_i32_i24 v133, v133, 3, v137\n"
            "v_sub_u32_dpp v136, v135, v136 quad_perm:[2,3,0,1] row_mask:0xf bank_mask:0xf\n"
            "v_add_u32 v134, -4, v134\n"
            "v_add_u32 v133, v133, v136\n"
            "v_ashrrev_i32 v133, 4, v133\n"
            "v_max_i32 v133, 4, v133\n"
            "v_min_i32 v133, v133, v134\n"
            "v_max_i32 v133, 0, v133\n"
            "s_add_i32 s69, s73, -1\n"
            "s_lshr_b32 s69, s69, 1\n"
            "v_mov_b32_dpp v134, v133 quad_perm:[3,0,1,2] row_mask:0xf bank_mask:0xf\n"
            "v_readlane_b32 s77, v133, s3\n"
            "v_sub_u32 v134, v130, v134\n"
            "v_add3_u32 v132, v132, v133, v131\n"
            "v_readlane_b32 s76, v134, s3\n"
            "v_add_u32 v132, v132, v134\n"
            "v_cmp_lt_u32 vcc, 72, v132\n"
            "v_readlane_b32 s73, v132, s3\n"
            "s_cbranch_vccnz Lslow_%=\n"
            "s_sub_u32 s43, 0, s73\n"
            "s_and_b32 s43, s43, 3\n"
            "s_min_u32 s43, s43, 3\n"
            "s_add_u32 s73, s73, s43\n"
            "s_sub_u32 s81, 8, s43\n"
            "s_max_i32 s69, s69, s81\n"
            "s_waitcnt lgkmcnt(3)\n"
            "v_max_u32 v140, 8, v140\n"
            "v_add_u32 v140, s43, v140\n"
            "v_sub_u32_sdwa v134, v140, v138 dst_sel:DWORD dst_unused:UNUSED_PAD src0_sel:DWORD src1_sel:BYTE_2\n"
            "v_sub_u32 v134, v206, v134\n"
            "v_add_u32 v136, 4, v134\n"
            "v_cmp_lt_u32_sdwa vcc, v134, v138 src0_sel:DWORD src1_sel:BYTE_2\n"
            "v_cmp_lt_u32_sdwa s[86:87], v136, v138 src0_sel:DWORD src1_sel:BYTE_2\n"
            "v_and_b32 v138, 0xffff, v138\n"
            "v_lshl_add_u32 v138, v134, 4, v138\n"
            "v_sub_u32_sdwa v135, v140, v139 dst_sel:DWORD dst_unused:UNUSED_PAD src0_sel:DWORD src1_sel:BYTE_2\n"
            "v_sub_u32 v135, v249, v135\n"
            "v_add_u32 v136, 4, v135\n"
            "v_cmp_lt_u32_sdwa s[70:71], v135, v139 src0_sel:DWORD src1_sel:BYTE_2\n"
            "v_cmp_lt_u32_sdwa s[88:89], v136, v139 src0_sel:DWORD src1_sel:BYTE_2\n"
            "v_and_b32 v139, 0xffff, v139\n"
            "v_lshl_add_u32 v139, v135, 4, v139\n"
            "v_cndmask_b32 v232, v251, v138, vcc\n"
            "v_cndmask_b32 v221, v251, v139, s[70:71]\n"
            "v_add_u32 v138, 64, v138\n"
            "v_add_u32 v139, 64, v139\n"
            "v_cndmask_b32 v233, v251, v138, s[86:87]\n"
            "v_cndmask_b32 v210, v251, v139, s[88:89]\n"
            "Lw_%=:\n"
            "ds_read_b32 v220, v219 offset:56\n"
            "s_waitcnt lgkmcnt(0)\n"
            "v_readfirstlane_b32 s68, v220\n"
            "s_cmp_lt_u32 s68, 4\n"
            "s_cbranch_scc1 Lw_%=\n"
            "ds_read_b32 v232, v232 offset:4\n"
            "ds_read_b128 v[196:199], v221\n"
            "ds_read_b32 v233, v233 offset:4\n"
            "s_waitcnt lgkmcnt(2)\n"
            "v_add_u32 v232, v232, v229\n"
            "buffer_load_dwordx4 v[160:163], v232, s[24:27], 0 offen\n"
            "s_waitcnt lgkmcnt(0)\n"
            "v_add_u32 v233, v233, v229\n"
            "v_add_u32_dpp v220, v196, v214 quad_perm:[0,0,0,0] row_mask:0xf bank_mask:0xf\n"
            "v_add_u32_dpp v221, v196, v214 quad_perm:[1,1,1,1] row_mask:0xf bank_mask:0xf\n"
            "v_add_u32_dpp v222, v196, v214 quad_perm:[2,2,2,2] row_mask:0xf bank_mask:0xf\n"
            "v_add_u32_dpp v223, v196, v214 quad_perm:[3,3,3,3] row_mask:0xf bank_mask:0xf\n"
            "buffer_load_dwordx4 v[32:35], v220, s[28:31], 0 offen\n"
            "buffer_load_dwordx4 v[36:39], v220, s[28:31], 0 offen offset:256\n"
            "v_mfma_f32_4x4x4_16b_bf16 v[204:207], v[202:203], v[198:199], 0\n"
            "buffer_load_dwordx4 v[48:51], v221, s[28:31], 0 offen\n"
            "buffer_load_dwordx4 v[52:55], v221, s[28:31], 0 offen offset:256\n"
            "buffer_load_dwordx4 v[232:235], v233, s[24:27], 0 offen\n"
            "buffer_load_dwordx4 v[64:67], v222, s[28:31], 0 offen\n"
            "buffer_load_dwordx4 v[68:71], v222, s[28:31], 0 offen offset:256\n"
            "v_mfma_f32_4x4x4_16b_bf16 v[204:207], v[200:201], v[198:199], v[204:207]\n"
            "buffer_load_dwordx4 v[80:83], v223, s[28:31], 0 offen\n"
            "buffer_load_dwordx4 v[84:87], v223, s[28:31], 0 offen offset:256\n"
            "buffer_load_dwordx4 v[40:43], v220, s[28:31], 0 offen offset:512\n"
            "buffer_load_dwordx4 v[44:47], v220, s[28:31], 0 offen offset:768\n"
            "buffer_load_dwordx4 v[56:59], v221, s[28:31], 0 offen offset:512\n"
            "buffer_load_dwordx4 v[60:63], v221, s[28:31], 0 offen offset:768\n"
            "buffer_load_dwordx4 v[72:75], v222, s[28:31], 0 offen offset:512\n"
            "buffer_load_dwordx4 v[76:79], v222, s[28:31], 0 offen offset:768\n"
            "buffer_load_dwordx4 v[88:91], v223, s[28:31], 0 offen offset:512\n"
            "buffer_load_dwordx4 v[92:95], v223, s[28:31], 0 offen offset:768\n"
            "s_add_u32 s78, s69, s77\n"
            "s_add_u32 s79, s78, s75\n"
            "s_sub_u32 s83, s79, s80\n"
            "s_add_u32 s83, s83, s76\n"
            "s_add_u32 s80, s79, s76\n"
            "s_sub_u32 s84, s80, s69\n"
            "s_pack_ll_b32_b16 s81, s40, s69\n"
            "s_pack_ll_b32_b16 s82, s65, s78\n"
            "s_pack_ll_b32_b16 s83, s66, s83\n"
            "s_pack_ll_b32_b16 s84, s64, s84\n"
            "v_subrev_u32 v128, s43, v243\n"
            "v_mov_b32 v136, s64\n"
            "v_cmpx_le_i32 vcc, s69, v128\n"
            "v_mov_b32 v136, s81\n"
            "v_cmpx_le_i32 vcc, s78, v128\n"
            "v_mov_b32 v136, s82\n"
            "v_cmpx_le_i32 vcc, s79, v128\n"
            "v_mov_b32 v136, s83\n"
            "v_cmpx_le_i32 vcc, s80, v128\n"
            "v_mov_b32 v136, s84\n"
            "s_mov_b64 exec, -1\n"
            "v_and_b32 v137, 0xffff, v136\n"
            "ds_read_b128 v[140:143], v137\n"
            "v_ashrrev_i32 v136, 16, v136\n"
            "v_sub_u32 v136, v128, v136\n"
            "v_cvt_pk_bf16_f32 v204, v204, v205\n"
            "v_cvt_pk_bf16_f32 v205, v206, v207\n"
            "ds_write_b64 v212, v[204:205]\n"
            "ds_read_b128 v[196:199], v210\n"
            "ds_read_b64 v[208:209], v213\n"
            "s_add_u32 s73, s73, 3\n"
            "s_and_b32 s74, s73, 3\n"
            "s_add_u32 s74, s74, 1\n"
            "s_lshr_b32 s73, s73, 2\n"
            "s_mov_b32 s68, 0x05040100\n"
            "v_add_u32 v217, v217, v214\n"
            "v_add3_u32 v215, v215, v214, s63\n"
            "v_add_u32 v218, v218, v214\n"
            "v_not_b32 v223, s90\n"
            "s_waitcnt lgkmcnt(3)\n"
            "v_cmp_eq_u32 vcc, s64, v137\n"
            "v_mov_b32 v132, 60\n"
            "s_nop 0\n"
            "v_cndmask_b32 v132, 0, v132, vcc\n"
            "v_bfe_u32 v137, v140, 16, 8\n"
            "v_max_u32 v137, 8, v137\n"
            "v_lshrrev_b32 v138, 1, v137\n"
            "v_add_u32 v138, v138, v132\n"
            "v_sub_u32_sdwa v132, v138, v140 dst_sel:DWORD dst_unused:UNUSED_PAD src0_sel:DWORD src1_sel:BYTE_3\n"
            "v_sub_u32_sdwa v133, v137, v140 dst_sel:DWORD dst_unused:UNUSED_PAD src0_sel:DWORD src1_sel:BYTE_2\n"
            "v_med3_i32 v132, v132, 0, v133\n"
            "v_sub_u32 v132, v136, v132\n"
            "v_cmp_lt_u32_sdwa vcc, v132, v140 src0_sel:DWORD src1_sel:BYTE_2\n"
            "v_lshl_add_u32 v140, v132, 4, v140\n"
            "v_sub_u32_sdwa v132, v138, v141 dst_sel:DWORD dst_unused:UNUSED_PAD src0_sel:DWORD src1_sel:BYTE_3\n"
            "v_sub_u32_sdwa v133, v137, v141 dst_sel:DWORD dst_unused:UNUSED_PAD src0_sel:DWORD src1_sel:BYTE_2\n"
            "v_med3_i32 v132, v132, 0, v133\n"
            "v_sub_u32 v132, v136, v132\n"
            "v_cmp_lt_u32_sdwa s[70:71], v132, v141 src0_sel:DWORD src1_sel:BYTE_2\n"
            "v_lshl_add_u32 v141, v132, 4, v141\n"
            "v_sub_u32_sdwa v132, v138, v142 dst_sel:DWORD dst_unused:UNUSED_PAD src0_sel:DWORD src1_sel:BYTE_3\n"
            "v_sub_u32_sdwa v133, v137, v142 dst_sel:DWORD dst_unused:UNUSED_PAD src0_sel:DWORD src1_sel:BYTE_2\n"
            "v_med3_i32 v132, v132, 0, v133\n"
            "v_sub_u32 v132, v136, v132\n"
            "v_cmp_lt_u32_sdwa s[86:87], v132, v142 src0_sel:DWORD src1_sel:BYTE_2\n"
            "v_lshl_add_u32 v142, v132, 4, v142\n"
            "v_sub_u32_sdwa v132, v138, v143 dst_sel:DWORD dst_unused:UNUSED_PAD src0_sel:DWORD src1_sel:BYTE_3\n"
            "v_sub_u32_sdwa v133, v137, v143 dst_sel:DWORD dst_unused:UNUSED_PAD src0_sel:DWORD src1_sel:BYTE_2\n"
            "v_med3_i32 v132, v132, 0, v133\n"
            "v_sub_u32 v132, v136, v132\n"
            "v_cmp_lt_u32_sdwa s[88:89], v132, v143 src0_sel:DWORD src1_sel:BYTE_2\n"
            "v_lshl_add_u32 v143, v132, 4, v143\n"
            "v_cndmask_b32 v140, v251, v140, vcc\n"
            "v_cndmask_b32 v141, v251, v141, s[70:71]\n"
            "v_cndmask_b32 v142, v251, v142, s[86:87]\n"
            "v_cndmask_b32 v143, v251, v143, s[88:89]\n"
            "v_perm_b32 v140, v141, v140, s68\n"
            "v_perm_b32 v141, v143, v142, s68\n"
            "ds_write_b64 v247, v[140:141]\n"
            "ds_read_u16 v211, v238\n"
            "s_cmp_gt_u32 s73, 13\n"
            "s_cbranch_scc1 Llong_%=\n"
            "Llong2_%=:\n"
            "s_add_u32 s69, s69, s43\n"
            "v_add_u32 v216, s63, v218\n"
            "s_lshl_b32 s72, s75, 8\n"
            "s_lshl_b32 s76, s76, 16\n"
            "s_add_u32 s72, s72, s76\n"
            "s_add_u32 s72, s72, s77\n"
            "s_add_u32 s72, s72, 0xfefbfbfc\n"
            "s_add_u32 s75, s75, 0xfefc\n"
            "s_cmp_lt_u32 s3, 4\n"
            "s_cselect_b32 s72, s75, s72\n"
            "s_cmp_lt_u32 s3, 6\n"
            "s_cselect_b32 s75, 1, 0\n"
            "v_lshl_or_b32 v218, s75, 31, v218\n"
            "s_mov_b32 s85, 15\n"
            "s_mov_b32 s42, 0\n"
            "s_waitcnt vmcnt(17)\n"
            "ds_bpermute_b32 v192, v253, v160\n"
            "ds_bpermute_b32 v193, v253, v161\n"
            "ds_bpermute_b32 v194, v253, v162\n"
            "ds_bpermute_b32 v195, v253, v163\n"
            "s_waitcnt lgkmcnt(0)\n"
            "v_mfma_f32_16x16x32_bf16 v[176:179], v[192:195], v[96:99], 0\n"
            "v_mfma_f32_16x16x32_bf16 v[180:183], v[192:195], v[100:103], 0\n"
            "v_mfma_f32_16x16x32_bf16 v[184:187], v[192:195], v[104:107], 0\n"
            "v_mfma_f32_16x16x32_bf16 v[188:191], v[192:195], v[108:111], 0\n"
            "v_mfma_f32_16x16x32_bf16 v[128:131], v[192:195], v[112:115], 0\n"
            "v_mfma_f32_16x16x32_bf16 v[132:135], v[192:195], v[116:119], 0\n"
            "s_nop 2\n"
            "v_exp_f32 v144, v176\n"
            "v_exp_f32 v145, v177\n"
            "v_exp_f32 v146, v178\n"
            "v_exp_f32 v147, v179\n"
            "v_exp_f32 v148, v180\n"
            "v_exp_f32 v149, v181\n"
            "v_exp_f32 v150, v182\n"
            "v_exp_f32 v151, v183\n"
            "v_exp_f32 v152, v184\n"
            "v_exp_f32 v153, v185\n"
            "v_exp_f32 v154, v186\n"
            "v_exp_f32 v155, v187\n"
            "v_exp_f32 v156, v188\n"
            "v_exp_f32 v157, v189\n"
            "v_exp_f32 v158, v190\n"
            "v_exp_f32 v159, v191\n"
            "v_pk_add_f32 v[144:145], v[144:145], 1.0 op_sel_hi:[1,0]\n"
            "v_pk_add_f32 v[146:147], v[146:147], 1.0 op_sel_hi:[1,0]\n"
            "v_pk_add_f32 v[148:149], v[148:149], 1.0 op_sel_hi:[1,0]\n"
            "v_pk_add_f32 v[150:151], v[150:151], 1.0 op_sel_hi:[1,0]\n"
            "v_pk_add_f32 v[152:153], v[152:153], 1.0 op_sel_hi:[1,0]\n"
            "v_pk_add_f32 v[154:155], v[154:155], 1.0 op_sel_hi:[1,0]\n"
            "v_pk_add_f32 v[156:157], v[156:157], 1.0 op_sel_hi:[1,0]\n"
            "v_pk_add_f32 v[158:159], v[158:159], 1.0 op_sel_hi:[1,0]\n"
            "v_rcp_f32 v144, v144\n"
            "v_rcp_f32 v145, v145\n"
            "v_rcp_f32 v146, v146\n"
            "v_rcp_f32 v147, v147\n"
            "v_rcp_f32 v148, v148\n"
            "v_rcp_f32 v149, v149\n"
            "v_rcp_f32 v150, v150\n"
            "v_rcp_f32 v151, v151\n"
            "v_rcp_f32 v152, v152\n"
            "v_rcp_f32 v153, v153\n"
            "v_rcp_f32 v154, v154\n"
            "v_rcp_f32 v155, v155\n"
            "v_rcp_f32 v156, v156\n"
            "v_rcp_f32 v157, v157\n"
            "v_rcp_f32 v158, v158\n"
            "v_rcp_f32 v159, v159\n"
            "v_pk_mul_f32 v[144:145], v[144:145], v[176:177]\n"
            "v_pk_mul_f32 v[146:147], v[146:147], v[178:179]\n"
            "v_pk_mul_f32 v[148:149], v[148:149], v[180:181]\n"
            "v_pk_mul_f32 v[150:151], v[150:151], v[182:183]\n"
            "v_pk_mul_f32 v[152:153], v[152:153], v[184:185]\n"
            "v_pk_mul_f32 v[154:155], v[154:155], v[186:187]\n"
            "v_pk_mul_f32 v[156:157], v[156:157], v[188:189]\n"
            "v_pk_mul_f32 v[158:159], v[158:159], v[190:191]\n"
            "s_branch Ldisp_%=\n"
            "Lfast_%=:\n"
            "v_mfma_f32_16x16x32_bf16 v[136:139], v[192:195], v[120:123], 0\n"
            "v_exp_f32 v160, v128\n"
            "v_add_u32 v237, 32, v237\n"
            "v_exp_f32 v161, v129\n"
            "s_waitcnt vmcnt(12) lgkmcnt(0)\n"
            "v_mfma_f32_4x4x4_16b_bf16 v[176:179], v[208:209], v[32:33], 0 cbsz:2\n"
            "v_exp_f32 v162, v130\n"
            "v_mfma_f32_4x4x4_16b_bf16 v[180:183], v[208:209], v[34:35], 0 cbsz:2\n"
            "v_exp_f32 v163, v131\n"
            "v_mfma_f32_4x4x4_16b_bf16 v[184:187], v[208:209], v[36:37], 0 cbsz:2\n"
            "v_exp_f32 v164, v132\n"
            "v_mfma_f32_4x4x4_16b_bf16 v[188:191], v[208:209], v[38:39], 0 cbsz:2\n"
            "v_exp_f32 v165, v133\n"
            "v_add_u32_dpp v219, v196, v214 quad_perm:[0,0,0,0] row_mask:0xf bank_mask:0xf\n"
            "v_mfma_f32_4x4x4_16b_bf16 v[204:207], v[202:203], v[198:199], 0\n"
            "v_exp_f32 v166, v134\n"
            "buffer_load_dwordx4 v[32:35], v219, s[28:31], 0 offen\n"
            "buffer_load_dwordx4 v[36:39], v219, s[28:31], 0 offen offset:256\n"
            "v_exp_f32 v167, v135\n"
            "v_pk_fma_f32 v[0:1], v[144:145], v[176:177], v[0:1] op_sel_hi:[0,1,1]\n"
            "v_pk_fma_f32 v[2:3], v[144:145], v[178:179], v[2:3] op_sel_hi:[0,1,1]\n"
            "v_pk_fma_f32 v[4:5], v[148:149], v[180:181], v[4:5] op_sel_hi:[0,1,1]\n"
            "v_pk_fma_f32 v[6:7], v[148:149], v[182:183], v[6:7] op_sel_hi:[0,1,1]\n"
            "v_pk_fma_f32 v[8:9], v[152:153], v[184:185], v[8:9] op_sel_hi:[0,1,1]\n"
            "v_pk_fma_f32 v[10:11], v[152:153], v[186:187], v[10:11] op_sel_hi:[0,1,1]\n"
            "v_pk_fma_f32 v[12:13], v[156:157], v[188:189], v[12:13] op_sel_hi:[0,1,1]\n"
            "v_pk_fma_f32 v[14:15], v[156:157], v[190:191], v[14:15] op_sel_hi:[0,1,1]\n"
            "v_pk_add_f32 v[160:161], v[160:161], 1.0 op_sel_hi:[1,0]\n"
            "v_pk_add_f32 v[162:163], v[162:163], 1.0 op_sel_hi:[1,0]\n"
            "v_pk_add_f32 v[164:165], v[164:165], 1.0 op_sel_hi:[1,0]\n"
            "v_pk_add_f32 v[166:167], v[166:167], 1.0 op_sel_hi:[1,0]\n"
            "v_mfma_f32_16x16x32_bf16 v[140:143], v[192:195], v[124:127], 0\n"
            "ds_bpermute_b32 v192, v253, v232\n"
            "ds_bpermute_b32 v193, v253, v233\n"
            "ds_bpermute_b32 v194, v253, v234\n"
            "ds_bpermute_b32 v195, v253, v235\n"
            "ds_read_b32 v230, v211 offset:4\n"
            "ds_read_u16 v210, v237\n"
            "v_rcp_f32 v160, v160\n"
            "v_rcp_f32 v161, v161\n"
            "v_mfma_f32_4x4x4_16b_bf16 v[176:179], v[208:209], v[48:49], 0 cbsz:2 abid:1\n"
            "v_rcp_f32 v162, v162\n"
            "v_mfma_f32_4x4x4_16b_bf16 v[180:183], v[208:209], v[50:51], 0 cbsz:2 abid:1\n"
            "v_rcp_f32 v163, v163\n"
            "v_mfma_f32_4x4x4_16b_bf16 v[184:187], v[208:209], v[52:53], 0 cbsz:2 abid:1\n"
            "v_rcp_f32 v164, v164\n"
            "v_mfma_f32_4x4x4_16b_bf16 v[188:191], v[208:209], v[54:55], 0 cbsz:2 abid:1\n"
            "v_rcp_f32 v165, v165\n"
            "v_add_u32_dpp v220, v196, v214 quad_perm:[1,1,1,1] row_mask:0xf bank_mask:0xf\n"
            "v_mfma_f32_4x4x4_16b_bf16 v[204:207], v[200:201], v[198:199], v[204:207]\n"
            "v_rcp_f32 v166, v166\n"
            "buffer_load_dwordx4 v[48:51], v220, s[28:31], 0 offen\n"
            "buffer_load_dwordx4 v[52:55], v220, s[28:31], 0 offen offset:256\n"
            "v_rcp_f32 v167, v167\n"
            "v_pk_fma_f32 v[0:1], v[144:145], v[176:177], v[0:1] op_sel:[1,0,0]\n"
            "v_pk_fma_f32 v[2:3], v[144:145], v[178:179], v[2:3] op_sel:[1,0,0]\n"
            "v_pk_fma_f32 v[4:5], v[148:149], v[180:181], v[4:5] op_sel:[1,0,0]\n"
            "v_pk_fma_f32 v[6:7], v[148:149], v[182:183], v[6:7] op_sel:[1,0,0]\n"
            "v_pk_fma_f32 v[8:9], v[152:153], v[184:185], v[8:9] op_sel:[1,0,0]\n"
            "v_pk_fma_f32 v[10:11], v[152:153], v[186:187], v[10:11] op_sel:[1,0,0]\n"
            "v_pk_fma_f32 v[12:13], v[156:157], v[188:189], v[12:13] op_sel:[1,0,0]\n"
            "v_pk_fma_f32 v[14:15], v[156:157], v[190:191], v[14:15] op_sel:[1,0,0]\n"
            "v_pk_mul_f32 v[160:161], v[160:161], v[128:129]\n"
            "v_pk_mul_f32 v[162:163], v[162:163], v[130:131]\n"
            "v_pk_mul_f32 v[164:165], v[164:165], v[132:133]\n"
            "v_pk_mul_f32 v[166:167], v[166:167], v[134:135]\n"
            "s_waitcnt vmcnt(12) lgkmcnt(0)\n"
            "v_add_u32 v230, v230, v229\n"
            "buffer_load_dwordx4 v[232:235], v230, s[24:27], 0 offen\n"
            "s_bitcmp0_b32 s85, 0\n"
            "s_cbranch_scc1 Lnpf02_%=\n"
            "s_buffer_load_dwordx2 s[86:87], s[28:31], s67 offset:60\n"
            "s_buffer_load_dwordx2 s[86:87], s[28:31], s67 offset:188\n"
            "s_buffer_load_dwordx2 s[86:87], s[28:31], s67 offset:316\n"
            "s_buffer_load_dwordx2 s[86:87], s[28:31], s67 offset:444\n"
            "s_add_u32 s67, s67, 524288\n"
            "Lnpf02_%=:\n"
            "s_lshr_b32 s85, s85, 1\n"
            "v_mfma_f32_16x16x32_bf16 v[128:131], v[192:195], v[96:99], 0\n"
            "v_exp_f32 v168, v136\n"
            "v_exp_f32 v169, v137\n"
            "v_mfma_f32_4x4x4_16b_bf16 v[176:179], v[208:209], v[64:65], 0 cbsz:2 abid:2\n"
            "v_exp_f32 v170, v138\n"
            "v_mfma_f32_4x4x4_16b_bf16 v[180:183], v[208:209], v[66:67], 0 cbsz:2 abid:2\n"
            "v_exp_f32 v171, v139\n"
            "v_mfma_f32_4x4x4_16b_bf16 v[184:187], v[208:209], v[68:69], 0 cbsz:2 abid:2\n"
            "v_exp_f32 v172, v140\n"
            "v_mfma_f32_4x4x4_16b_bf16 v[188:191], v[208:209], v[70:71], 0 cbsz:2 abid:2\n"
            "v_exp_f32 v173, v141\n"
            "v_add_u32_dpp v221, v196, v214 quad_perm:[2,2,2,2] row_mask:0xf bank_mask:0xf\n"
            "v_exp_f32 v174, v142\n"
            "buffer_load_dwordx4 v[64:67], v221, s[28:31], 0 offen\n"
            "buffer_load_dwordx4 v[68:71], v221, s[28:31], 0 offen offset:256\n"
            "v_exp_f32 v175, v143\n"
            "v_pk_fma_f32 v[0:1], v[146:147], v[176:177], v[0:1] op_sel_hi:[0,1,1]\n"
            "v_pk_fma_f32 v[2:3], v[146:147], v[178:179], v[2:3] op_sel_hi:[0,1,1]\n"
            "v_pk_fma_f32 v[4:5], v[150:151], v[180:181], v[4:5] op_sel_hi:[0,1,1]\n"
            "v_pk_fma_f32 v[6:7], v[150:151], v[182:183], v[6:7] op_sel_hi:[0,1,1]\n"
            "v_pk_fma_f32 v[8:9], v[154:155], v[184:185], v[8:9] op_sel_hi:[0,1,1]\n"
            "v_pk_fma_f32 v[10:11], v[154:155], v[186:187], v[10:11] op_sel_hi:[0,1,1]\n"
            "v_pk_fma_f32 v[12:13], v[158:159], v[188:189], v[12:13] op_sel_hi:[0,1,1]\n"
            "v_pk_fma_f32 v[14:15], v[158:159], v[190:191], v[14:15] op_sel_hi:[0,1,1]\n"
            "v_pk_add_f32 v[168:169], v[168:169], 1.0 op_sel_hi:[1,0]\n"
            "v_pk_add_f32 v[170:171], v[170:171], 1.0 op_sel_hi:[1,0]\n"
            "v_pk_add_f32 v[172:173], v[172:173], 1.0 op_sel_hi:[1,0]\n"
            "v_pk_add_f32 v[174:175], v[174:175], 1.0 op_sel_hi:[1,0]\n"
            "v_cvt_pk_bf16_f32 v204, v204, v205\n"
            "v_cvt_pk_bf16_f32 v205, v206, v207\n"
            "ds_write_b64 v212, v[204:205]\n"
            "v_mfma_f32_16x16x32_bf16 v[132:135], v[192:195], v[100:103], 0\n"
            "v_rcp_f32 v168, v168\n"
            "v_rcp_f32 v169, v169\n"
            "v_mfma_f32_4x4x4_16b_bf16 v[176:179], v[208:209], v[80:81], 0 cbsz:2 abid:3\n"
            "v_rcp_f32 v170, v170\n"
            "v_mfma_f32_4x4x4_16b_bf16 v[180:183], v[208:209], v[82:83], 0 cbsz:2 abid:3\n"
            "v_rcp_f32 v171, v171\n"
            "v_mfma_f32_4x4x4_16b_bf16 v[184:187], v[208:209], v[84:85], 0 cbsz:2 abid:3\n"
            "v_rcp_f32 v172, v172\n"
            "v_mfma_f32_4x4x4_16b_bf16 v[188:191], v[208:209], v[86:87], 0 cbsz:2 abid:3\n"
            "v_rcp_f32 v173, v173\n"
            "v_add_u32_dpp v222, v196, v214 quad_perm:[3,3,3,3] row_mask:0xf bank_mask:0xf\n"
            "v_rcp_f32 v174, v174\n"
            "buffer_load_dwordx4 v[80:83], v222, s[28:31], 0 offen\n"
            "buffer_load_dwordx4 v[84:87], v222, s[28:31], 0 offen offset:256\n"
            "v_rcp_f32 v175, v175\n"
            "v_pk_fma_f32 v[0:1], v[146:147], v[176:177], v[0:1] op_sel:[1,0,0]\n"
            "v_pk_fma_f32 v[2:3], v[146:147], v[178:179], v[2:3] op_sel:[1,0,0]\n"
            "v_pk_fma_f32 v[4:5], v[150:151], v[180:181], v[4:5] op_sel:[1,0,0]\n"
            "v_pk_fma_f32 v[6:7], v[150:151], v[182:183], v[6:7] op_sel:[1,0,0]\n"
            "v_pk_fma_f32 v[8:9], v[154:155], v[184:185], v[8:9] op_sel:[1,0,0]\n"
            "v_pk_fma_f32 v[10:11], v[154:155], v[186:187], v[10:11] op_sel:[1,0,0]\n"
            "v_pk_fma_f32 v[12:13], v[158:159], v[188:189], v[12:13] op_sel:[1,0,0]\n"
            "v_pk_fma_f32 v[14:15], v[158:159], v[190:191], v[14:15] op_sel:[1,0,0]\n"
            "v_pk_mul_f32 v[168:169], v[168:169], v[136:137]\n"
            "v_pk_mul_f32 v[170:171], v[170:171], v[138:139]\n"
            "v_pk_mul_f32 v[172:173], v[172:173], v[140:141]\n"
            "v_pk_mul_f32 v[174:175], v[174:175], v[142:143]\n"
            "v_add_u32 v238, 32, v238\n"
            "ds_read_b128 v[196:199], v210\n"
            "ds_read_u16 v211, v238\n"
            "v_mfma_f32_16x16x32_bf16 v[136:139], v[192:195], v[104:107], 0\n"
            "v_exp_f32 v144, v128\n"
            "v_exp_f32 v145, v129\n"
            "s_waitcnt vmcnt(13)\n"
            "v_mfma_f32_4x4x4_16b_bf16 v[176:179], v[208:209], v[40:41], 0 cbsz:2\n"
            "v_exp_f32 v146, v130\n"
            "v_mfma_f32_4x4x4_16b_bf16 v[180:183], v[208:209], v[42:43], 0 cbsz:2\n"
            "v_exp_f32 v147, v131\n"
            "v_mfma_f32_4x4x4_16b_bf16 v[184:187], v[208:209], v[44:45], 0 cbsz:2\n"
            "v_exp_f32 v148, v132\n"
            "v_mfma_f32_4x4x4_16b_bf16 v[188:191], v[208:209], v[46:47], 0 cbsz:2\n"
            "v_exp_f32 v149, v133\n"
            "v_exp_f32 v150, v134\n"
            "buffer_load_dwordx4 v[40:43], v219, s[28:31], 0 offen offset:512\n"
            "buffer_load_dwordx4 v[44:47], v219, s[28:31], 0 offen offset:768\n"
            "v_exp_f32 v151, v135\n"
            "v_pk_fma_f32 v[16:17], v[160:161], v[176:177], v[16:17] op_sel_hi:[0,1,1]\n"
            "v_pk_fma_f32 v[18:19], v[160:161], v[178:179], v[18:19] op_sel_hi:[0,1,1]\n"
            "v_pk_fma_f32 v[20:21], v[164:165], v[180:181], v[20:21] op_sel_hi:[0,1,1]\n"
            "v_pk_fma_f32 v[22:23], v[164:165], v[182:183], v[22:23] op_sel_hi:[0,1,1]\n"
            "v_pk_fma_f32 v[24:25], v[168:169], v[184:185], v[24:25] op_sel_hi:[0,1,1]\n"
            "v_pk_fma_f32 v[26:27], v[168:169], v[186:187], v[26:27] op_sel_hi:[0,1,1]\n"
            "v_pk_fma_f32 v[28:29], v[172:173], v[188:189], v[28:29] op_sel_hi:[0,1,1]\n"
            "v_pk_fma_f32 v[30:31], v[172:173], v[190:191], v[30:31] op_sel_hi:[0,1,1]\n"
            "v_pk_add_f32 v[144:145], v[144:145], 1.0 op_sel_hi:[1,0]\n"
            "v_pk_add_f32 v[146:147], v[146:147], 1.0 op_sel_hi:[1,0]\n"
            "v_pk_add_f32 v[148:149], v[148:149], 1.0 op_sel_hi:[1,0]\n"
            "v_pk_add_f32 v[150:151], v[150:151], 1.0 op_sel_hi:[1,0]\n"
            "v_mfma_f32_16x16x32_bf16 v[140:143], v[192:195], v[108:111], 0\n"
            "v_rcp_f32 v144, v144\n"
            "v_rcp_f32 v145, v145\n"
            "v_mfma_f32_4x4x4_16b_bf16 v[176:179], v[208:209], v[56:57], 0 cbsz:2 abid:1\n"
            "v_rcp_f32 v146, v146\n"
            "v_mfma_f32_4x4x4_16b_bf16 v[180:183], v[208:209], v[58:59], 0 cbsz:2 abid:1\n"
            "v_rcp_f32 v147, v147\n"
            "v_mfma_f32_4x4x4_16b_bf16 v[184:187], v[208:209], v[60:61], 0 cbsz:2 abid:1\n"
            "v_rcp_f32 v148, v148\n"
            "v_mfma_f32_4x4x4_16b_bf16 v[188:191], v[208:209], v[62:63], 0 cbsz:2 abid:1\n"
            "v_rcp_f32 v149, v149\n"
            "v_rcp_f32 v150, v150\n"
            "buffer_load_dwordx4 v[56:59], v220, s[28:31], 0 offen offset:512\n"
            "buffer_load_dwordx4 v[60:63], v220, s[28:31], 0 offen offset:768\n"
            "v_rcp_f32 v151, v151\n"
            "v_pk_fma_f32 v[16:17], v[160:161], v[176:177], v[16:17] op_sel:[1,0,0]\n"
            "v_pk_fma_f32 v[18:19], v[160:161], v[178:179], v[18:19] op_sel:[1,0,0]\n"
            "v_pk_fma_f32 v[20:21], v[164:165], v[180:181], v[20:21] op_sel:[1,0,0]\n"
            "v_pk_fma_f32 v[22:23], v[164:165], v[182:183], v[22:23] op_sel:[1,0,0]\n"
            "v_pk_fma_f32 v[24:25], v[168:169], v[184:185], v[24:25] op_sel:[1,0,0]\n"
            "v_pk_fma_f32 v[26:27], v[168:169], v[186:187], v[26:27] op_sel:[1,0,0]\n"
            "v_pk_fma_f32 v[28:29], v[172:173], v[188:189], v[28:29] op_sel:[1,0,0]\n"
            "v_pk_fma_f32 v[30:31], v[172:173], v[190:191], v[30:31] op_sel:[1,0,0]\n"
            "v_pk_mul_f32 v[144:145], v[144:145], v[128:129]\n"
            "v_pk_mul_f32 v[146:147], v[146:147], v[130:131]\n"
            "v_pk_mul_f32 v[148:149], v[148:149], v[132:133]\n"
            "v_pk_mul_f32 v[150:151], v[150:151], v[134:135]\n"
            "v_mfma_f32_16x16x32_bf16 v[128:131], v[192:195], v[112:115], 0\n"
            "v_exp_f32 v152, v136\n"
            "v_exp_f32 v153, v137\n"
            "s_waitcnt vmcnt(13)\n"
            "v_mfma_f32_4x4x4_16b_bf16 v[176:179], v[208:209], v[72:73], 0 cbsz:2 abid:2\n"
            "v_exp_f32 v154, v138\n"
            "v_mfma_f32_4x4x4_16b_bf16 v[180:183], v[208:209], v[74:75], 0 cbsz:2 abid:2\n"
            "v_exp_f32 v155, v139\n"
            "v_mfma_f32_4x4x4_16b_bf16 v[184:187], v[208:209], v[76:77], 0 cbsz:2 abid:2\n"
            "v_exp_f32 v156, v140\n"
            "v_mfma_f32_4x4x4_16b_bf16 v[188:191], v[208:209], v[78:79], 0 cbsz:2 abid:2\n"
            "v_exp_f32 v157, v141\n"
            "v_exp_f32 v158, v142\n"
            "buffer_load_dwordx4 v[72:75], v221, s[28:31], 0 offen offset:512\n"
            "buffer_load_dwordx4 v[76:79], v221, s[28:31], 0 offen offset:768\n"
            "v_exp_f32 v159, v143\n"
            "v_pk_fma_f32 v[16:17], v[162:163], v[176:177], v[16:17] op_sel_hi:[0,1,1]\n"
            "v_pk_fma_f32 v[18:19], v[162:163], v[178:179], v[18:19] op_sel_hi:[0,1,1]\n"
            "v_pk_fma_f32 v[20:21], v[166:167], v[180:181], v[20:21] op_sel_hi:[0,1,1]\n"
            "v_pk_fma_f32 v[22:23], v[166:167], v[182:183], v[22:23] op_sel_hi:[0,1,1]\n"
            "v_pk_fma_f32 v[24:25], v[170:171], v[184:185], v[24:25] op_sel_hi:[0,1,1]\n"
            "v_pk_fma_f32 v[26:27], v[170:171], v[186:187], v[26:27] op_sel_hi:[0,1,1]\n"
            "v_pk_fma_f32 v[28:29], v[174:175], v[188:189], v[28:29] op_sel_hi:[0,1,1]\n"
            "v_pk_fma_f32 v[30:31], v[174:175], v[190:191], v[30:31] op_sel_hi:[0,1,1]\n"
            "v_pk_add_f32 v[152:153], v[152:153], 1.0 op_sel_hi:[1,0]\n"
            "v_pk_add_f32 v[154:155], v[154:155], 1.0 op_sel_hi:[1,0]\n"
            "v_pk_add_f32 v[156:157], v[156:157], 1.0 op_sel_hi:[1,0]\n"
            "v_pk_add_f32 v[158:159], v[158:159], 1.0 op_sel_hi:[1,0]\n"
            "v_mfma_f32_16x16x32_bf16 v[132:135], v[192:195], v[116:119], 0\n"
            "v_rcp_f32 v152, v152\n"
            "v_rcp_f32 v153, v153\n"
            "v_mfma_f32_4x4x4_16b_bf16 v[176:179], v[208:209], v[88:89], 0 cbsz:2 abid:3\n"
            "v_rcp_f32 v154, v154\n"
            "v_mfma_f32_4x4x4_16b_bf16 v[180:183], v[208:209], v[90:91], 0 cbsz:2 abid:3\n"
            "v_rcp_f32 v155, v155\n"
            "v_mfma_f32_4x4x4_16b_bf16 v[184:187], v[208:209], v[92:93], 0 cbsz:2 abid:3\n"
            "v_rcp_f32 v156, v156\n"
            "v_mfma_f32_4x4x4_16b_bf16 v[188:191], v[208:209], v[94:95], 0 cbsz:2 abid:3\n"
            "ds_read_b64 v[208:209], v213\n"
            "v_rcp_f32 v157, v157\n"
            "v_rcp_f32 v158, v158\n"
            "buffer_load_dwordx4 v[88:91], v222, s[28:31], 0 offen offset:512\n"
            "buffer_load_dwordx4 v[92:95], v222, s[28:31], 0 offen offset:768\n"
            "v_rcp_f32 v159, v159\n"
            "v_pk_fma_f32 v[16:17], v[162:163], v[176:177], v[16:17] op_sel:[1,0,0]\n"
            "v_pk_fma_f32 v[18:19], v[162:163], v[178:179], v[18:19] op_sel:[1,0,0]\n"
            "v_pk_fma_f32 v[20:21], v[166:167], v[180:181], v[20:21] op_sel:[1,0,0]\n"
            "v_pk_fma_f32 v[22:23], v[166:167], v[182:183], v[22:23] op_sel:[1,0,0]\n"
            "v_pk_fma_f32 v[24:25], v[170:171], v[184:185], v[24:25] op_sel:[1,0,0]\n"
            "v_pk_fma_f32 v[26:27], v[170:171], v[186:187], v[26:27] op_sel:[1,0,0]\n"
            "v_pk_fma_f32 v[28:29], v[174:175], v[188:189], v[28:29] op_sel:[1,0,0]\n"
            "v_pk_fma_f32 v[30:31], v[174:175], v[190:191], v[30:31] op_sel:[1,0,0]\n"
            "v_pk_mul_f32 v[152:153], v[152:153], v[136:137]\n"
            "v_pk_mul_f32 v[154:155], v[154:155], v[138:139]\n"
            "v_pk_mul_f32 v[156:157], v[156:157], v[140:141]\n"
            "v_pk_mul_f32 v[158:159], v[158:159], v[142:143]\n"
            "s_sub_u32 s69, s69, 4\n"
            "Lnext_%=:\n"
            "s_sub_u32 s73, s73, 1\n"
            "Ldisp_%=:\n"
            "s_bitcmp1_b32 s60, s73\n"
            "s_cbranch_scc1 Lprio_%=\n"
            "Ld2_%=:\n"
            "s_cmp_gt_u32 s69, 4\n"
            "s_cbranch_scc1 Lfast_%=\n"
            "v_mfma_f32_16x16x32_bf16 v[136:139], v[192:195], v[120:123], 0\n"
            "v_exp_f32 v160, v128\n"
            "v_add_u32 v237, 32, v237\n"
            "v_exp_f32 v161, v129\n"
            "s_waitcnt vmcnt(12) lgkmcnt(0)\n"
            "v_mfma_f32_4x4x4_16b_bf16 v[176:179], v[208:209], v[32:33], 0 cbsz:2\n"
            "v_exp_f32 v162, v130\n"
            "v_mfma_f32_4x4x4_16b_bf16 v[180:183], v[208:209], v[34:35], 0 cbsz:2\n"
            "v_exp_f32 v163, v131\n"
            "v_mfma_f32_4x4x4_16b_bf16 v[184:187], v[208:209], v[36:37], 0 cbsz:2\n"
            "v_exp_f32 v164, v132\n"
            "v_mfma_f32_4x4x4_16b_bf16 v[188:191], v[208:209], v[38:39], 0 cbsz:2\n"
            "v_exp_f32 v165, v133\n"
            "v_add_u32_dpp v219, v196, v214 quad_perm:[0,0,0,0] row_mask:0xf bank_mask:0xf\n"
            "v_mfma_f32_4x4x4_16b_bf16 v[204:207], v[202:203], v[198:199], 0\n"
            "v_exp_f32 v166, v134\n"
            "buffer_load_dwordx4 v[32:35], v219, s[28:31], 0 offen\n"
            "buffer_load_dwordx4 v[36:39], v219, s[28:31], 0 offen offset:256\n"
            "v_exp_f32 v167, v135\n"
            "v_pk_fma_f32 v[0:1], v[144:145], v[176:177], v[0:1] op_sel_hi:[0,1,1]\n"
            "v_pk_fma_f32 v[2:3], v[144:145], v[178:179], v[2:3] op_sel_hi:[0,1,1]\n"
            "v_pk_fma_f32 v[4:5], v[148:149], v[180:181], v[4:5] op_sel_hi:[0,1,1]\n"
            "v_pk_fma_f32 v[6:7], v[148:149], v[182:183], v[6:7] op_sel_hi:[0,1,1]\n"
            "v_pk_fma_f32 v[8:9], v[152:153], v[184:185], v[8:9] op_sel_hi:[0,1,1]\n"
            "v_pk_fma_f32 v[10:11], v[152:153], v[186:187], v[10:11] op_sel_hi:[0,1,1]\n"
            "v_pk_fma_f32 v[12:13], v[156:157], v[188:189], v[12:13] op_sel_hi:[0,1,1]\n"
            "v_pk_fma_f32 v[14:15], v[156:157], v[190:191], v[14:15] op_sel_hi:[0,1,1]\n"
            "v_pk_add_f32 v[160:161], v[160:161], 1.0 op_sel_hi:[1,0]\n"
            "v_pk_add_f32 v[162:163], v[162:163], 1.0 op_sel_hi:[1,0]\n"
            "v_pk_add_f32 v[164:165], v[164:165], 1.0 op_sel_hi:[1,0]\n"
            "v_pk_add_f32 v[166:167], v[166:167], 1.0 op_sel_hi:[1,0]\n"
            "s_cmp_eq_u32 s69, 1\n"
            "s_cbranch_scc1 Lf00_%=\n"
            "Lr00_%=:\n"
            "v_mfma_f32_16x16x32_bf16 v[140:143], v[192:195], v[124:127], 0\n"
            "ds_bpermute_b32 v192, v253, v232\n"
            "ds_bpermute_b32 v193, v253, v233\n"
            "ds_bpermute_b32 v194, v253, v234\n"
            "ds_bpermute_b32 v195, v253, v235\n"
            "ds_read_b32 v230, v211 offset:4\n"
            "ds_read_u16 v210, v237\n"
            "v_rcp_f32 v160, v160\n"
            "v_rcp_f32 v161, v161\n"
            "v_mfma_f32_4x4x4_16b_bf16 v[176:179], v[208:209], v[48:49], 0 cbsz:2 abid:1\n"
            "v_rcp_f32 v162, v162\n"
            "v_mfma_f32_4x4x4_16b_bf16 v[180:183], v[208:209], v[50:51], 0 cbsz:2 abid:1\n"
            "v_rcp_f32 v163, v163\n"
            "v_mfma_f32_4x4x4_16b_bf16 v[184:187], v[208:209], v[52:53], 0 cbsz:2 abid:1\n"
            "v_rcp_f32 v164, v164\n"
            "v_mfma_f32_4x4x4_16b_bf16 v[188:191], v[208:209], v[54:55], 0 cbsz:2 abid:1\n"
            "v_rcp_f32 v165, v165\n"
            "v_add_u32_dpp v220, v196, v214 quad_perm:[1,1,1,1] row_mask:0xf bank_mask:0xf\n"
            "v_mfma_f32_4x4x4_16b_bf16 v[204:207], v[200:201], v[198:199], v[204:207]\n"
            "v_rcp_f32 v166, v166\n"
            "buffer_load_dwordx4 v[48:51], v220, s[28:31], 0 offen\n"
            "buffer_load_dwordx4 v[52:55], v220, s[28:31], 0 offen offset:256\n"
            "v_rcp_f32 v167, v167\n"
            "v_pk_fma_f32 v[0:1], v[144:145], v[176:177], v[0:1] op_sel:[1,0,0]\n"
            "v_pk_fma_f32 v[2:3], v[144:145], v[178:179], v[2:3] op_sel:[1,0,0]\n"
            "v_pk_fma_f32 v[4:5], v[148:149], v[180:181], v[4:5] op_sel:[1,0,0]\n"
            "v_pk_fma_f32 v[6:7], v[148:149], v[182:183], v[6:7] op_sel:[1,0,0]\n"
            "v_pk_fma_f32 v[8:9], v[152:153], v[184:185], v[8:9] op_sel:[1,0,0]\n"
            "v_pk_fma_f32 v[10:11], v[152:153], v[186:187], v[10:11] op_sel:[1,0,0]\n"
            "v_pk_fma_f32 v[12:13], v[156:157], v[188:189], v[12:13] op_sel:[1,0,0]\n"
            "v_pk_fma_f32 v[14:15], v[156:157], v[190:191], v[14:15] op_sel:[1,0,0]\n"
            "v_pk_mul_f32 v[160:161], v[160:161], v[128:129]\n"
            "v_pk_mul_f32 v[162:163], v[162:163], v[130:131]\n"
            "v_pk_mul_f32 v[164:165], v[164:165], v[132:133]\n"
            "v_pk_mul_f32 v[166:167], v[166:167], v[134:135]\n"
            "s_cmp_eq_u32 s69, 2\n"
            "s_cbranch_scc1 Lf01_%=\n"
            "Lr01_%=:\n"
            "s_waitcnt vmcnt(12) lgkmcnt(0)\n"
            "v_add_u32 v230, v230, v229\n"
            "buffer_load_dwordx4 v[232:235], v230, s[24:27], 0 offen\n"
            "s_bitcmp0_b32 s85, 0\n"
            "s_cbranch_scc1 Lnps02_%=\n"
            "s_buffer_load_dwordx2 s[86:87], s[28:31], s67 offset:60\n"
            "s_buffer_load_dwordx2 s[86:87], s[28:31], s67 offset:188\n"
            "s_buffer_load_dwordx2 s[86:87], s[28:31], s67 offset:316\n"
            "s_buffer_load_dwordx2 s[86:87], s[28:31], s67 offset:444\n"
            "s_add_u32 s67, s67, 524288\n"
            "Lnps02_%=:\n"
            "s_lshr_b32 s85, s85, 1\n"
            "v_mfma_f32_16x16x32_bf16 v[128:131], v[192:195], v[96:99], 0\n"
            "v_exp_f32 v168, v136\n"
            "v_exp_f32 v169, v137\n"
            "v_mfma_f32_4x4x4_16b_bf16 v[176:179], v[208:209], v[64:65], 0 cbsz:2 abid:2\n"
            "v_exp_f32 v170, v138\n"
            "v_mfma_f32_4x4x4_16b_bf16 v[180:183], v[208:209], v[66:67], 0 cbsz:2 abid:2\n"
            "v_exp_f32 v171, v139\n"
            "v_mfma_f32_4x4x4_16b_bf16 v[184:187], v[208:209], v[68:69], 0 cbsz:2 abid:2\n"
            "v_exp_f32 v172, v140\n"
            "v_mfma_f32_4x4x4_16b_bf16 v[188:191], v[208:209], v[70:71], 0 cbsz:2 abid:2\n"
            "v_exp_f32 v173, v141\n"
            "v_add_u32_dpp v221, v196, v214 quad_perm:[2,2,2,2] row_mask:0xf bank_mask:0xf\n"
            "v_exp_f32 v174, v142\n"
            "buffer_load_dwordx4 v[64:67], v221, s[28:31], 0 offen\n"
            "buffer_load_dwordx4 v[68:71], v221, s[28:31], 0 offen offset:256\n"
            "v_exp_f32 v175, v143\n"
            "v_pk_fma_f32 v[0:1], v[146:147], v[176:177], v[0:1] op_sel_hi:[0,1,1]\n"
            "v_pk_fma_f32 v[2:3], v[146:147], v[178:179], v[2:3] op_sel_hi:[0,1,1]\n"
            "v_pk_fma_f32 v[4:5], v[150:151], v[180:181], v[4:5] op_sel_hi:[0,1,1]\n"
            "v_pk_fma_f32 v[6:7], v[150:151], v[182:183], v[6:7] op_sel_hi:[0,1,1]\n"
            "v_pk_fma_f32 v[8:9], v[154:155], v[184:185], v[8:9] op_sel_hi:[0,1,1]\n"
            "v_pk_fma_f32 v[10:11], v[154:155], v[186:187], v[10:11] op_sel_hi:[0,1,1]\n"
            "v_pk_fma_f32 v[12:13], v[158:159], v[188:189], v[12:13] op_sel_hi:[0,1,1]\n"
            "v_pk_fma_f32 v[14:15], v[158:159], v[190:191], v[14:15] op_sel_hi:[0,1,1]\n"
            "v_pk_add_f32 v[168:169], v[168:169], 1.0 op_sel_hi:[1,0]\n"
            "v_pk_add_f32 v[170:171], v[170:171], 1.0 op_sel_hi:[1,0]\n"
            "v_pk_add_f32 v[172:173], v[172:173], 1.0 op_sel_hi:[1,0]\n"
            "v_pk_add_f32 v[174:175], v[174:175], 1.0 op_sel_hi:[1,0]\n"
            "v_cvt_pk_bf16_f32 v204, v204, v205\n"
            "v_cvt_pk_bf16_f32 v205, v206, v207\n"
            "ds_write_b64 v212, v[204:205]\n"
            "s_cmp_eq_u32 s69, 3\n"
            "s_cbranch_scc1 Lf02_%=\n"
            "Lr02_%=:\n"
            "v_mfma_f32_16x16x32_bf16 v[132:135], v[192:195], v[100:103], 0\n"
            "v_rcp_f32 v168, v168\n"
            "v_rcp_f32 v169, v169\n"
            "v_mfma_f32_4x4x4_16b_bf16 v[176:179], v[208:209], v[80:81], 0 cbsz:2 abid:3\n"
            "v_rcp_f32 v170, v170\n"
            "v_mfma_f32_4x4x4_16b_bf16 v[180:183], v[208:209], v[82:83], 0 cbsz:2 abid:3\n"
            "v_rcp_f32 v171, v171\n"
            "v_mfma_f32_4x4x4_16b_bf16 v[184:187], v[208:209], v[84:85], 0 cbsz:2 abid:3\n"
            "v_rcp_f32 v172, v172\n"
            "v_mfma_f32_4x4x4_16b_bf16 v[188:191], v[208:209], v[86:87], 0 cbsz:2 abid:3\n"
            "v_rcp_f32 v173, v173\n"
            "v_add_u32_dpp v222, v196, v214 quad_perm:[3,3,3,3] row_mask:0xf bank_mask:0xf\n"
            "v_rcp_f32 v174, v174\n"
            "buffer_load_dwordx4 v[80:83], v222, s[28:31], 0 offen\n"
            "buffer_load_dwordx4 v[84:87], v222, s[28:31], 0 offen offset:256\n"
            "v_rcp_f32 v175, v175\n"
            "v_pk_fma_f32 v[0:1], v[146:147], v[176:177], v[0:1] op_sel:[1,0,0]\n"
            "v_pk_fma_f32 v[2:3], v[146:147], v[178:179], v[2:3] op_sel:[1,0,0]\n"
            "v_pk_fma_f32 v[4:5], v[150:151], v[180:181], v[4:5] op_sel:[1,0,0]\n"
            "v_pk_fma_f32 v[6:7], v[150:151], v[182:183], v[6:7] op_sel:[1,0,0]\n"
            "v_pk_fma_f32 v[8:9], v[154:155], v[184:185], v[8:9] op_sel:[1,0,0]\n"
            "v_pk_fma_f32 v[10:11], v[154:155], v[186:187], v[10:11] op_sel:[1,0,0]\n"
            "v_pk_fma_f32 v[12:13], v[158:159], v[188:189], v[12:13] op_sel:[1,0,0]\n"
            "v_pk_fma_f32 v[14:15], v[158:159], v[190:191], v[14:15] op_sel:[1,0,0]\n"
            "v_pk_mul_f32 v[168:169], v[168:169], v[136:137]\n"
            "v_pk_mul_f32 v[170:171], v[170:171], v[138:139]\n"
            "v_pk_mul_f32 v[172:173], v[172:173], v[140:141]\n"
            "v_pk_mul_f32 v[174:175], v[174:175], v[142:143]\n"
            "s_cmp_eq_u32 s69, 4\n"
            "s_cbranch_scc1 Lf03_%=\n"
            "Lr03_%=:\n"
            "v_add_u32 v238, 32, v238\n"
            "ds_read_b128 v[196:199], v210\n"
            "ds_read_u16 v211, v238\n"
            "v_mfma_f32_16x16x32_bf16 v[136:139], v[192:195], v[104:107], 0\n"
            "v_exp_f32 v144, v128\n"
            "v_exp_f32 v145, v129\n"
            "s_waitcnt vmcnt(13)\n"
            "v_mfma_f32_4x4x4_16b_bf16 v[176:179], v[208:209], v[40:41], 0 cbsz:2\n"
            "v_exp_f32 v146, v130\n"
            "v_mfma_f32_4x4x4_16b_bf16 v[180:183], v[208:209], v[42:43], 0 cbsz:2\n"
            "v_exp_f32 v147, v131\n"
            "v_mfma_f32_4x4x4_16b_bf16 v[184:187], v[208:209], v[44:45], 0 cbsz:2\n"
            "v_exp_f32 v148, v132\n"
            "v_mfma_f32_4x4x4_16b_bf16 v[188:191], v[208:209], v[46:47], 0 cbsz:2\n"
            "v_exp_f32 v149, v133\n"
            "v_exp_f32 v150, v134\n"
            "buffer_load_dwordx4 v[40:43], v219, s[28:31], 0 offen offset:512\n"
            "buffer_load_dwordx4 v[44:47], v219, s[28:31], 0 offen offset:768\n"
            "v_exp_f32 v151, v135\n"
            "v_pk_fma_f32 v[16:17], v[160:161], v[176:177], v[16:17] op_sel_hi:[0,1,1]\n"
            "v_pk_fma_f32 v[18:19], v[160:161], v[178:179], v[18:19] op_sel_hi:[0,1,1]\n"
            "v_pk_fma_f32 v[20:21], v[164:165], v[180:181], v[20:21] op_sel_hi:[0,1,1]\n"
            "v_pk_fma_f32 v[22:23], v[164:165], v[182:183], v[22:23] op_sel_hi:[0,1,1]\n"
            "v_pk_fma_f32 v[24:25], v[168:169], v[184:185], v[24:25] op_sel_hi:[0,1,1]\n"
            "v_pk_fma_f32 v[26:27], v[168:169], v[186:187], v[26:27] op_sel_hi:[0,1,1]\n"
            "v_pk_fma_f32 v[28:29], v[172:173], v[188:189], v[28:29] op_sel_hi:[0,1,1]\n"
            "v_pk_fma_f32 v[30:31], v[172:173], v[190:191], v[30:31] op_sel_hi:[0,1,1]\n"
            "v_pk_add_f32 v[144:145], v[144:145], 1.0 op_sel_hi:[1,0]\n"
            "v_pk_add_f32 v[146:147], v[146:147], 1.0 op_sel_hi:[1,0]\n"
            "v_pk_add_f32 v[148:149], v[148:149], 1.0 op_sel_hi:[1,0]\n"
            "v_pk_add_f32 v[150:151], v[150:151], 1.0 op_sel_hi:[1,0]\n"
            "s_cmp_eq_u32 s69, 1\n"
            "s_cbranch_scc1 Lf10_%=\n"
            "Lr10_%=:\n"
            "v_mfma_f32_16x16x32_bf16 v[140:143], v[192:195], v[108:111], 0\n"
            "v_rcp_f32 v144, v144\n"
            "v_rcp_f32 v145, v145\n"
            "v_mfma_f32_4x4x4_16b_bf16 v[176:179], v[208:209], v[56:57], 0 cbsz:2 abid:1\n"
            "v_rcp_f32 v146, v146\n"
            "v_mfma_f32_4x4x4_16b_bf16 v[180:183], v[208:209], v[58:59], 0 cbsz:2 abid:1\n"
            "v_rcp_f32 v147, v147\n"
            "v_mfma_f32_4x4x4_16b_bf16 v[184:187], v[208:209], v[60:61], 0 cbsz:2 abid:1\n"
            "v_rcp_f32 v148, v148\n"
            "v_mfma_f32_4x4x4_16b_bf16 v[188:191], v[208:209], v[62:63], 0 cbsz:2 abid:1\n"
            "v_rcp_f32 v149, v149\n"
            "v_rcp_f32 v150, v150\n"
            "buffer_load_dwordx4 v[56:59], v220, s[28:31], 0 offen offset:512\n"
            "buffer_load_dwordx4 v[60:63], v220, s[28:31], 0 offen offset:768\n"
            "v_rcp_f32 v151, v151\n"
            "v_pk_fma_f32 v[16:17], v[160:161], v[176:177], v[16:17] op_sel:[1,0,0]\n"
            "v_pk_fma_f32 v[18:19], v[160:161], v[178:179], v[18:19] op_sel:[1,0,0]\n"
            "v_pk_fma_f32 v[20:21], v[164:165], v[180:181], v[20:21] op_sel:[1,0,0]\n"
            "v_pk_fma_f32 v[22:23], v[164:165], v[182:183], v[22:23] op_sel:[1,0,0]\n"
            "v_pk_fma_f32 v[24:25], v[168:169], v[184:185], v[24:25] op_sel:[1,0,0]\n"
            "v_pk_fma_f32 v[26:27], v[168:169], v[186:187], v[26:27] op_sel:[1,0,0]\n"
            "v_pk_fma_f32 v[28:29], v[172:173], v[188:189], v[28:29] op_sel:[1,0,0]\n"
            "v_pk_fma_f32 v[30:31], v[172:173], v[190:191], v[30:31] op_sel:[1,0,0]\n"
            "v_pk_mul_f32 v[144:145], v[144:145], v[128:129]\n"
            "v_pk_mul_f32 v[146:147], v[146:147], v[130:131]\n"
            "v_pk_mul_f32 v[148:149], v[148:149], v[132:133]\n"
            "v_pk_mul_f32 v[150:151], v[150:151], v[134:135]\n"
            "s_cmp_eq_u32 s69, 2\n"
            "s_cbranch_scc1 Lf11_%=\n"
            "Lr11_%=:\n"
            "v_mfma_f32_16x16x32_bf16 v[128:131], v[192:195], v[112:115], 0\n"
            "v_exp_f32 v152, v136\n"
            "v_exp_f32 v153, v137\n"
            "s_waitcnt vmcnt(13)\n"
            "v_mfma_f32_4x4x4_16b_bf16 v[176:179], v[208:209], v[72:73], 0 cbsz:2 abid:2\n"
            "v_exp_f32 v154, v138\n"
            "v_mfma_f32_4x4x4_16b_bf16 v[180:183], v[208:209], v[74:75], 0 cbsz:2 abid:2\n"
            "v_exp_f32 v155, v139\n"
            "v_mfma_f32_4x4x4_16b_bf16 v[184:187], v[208:209], v[76:77], 0 cbsz:2 abid:2\n"
            "v_exp_f32 v156, v140\n"
            "v_mfma_f32_4x4x4_16b_bf16 v[188:191], v[208:209], v[78:79], 0 cbsz:2 abid:2\n"
            "v_exp_f32 v157, v141\n"
            "v_exp_f32 v158, v142\n"
            "buffer_load_dwordx4 v[72:75], v221, s[28:31], 0 offen offset:512\n"
            "buffer_load_dwordx4 v[76:79], v221, s[28:31], 0 offen offset:768\n"
            "v_exp_f32 v159, v143\n"
            "v_pk_fma_f32 v[16:17], v[162:163], v[176:177], v[16:17] op_sel_hi:[0,1,1]\n"
            "v_pk_fma_f32 v[18:19], v[162:163], v[178:179], v[18:19] op_sel_hi:[0,1,1]\n"
            "v_pk_fma_f32 v[20:21], v[166:167], v[180:181], v[20:21] op_sel_hi:[0,1,1]\n"
            "v_pk_fma_f32 v[22:23], v[166:167], v[182:183], v[22:23] op_sel_hi:[0,1,1]\n"
            "v_pk_fma_f32 v[24:25], v[170:171], v[184:185], v[24:25] op_sel_hi:[0,1,1]\n"
            "v_pk_fma_f32 v[26:27], v[170:171], v[186:187], v[26:27] op_sel_hi:[0,1,1]\n"
            "v_pk_fma_f32 v[28:29], v[174:175], v[188:189], v[28:29] op_sel_hi:[0,1,1]\n"
            "v_pk_fma_f32 v[30:31], v[174:175], v[190:191], v[30:31] op_sel_hi:[0,1,1]\n"
            "v_pk_add_f32 v[152:153], v[152:153], 1.0 op_sel_hi:[1,0]\n"
            "v_pk_add_f32 v[154:155], v[154:155], 1.0 op_sel_hi:[1,0]\n"
            "v_pk_add_f32 v[156:157], v[156:157], 1.0 op_sel_hi:[1,0]\n"
            "v_pk_add_f32 v[158:159], v[158:159], 1.0 op_sel_hi:[1,0]\n"
            "s_cmp_eq_u32 s69, 3\n"
            "s_cbranch_scc1 Lf12_%=\n"
            "Lr12_%=:\n"
            "v_mfma_f32_16x16x32_bf16 v[132:135], v[192:195], v[116:119], 0\n"
            "v_rcp_f32 v152, v152\n"
            "v_rcp_f32 v153, v153\n"
            "v_mfma_f32_4x4x4_16b_bf16 v[176:179], v[208:209], v[88:89], 0 cbsz:2 abid:3\n"
            "v_rcp_f32 v154, v154\n"
            "v_mfma_f32_4x4x4_16b_bf16 v[180:183], v[208:209], v[90:91], 0 cbsz:2 abid:3\n"
            "v_rcp_f32 v155, v155\n"
            "v_mfma_f32_4x4x4_16b_bf16 v[184:187], v[208:209], v[92:93], 0 cbsz:2 abid:3\n"
            "v_rcp_f32 v156, v156\n"
            "v_mfma_f32_4x4x4_16b_bf16 v[188:191], v[208:209], v[94:95], 0 cbsz:2 abid:3\n"
            "ds_read_b64 v[208:209], v213\n"
            "v_rcp_f32 v157, v157\n"
            "v_rcp_f32 v158, v158\n"
            "buffer_load_dwordx4 v[88:91], v222, s[28:31], 0 offen offset:512\n"
            "buffer_load_dwordx4 v[92:95], v222, s[28:31], 0 offen offset:768\n"
            "v_rcp_f32 v159, v159\n"
            "v_pk_fma_f32 v[16:17], v[162:163], v[176:177], v[16:17] op_sel:[1,0,0]\n"
            "v_pk_fma_f32 v[18:19], v[162:163], v[178:179], v[18:19] op_sel:[1,0,0]\n"
            "v_pk_fma_f32 v[20:21], v[166:167], v[180:181], v[20:21] op_sel:[1,0,0]\n"
            "v_pk_fma_f32 v[22:23], v[166:167], v[182:183], v[22:23] op_sel:[1,0,0]\n"
            "v_pk_fma_f32 v[24:25], v[170:171], v[184:185], v[24:25] op_sel:[1,0,0]\n"
            "v_pk_fma_f32 v[26:27], v[170:171], v[186:187], v[26:27] op_sel:[1,0,0]\n"
            "v_pk_fma_f32 v[28:29], v[174:175], v[188:189], v[28:29] op_sel:[1,0,0]\n"
            "v_pk_fma_f32 v[30:31], v[174:175], v[190:191], v[30:31] op_sel:[1,0,0]\n"
            "v_pk_mul_f32 v[152:153], v[152:153], v[136:137]\n"
            "v_pk_mul_f32 v[154:155], v[154:155], v[138:139]\n"
            "v_pk_mul_f32 v[156:157], v[156:157], v[140:141]\n"
            "v_pk_mul_f32 v[158:159], v[158:159], v[142:143]\n"
            "s_cmp_eq_u32 s69, 4\n"
            "s_cbranch_scc1 Lf13_%=\n"
            "Lr13_%=:\n"
            "s_mov_b32 s81, s69\n"
            "s_and_b32 s68, s72, 0xff\n"
            "s_add_u32 s69, s69, s68\n"
            "s_lshr_b32 s72, s72, 8\n"
            "v_mov_b32 v231, v239\n"
            "v_mov_b32 v239, v252\n"
            "s_and_b32 s68, s62, 14\n"
            "s_lshr_b32 s62, s62, 4\n"
            "s_cmp_lg_u32 s68, 0\n"
            "s_cbranch_scc1 Lb1_%=\n"
            "v_mov_b32 v215, v216\n"
            "s_branch Lnext_%=\n"
            "Lb1_%=:\n"
            "s_cmp_lg_u32 s68, 6\n"
            "s_cbranch_scc1 Lb2_%=\n"
            "s_lshl_b32 s68, s3, 2\n"
            "v_mov_b32 v176, s68\n"
            "ds_write_b32 v176, v176\n"
            "s_branch Lnext_%=\n"
            "Lb2_%=:\n"
            "s_branch Lnext_%=\n"
            "Lpub_%=:\n"
            "s_waitcnt vmcnt(0)\n"
            "v_mov_b32 v176, 4\n"
            "v_mov_b32 v177, 1\n"
            "s_mov_b64 exec, 1\n"
            "ds_add_rtn_u32 v177, v176, v177\n"
            "s_waitcnt lgkmcnt(0)\n"
            "v_readfirstlane_b32 s68, v177\n"
            "s_mov_b64 exec, 0xff\n"
            "v_mov_b32 v176, s90\n"
            "v_mov_b32 v177, s90\n"
            "v_mov_b32 v178, s90\n"
            "v_mov_b32 v179, s90\n"
            "s_cmp_lg_u32 s68, 7\n"
            "s_cbranch_scc1 Lpub2_%=\n"
            "global_store_dwordx4 v214, v[176:179], s[56:57] sc1\n"
            "Lpub2_%=:\n"
            "s_mov_b64 exec, -1\n"
            "s_nop 0\n"
            "s_setpc_b64 s[70:71]\n"
            "Lprio_%=:\n"
            "s_cmp_eq_u32 s73, 1\n"
            "s_cbranch_scc1 Llast_%=\n"
            "s_lshl_b32 s68, s73, 1\n"
            "s_lshr_b32 s68, s61, s68\n"
            "s_bitcmp1_b32 s68, 1\n"
            "s_cbranch_scc1 Lq2_%=\n"
            "s_bitcmp1_b32 s68, 0\n"
            "s_setprio 0\n"
            "s_cbranch_scc0 Lq9_%=\n"
            "s_setprio 1\n"
            "s_branch Lq9_%=\n"
            "Lq2_%=:\n"
            "s_bitcmp1_b32 s68, 0\n"
            "s_setprio 2\n"
            "s_cbranch_scc0 Lq9_%=\n"
            "s_setprio 3\n"
            "Lq9_%=:\n"
            "s_cmp_lg_u32 s73, 2\n"
            "s_cbranch_scc1 Ld2_%=\n"
            "v_mov_b32 v223, 0\n"
            "global_load_dword v223, v223, s[58:59] sc1\n"
            "s_branch Ld2_%=\n"
            "Lf00_%=:\n"
            "s_call_b64 s[70:71], Lflush0_%=\n"
            "s_branch Lr00_%=\n"
            "Lf01_%=:\n"
            "s_call_b64 s[70:71], Lflush0_%=\n"
            "s_branch Lr01_%=\n"
            "Lf02_%=:\n"
            "s_call_b64 s[70:71], Lflush0_%=\n"
            "s_branch Lr02_%=\n"
            "Lf03_%=:\n"
            "s_call_b64 s[70:71], Lflush0_%=\n"
            "s_branch Lr03_%=\n"
            "Lflush0_%=:\n"
            "s_bitcmp0_b32 s62, 0\n"
            "s_cbranch_scc1 Lfa0_%=\n"
            "Lfw0_%=:\n"
            "v_mov_b32 v184, s41\n"
            "ds_read_b32 v184, v184\n"
            "ds_read_b128 v[176:179], v231 offset:0\n"
            "ds_read_b128 v[180:183], v231 offset:1024\n"
            "s_waitcnt lgkmcnt(0)\n"
            "v_readfirstlane_b32 s68, v184\n"
            "s_cmp_eq_u32 s68, 0\n"
            "s_cbranch_scc1 Lfw0_%=\n"
            "v_mfma_f32_4x4x4_16b_bf16 v[0:3], v[224:225], v[176:177], v[0:3]\n"
            "v_mfma_f32_4x4x4_16b_bf16 v[4:7], v[224:225], v[178:179], v[4:7]\n"
            "v_mfma_f32_4x4x4_16b_bf16 v[8:11], v[224:225], v[180:181], v[8:11]\n"
            "v_mfma_f32_4x4x4_16b_bf16 v[12:15], v[224:225], v[182:183], v[12:15]\n"
            "s_nop 1\n"
            "Lfa0_%=:\n"
            "s_cmp_lg_u32 s72, 0xff\n"
            "s_cbranch_scc1 Lfb0_%=\n"
            "ds_read_b128 v[184:187], v236 offset:0\n"
            "ds_read_b128 v[188:191], v236 offset:1024\n"
            "Lfb0_%=:\n"
            "v_cvt_pk_bf16_f32 v176, v0, v1\n"
            "v_cvt_pk_bf16_f32 v177, v2, v3\n"
            "v_cvt_pk_bf16_f32 v178, v4, v5\n"
            "v_cvt_pk_bf16_f32 v179, v6, v7\n"
            "v_cvt_pk_bf16_f32 v180, v8, v9\n"
            "v_cvt_pk_bf16_f32 v181, v10, v11\n"
            "v_cvt_pk_bf16_f32 v182, v12, v13\n"
            "v_cvt_pk_bf16_f32 v183, v14, v15\n"
            "s_bitcmp1_b32 s62, 1\n"
            "s_cbranch_scc1 Lpark0_%=\n"
            "buffer_store_dwordx4 v[176:179], v215, s[20:23], 0 offen offset:0 sc1\n"
            "buffer_store_dwordx4 v[180:183], v215, s[20:23], 0 offen offset:256 sc1\n"
            "s_branch Lzero0_%=\n"
            "Lpark0_%=:\n"
            "ds_write_b128 v231, v[176:179] offset:0\n"
            "ds_write_b128 v231, v[180:183] offset:1024\n"
            "Lzero0_%=:\n"
            "s_cmp_lg_u32 s72, 0xff\n"
            "s_cbranch_scc1 Lzz0_%=\n"
            "s_waitcnt lgkmcnt(0)\n"
            "v_mfma_f32_4x4x4_16b_bf16 v[0:3], v[224:225], v[184:185], 0\n"
            "v_mfma_f32_4x4x4_16b_bf16 v[4:7], v[224:225], v[186:187], 0\n"
            "v_mfma_f32_4x4x4_16b_bf16 v[8:11], v[224:225], v[188:189], 0\n"
            "v_mfma_f32_4x4x4_16b_bf16 v[12:15], v[224:225], v[190:191], 0\n"
            "s_setpc_b64 s[70:71]\n"
            "Lzz0_%=:\n"
            "v_mov_b64 v[0:1], 0\n"
            "v_mov_b64 v[2:3], 0\n"
            "v_mov_b64 v[4:5], 0\n"
            "v_mov_b64 v[6:7], 0\n"
            "v_mov_b64 v[8:9], 0\n"
            "v_mov_b64 v[10:11], 0\n"
            "v_mov_b64 v[12:13], 0\n"
            "v_mov_b64 v[14:15], 0\n"
            "s_setpc_b64 s[70:71]\n"
            "Lf10_%=:\n"
            "s_call_b64 s[70:71], Lflush1_%=\n"
            "s_branch Lr10_%=\n"
            "Lf11_%=:\n"
            "s_call_b64 s[70:71], Lflush1_%=\n"
            "s_branch Lr11_%=\n"
            "Lf12_%=:\n"
            "s_call_b64 s[70:71], Lflush1_%=\n"
            "s_branch Lr12_%=\n"
            "Lf13_%=:\n"
            "s_call_b64 s[70:71], Lflush1_%=\n"
            "s_branch Lr13_%=\n"
            "Lflush1_%=:\n"
            "s_bitcmp0_b32 s62, 0\n"
            "s_cbranch_scc1 Lfa1_%=\n"
            "Lfw1_%=:\n"
            "v_mov_b32 v184, s41\n"
            "ds_read_b32 v184, v184\n"
            "ds_read_b128 v[176:179], v231 offset:2048\n"
            "ds_read_b128 v[180:183], v231 offset:3072\n"
            "s_waitcnt lgkmcnt(0)\n"
            "v_readfirstlane_b32 s68, v184\n"
            "s_cmp_eq_u32 s68, 0\n"
            "s_cbranch_scc1 Lfw1_%=\n"
            "v_mfma_f32_4x4x4_16b_bf16 v[16:19], v[224:225], v[176:177], v[16:19]\n"
            "v_mfma_f32_4x4x4_16b_bf16 v[20:23], v[224:225], v[178:179], v[20:23]\n"
            "v_mfma_f32_4x4x4_16b_bf16 v[24:27], v[224:225], v[180:181], v[24:27]\n"
            "v_mfma_f32_4x4x4_16b_bf16 v[28:31], v[224:225], v[182:183], v[28:31]\n"
            "s_nop 1\n"
            "Lfa1_%=:\n"
            "s_cmp_lg_u32 s72, 0xff\n"
            "s_cbranch_scc1 Lfb1_%=\n"
            "ds_read_b128 v[184:187], v236 offset:2048\n"
            "ds_read_b128 v[188:191], v236 offset:3072\n"
            "Lfb1_%=:\n"
            "v_cvt_pk_bf16_f32 v176, v16, v17\n"
            "v_cvt_pk_bf16_f32 v177, v18, v19\n"
            "v_cvt_pk_bf16_f32 v178, v20, v21\n"
            "v_cvt_pk_bf16_f32 v179, v22, v23\n"
            "v_cvt_pk_bf16_f32 v180, v24, v25\n"
            "v_cvt_pk_bf16_f32 v181, v26, v27\n"
            "v_cvt_pk_bf16_f32 v182, v28, v29\n"
            "v_cvt_pk_bf16_f32 v183, v30, v31\n"
            "s_bitcmp1_b32 s62, 1\n"
            "s_cbranch_scc1 Lpark1_%=\n"
            "buffer_store_dwordx4 v[176:179], v215, s[20:23], 0 offen offset:512 sc1\n"
            "buffer_store_dwordx4 v[180:183], v215, s[20:23], 0 offen offset:768 sc1\n"
            "s_branch Lzero1_%=\n"
            "Lpark1_%=:\n"
            "ds_write_b128 v231, v[176:179] offset:2048\n"
            "ds_write_b128 v231, v[180:183] offset:3072\n"
            "Lzero1_%=:\n"
            "s_cmp_lg_u32 s72, 0xff\n"
            "s_cbranch_scc1 Lzz1_%=\n"
            "s_waitcnt lgkmcnt(0)\n"
            "v_mfma_f32_4x4x4_16b_bf16 v[16:19], v[224:225], v[184:185], 0\n"
            "v_mfma_f32_4x4x4_16b_bf16 v[20:23], v[224:225], v[186:187], 0\n"
            "v_mfma_f32_4x4x4_16b_bf16 v[24:27], v[224:225], v[188:189], 0\n"
            "v_mfma_f32_4x4x4_16b_bf16 v[28:31], v[224:225], v[190:191], 0\n"
            "s_bitcmp1_b32 s62, 3\n"
            "s_cbranch_scc1 Lpub_%=\n"
            "s_setpc_b64 s[70:71]\n"
            "Lzz1_%=:\n"
            "v_mov_b64 v[16:17], 0\n"
            "v_mov_b64 v[18:19], 0\n"
            "v_mov_b64 v[20:21], 0\n"
            "v_mov_b64 v[22:23], 0\n"
            "v_mov_b64 v[24:25], 0\n"
            "v_mov_b64 v[26:27], 0\n"
            "v_mov_b64 v[28:29], 0\n"
            "v_mov_b64 v[30:31], 0\n"
            "s_bitcmp1_b32 s62, 3\n"
            "s_cbranch_scc1 Lpub_%=\n"
            "s_setpc_b64 s[70:71]\n"
            "Llk_%=:\n"
            "s_waitcnt vmcnt(4)\n"
            "v_readfirstlane_b32 s73, v223\n"
            "s_cmp_lg_u32 s73, s90\n"
            "s_cbranch_scc1 Llk2_%=\n"
            "buffer_load_dwordx4 v[32:35], v217, s[20:23], s63 offen offset:0 sc1\n"
            "buffer_load_dwordx4 v[36:39], v217, s[20:23], s63 offen offset:256 sc1\n"
            "buffer_load_dwordx4 v[48:51], v217, s[20:23], s63 offen offset:512 sc1\n"
            "buffer_load_dwordx4 v[52:55], v217, s[20:23], s63 offen offset:768 sc1\n"
            "s_branch Llkr_%=\n"
            "Llk2_%=:\n"
            "v_mov_b32 v223, 0\n"
            "global_load_dword v223, v223, s[58:59] sc1\n"
            "s_branch Llkr_%=\n"
            "Lspin_%=:\n"
            "s_waitcnt vmcnt(0)\n"
            "s_mov_b32 s72, 0x400000\n"
            "Lsp1_%=:\n"
            "v_readfirstlane_b32 s73, v223\n"
            "s_sub_u32 s72, s72, 1\n"
            "s_cmp_eq_u32 s72, 0\n"
            "s_cbranch_scc1 Lsp2_%=\n"
            "s_cmp_eq_u32 s73, s90\n"
            "s_cbranch_scc1 Lsp2_%=\n"
            "s_sleep 1\n"
            "v_mov_b32 v223, 0\n"
            "global_load_dword v223, v223, s[58:59] sc1\n"
            "s_waitcnt vmcnt(0)\n"
            "s_branch Lsp1_%=\n"
            "Lsp2_%=:\n"
            "buffer_load_dwordx4 v[32:35], v217, s[20:23], s63 offen offset:0 sc1\n"
            "buffer_load_dwordx4 v[36:39], v217, s[20:23], s63 offen offset:256 sc1\n"
            "buffer_load_dwordx4 v[48:51], v217, s[20:23], s63 offen offset:512 sc1\n"
            "buffer_load_dwordx4 v[52:55], v217, s[20:23], s63 offen offset:768 sc1\n"
            "s_branch Lgo_%=\n"
            "Llast_%=:\n"
            "s_waitcnt vmcnt(16)\n"
            "v_readfirstlane_b32 s73, v223\n"
            "v_mov_b32 v222, v217\n"
            "s_cmp_eq_u32 s73, s90\n"
            "s_cbranch_scc1 Lp4_%=\n"
            "v_bfrev_b32 v222, 1\n"
            "v_mov_b32 v223, 0\n"
            "global_load_dword v223, v223, s[58:59] sc1\n"
            "Lp4_%=:\n"
            "v_mfma_f32_16x16x32_bf16 v[136:139], v[192:195], v[120:123], 0\n"
            "v_exp_f32 v160, v128\n"
            "v_exp_f32 v161, v129\n"
            "s_waitcnt vmcnt(13) lgkmcnt(0)\n"
            "v_mfma_f32_4x4x4_16b_bf16 v[176:179], v[208:209], v[32:33], 0 cbsz:2\n"
            "v_exp_f32 v162, v130\n"
            "v_mfma_f32_4x4x4_16b_bf16 v[180:183], v[208:209], v[34:35], 0 cbsz:2\n"
            "v_exp_f32 v163, v131\n"
            "v_mfma_f32_4x4x4_16b_bf16 v[184:187], v[208:209], v[36:37], 0 cbsz:2\n"
            "v_exp_f32 v164, v132\n"
            "v_mfma_f32_4x4x4_16b_bf16 v[188:191], v[208:209], v[38:39], 0 cbsz:2\n"
            "v_exp_f32 v165, v133\n"
            "v_exp_f32 v166, v134\n"
            "buffer_load_dwordx4 v[32:35], v222, s[20:23], s63 offen offset:0 sc1\n"
            "buffer_load_dwordx4 v[36:39], v222, s[20:23], s63 offen offset:256 sc1\n"
            "ds_read_b128 v[236:239], v228 offset:0\n"
            "ds_read_b128 v[240:243], v228 offset:1024\n"
            "ds_read_b128 v[244:247], v228 offset:2048\n"
            "ds_read_b128 v[248:251], v228 offset:3072\n"
            "ds_read_b128 v[252:255], v228 offset:4096\n"
            "ds_read_b128 v[96:99], v228 offset:5120\n"
            "v_exp_f32 v167, v135\n"
            "v_pk_fma_f32 v[0:1], v[144:145], v[176:177], v[0:1] op_sel_hi:[0,1,1]\n"
            "v_pk_fma_f32 v[2:3], v[144:145], v[178:179], v[2:3] op_sel_hi:[0,1,1]\n"
            "v_pk_fma_f32 v[4:5], v[148:149], v[180:181], v[4:5] op_sel_hi:[0,1,1]\n"
            "v_pk_fma_f32 v[6:7], v[148:149], v[182:183], v[6:7] op_sel_hi:[0,1,1]\n"
            "v_pk_fma_f32 v[8:9], v[152:153], v[184:185], v[8:9] op_sel_hi:[0,1,1]\n"
            "v_pk_fma_f32 v[10:11], v[152:153], v[186:187], v[10:11] op_sel_hi:[0,1,1]\n"
            "v_pk_fma_f32 v[12:13], v[156:157], v[188:189], v[12:13] op_sel_hi:[0,1,1]\n"
            "v_pk_fma_f32 v[14:15], v[156:157], v[190:191], v[14:15] op_sel_hi:[0,1,1]\n"
            "v_pk_add_f32 v[160:161], v[160:161], 1.0 op_sel_hi:[1,0]\n"
            "v_pk_add_f32 v[162:163], v[162:163], 1.0 op_sel_hi:[1,0]\n"
            "v_pk_add_f32 v[164:165], v[164:165], 1.0 op_sel_hi:[1,0]\n"
            "v_pk_add_f32 v[166:167], v[166:167], 1.0 op_sel_hi:[1,0]\n"
            "v_mfma_f32_16x16x32_bf16 v[140:143], v[192:195], v[124:127], 0\n"
            "v_rcp_f32 v160, v160\n"
            "v_rcp_f32 v161, v161\n"
            "v_mfma_f32_4x4x4_16b_bf16 v[176:179], v[208:209], v[48:49], 0 cbsz:2 abid:1\n"
            "v_rcp_f32 v162, v162\n"
            "v_mfma_f32_4x4x4_16b_bf16 v[180:183], v[208:209], v[50:51], 0 cbsz:2 abid:1\n"
            "v_rcp_f32 v163, v163\n"
            "v_mfma_f32_4x4x4_16b_bf16 v[184:187], v[208:209], v[52:53], 0 cbsz:2 abid:1\n"
            "v_rcp_f32 v164, v164\n"
            "v_mfma_f32_4x4x4_16b_bf16 v[188:191], v[208:209], v[54:55], 0 cbsz:2 abid:1\n"
            "v_rcp_f32 v165, v165\n"
            "v_rcp_f32 v166, v166\n"
            "buffer_load_dwordx4 v[48:51], v222, s[20:23], s63 offen offset:512 sc1\n"
            "buffer_load_dwordx4 v[52:55], v222, s[20:23], s63 offen offset:768 sc1\n"
            "ds_read_b128 v[100:103], v228 offset:6144\n"
            "ds_read_b128 v[104:107], v228 offset:7168\n"
            "ds_read_b128 v[108:111], v228 offset:8192\n"
            "ds_read_b128 v[112:115], v228 offset:9216\n"
            "ds_read_b128 v[116:119], v228 offset:10240\n"
            "ds_read_b128 v[196:199], v228 offset:11264\n"
            "v_rcp_f32 v167, v167\n"
            "v_pk_fma_f32 v[0:1], v[144:145], v[176:177], v[0:1] op_sel:[1,0,0]\n"
            "v_pk_fma_f32 v[2:3], v[144:145], v[178:179], v[2:3] op_sel:[1,0,0]\n"
            "v_pk_fma_f32 v[4:5], v[148:149], v[180:181], v[4:5] op_sel:[1,0,0]\n"
            "v_pk_fma_f32 v[6:7], v[148:149], v[182:183], v[6:7] op_sel:[1,0,0]\n"
            "v_pk_fma_f32 v[8:9], v[152:153], v[184:185], v[8:9] op_sel:[1,0,0]\n"
            "v_pk_fma_f32 v[10:11], v[152:153], v[186:187], v[10:11] op_sel:[1,0,0]\n"
            "v_pk_fma_f32 v[12:13], v[156:157], v[188:189], v[12:13] op_sel:[1,0,0]\n"
            "v_pk_fma_f32 v[14:15], v[156:157], v[190:191], v[14:15] op_sel:[1,0,0]\n"
            "v_pk_mul_f32 v[160:161], v[160:161], v[128:129]\n"
            "v_pk_mul_f32 v[162:163], v[162:163], v[130:131]\n"
            "v_pk_mul_f32 v[164:165], v[164:165], v[132:133]\n"
            "v_pk_mul_f32 v[166:167], v[166:167], v[134:135]\n"
            "v_exp_f32 v168, v136\n"
            "v_exp_f32 v169, v137\n"
            "s_waitcnt vmcnt(12)\n"
            "v_mfma_f32_4x4x4_16b_bf16 v[176:179], v[208:209], v[64:65], 0 cbsz:2 abid:2\n"
            "v_exp_f32 v170, v138\n"
            "v_mfma_f32_4x4x4_16b_bf16 v[180:183], v[208:209], v[66:67], 0 cbsz:2 abid:2\n"
            "v_exp_f32 v171, v139\n"
            "v_mfma_f32_4x4x4_16b_bf16 v[184:187], v[208:209], v[68:69], 0 cbsz:2 abid:2\n"
            "v_exp_f32 v172, v140\n"
            "v_mfma_f32_4x4x4_16b_bf16 v[188:191], v[208:209], v[70:71], 0 cbsz:2 abid:2\n"
            "v_exp_f32 v173, v141\n"
            "v_exp_f32 v174, v142\n"
            "ds_read_b128 v[200:203], v228 offset:12288\n"
            "ds_read_b128 v[204:207], v228 offset:13312\n"
            "ds_read_b128 v[232:235], v228 offset:14336\n"
            "ds_read_b128 v[120:123], v228 offset:15360\n"
            "ds_read_b128 v[64:67], v228 offset:16384\n"
            "ds_read_b128 v[68:71], v228 offset:17408\n"
            "v_exp_f32 v175, v143\n"
            "v_pk_fma_f32 v[0:1], v[146:147], v[176:177], v[0:1] op_sel_hi:[0,1,1]\n"
            "v_pk_fma_f32 v[2:3], v[146:147], v[178:179], v[2:3] op_sel_hi:[0,1,1]\n"
            "v_pk_fma_f32 v[4:5], v[150:151], v[180:181], v[4:5] op_sel_hi:[0,1,1]\n"
            "v_pk_fma_f32 v[6:7], v[150:151], v[182:183], v[6:7] op_sel_hi:[0,1,1]\n"
            "v_pk_fma_f32 v[8:9], v[154:155], v[184:185], v[8:9] op_sel_hi:[0,1,1]\n"
            "v_pk_fma_f32 v[10:11], v[154:155], v[186:187], v[10:11] op_sel_hi:[0,1,1]\n"
            "v_pk_fma_f32 v[12:13], v[158:159], v[188:189], v[12:13] op_sel_hi:[0,1,1]\n"
            "v_pk_fma_f32 v[14:15], v[158:159], v[190:191], v[14:15] op_sel_hi:[0,1,1]\n"
            "v_pk_add_f32 v[168:169], v[168:169], 1.0 op_sel_hi:[1,0]\n"
            "v_pk_add_f32 v[170:171], v[170:171], 1.0 op_sel_hi:[1,0]\n"
            "v_pk_add_f32 v[172:173], v[172:173], 1.0 op_sel_hi:[1,0]\n"
            "v_pk_add_f32 v[174:175], v[174:175], 1.0 op_sel_hi:[1,0]\n"
            "v_rcp_f32 v168, v168\n"
            "v_rcp_f32 v169, v169\n"
            "v_mfma_f32_4x4x4_16b_bf16 v[176:179], v[208:209], v[80:81], 0 cbsz:2 abid:3\n"
            "v_rcp_f32 v170, v170\n"
            "v_mfma_f32_4x4x4_16b_bf16 v[180:183], v[208:209], v[82:83], 0 cbsz:2 abid:3\n"
            "v_rcp_f32 v171, v171\n"
            "v_mfma_f32_4x4x4_16b_bf16 v[184:187], v[208:209], v[84:85], 0 cbsz:2 abid:3\n"
            "v_rcp_f32 v172, v172\n"
            "v_mfma_f32_4x4x4_16b_bf16 v[188:191], v[208:209], v[86:87], 0 cbsz:2 abid:3\n"
            "v_rcp_f32 v173, v173\n"
            "v_rcp_f32 v174, v174\n"
            "ds_read_b128 v[124:127], v228 offset:18432\n"
            "ds_read_b128 v[192:195], v228 offset:19456\n"
            "ds_read_b128 v[128:131], v228 offset:20480\n"
            "ds_read_b128 v[132:135], v228 offset:21504\n"
            "ds_read_b128 v[80:83], v228 offset:22528\n"
            "ds_read_b128 v[84:87], v228 offset:23552\n"
            "v_rcp_f32 v175, v175\n"
            "v_pk_fma_f32 v[0:1], v[146:147], v[176:177], v[0:1] op_sel:[1,0,0]\n"
            "v_pk_fma_f32 v[2:3], v[146:147], v[178:179], v[2:3] op_sel:[1,0,0]\n"
            "v_pk_fma_f32 v[4:5], v[150:151], v[180:181], v[4:5] op_sel:[1,0,0]\n"
            "v_pk_fma_f32 v[6:7], v[150:151], v[182:183], v[6:7] op_sel:[1,0,0]\n"
            "v_pk_fma_f32 v[8:9], v[154:155], v[184:185], v[8:9] op_sel:[1,0,0]\n"
            "v_pk_fma_f32 v[10:11], v[154:155], v[186:187], v[10:11] op_sel:[1,0,0]\n"
            "v_pk_fma_f32 v[12:13], v[158:159], v[188:189], v[12:13] op_sel:[1,0,0]\n"
            "v_pk_fma_f32 v[14:15], v[158:159], v[190:191], v[14:15] op_sel:[1,0,0]\n"
            "v_pk_mul_f32 v[168:169], v[168:169], v[136:137]\n"
            "v_pk_mul_f32 v[170:171], v[170:171], v[138:139]\n"
            "v_pk_mul_f32 v[172:173], v[172:173], v[140:141]\n"
            "v_pk_mul_f32 v[174:175], v[174:175], v[142:143]\n"
            "s_cmp_lg_u32 s73, s90\n"
            "s_cbranch_scc1 Llk_%=\n"
            "Llkr_%=:\n"
            "s_waitcnt vmcnt(8)\n"
            "v_mfma_f32_4x4x4_16b_bf16 v[176:179], v[208:209], v[40:41], 0 cbsz:2\n"
            "v_mfma_f32_4x4x4_16b_bf16 v[180:183], v[208:209], v[42:43], 0 cbsz:2\n"
            "v_mfma_f32_4x4x4_16b_bf16 v[184:187], v[208:209], v[44:45], 0 cbsz:2\n"
            "v_mfma_f32_4x4x4_16b_bf16 v[188:191], v[208:209], v[46:47], 0 cbsz:2\n"
            "ds_read_b128 v[136:139], v228 offset:24576\n"
            "ds_read_b128 v[140:143], v228 offset:25600\n"
            "ds_read_b128 v[144:147], v228 offset:26624\n"
            "ds_read_b128 v[148:151], v228 offset:27648\n"
            "ds_read_b128 v[152:155], v228 offset:28672\n"
            "ds_read_b128 v[156:159], v228 offset:29696\n"
            "ds_read_b128 v[40:43], v228 offset:30720\n"
            "ds_read_b128 v[44:47], v228 offset:31744\n"
            "s_nop 1\n"
            "v_pk_fma_f32 v[16:17], v[160:161], v[176:177], v[16:17] op_sel_hi:[0,1,1]\n"
            "v_pk_fma_f32 v[18:19], v[160:161], v[178:179], v[18:19] op_sel_hi:[0,1,1]\n"
            "v_pk_fma_f32 v[20:21], v[164:165], v[180:181], v[20:21] op_sel_hi:[0,1,1]\n"
            "v_pk_fma_f32 v[22:23], v[164:165], v[182:183], v[22:23] op_sel_hi:[0,1,1]\n"
            "v_pk_fma_f32 v[24:25], v[168:169], v[184:185], v[24:25] op_sel_hi:[0,1,1]\n"
            "v_pk_fma_f32 v[26:27], v[168:169], v[186:187], v[26:27] op_sel_hi:[0,1,1]\n"
            "v_pk_fma_f32 v[28:29], v[172:173], v[188:189], v[28:29] op_sel_hi:[0,1,1]\n"
            "v_pk_fma_f32 v[30:31], v[172:173], v[190:191], v[30:31] op_sel_hi:[0,1,1]\n"
            "s_cmp_le_u32 s74, 1\n"
            "s_cbranch_scc1 Ltail_%=\n"
            "v_mfma_f32_4x4x4_16b_bf16 v[176:179], v[208:209], v[56:57], 0 cbsz:2 abid:1\n"
            "v_mfma_f32_4x4x4_16b_bf16 v[180:183], v[208:209], v[58:59], 0 cbsz:2 abid:1\n"
            "v_mfma_f32_4x4x4_16b_bf16 v[184:187], v[208:209], v[60:61], 0 cbsz:2 abid:1\n"
            "v_mfma_f32_4x4x4_16b_bf16 v[188:191], v[208:209], v[62:63], 0 cbsz:2 abid:1\n"
            "s_nop 1\n"
            "v_pk_fma_f32 v[16:17], v[160:161], v[176:177], v[16:17] op_sel:[1,0,0]\n"
            "v_pk_fma_f32 v[18:19], v[160:161], v[178:179], v[18:19] op_sel:[1,0,0]\n"
            "v_pk_fma_f32 v[20:21], v[164:165], v[180:181], v[20:21] op_sel:[1,0,0]\n"
            "v_pk_fma_f32 v[22:23], v[164:165], v[182:183], v[22:23] op_sel:[1,0,0]\n"
            "v_pk_fma_f32 v[24:25], v[168:169], v[184:185], v[24:25] op_sel:[1,0,0]\n"
            "v_pk_fma_f32 v[26:27], v[168:169], v[186:187], v[26:27] op_sel:[1,0,0]\n"
            "v_pk_fma_f32 v[28:29], v[172:173], v[188:189], v[28:29] op_sel:[1,0,0]\n"
            "v_pk_fma_f32 v[30:31], v[172:173], v[190:191], v[30:31] op_sel:[1,0,0]\n"
            "s_cmp_le_u32 s74, 2\n"
            "s_cbranch_scc1 Ltail_%=\n"
            "s_waitcnt vmcnt(4)\n"
            "v_mfma_f32_4x4x4_16b_bf16 v[176:179], v[208:209], v[72:73], 0 cbsz:2 abid:2\n"
            "v_mfma_f32_4x4x4_16b_bf16 v[180:183], v[208:209], v[74:75], 0 cbsz:2 abid:2\n"
            "v_mfma_f32_4x4x4_16b_bf16 v[184:187], v[208:209], v[76:77], 0 cbsz:2 abid:2\n"
            "v_mfma_f32_4x4x4_16b_bf16 v[188:191], v[208:209], v[78:79], 0 cbsz:2 abid:2\n"
            "s_nop 1\n"
            "v_pk_fma_f32 v[16:17], v[162:163], v[176:177], v[16:17] op_sel_hi:[0,1,1]\n"
            "v_pk_fma_f32 v[18:19], v[162:163], v[178:179], v[18:19] op_sel_hi:[0,1,1]\n"
            "v_pk_fma_f32 v[20:21], v[166:167], v[180:181], v[20:21] op_sel_hi:[0,1,1]\n"
            "v_pk_fma_f32 v[22:23], v[166:167], v[182:183], v[22:23] op_sel_hi:[0,1,1]\n"
            "v_pk_fma_f32 v[24:25], v[170:171], v[184:185], v[24:25] op_sel_hi:[0,1,1]\n"
            "v_pk_fma_f32 v[26:27], v[170:171], v[186:187], v[26:27] op_sel_hi:[0,1,1]\n"
            "v_pk_fma_f32 v[28:29], v[174:175], v[188:189], v[28:29] op_sel_hi:[0,1,1]\n"
            "v_pk_fma_f32 v[30:31], v[174:175], v[190:191], v[30:31] op_sel_hi:[0,1,1]\n"
            "s_cmp_le_u32 s74, 3\n"
            "s_cbranch_scc1 Ltail_%=\n"
            "v_mfma_f32_4x4x4_16b_bf16 v[176:179], v[208:209], v[88:89], 0 cbsz:2 abid:3\n"
            "v_mfma_f32_4x4x4_16b_bf16 v[180:183], v[208:209], v[90:91], 0 cbsz:2 abid:3\n"
            "v_mfma_f32_4x4x4_16b_bf16 v[184:187], v[208:209], v[92:93], 0 cbsz:2 abid:3\n"
            "v_mfma_f32_4x4x4_16b_bf16 v[188:191], v[208:209], v[94:95], 0 cbsz:2 abid:3\n"
            "s_nop 1\n"
            "v_pk_fma_f32 v[16:17], v[162:163], v[176:177], v[16:17] op_sel:[1,0,0]\n"
            "v_pk_fma_f32 v[18:19], v[162:163], v[178:179], v[18:19] op_sel:[1,0,0]\n"
            "v_pk_fma_f32 v[20:21], v[166:167], v[180:181], v[20:21] op_sel:[1,0,0]\n"
            "v_pk_fma_f32 v[22:23], v[166:167], v[182:183], v[22:23] op_sel:[1,0,0]\n"
            "v_pk_fma_f32 v[24:25], v[170:171], v[184:185], v[24:25] op_sel:[1,0,0]\n"
            "v_pk_fma_f32 v[26:27], v[170:171], v[186:187], v[26:27] op_sel:[1,0,0]\n"
            "v_pk_fma_f32 v[28:29], v[174:175], v[188:189], v[28:29] op_sel:[1,0,0]\n"
            "v_pk_fma_f32 v[30:31], v[174:175], v[190:191], v[30:31] op_sel:[1,0,0]\n"
            "Ltail_%=:\n"
            "s_setprio 3\n"
            "s_cmp_lg_u32 s73, s90\n"
            "s_cbranch_scc1 Lspin_%=\n"
            "Lgo_%=:\n"
            "s_waitcnt vmcnt(0)\n"
            "v_mfma_f32_4x4x4_16b_bf16 v[0:3], v[224:225], v[32:33], v[0:3]\n"
            "v_mfma_f32_4x4x4_16b_bf16 v[4:7], v[224:225], v[34:35], v[4:7]\n"
            "v_mfma_f32_4x4x4_16b_bf16 v[8:11], v[224:225], v[36:37], v[8:11]\n"
            "v_mfma_f32_4x4x4_16b_bf16 v[12:15], v[224:225], v[38:39], v[12:15]\n"
            "v_mfma_f32_4x4x4_16b_bf16 v[16:19], v[224:225], v[48:49], v[16:19]\n"
            "v_mfma_f32_4x4x4_16b_bf16 v[20:23], v[224:225], v[50:51], v[20:23]\n"
            "v_mfma_f32_4x4x4_16b_bf16 v[24:27], v[224:225], v[52:53], v[24:27]\n"
            "v_mfma_f32_4x4x4_16b_bf16 v[28:31], v[224:225], v[54:55], v[28:31]\n"
            "v_cvt_pk_bf16_f32 v176, v0, v4\n"
            "v_cvt_pk_bf16_f32 v177, v8, v12\n"
            "ds_write_b64 v226, v[176:177] offset:0\n"
            "v_cvt_pk_bf16_f32 v178, v1, v5\n"
            "v_cvt_pk_bf16_f32 v179, v9, v13\n"
            "ds_write_b64 v226, v[178:179] offset:272\n"
            "v_cvt_pk_bf16_f32 v180, v2, v6\n"
            "v_cvt_pk_bf16_f32 v181, v10, v14\n"
            "ds_write_b64 v226, v[180:181] offset:544\n"
            "v_cvt_pk_bf16_f32 v182, v3, v7\n"
            "v_cvt_pk_bf16_f32 v183, v11, v15\n"
            "ds_write_b64 v226, v[182:183] offset:816\n"
            "ds_read_b128 v[176:179], v227 offset:0\n"
            "ds_read_b128 v[180:183], v227 offset:64\n"
            "v_cvt_pk_bf16_f32 v184, v16, v20\n"
            "v_cvt_pk_bf16_f32 v185, v24, v28\n"
            "ds_write_b64 v226, v[184:185] offset:128\n"
            "v_cvt_pk_bf16_f32 v186, v17, v21\n"
            "v_cvt_pk_bf16_f32 v187, v25, v29\n"
            "ds_write_b64 v226, v[186:187] offset:400\n"
            "v_cvt_pk_bf16_f32 v188, v18, v22\n"
            "v_cvt_pk_bf16_f32 v189, v26, v30\n"
            "ds_write_b64 v226, v[188:189] offset:672\n"
            "v_cvt_pk_bf16_f32 v190, v19, v23\n"
            "v_cvt_pk_bf16_f32 v191, v27, v31\n"
            "ds_write_b64 v226, v[190:191] offset:944\n"
            "ds_read_b128 v[184:187], v227 offset:128\n"
            "ds_read_b128 v[188:191], v227 offset:192\n"
            "s_waitcnt lgkmcnt(7)\n"
            "v_mfma_f32_16x16x32_bf16 v[0:3], v[176:179], v[236:239], 0\n"
            "v_mfma_f32_16x16x32_bf16 v[4:7], v[176:179], v[252:255], 0\n"
            "s_waitcnt lgkmcnt(6)\n"
            "v_mfma_f32_16x16x32_bf16 v[0:3], v[180:183], v[240:243], v[0:3]\n"
            "v_mfma_f32_16x16x32_bf16 v[4:7], v[180:183], v[96:99], v[4:7]\n"
            "buffer_load_dwordx4 v[72:75], v218, s[20:23], s63 offen offset:0 sc1\n"
            "v_mfma_f32_16x16x32_bf16 v[8:11], v[176:179], v[108:111], 0\n"
            "v_mfma_f32_16x16x32_bf16 v[12:15], v[176:179], v[200:203], 0\n"
            "v_mfma_f32_16x16x32_bf16 v[8:11], v[180:183], v[112:115], v[8:11]\n"
            "v_mfma_f32_16x16x32_bf16 v[12:15], v[180:183], v[204:207], v[12:15]\n"
            "buffer_load_dwordx4 v[76:79], v218, s[20:23], s63 offen offset:256 sc1\n"
            "v_mfma_f32_16x16x32_bf16 v[16:19], v[176:179], v[64:67], 0\n"
            "v_mfma_f32_16x16x32_bf16 v[20:23], v[176:179], v[128:131], 0\n"
            "v_mfma_f32_16x16x32_bf16 v[16:19], v[180:183], v[68:71], v[16:19]\n"
            "v_mfma_f32_16x16x32_bf16 v[20:23], v[180:183], v[132:135], v[20:23]\n"
            "buffer_load_dwordx4 v[88:91], v218, s[20:23], s63 offen offset:512 sc1\n"
            "v_mfma_f32_16x16x32_bf16 v[24:27], v[176:179], v[136:139], 0\n"
            "v_mfma_f32_16x16x32_bf16 v[28:31], v[176:179], v[152:155], 0\n"
            "v_mfma_f32_16x16x32_bf16 v[24:27], v[180:183], v[140:143], v[24:27]\n"
            "v_mfma_f32_16x16x32_bf16 v[28:31], v[180:183], v[156:159], v[28:31]\n"
            "buffer_load_dwordx4 v[92:95], v218, s[20:23], s63 offen offset:768 sc1\n"
            "s_waitcnt lgkmcnt(1)\n"
            "v_mfma_f32_16x16x32_bf16 v[0:3], v[184:187], v[244:247], v[0:3]\n"
            "v_mfma_f32_16x16x32_bf16 v[4:7], v[184:187], v[100:103], v[4:7]\n"
            "s_waitcnt lgkmcnt(0)\n"
            "v_mfma_f32_16x16x32_bf16 v[0:3], v[188:191], v[248:251], v[0:3]\n"
            "v_mfma_f32_16x16x32_bf16 v[4:7], v[188:191], v[104:107], v[4:7]\n"
            "v_mfma_f32_16x16x32_bf16 v[8:11], v[184:187], v[116:119], v[8:11]\n"
            "v_mfma_f32_16x16x32_bf16 v[12:15], v[184:187], v[232:235], v[12:15]\n"
            "v_mfma_f32_16x16x32_bf16 v[8:11], v[188:191], v[196:199], v[8:11]\n"
            "v_mfma_f32_16x16x32_bf16 v[12:15], v[188:191], v[120:123], v[12:15]\n"
            "v_mfma_f32_16x16x32_bf16 v[16:19], v[184:187], v[124:127], v[16:19]\n"
            "v_mfma_f32_16x16x32_bf16 v[20:23], v[184:187], v[80:83], v[20:23]\n"
            "v_mfma_f32_16x16x32_bf16 v[16:19], v[188:191], v[192:195], v[16:19]\n"
            "v_mfma_f32_16x16x32_bf16 v[20:23], v[188:191], v[84:87], v[20:23]\n"
            "v_cvt_pk_bf16_f32 v0, v0, v1\n"
            "v_cvt_pk_bf16_f32 v1, v2, v3\n"
            "v_cvt_pk_bf16_f32 v2, v4, v5\n"
            "v_cvt_pk_bf16_f32 v3, v6, v7\n"
            "buffer_store_dwordx4 v[0:3], v217, s[32:35], 0 offen offset:0 sc1\n"
            "v_mfma_f32_16x16x32_bf16 v[24:27], v[184:187], v[144:147], v[24:27]\n"
            "v_mfma_f32_16x16x32_bf16 v[28:31], v[184:187], v[40:43], v[28:31]\n"
            "v_mfma_f32_16x16x32_bf16 v[24:27], v[188:191], v[148:151], v[24:27]\n"
            "v_mfma_f32_16x16x32_bf16 v[28:31], v[188:191], v[44:47], v[28:31]\n"
            "v_cvt_pk_bf16_f32 v8, v8, v9\n"
            "v_cvt_pk_bf16_f32 v9, v10, v11\n"
            "v_cvt_pk_bf16_f32 v10, v12, v13\n"
            "v_cvt_pk_bf16_f32 v11, v14, v15\n"
            "buffer_store_dwordx4 v[8:11], v217, s[32:35], 0 offen offset:256 sc1\n"
            "v_cvt_pk_bf16_f32 v16, v16, v17\n"
            "v_cvt_pk_bf16_f32 v17, v18, v19\n"
            "v_cvt_pk_bf16_f32 v18, v20, v21\n"
            "v_cvt_pk_bf16_f32 v19, v22, v23\n"
            "buffer_store_dwordx4 v[16:19], v217, s[32:35], 0 offen offset:512 sc1\n"
            "v_cvt_pk_bf16_f32 v24, v24, v25\n"
            "v_cvt_pk_bf16_f32 v25, v26, v27\n"
            "v_cvt_pk_bf16_f32 v26, v28, v29\n"
            "v_cvt_pk_bf16_f32 v27, v30, v31\n"
            "buffer_store_dwordx4 v[24:27], v217, s[32:35], 0 offen offset:768 sc1\n"
            "s_cmp_lt_u32 s3, 6\n"
            "s_cbranch_scc0 Ltwo_%=\n"
            "s_endpgm\n"
            "Ltwo_%=:\n"
            "ds_read_b128 v[32:35], v231 offset:0\n"
            "ds_read_b128 v[36:39], v231 offset:1024\n"
            "ds_read_b128 v[48:51], v231 offset:2048\n"
            "ds_read_b128 v[52:55], v231 offset:3072\n"
            "s_waitcnt vmcnt(4)\n"
            "v_mfma_f32_4x4x4_16b_bf16 v[0:3], v[224:225], v[72:73], 0\n"
            "v_mfma_f32_4x4x4_16b_bf16 v[4:7], v[224:225], v[74:75], 0\n"
            "v_mfma_f32_4x4x4_16b_bf16 v[8:11], v[224:225], v[76:77], 0\n"
            "v_mfma_f32_4x4x4_16b_bf16 v[12:15], v[224:225], v[78:79], 0\n"
            "v_mfma_f32_4x4x4_16b_bf16 v[16:19], v[224:225], v[88:89], 0\n"
            "v_mfma_f32_4x4x4_16b_bf16 v[20:23], v[224:225], v[90:91], 0\n"
            "v_mfma_f32_4x4x4_16b_bf16 v[24:27], v[224:225], v[92:93], 0\n"
            "v_mfma_f32_4x4x4_16b_bf16 v[28:31], v[224:225], v[94:95], 0\n"
            "s_waitcnt lgkmcnt(0)\n"
            "v_mfma_f32_4x4x4_16b_bf16 v[0:3], v[224:225], v[32:33], v[0:3]\n"
            "v_mfma_f32_4x4x4_16b_bf16 v[4:7], v[224:225], v[34:35], v[4:7]\n"
            "v_mfma_f32_4x4x4_16b_bf16 v[8:11], v[224:225], v[36:37], v[8:11]\n"
            "v_mfma_f32_4x4x4_16b_bf16 v[12:15], v[224:225], v[38:39], v[12:15]\n"
            "v_mfma_f32_4x4x4_16b_bf16 v[16:19], v[224:225], v[48:49], v[16:19]\n"
            "v_mfma_f32_4x4x4_16b_bf16 v[20:23], v[224:225], v[50:51], v[20:23]\n"
            "v_mfma_f32_4x4x4_16b_bf16 v[24:27], v[224:225], v[52:53], v[24:27]\n"
            "v_mfma_f32_4x4x4_16b_bf16 v[28:31], v[224:225], v[54:55], v[28:31]\n"
            "v_cvt_pk_bf16_f32 v176, v0, v4\n"
            "v_cvt_pk_bf16_f32 v177, v8, v12\n"
            "ds_write_b64 v226, v[176:177] offset:0\n"
            "v_cvt_pk_bf16_f32 v178, v1, v5\n"
            "v_cvt_pk_bf16_f32 v179, v9, v13\n"
            "ds_write_b64 v226, v[178:179] offset:272\n"
            "v_cvt_pk_bf16_f32 v180, v2, v6\n"
            "v_cvt_pk_bf16_f32 v181, v10, v14\n"
            "ds_write_b64 v226, v[180:181] offset:544\n"
            "v_cvt_pk_bf16_f32 v182, v3, v7\n"
            "v_cvt_pk_bf16_f32 v183, v11, v15\n"
            "ds_write_b64 v226, v[182:183] offset:816\n"
            "ds_read_b128 v[176:179], v227 offset:0\n"
            "ds_read_b128 v[180:183], v227 offset:64\n"
            "v_cvt_pk_bf16_f32 v184, v16, v20\n"
            "v_cvt_pk_bf16_f32 v185, v24, v28\n"
            "ds_write_b64 v226, v[184:185] offset:128\n"
            "v_cvt_pk_bf16_f32 v186, v17, v21\n"
            "v_cvt_pk_bf16_f32 v187, v25, v29\n"
            "ds_write_b64 v226, v[186:187] offset:400\n"
            "v_cvt_pk_bf16_f32 v188, v18, v22\n"
            "v_cvt_pk_bf16_f32 v189, v26, v30\n"
            "ds_write_b64 v226, v[188:189] offset:672\n"
            "v_cvt_pk_bf16_f32 v190, v19, v23\n"
            "v_cvt_pk_bf16_f32 v191, v27, v31\n"
            "ds_write_b64 v226, v[190:191] offset:944\n"
            "ds_read_b128 v[184:187], v227 offset:128\n"
            "ds_read_b128 v[188:191], v227 offset:192\n"
            "s_waitcnt lgkmcnt(7)\n"
            "v_mfma_f32_16x16x32_bf16 v[0:3], v[176:179], v[236:239], 0\n"
            "v_mfma_f32_16x16x32_bf16 v[4:7], v[176:179], v[252:255], 0\n"
            "s_waitcnt lgkmcnt(6)\n"
            "v_mfma_f32_16x16x32_bf16 v[0:3], v[180:183], v[240:243], v[0:3]\n"
            "v_mfma_f32_16x16x32_bf16 v[4:7], v[180:183], v[96:99], v[4:7]\n"
            "v_mfma_f32_16x16x32_bf16 v[8:11], v[176:179], v[108:111], 0\n"
            "v_mfma_f32_16x16x32_bf16 v[12:15], v[176:179], v[200:203], 0\n"
            "v_mfma_f32_16x16x32_bf16 v[8:11], v[180:183], v[112:115], v[8:11]\n"
            "v_mfma_f32_16x16x32_bf16 v[12:15], v[180:183], v[204:207], v[12:15]\n"
            "v_mfma_f32_16x16x32_bf16 v[16:19], v[176:179], v[64:67], 0\n"
            "v_mfma_f32_16x16x32_bf16 v[20:23], v[176:179], v[128:131], 0\n"
            "v_mfma_f32_16x16x32_bf16 v[16:19], v[180:183], v[68:71], v[16:19]\n"
            "v_mfma_f32_16x16x32_bf16 v[20:23], v[180:183], v[132:135], v[20:23]\n"
            "v_mfma_f32_16x16x32_bf16 v[24:27], v[176:179], v[136:139], 0\n"
            "v_mfma_f32_16x16x32_bf16 v[28:31], v[176:179], v[152:155], 0\n"
            "v_mfma_f32_16x16x32_bf16 v[24:27], v[180:183], v[140:143], v[24:27]\n"
            "v_mfma_f32_16x16x32_bf16 v[28:31], v[180:183], v[156:159], v[28:31]\n"
            "s_waitcnt lgkmcnt(1)\n"
            "v_mfma_f32_16x16x32_bf16 v[0:3], v[184:187], v[244:247], v[0:3]\n"
            "v_mfma_f32_16x16x32_bf16 v[4:7], v[184:187], v[100:103], v[4:7]\n"
            "s_waitcnt lgkmcnt(0)\n"
            "v_mfma_f32_16x16x32_bf16 v[0:3], v[188:191], v[248:251], v[0:3]\n"
            "v_mfma_f32_16x16x32_bf16 v[4:7], v[188:191], v[104:107], v[4:7]\n"
            "v_mfma_f32_16x16x32_bf16 v[8:11], v[184:187], v[116:119], v[8:11]\n"
            "v_mfma_f32_16x16x32_bf16 v[12:15], v[184:187], v[232:235], v[12:15]\n"
            "v_mfma_f32_16x16x32_bf16 v[8:11], v[188:191], v[196:199], v[8:11]\n"
            "v_mfma_f32_16x16x32_bf16 v[12:15], v[188:191], v[120:123], v[12:15]\n"
            "v_mfma_f32_16x16x32_bf16 v[16:19], v[184:187], v[124:127], v[16:19]\n"
            "v_mfma_f32_16x16x32_bf16 v[20:23], v[184:187], v[80:83], v[20:23]\n"
            "v_mfma_f32_16x16x32_bf16 v[16:19], v[188:191], v[192:195], v[16:19]\n"
            "v_mfma_f32_16x16x32_bf16 v[20:23], v[188:191], v[84:87], v[20:23]\n"
            "v_cvt_pk_bf16_f32 v0, v0, v1\n"
            "v_cvt_pk_bf16_f32 v1, v2, v3\n"
            "v_cvt_pk_bf16_f32 v2, v4, v5\n"
            "v_cvt_pk_bf16_f32 v3, v6, v7\n"
            "buffer_store_dwordx4 v[0:3], v218, s[32:35], 0 offen offset:0 sc1\n"
            "v_mfma_f32_16x16x32_bf16 v[24:27], v[184:187], v[144:147], v[24:27]\n"
            "v_mfma_f32_16x16x32_bf16 v[28:31], v[184:187], v[40:43], v[28:31]\n"
            "v_mfma_f32_16x16x32_bf16 v[24:27], v[188:191], v[148:151], v[24:27]\n"
            "v_mfma_f32_16x16x32_bf16 v[28:31], v[188:191], v[44:47], v[28:31]\n"
            "v_cvt_pk_bf16_f32 v8, v8, v9\n"
            "v_cvt_pk_bf16_f32 v9, v10, v11\n"
            "v_cvt_pk_bf16_f32 v10, v12, v13\n"
            "v_cvt_pk_bf16_f32 v11, v14, v15\n"
            "buffer_store_dwordx4 v[8:11], v218, s[32:35], 0 offen offset:256 sc1\n"
            "v_cvt_pk_bf16_f32 v16, v16, v17\n"
            "v_cvt_pk_bf16_f32 v17, v18, v19\n"
            "v_cvt_pk_bf16_f32 v18, v20, v21\n"
            "v_cvt_pk_bf16_f32 v19, v22, v23\n"
            "buffer_store_dwordx4 v[16:19], v218, s[32:35], 0 offen offset:512 sc1\n"
            "v_cvt_pk_bf16_f32 v24, v24, v25\n"
            "v_cvt_pk_bf16_f32 v25, v26, v27\n"
            "v_cvt_pk_bf16_f32 v26, v28, v29\n"
            "v_cvt_pk_bf16_f32 v27, v30, v31\n"
            "buffer_store_dwordx4 v[24:27], v218, s[32:35], 0 offen offset:768 sc1\n"
            "Lend_%=:\n"
            "s_endpgm\n"
            "s_mov_b32 s68, 0\n"
            "s_branch Lout_%=\n"
            "Lpoll2_%=:\n"
            "s_sub_u32 s99, s99, 1\n"
            "s_cmp_lt_i32 s99, 0\n"
            "s_cbranch_scc1 Lpoll3_%=\n"
            "s_sleep 2\n"
            "buffer_load_dwordx2 v[142:143], v180, s[20:23], 0 offen sc1\n"
            "s_branch Lpoll_%=\n"
            "Lover_%=:\n"
            "s_mov_b32 s37, 0\n"
            "v_mov_b32 v144, 255\n"
            "ds_write_b32 v219, v144\n"
            "s_branch Lover2_%=\n"
            "Llng_%=:\n"
            "s_nop 1\n"
            "s_mov_b32 s38, 0\n"
            "v_max_u32_dpp v74, v74, v74 row_bcast:15 row_mask:0xa bank_mask:0xf\n"
            "s_nop 1\n"
            "v_max_u32_dpp v74, v74, v74 row_bcast:31 row_mask:0xc bank_mask:0xf\n"
            "v_max_u32 v74, s38, v74\n"
            "s_nop 0\n"
            "v_readlane_b32 s38, v74, 63\n"
            "v_max_u32_dpp v75, v75, v75 row_bcast:15 row_mask:0xa bank_mask:0xf\n"
            "s_nop 1\n"
            "v_max_u32_dpp v75, v75, v75 row_bcast:31 row_mask:0xc bank_mask:0xf\n"
            "v_max_u32 v75, s38, v75\n"
            "s_nop 0\n"
            "v_readlane_b32 s38, v75, 63\n"
            "v_max_u32_dpp v76, v76, v76 row_bcast:15 row_mask:0xa bank_mask:0xf\n"
            "s_nop 1\n"
            "v_max_u32_dpp v76, v76, v76 row_bcast:31 row_mask:0xc bank_mask:0xf\n"
            "v_max_u32 v76, s38, v76\n"
            "s_nop 0\n"
            "v_readlane_b32 s38, v76, 63\n"
            "v_max_u32_dpp v77, v77, v77 row_bcast:15 row_mask:0xa bank_mask:0xf\n"
            "s_nop 1\n"
            "v_max_u32_dpp v77, v77, v77 row_bcast:31 row_mask:0xc bank_mask:0xf\n"
            "v_max_u32 v77, s38, v77\n"
            "s_nop 0\n"
            "v_readlane_b32 s38, v77, 63\n"
            "v_max_u32_dpp v78, v78, v78 row_bcast:15 row_mask:0xa bank_mask:0xf\n"
            "s_nop 1\n"
            "v_max_u32_dpp v78, v78, v78 row_bcast:31 row_mask:0xc bank_mask:0xf\n"
            "v_max_u32 v78, s38, v78\n"
            "s_nop 0\n"
            "v_readlane_b32 s38, v78, 63\n"
            "v_max_u32_dpp v79, v79, v79 row_bcast:15 row_mask:0xa bank_mask:0xf\n"
            "s_nop 1\n"
            "v_max_u32_dpp v79, v79, v79 row_bcast:31 row_mask:0xc bank_mask:0xf\n"
            "v_max_u32 v79, s38, v79\n"
            "s_nop 0\n"
            "v_readlane_b32 s38, v79, 63\n"
            "v_max_u32_dpp v80, v80, v80 row_bcast:15 row_mask:0xa bank_mask:0xf\n"
            "s_nop 1\n"
            "v_max_u32_dpp v80, v80, v80 row_bcast:31 row_mask:0xc bank_mask:0xf\n"
            "v_max_u32 v80, s38, v80\n"
            "s_nop 0\n"
            "v_readlane_b32 s38, v80, 63\n"
            "s_branch Llng2_%=\n"
            "Lre0_%=:\n"
            "s_sub_u32 s99, s99, 1\n"
            "s_cmp_lt_i32 s99, 0\n"
            "s_cbranch_scc1 Lok0_%=\n"
            "s_sleep 1\n"
            "buffer_load_dwordx4 v[34:37], v81, s[20:23], s15 offen offset:0 sc1\n"
            "s_waitcnt vmcnt(0)\n"
            "s_branch Lchk0_%=\n"
            "Lre1_%=:\n"
            "s_sub_u32 s99, s99, 1\n"
            "s_cmp_lt_i32 s99, 0\n"
            "s_cbranch_scc1 Lok1_%=\n"
            "s_sleep 1\n"
            "buffer_load_dwordx4 v[40:43], v82, s[20:23], s15 offen offset:1024 sc1\n"
            "s_waitcnt vmcnt(0)\n"
            "s_branch Lchk1_%=\n"
            "Lre2_%=:\n"
            "s_sub_u32 s99, s99, 1\n"
            "s_cmp_lt_i32 s99, 0\n"
            "s_cbranch_scc1 Lok2_%=\n"
            "s_sleep 1\n"
            "buffer_load_dwordx4 v[46:49], v83, s[20:23], s15 offen offset:2048 sc1\n"
            "s_waitcnt vmcnt(0)\n"
            "s_branch Lchk2_%=\n"
            "Lre3_%=:\n"
            "s_sub_u32 s99, s99, 1\n"
            "s_cmp_lt_i32 s99, 0\n"
            "s_cbranch_scc1 Lok3_%=\n"
            "s_sleep 1\n"
            "buffer_load_dwordx4 v[52:55], v84, s[20:23], s15 offen offset:3072 sc1\n"
            "s_waitcnt vmcnt(0)\n"
            "s_branch Lchk3_%=\n"
            "Lre4_%=:\n"
            "s_sub_u32 s99, s99, 1\n"
            "s_cmp_lt_i32 s99, 0\n"
            "s_cbranch_scc1 Lok4_%=\n"
            "s_sleep 1\n"
            "buffer_load_dwordx4 v[58:61], v85, s[20:23], s16 offen offset:0 sc1\n"
            "s_waitcnt vmcnt(0)\n"
            "s_branch Lchk4_%=\n"
            "Lre5_%=:\n"
            "s_sub_u32 s99, s99, 1\n"
            "s_cmp_lt_i32 s99, 0\n"
            "s_cbranch_scc1 Lok5_%=\n"
            "s_sleep 1\n"
            "buffer_load_dwordx4 v[64:67], v86, s[20:23], s16 offen offset:1024 sc1\n"
            "s_waitcnt vmcnt(0)\n"
            "s_branch Lchk5_%=\n"
            "Lre6_%=:\n"
            "s_sub_u32 s99, s99, 1\n"
            "s_cmp_lt_i32 s99, 0\n"
            "s_cbranch_scc1 Lok6_%=\n"
            "s_sleep 1\n"
            "buffer_load_dwordx4 v[70:73], v87, s[20:23], s16 offen offset:2048 sc1\n"
            "s_waitcnt vmcnt(0)\n"
            "s_branch Lchk6_%=\n"
            "Llong_%=:\n"
            "v_and_b32 v243, 31, v243\n"
            "v_readfirstlane_b32 s86, v247\n"
            "v_add_u32 v243, 64, v243\n"
            "s_nop 0\n"
            "v_lshl_add_u32 v247, v243, 3, s86\n"
            "v_subrev_u32 v128, s43, v243\n"
            "v_mov_b32 v136, s64\n"
            "v_cmpx_le_i32 vcc, s69, v128\n"
            "v_mov_b32 v136, s81\n"
            "v_cmpx_le_i32 vcc, s78, v128\n"
            "v_mov_b32 v136, s82\n"
            "v_cmpx_le_i32 vcc, s79, v128\n"
            "v_mov_b32 v136, s83\n"
            "v_cmpx_le_i32 vcc, s80, v128\n"
            "v_mov_b32 v136, s84\n"
            "s_mov_b64 exec, -1\n"
            "v_and_b32 v137, 0xffff, v136\n"
            "ds_read_b128 v[140:143], v137\n"
            "v_ashrrev_i32 v136, 16, v136\n"
            "v_sub_u32 v136, v128, v136\n"
            "s_waitcnt lgkmcnt(0)\n"
            "v_cmp_eq_u32 vcc, s64, v137\n"
            "v_mov_b32 v132, 60\n"
            "s_nop 0\n"
            "v_cndmask_b32 v132, 0, v132, vcc\n"
            "v_bfe_u32 v137, v140, 16, 8\n"
            "v_max_u32 v137, 8, v137\n"
            "v_lshrrev_b32 v138, 1, v137\n"
            "v_add_u32 v138, v138, v132\n"
            "v_sub_u32_sdwa v132, v138, v140 dst_sel:DWORD dst_unused:UNUSED_PAD src0_sel:DWORD src1_sel:BYTE_3\n"
            "v_sub_u32_sdwa v133, v137, v140 dst_sel:DWORD dst_unused:UNUSED_PAD src0_sel:DWORD src1_sel:BYTE_2\n"
            "v_med3_i32 v132, v132, 0, v133\n"
            "v_sub_u32 v132, v136, v132\n"
            "v_cmp_lt_u32_sdwa vcc, v132, v140 src0_sel:DWORD src1_sel:BYTE_2\n"
            "v_lshl_add_u32 v140, v132, 4, v140\n"
            "v_sub_u32_sdwa v132, v138, v141 dst_sel:DWORD dst_unused:UNUSED_PAD src0_sel:DWORD src1_sel:BYTE_3\n"
            "v_sub_u32_sdwa v133, v137, v141 dst_sel:DWORD dst_unused:UNUSED_PAD src0_sel:DWORD src1_sel:BYTE_2\n"
            "v_med3_i32 v132, v132, 0, v133\n"
            "v_sub_u32 v132, v136, v132\n"
            "v_cmp_lt_u32_sdwa s[70:71], v132, v141 src0_sel:DWORD src1_sel:BYTE_2\n"
            "v_lshl_add_u32 v141, v132, 4, v141\n"
            "v_sub_u32_sdwa v132, v138, v142 dst_sel:DWORD dst_unused:UNUSED_PAD src0_sel:DWORD src1_sel:BYTE_3\n"
            "v_sub_u32_sdwa v133, v137, v142 dst_sel:DWORD dst_unused:UNUSED_PAD src0_sel:DWORD src1_sel:BYTE_2\n"
            "v_med3_i32 v132, v132, 0, v133\n"
            "v_sub_u32 v132, v136, v132\n"
            "v_cmp_lt_u32_sdwa s[86:87], v132, v142 src0_sel:DWORD src1_sel:BYTE_2\n"
            "v_lshl_add_u32 v142, v132, 4, v142\n"
            "v_sub_u32_sdwa v132, v138, v143 dst_sel:DWORD dst_unused:UNUSED_PAD src0_sel:DWORD src1_sel:BYTE_3\n"
            "v_sub_u32_sdwa v133, v137, v143 dst_sel:DWORD dst_unused:UNUSED_PAD src0_sel:DWORD src1_sel:BYTE_2\n"
            "v_med3_i32 v132, v132, 0, v133\n"
            "v_sub_u32 v132, v136, v132\n"
            "v_cmp_lt_u32_sdwa s[88:89], v132, v143 src0_sel:DWORD src1_sel:BYTE_2\n"
            "v_lshl_add_u32 v143, v132, 4, v143\n"
            "v_cndmask_b32 v140, v251, v140, vcc\n"
            "v_cndmask_b32 v141, v251, v141, s[70:71]\n"
            "v_cndmask_b32 v142, v251, v142, s[86:87]\n"
            "v_cndmask_b32 v143, v251, v143, s[88:89]\n"
            "v_perm_b32 v140, v141, v140, s68\n"
            "v_perm_b32 v141, v143, v142, s68\n"
            "ds_write_b64 v247, v[140:141]\n"
            "s_branch Llong2_%=\n"
            "Lslow_%=:\n"
            "s_waitcnt vmcnt(0) lgkmcnt(0)\n"
            "s_mov_b32 s68, -1\n"
            "Lout_%=:\n"
            : "+{v0}"(tid), "={s68}"(slow), "={s90}"(tag), "={s3}"(wq)
            : "{s[0:1]}"(ka), "{s2}"(bx)
            : "memory", "vcc", "scc", "v1", "v2", "v3", "v4", "v5", "v6", "v7", "v8", "v9", "v10", "v11", "v12", "v13", "v14", "v15", "v16", "v17", "v18", "v19", "v20", "v21", "v22", "v23", "v24", "v25", "v26", "v27", "v28", "v29", "v30", "v31", "v32", "v33", "v34", "v35", "v36", "v37", "v38", "v39", "v40", "v41", "v42", "v43", "v44", "v45", "v46", "v47", "v48", "v49", "v50", "v51", "v52", "v53", "v54", "v55", "v56", "v57", "v58", "v59", "v60", "v61", "v62", "v63", "v64", "v65", "v66", "v67", "v68", "v69", "v70", "v71", "v72", "v73", "v74", "v75", "v76", "v77", "v78", "v79", "v80", "v81", "v82", "v83", "v84", "v85", "v86", "v87", "v88", "v89", "v90", "v91", "v92", "v93", "v94", "v95", "v96", "v97", "v98", "v99", "v100", "v101", "v102", "v103", "v104", "v105", "v106", "v107", "v108", "v109", "v110", "v111", "v112", "v113", "v114", "v115", "v116", "v117", "v118", "v119", "v120", "v121", "v122", "v123", "v124", "v125", "v126", "v127", "v128", "v129", "v130", "v131", "v132", "v133", "v134", "v135", "v136", "v137", "v138", "v139", "v140", "v141", "v142", "v143", "v144", "v145", "v146", "v147", "v148", "v149", "v150", "v151", "v152", "v153", "v154", "v155", "v156", "v157", "v158", "v159", "v160", "v161", "v162", "v163", "v164", "v165", "v166", "v167", "v168", "v169", "v170", "v171", "v172", "v173", "v174", "v175", "v176", "v177", "v178", "v179", "v180", "v181", "v182", "v183", "v184", "v185", "v186", "v187", "v188", "v189", "v190", "v191", "v192", "v193", "v194", "v195", "v196", "v197", "v198", "v199", "v200", "v201", "v202", "v203", "v204", "v205", "v206", "v207", "v208", "v209", "v210", "v211", "v212", "v213", "v214", "v215", "v216", "v217", "v218", "v219", "v220", "v221", "v222", "v223", "v224", "v225", "v226", "v227", "v228", "v229", "v230", "v231", "v232", "v233", "v234", "v235", "v236", "v237", "v238", "v239", "v240", "v241", "v242", "v243", "v244", "v245", "v246", "v247", "v248", "v249", "v250", "v251", "v252", "v253", "v254", "v255", "s4", "s5", "s6", "s7", "s8", "s9", "s10", "s11", "s12", "s13", "s14", "s15", "s16", "s17", "s18", "s19", "s20", "s21", "s22", "s23", "s24", "s25", "s26", "s27", "s28", "s29", "s30", "s31", "s33", "s34", "s35", "s36", "s37", "s38", "s39", "s40", "s41", "s42", "s43", "s44", "s45", "s46", "s47", "s48", "s49", "s50", "s51", "s52", "s53", "s54", "s55", "s56", "s57", "s58", "s59", "s60", "s61", "s62", "s63", "s64", "s65", "s66", "s67", "s69", "s70", "s71", "s72", "s73", "s74", "s75", "s76", "s77", "s78", "s79", "s80", "s81", "s82", "s83", "s84", "s85", "s86", "s87", "s88", "s89", "s91", "s92", "s93", "s94", "s95", "s96", "s97", "s98", "s99");
    const int w = __builtin_amdgcn_readfirstlane((int)wq);
    const unsigned lds0 = (unsigned)(uintptr_t)(__attribute__((address_space(3))) unsigned char*)smem;
    asm volatile("" : "+s"(ka));
    unsigned char* ws = (unsigned char*)ka[6];
    if (__builtin_amdgcn_readfirstlane((int)slow) == -1) {
        const int lane = lane_opaque();
        tag = (unsigned)__builtin_amdgcn_readfirstlane((int)tag);
        const int* dst = (const int*)ka[0]; const int* src = (const int*)ka[2];
        const unsigned short *Y = (const unsigned short*)ka[4], *ef = (const unsigned short*)ka[8], *x = (const unsigned short*)ka[10], *w_rad = (const unsigned short*)ka[14],
                             *cg = (const unsigned short*)ka[16], *w_mix = (const unsigned short*)ka[18];
        unsigned short* out = (unsigned short*)ka[12];
        const int N = (int)ka[20], E = (int)(ka[20] >> 32), nh = (int)ka[23];
        const int half = (bx >> 2) & 1, b = (bx & 3) * 32 + (bx >> 3), sS = 40 * (1 - half), sK = 40 * half;
        const int node0 = (b * N + 127) >> 7, npb = (((b + 1) * N + 127) >> 7) - node0;
        const __amdgpu_buffer_rsrc_t rsw = __builtin_amdgcn_make_buffer_rsrc((void*)ws, 0, W_END, 0x00020000);
        lds_wait(lds0 + SY_C3, 4);
        const __amdgpu_buffer_rsrc_t rso = __builtin_amdgcn_make_buffer_rsrc((void*)out, 0, N * 1024, 0x00020000);
#pragma unroll 1
        for (int it = 0; it < 10; ++it) {
            const int nl = (it < 5 ? sS + 8 * it : sK + 8 * (it - 5)) + w, node = node0 + nl;
            const bool nv = nl < npb;
            const unsigned ro = nv ? (unsigned)node * 1024u + (unsigned)lane * 16u : OOB_OFF;
            float a8[8] = {0.f, 0.f, 0.f, 0.f, 0.f, 0.f, 0.f, 0.f};
#pragma unroll 1
            for (int e0 = 0; e0 < (nv ? E : 0); e0 += 64) {
                const int ec = min(e0 + lane, E - 1);
                u64 mm = __ballot(e0 + lane < E && dst[ec] == node && (src[ec] >= nh) == (half != 0));
#pragma unroll 1
                while (mm) {
                    const int e = e0 + __builtin_ctzll(mm);
                    mm &= mm - 1;
                    const unsigned short* fe = (const unsigned short*)ef + (size_t)e * DE_DIM;
                    const unsigned short* ye = (const unsigned short*)Y + (size_t)e * M_DIM;
                    const unsigned short* xp = x + (size_t)src[e] * (C_DIM * M_DIM) + lane * 8;
                    float z0 = 0.f, z1 = 0.f;
#pragma unroll 1
                    for (int f = 0; f < DE_DIM; ++f) { z0 += bf2f(fe[f]) * bf2f(w_rad[f * C_DIM + 2 * lane]); z1 += bf2f(fe[f]) * bf2f(w_rad[f * C_DIM + 2 * lane + 1]); }
                    const float r0 = z0 / (1.f + __expf(-z0)), r1 = z1 / (1.f + __expf(-z1));
#pragma unroll 1
                    for (int i = 0; i < 4; ++i) {
                        const float x0 = bf2f(xp[i]) * r0, x1 = bf2f(xp[4 + i]) * r1;
#pragma unroll
                        for (int o = 0; o < 4; ++o) {
                            float wv4 = 0.f;
#pragma unroll
                            for (int f = 0; f < 4; ++f) wv4 += bf2f(cg[(i * 4 + f) * 4 + o]) * bf2f(ye[f]);
                            a8[o] += x0 * wv4; a8[4 + o] += x1 * wv4;
                        }
                    }
                }
            }
            if (it >= 5) {
                const u32x4 pv = __builtin_amdgcn_raw_buffer_load_b128(rsw, ro, W_AGG, SC1);
#pragma unroll
                for (int i = 0; i < 4; ++i) { a8[2 * i] += __uint_as_float(pv[i] << 16); a8[2 * i + 1] += __uint_as_float(pv[i] & 0xffff0000u); }
            }
            const u32x4 rv = (u32x4){pack2bf(a8[0], a8[1]), pack2bf(a8[2], a8[3]), pack2bf(a8[4], a8[5]), pack2bf(a8[6], a8[7])};
            if (it < 5) {
                __builtin_amdgcn_raw_buffer_store_b128(rv, rsw, ro, W_AGG, SC1);
                if (it == 4) {
                    asm volatile("s_waitcnt vmcnt(0)" ::: "memory");
                    if (lds_arrive_rtn(lds0 + 4) == 7u) flag_store(ws + W_FLG + bx * 128, tag);
                    poll_tag(ws + W_FLG + (bx ^ 4) * 128, tag);
                }
            } else {
                *(u32x4*)(smem + L_LST + w * 1024 + lane * 16) = rv;
#pragma unroll
                for (int i = 0; i < 8; ++i) a8[i] = 0.f;
#pragma unroll 1
                for (int c = 0; c < C_DIM; ++c) {
                    const unsigned short* ap = (const unsigned short*)(smem + L_LST + w * 1024 + c * 8);
                    const float w0 = bf2f(w_mix[c * C_DIM + 2 * lane]), w1 = bf2f(w_mix[c * C_DIM + 2 * lane + 1]);
#pragma unroll
                    for (int m = 0; m < 4; ++m) { a8[m] += bf2f(ap[m]) * w0; a8[4 + m] += bf2f(ap[m]) * w1; }
                }
                __builtin_amdgcn_raw_buffer_store_b128((u32x4){pack2bf(a8[0], a8[1]), pack2bf(a8[2], a8[3]), pack2bf(a8[4], a8[5]), pack2bf(a8[6], a8[7])}, rso, ro, 0, 0);
            }
        }
    }
}

__global__ void kf_zero(float* __restrict__ agg, size_t n) {
    for (size_t i = (size_t)blockIdx.x * blockDim.x + threadIdx.x; i < n; i += (size_t)gridDim.x * blockDim.x) agg[i] = 0.f;
}
__global__ void kf_edges(const unsigned short* __restrict__ x, const int* __restrict__ src, const int* __restrict__ dst,
                         const unsigned short* __restrict__ Y, const unsigned short* __restrict__ ef,
                         const unsigned short* __restrict__ w_rad, const unsigned short* __restrict__ cg,
                         float* __restrict__ agg, int E) {
    const int ch = threadIdx.x;
    for (int e = blockIdx.x; e < E; e += gridDim.x) {
        float z = 0.f;
        for (int f = 0; f < DE_DIM; ++f) z += bf2f(ef[(size_t)e * DE_DIM + f]) * bf2f(w_rad[f * C_DIM + ch]);
        const float r = z / (1.f + __expf(-z));
        float a4[4] = {0.f, 0.f, 0.f, 0.f};
        for (int ii = 0; ii < 4; ++ii) {
            const float xv = bf2f(x[((size_t)src[e] * C_DIM + ch) * M_DIM + ii]) * r;
            for (int o = 0; o < 4; ++o) {
                float wv = 0.f;
                for (int f = 0; f < 4; ++f) wv += bf2f(cg[(ii * 4 + f) * 4 + o]) * bf2f(Y[(size_t)e * 4 + f]);
                a4[o] += xv * wv;
            }
        }
        for (int o = 0; o < 4; ++o) atomicAdd(&agg[((size_t)dst[e] * C_DIM + ch) * M_DIM + o], a4[o]);
    }
}
__global__ void kf_mix(const float* __restrict__ agg, const unsigned short* __restrict__ w_mix, unsigned short* __restrict__ out, int N) {
    const int d = threadIdx.x;
    for (int n = blockIdx.x; n < N; n += gridDim.x) {
        float a4[4] = {0.f, 0.f, 0.f, 0.f};
        for (int c = 0; c < C_DIM; ++c) {
            const float wv = bf2f(w_mix[c * C_DIM + d]);
            for (int m = 0; m < 4; ++m) a4[m] += bf2f(f2bf(agg[((size_t)n * C_DIM + c) * M_DIM + m])) * wv;
        }
        for (int m = 0; m < 4; ++m) out[((size_t)n * C_DIM + d) * M_DIM + m] = f2bf(a4[m]);
    }
}

extern "C" void kernel_launch(void* const* d_in, const int* in_sizes, int n_in,
                              void* d_out, int out_size, void* d_ws, size_t ws_size,
                              hipStream_t stream) {
    const unsigned short* x = (const unsigned short*)d_in[0];
    const int* src = (const int*)d_in[1];
    const int* dst = (const int*)d_in[2];
    const unsigned short* Y = (const unsigned short*)d_in[3];
    const unsigned short* ef = (const unsigned short*)d_in[4];
    const unsigned short* w_rad = (const unsigned short*)d_in[5];
    const unsigned short* cg = (const unsigned short*)d_in[6];
    const unsigned short* w_mix = (const unsigned short*)d_in[7];
    unsigned short* out = (unsigned short*)d_out;
    const int N = in_sizes[0] / (C_DIM * M_DIM);
    const int E = in_sizes[1];
    const int epb = (E + 255) / 256;
    const int nh = (N + 1) / 2;
    u64 hi = (u64)(uintptr_t)d_out | (u64)(uintptr_t)d_ws;
    for (int i = 0; i < 8; ++i) hi |= (u64)(uintptr_t)d_in[i];
    const bool fast = (hi >> 48) == 0 && N >= 256 && N <= NB_MAX * NPB_MAX && E > 0 && epb <= EPB_MAX && ws_size >= (size_t)W_END;
    if (!fast) {
        float* agg = (float*)d_ws;
        if (N <= 0) return;
        kf_zero<<<1024, 256, 0, stream>>>(agg, (size_t)N * C_DIM * M_DIM);
        if (E > 0) kf_edges<<<4096, C_DIM, 0, stream>>>(x, src, dst, Y, ef, w_rad, cg, agg, E);
        kf_mix<<<2048, C_DIM, 0, stream>>>(agg, w_mix, out, N);
        return;
    }
    int k = 0;
    while ((N << k) <= 32768) ++k;
    const unsigned magic = (unsigned)(((1ull << (39 - k)) + N - 1) / N);
    const unsigned F = 0x00020000u;
    k_fused<<<256, 512, 0, stream>>>(dst, E * 4u, F, src, E * 4u, F, Y, E * 8u, F, d_ws, W_END, F, ef, E * 32u, F, x, N * 1024u, F, out, N * 1024u, F, w_rad, DE_DIM * C_DIM * 2u, F,
                                     cg, 128u, F, w_mix, C_DIM * C_DIM * 2u, F, N, E, epb, magic, k, nh * 1024u, nh, 0u);
}
```

```cpp
#include <hip/hip_runtime.h>
#include <hip/hip_bf16.h>
#include <stdint.h>

typedef __attribute__((ext_vector_type(4))) unsigned u32x4;
typedef unsigned long long u64;

#define C_DIM 128
#define M_DIM 4
#define DE_DIM 16
#define NPB_MAX 80
#define NB_MAX 128
#define EPB_MAX 1280
#define OOB_OFF 0x80000000u
#define SC1 16
#define SPIN_MAX (1u << 22)
#define W_FLG 4096
#define W_AGG 5804032
#define W_END 16289792
#define L_LST 5120
#define L_TOTAL 156672
#define SY_C3 56

static __device__ __forceinline__ int lane_opaque() {
    int l;
    asm volatile("v_mbcnt_lo_u32_b32 %0, -1, 0\n\tv_mbcnt_hi_u32_b32 %0, -1, %0" : "=v"(l));
    return l;
}

static __device__ __forceinline__ float bf2f(unsigned short v) { return __uint_as_float(((unsigned)v) << 16); }
typedef __bf16 bf16v2 __attribute__((ext_vector_type(2)));
static __device__ __forceinline__ unsigned pack2bf(float lo, float hi) {
    bf16v2 r = (bf16v2){(__bf16)lo, (__bf16)hi};
    return __builtin_bit_cast(unsigned, r);
}
static __device__ __forceinline__ unsigned short f2bf(float v) { return (unsigned short)(pack2bf(v, 0.f) & 0xffffu); }
static __device__ __forceinline__ void flag_store(const void* p, unsigned v) {
    unsigned t0, t1; u64 sv;
    asm volatile("v_mov_b32 %0, 0\n\tv_mov_b32 %1, %4\n\ts_mov_b64 %2, exec\n\ts_mov_b64 exec, 1\n\t"
                 "global_store_dword %0, %1, %3 sc1\n\ts_mov_b64 exec, %2" : "=&v"(t0), "=&v"(t1), "=&s"(sv) : "s"(p), "s"(v) : "memory");
}
static __device__ __forceinline__ unsigned lds_arrive_rtn(unsigned a) {
    unsigned t0, t1; u64 sv;
    asm volatile("v_mov_b32 %0, %3\n\tv_mov_b32 %1, 1\n\ts_mov_b64 %2, exec\n\ts_mov_b64 exec, 1\n\t"
                 "ds_add_rtn_u32 %1, %0, %1\n\ts_mov_b64 exec, %2\n\ts_waitcnt lgkmcnt(0)" : "=&v"(t0), "=&v"(t1), "=&s"(sv) : "s"(a) : "memory");
    return (unsigned)__builtin_amdgcn_readfirstlane((int)t1);
}
template <int SLEEP = 0> static __device__ __forceinline__ void lds_wait(unsigned a, unsigned n) {
    for (;;) {
        unsigned v;
        asm volatile("ds_read_b32 %0, %1\n\ts_waitcnt lgkmcnt(0)" : "=v"(v) : "v"(a) : "memory");
        if ((unsigned)__builtin_amdgcn_readfirstlane((int)v) >= n) break;
        if (SLEEP) __builtin_amdgcn_s_sleep(SLEEP);
    }
}
static __device__ __forceinline__ unsigned ld_agent(const void* p) { return __hip_atomic_load((const unsigned*)p, __ATOMIC_RELAXED, __HIP_MEMORY_SCOPE_AGENT); }
static __device__ __forceinline__ void poll_tag(const void* p, unsigned tag) {
#pragma unroll 1
    for (unsigned spin = 0; spin < SPIN_MAX; ++spin) {
        if (__all(ld_agent(p) == tag)) break;
        __builtin_amdgcn_s_sleep(2);
    }
    __builtin_amdgcn_fence(__ATOMIC_ACQUIRE, "wavefront");
}

#define BUF(t, n) t n, unsigned n##_bytes, unsigned n##_fmt
__global__ __launch_bounds__(512, 2) void k_fused(
    BUF(const void*, dst_), BUF(const void*, src_), BUF(const void*, Y_), BUF(void*, ws_), BUF(const void*, ef_), BUF(const void*, x_), BUF(void*, out_), BUF(const void*, w_rad_),
    BUF(const void*, cg_), BUF(const void*, w_mix_), unsigned N_, unsigned E_, unsigned epb_, unsigned magic_, unsigned shift_, unsigned nhb_, unsigned nh_, unsigned zero_)
{
    __shared__ __attribute__((aligned(256))) unsigned char smem[L_TOTAL];
    const __attribute__((address_space(4))) u64* ka = (const __attribute__((address_space(4))) u64*)__builtin_amdgcn_kernarg_segment_ptr();
    const int bx = blockIdx.x;
    unsigned slow, tag, wq, tid = threadIdx.x;
    asm volatile(
            "s_load_dwordx16 s[8:23], s[0:1], 0\n"
            "s_load_dwordx16 s[40:55], s[0:1], 128\n"
            "s_load_dwordx16 s[24:39], s[0:1], 64\n"
            "s_getpc_b64 s[4:5]\n"
            "v_readfirstlane_b32 s3, v0\n"
            "v_and_b32 v243, 63, v0\n"
            "s_setprio 3\n"
            "s_lshr_b32 s3, s3, 6\n"
            "v_mul_u32_u24 v54, 3, v243\n"
            "s_cmp_lg_u32 s3, 4\n"
            "s_cbranch_scc1 Lncp_%=\n"
            "s_and_b32 s4, s4, 0xffffff80\n"
            "s_movk_i32 s6, 17664\n"
            "s_mov_b32 s7, 0x20000\n"
            "s_lshr_b32 s68, s2, 3\n"
            "s_lshl_b32 s68, s68, 7\n"
            "v_lshlrev_b32 v81, 12, v243\n"
            "buffer_load_dword v53, v81, s[4:7], s68 offen\n"
            "Lncp_%=:\n"
            "s_mul_i32 s68, s3, 162\n"
            "v_cmp_gt_u32 vcc, 54, v243\n"
            "v_add_u32 v54, s68, v54\n"
            "v_mov_b32 v82, 0x1000000\n"
            "v_cndmask_b32 v54, v82, v54, vcc\n"
            "v_mov_b32 v219, 0\n"
            "s_waitcnt lgkmcnt(0)\n"
            "s_mul_i32 s95, s2, s50\n"
            "v_add_lshl_u32 v81, v54, s95, 2\n"
            "buffer_load_dwordx3 v[32:34], v81, s[8:11], 0 offen nt\n"
            "buffer_load_dwordx3 v[36:38], v81, s[12:15], 0 offen nt\n"
            "v_lshlrev_b32 v82, 1, v81\n"
            "buffer_load_dword v40, off, s[20:23], 0\n"
            "buffer_load_dwordx4 v[42:45], v82, s[16:19], 0 offen nt\n"
            "buffer_load_dwordx2 v[46:47], v82, s[16:19], 0 offen offset:16 nt\n"
            "v_lshlrev_b32 v193, 7, v243\n"
            "s_lshl_b32 s54, s95, 5\n"
            "s_bfe_u32 s8, s2, 0x10002\n"
            "s_and_b32 s9, s2, 3\n"
            "s_lshl_b32 s9, s9, 5\n"
            "s_lshr_b32 s68, s2, 3\n"
            "s_add_u32 s9, s9, s68\n"
            "s_mul_i32 s55, s8, s53\n"
            "s_lshl_b32 s69, s9, 13\n"
            "s_add_u32 s69, s69, s55\n"
            "s_cmp_lt_u32 s3, 4\n"
            "s_cbranch_scc1 Lx0_%=\n"
            "v_lshlrev_b32 v81, 4, v0\n"
            "v_and_b32 v81, 0xe00, v81\n"
            "v_and_b32 v82, 31, v0\n"
            "v_lshl_or_b32 v81, v82, 3, v81\n"
            "buffer_load_dwordx2 v[48:49], v81, s[36:39], 0 offen\n"
            "buffer_load_dwordx2 v[50:51], v81, s[36:39], 0 offen offset:256\n"
            "s_cmp_eq_u32 s3, 4\n"
            "s_cbranch_scc1 Lx4_%=\n"
            "s_sub_u32 s68, 7, s3\n"
            "s_cmp_lt_u32 s68, 3\n"
            "s_cselect_b32 s68, s68, 0x20000\n"
            "s_lshl_b32 s68, s68, 13\n"
            "s_add_u32 s68, s68, s54\n"
            "buffer_load_dword v52, v193, s[24:27], s68 offen\n"
            "s_branch Lx1_%=\n"
            "Lx4_%=:\n"
            "v_lshlrev_b32 v81, 2, v243\n"
            "buffer_load_dword v52, v81, s[40:43], 0 offen\n"
            "s_branch Lx1_%=\n"
            "Lx0_%=:\n"
            "s_add_u32 s68, s3, 3\n"
            "s_cmp_lt_u32 s68, 3\n"
            "s_cselect_b32 s68, s68, 0x20000\n"
            "s_lshl_b32 s68, s68, 13\n"
            "s_add_u32 s68, s68, s54\n"
            "buffer_load_dword v52, v193, s[24:27], s68 offen\n"
            "s_and_b32 s68, s9, 31\n"
            "s_lshl_b32 s68, s68, 7\n"
            "s_cmp_eq_u32 s3, 3\n"
            "s_cselect_b32 s68, s68, 0x40000000\n"
            "v_lshlrev_b32 v81, 5, v193\n"
            "buffer_load_dword v48, v81, s[44:47], s68 offen\n"
            "buffer_load_dword v49, off, s[20:23], 0\n"
            "Lx1_%=:\n"
            "v_mov_b64 v[82:83], 0\n"
            "v_mov_b64 v[84:85], 0\n"
            "v_lshlrev_b32 v81, 4, v0\n"
            "s_cmp_lt_u32 s3, 4\n"
            "s_cselect_b32 s68, 0, 72704\n"
            "v_add_u32 v86, s68, v81\n"
            "ds_write_b128 v86, v[82:85]\n"
            "s_cselect_b32 s68, 80896, 0\n"
            "v_add_u32 v86, s68, v81\n"
            "ds_write_b128 v86, v[82:85]\n"
            "v_and_b32 v196, 15, v0\n"
            "v_mov_b32 v209, 1\n"
            "s_cmp_lg_u32 s3, 0\n"
            "s_cbranch_scc1 Lnk_%=\n"
            "v_lshlrev_b32 v81, 2, v243\n"
            "v_sub_u32 v82, 127, v243\n"
            "ds_write_b32 v81, v82 offset:96\n"
            "v_lshlrev_b32 v81, 2, v196\n"
            "v_sub_u32 v82, 63, v196\n"
            "ds_write_b32 v81, v82 offset:352\n"
            "v_bfrev_b32 v82, 1\n"
            "ds_write2_b32 v219, v82, v82 offset0:20 offset1:21\n"
            "Lnk_%=:\n"
            "s_sub_i32 s96, s49, s95\n"
            "s_min_i32 s96, s96, s50\n"
            "s_max_i32 s96, s96, 0\n"
            "s_and_b32 s12, s3, 3\n"
            "s_lshl_b32 s12, s12, 3\n"
            "s_lshl_b32 s68, 1, s12\n"
            "v_mov_b32 v80, s68\n"
            "s_sub_u32 s68, s68, 1\n"
            "s_and_b32 s68, s68, 0x10101010\n"
            "s_cmp_lt_u32 s3, 4\n"
            "s_cselect_b32 s13, s68, 0x10101010\n"
            "s_cselect_b32 s14, 0, s68\n"
            "s_mov_b32 s39, 0x10101010\n"
            "v_cmp_gt_u32 s[72:73], s96, v54\n"
            "v_lshlrev_b32 v77, 10, v54\n"
            "v_add_u32 v81, 1, v54\n"
            "v_cmp_gt_u32 s[74:75], s96, v81\n"
            "v_lshlrev_b32 v78, 10, v81\n"
            "v_add_u32 v81, 2, v54\n"
            "v_cmp_gt_u32 s[76:77], s96, v81\n"
            "v_lshlrev_b32 v79, 10, v81\n"
            "s_mul_i32 s10, s9, s48\n"
            "s_add_u32 s10, s10, 127\n"
            "s_add_u32 s11, s10, s48\n"
            "s_lshr_b32 s10, s10, 7\n"
            "s_lshr_b32 s11, s11, 7\n"
            "s_sub_u32 s11, s11, s10\n"
            "v_lshlrev_b32 v192, 4, v243\n"
            "v_lshlrev_b32 v194, 3, v243\n"
            "v_lshrrev_b32 v195, 4, v243\n"
            "v_lshlrev_b32 v214, 4, v196\n"
            "v_and_b32 v249, 3, v0\n"
            "v_lshlrev_b32 v250, 4, v249\n"
            "v_mov_b32 v251, 80\n"
            "v_lshrrev_b32 v83, 2, v196\n"
            "s_lshl_b32 s68, s3, 9\n"
            "s_add_u32 s68, s68, 62464\n"
            "v_add_u32 v212, s68, v194\n"
            "v_lshl_add_u32 v213, v195, 7, s68\n"
            "v_lshl_add_u32 v213, v249, 5, v213\n"
            "v_lshl_add_u32 v213, v83, 3, v213\n"
            "v_mov_b32 v224, 0x3f80\n"
            "v_mov_b32 v225, 0\n"
            "v_lshlrev_b64 v[224:225], v250, v[224:225]\n"
            "s_mul_i32 s68, s3, 4352\n"
            "s_sub_u32 s38, s3, 3\n"
            "s_max_i32 s38, s38, 0\n"
            "s_lshl_b32 s38, s38, 12\n"
            "s_add_u32 s68, s68, s38\n"
            "s_add_u32 s68, s68, 72704\n"
            "v_mov_b32 v81, s68\n"
            "v_add_u32 v231, s68, v192\n"
            "v_add_u32 v239, 0xfffff000, v231\n"
            "v_mov_b32 v236, v231\n"
            "s_add_u32 s38, s3, 3\n"
            "s_and_b32 s38, s38, 3\n"
            "s_lshl2_add_u32 s41, s38, 16\n"
            "s_mul_i32 s38, s38, 8448\n"
            "s_add_u32 s38, s38, 90112\n"
            "v_add_u32 v252, s38, v192\n"
            "s_movk_i32 s38, 272\n"
            "v_mad_u32_u24 v227, v196, s38, v81\n"
            "v_lshl_add_u32 v227, v195, 4, v227\n"
            "s_movk_i32 s38, 1088\n"
            "v_mad_u32_u24 v226, v195, s38, v81\n"
            "v_lshl_add_u32 v226, v196, 3, v226\n"
            "v_add_u32 v228, 123904, v192\n"
            "v_and_b32 v253, 1, v195\n"
            "v_lshl_add_u32 v253, v196, 1, v253\n"
            "v_lshlrev_b32 v253, 2, v253\n"
            "v_and_b32 v229, 1, v0\n"
            "v_lshlrev_b32 v229, 4, v229\n"
            "v_lshlrev_b32 v81, 25, v0\n"
            "v_and_b32 v81, 2.0, v81\n"
            "v_or_b32 v229, v229, v81\n"
            "v_bfe_u32 v206, v0, 1, 2\n"
            "v_lshlrev_b32 v207, 4, v206\n"
            "v_bfe_u32 v83, v0, 3, 2\n"
            "s_mul_i32 s68, s3, 768\n"
            "s_add_u32 s68, s68, 66560\n"
            "v_add_u32 v247, s68, v194\n"
            "v_lshl_add_u32 v81, v249, 3, s68\n"
            "v_lshl_add_u32 v237, v195, 1, v81\n"
            "v_add_u32 v237, 32, v237\n"
            "v_lshl_add_u32 v81, v206, 3, s68\n"
            "v_lshl_add_u32 v238, v83, 1, v81\n"
            "v_add_u32 v238, 64, v238\n"
            "v_and_b32 v81, 7, v0\n"
            "v_lshlrev_b32 v82, 2, v81\n"
            "s_mov_b32 s68, 1982153488\n"
            "v_bfe_u32 v240, s68, v82, 4\n"
            "v_lshlrev_b32 v240, 4, v240\n"
            "v_add_u32 v240, 3328, v240\n"
            "s_mov_b32 s68, 2205745445\n"
            "v_bfe_u32 v241, s68, v82, 4\n"
            "v_lshlrev_b32 v241, 4, v241\n"
            "v_add_u32 v241, 3072, v241\n"
            "s_mov_b32 s68, 2305097727\n"
            "v_bfe_u32 v242, s68, v82, 4\n"
            "v_lshlrev_b32 v242, 4, v242\n"
            "v_add_u32 v242, 3072, v242\n"
            "v_cmp_lt_u32 vcc, 5, v81\n"
            "v_add_u32 v82, 256, v242\n"
            "s_nop 0\n"
            "v_cndmask_b32 v242, v242, v82, vcc\n"
            "v_cmp_lt_u32 vcc, 3, v81\n"
            "s_nop 1\n"
            "v_cndmask_b32 v242, 8, v242, vcc\n"
            "s_mov_b32 s68, 1998716928\n"
            "v_lshlrev_b32 v82, 2, v81\n"
            "v_bfe_u32 v208, s68, v82, 4\n"
            "s_add_u32 s38, s3, 1\n"
            "s_and_b32 s38, s38, 3\n"
            "s_add_u32 s38, s38, 4\n"
            "v_readlane_b32 s64, v240, s3\n"
            "v_readlane_b32 s65, v241, s3\n"
            "v_readlane_b32 s66, v242, s3\n"
            "v_readlane_b32 s40, v242, s38\n"
            "v_lshl_add_u32 v244, v195, 2, s64\n"
            "v_lshl_add_u32 v245, v195, 2, s65\n"
            "v_lshl_add_u32 v246, v195, 2, s66\n"
            "v_lshl_add_u32 v248, v83, 2, s64\n"
            "s_add_u32 s64, s64, 512\n"
            "s_add_u32 s65, s65, 512\n"
            "s_add_u32 s66, s66, 512\n"
            "s_add_u32 s40, s40, 512\n"
            "s_lshl_b32 s68, s2, 7\n"
            "s_add_u32 s38, s68, 4096\n"
            "s_add_u32 s56, s20, s38\n"
            "s_addc_u32 s57, s21, 0\n"
            "s_xor_b32 s38, s38, 512\n"
            "s_add_u32 s58, s20, s38\n"
            "s_addc_u32 s59, s21, 0\n"
            "s_cmp_lt_u32 s3, 6\n"
            "s_mov_b32 s60, 78\n"
            "s_cmov_b32 s60, 78\n"
            "s_mov_b32 s61, 4294961748\n"
            "s_cmov_b32 s61, 4294961748\n"
            "s_movk_i32 s62, 0x3862\n"
            "s_cmov_b32 s62, 0x9062\n"
            "s_cmp_lt_u32 s3, 4\n"
            "s_cmov_b32 s60, 590\n"
            "s_cmov_b32 s61, 4294612224\n"
            "s_cmovk_i32 s62, 0x82\n"
            "s_mov_b32 s63, 5804032\n"
            "s_and_b32 s68, s9, 127\n"
            "s_lshl3_add_u32 s68, s68, s3\n"
            "s_mul_i32 s68, s68, 512\n"
            "s_add_u32 s67, s68, s55\n"
            "s_add_u32 s67, s67, 3145728\n"
            "s_lshr_b32 s38, s3, 2\n"
            "s_lshl_b32 s38, s38, 10\n"
            "s_lshl_b32 s15, s3, 9\n"
            "s_add_u32 s15, s15, 29696\n"
            "v_add_u32 v94, s15, v194\n"
            "s_lshr_b32 s68, s38, 1\n"
            "s_sub_u32 s15, s15, s68\n"
            "s_and_b32 s68, s3, 1\n"
            "s_lshl1_add_u32 s68, s2, s68\n"
            "s_lshl_b32 s68, s68, 10\n"
            "s_add_u32 s36, s68, 36864\n"
            "v_mul_u32_u24 v144, 48, v243\n"
            "s_mul_i32 s68, s3, 2592\n"
            "s_add_u32 s68, s68, 8192\n"
            "v_add_u32 v144, s68, v144\n"
            "v_add_u32 v145, 16, v144\n"
            "v_add_u32 v146, 32, v144\n"
            "v_lshlrev_b32 v147, 1, v0\n"
            "v_lshlrev_b32 v95, 4, v0\n"
            "s_mul_i32 s68, s2, 20480\n"
            "s_add_u32 s68, s68, 557056\n"
            "s_add_u32 s4, s20, s68\n"
            "s_addc_u32 s5, s21, 0\n"
            "s_lshl_b32 s6, s96, 4\n"
            "s_add_u32 s6, s6, 4223\n"
            "s_and_b32 s6, s6, 0xffffff80\n"
            "s_mov_b32 s7, 0x20000\n"
            "v_mov_b64 v[0:1], 0\n"
            "v_mov_b64 v[2:3], 0\n"
            "v_mov_b64 v[4:5], 0\n"
            "v_mov_b64 v[6:7], 0\n"
            "v_mov_b64 v[8:9], 0\n"
            "v_mov_b64 v[10:11], 0\n"
            "v_mov_b64 v[12:13], 0\n"
            "v_mov_b64 v[14:15], 0\n"
            "v_mov_b64 v[16:17], 0\n"
            "v_mov_b64 v[18:19], 0\n"
            "v_mov_b64 v[20:21], 0\n"
            "v_mov_b64 v[22:23], 0\n"
            "v_mov_b64 v[24:25], 0\n"
            "v_mov_b64 v[26:27], 0\n"
            "v_mov_b64 v[28:29], 0\n"
            "v_mov_b64 v[30:31], 0\n"
            "s_waitcnt lgkmcnt(0)\n"
            "s_barrier\n"
            "s_waitcnt vmcnt(6)\n"
            "v_lshlrev_b32 v81, s52, v32\n"
            "v_lshlrev_b32 v84, s52, v36\n"
            "v_lshlrev_b32 v82, s52, v33\n"
            "v_lshlrev_b32 v85, s52, v37\n"
            "v_lshlrev_b32 v83, s52, v34\n"
            "v_lshlrev_b32 v86, s52, v38\n"
            "v_mul_hi_u32_u24 v81, s51, v81\n"
            "v_mul_hi_u32_u24 v84, s51, v84\n"
            "v_lshl_add_u32 v68, v81, 2, s38\n"
            "v_and_b32 v81, 64, v84\n"
            "v_lshl_add_u32 v68, v81, 3, v68\n"
            "s_mov_b64 exec, s[72:73]\n"
            "ds_add_rtn_u32 v71, v68, v80 offset:3072\n"
            "s_mov_b64 exec, -1\n"
            "v_mul_hi_u32_u24 v82, s51, v82\n"
            "v_mul_hi_u32_u24 v85, s51, v85\n"
            "v_lshl_add_u32 v69, v82, 2, s38\n"
            "v_and_b32 v82, 64, v85\n"
            "v_lshl_add_u32 v69, v82, 3, v69\n"
            "s_mov_b64 exec, s[74:75]\n"
            "ds_add_rtn_u32 v72, v69, v80 offset:3072\n"
            "s_mov_b64 exec, -1\n"
            "v_mul_hi_u32_u24 v83, s51, v83\n"
            "v_mul_hi_u32_u24 v86, s51, v86\n"
            "v_lshl_add_u32 v70, v83, 2, s38\n"
            "v_and_b32 v83, 64, v86\n"
            "v_lshl_add_u32 v70, v83, 3, v70\n"
            "s_mov_b64 exec, s[76:77]\n"
            "ds_add_rtn_u32 v73, v70, v80 offset:3072\n"
            "s_mov_b64 exec, -1\n"
            "v_bfe_u32 v84, v84, 3, 3\n"
            "v_and_b32 v81, 127, v32\n"
            "v_lshl_or_b32 v81, v81, 3, v84\n"
            "v_bfe_u32 v85, v85, 3, 3\n"
            "v_and_b32 v82, 127, v33\n"
            "v_lshl_or_b32 v82, v82, 3, v85\n"
            "v_bfe_u32 v86, v86, 3, 3\n"
            "v_and_b32 v83, 127, v34\n"
            "v_lshl_or_b32 v83, v83, 3, v86\n"
            "s_waitcnt vmcnt(5)\n"
            "v_readfirstlane_b32 s90, v40\n"
            "s_mul_i32 s90, s90, 1664525\n"
            "s_add_u32 s90, s90, 1013904223\n"
            "s_lshl_b32 s93, s90, 14\n"
            "s_and_b32 s94, s90, 0xffe00000\n"
            "v_or3_b32 v59, v81, v77, s94\n"
            "v_or_b32 v57, s93, v36\n"
            "v_or3_b32 v63, v82, v78, s94\n"
            "v_or_b32 v61, s93, v37\n"
            "v_or3_b32 v67, v83, v79, s94\n"
            "v_or_b32 v65, s93, v38\n"
            "s_waitcnt vmcnt(3)\n"
            "v_mov_b32 v56, v42\n"
            "v_mov_b32 v58, v43\n"
            "v_mov_b32 v60, v44\n"
            "v_mov_b32 v62, v45\n"
            "v_mov_b32 v64, v46\n"
            "v_mov_b32 v66, v47\n"
            "s_mov_b64 exec, s[72:73]\n"
            "ds_write_b128 v144, v[56:59] offset:0\n"
            "ds_write_b128 v144, v[60:63] offset:16\n"
            "ds_write_b128 v144, v[64:67] offset:32\n"
            "s_mov_b64 exec, -1\n"
            "s_waitcnt lgkmcnt(3)\n"
            "s_barrier\n"
            "ds_read_b128 v[128:131], v192 offset:3072\n"
            "ds_read_b128 v[132:135], v192 offset:4096\n"
            "v_lshrrev_b32 v74, 1, v68\n"
            "v_add_u32 v74, s15, v74\n"
            "v_bfe_u32 v71, v71, s12, 8\n"
            "v_lshrrev_b32 v75, 1, v69\n"
            "v_add_u32 v75, s15, v75\n"
            "v_bfe_u32 v72, v72, s12, 8\n"
            "v_lshrrev_b32 v76, 1, v70\n"
            "v_add_u32 v76, s15, v76\n"
            "v_bfe_u32 v73, v73, s12, 8\n"
            "s_waitcnt lgkmcnt(1)\n"
            "v_dot4_u32_u8 v136, v128, s39, 0\n"
            "v_dot4_u32_u8 v137, v129, s39, 0\n"
            "v_dot4_u32_u8 v138, v130, s39, 0\n"
            "v_dot4_u32_u8 v139, v131, s39, 0\n"
            "s_waitcnt lgkmcnt(0)\n"
            "v_dot4_u32_u8 v136, v132, s39, v136\n"
            "v_dot4_u32_u8 v137, v133, s39, v137\n"
            "v_dot4_u32_u8 v138, v134, s39, v138\n"
            "v_dot4_u32_u8 v139, v135, s39, v139\n"
            "v_dot4_u32_u8 v140, v128, s13, 0\n"
            "v_dot4_u32_u8 v141, v129, s13, v136\n"
            "v_dot4_u32_u8 v142, v130, s13, 0\n"
            "v_dot4_u32_u8 v143, v131, s13, v138\n"
            "v_add_u32 v87, v136, v137\n"
            "v_add3_u32 v88, v87, v138, v139\n"
            "v_dot4_u32_u8 v140, v132, s14, v140\n"
            "v_dot4_u32_u8 v141, v133, s14, v141\n"
            "s_mov_b32 s68, 0x10001\n"
            "v_add_u32_dpp v89, v88, v88 row_shr:1 row_mask:0xf bank_mask:0xf bound_ctrl:1\n"
            "v_dot4_u32_u8 v142, v134, s14, v142\n"
            "v_dot4_u32_u8 v143, v135, s14, v143\n"
            "v_add_u32_dpp v89, v89, v89 row_shr:2 row_mask:0xf bank_mask:0xf bound_ctrl:1\n"
            "v_lshl_add_u32 v140, v141, 16, v140\n"
            "s_nop 0\n"
            "v_add_u32_dpp v89, v89, v89 row_shr:4 row_mask:0xf bank_mask:0xf bound_ctrl:1\n"
            "v_lshl_add_u32 v141, v143, 16, v142\n"
            "s_nop 0\n"
            "v_add_u32_dpp v89, v89, v89 row_shr:8 row_mask:0xf bank_mask:0xf bound_ctrl:1\n"
            "v_mad_u32_u24 v141, v87, s68, v141\n"
            "s_and_b32 s38, s3, 6\n"
            "v_add_u32_dpp v89, v89, v89 row_bcast:15 row_mask:0xa bank_mask:0xf\n"
            "s_cmp_lg_u32 s38, 4\n"
            "s_nop 0\n"
            "v_add_u32_dpp v89, v89, v89 row_bcast:31 row_mask:0xc bank_mask:0xf\n"
            "v_sub_u32 v89, v89, v88\n"
            "v_mad_u32_u24 v140, v89, s68, v140\n"
            "v_mad_u32_u24 v141, v89, s68, v141\n"
            "ds_write_b64 v94, v[140:141]\n"
            "ds_read_u16 v74, v74\n"
            "ds_read_u16 v75, v75\n"
            "ds_read_u16 v76, v76\n"
            "s_cbranch_scc1 Lnt_%=\n"
            "s_bitcmp1_b32 s3, 0\n"
            "s_cbranch_scc0 Lt0_%=\n"
            "v_add_u32 v89, v89, v87\n"
            "v_mov_b64 v[136:137], v[138:139]\n"
            "Lt0_%=:\n"
            "v_lshl_add_u32 v128, v136, 16, v89\n"
            "v_add_u32 v89, v89, v136\n"
            "v_lshl_add_u32 v130, v137, 16, v89\n"
            "v_mov_b32 v129, s90\n"
            "v_mov_b32 v131, s90\n"
            "buffer_store_dwordx4 v[128:131], v192, s[20:23], s36 offen sc1\n"
            "s_waitcnt vmcnt(4)\n"
            "s_branch Lt1_%=\n"
            "Lnt_%=:\n"
            "s_waitcnt vmcnt(3)\n"
            "Lt1_%=:\n"
            "s_waitcnt lgkmcnt(0)\n"
            "v_lshrrev_b32 v74, 3, v74\n"
            "v_lshrrev_b32 v75, 3, v75\n"
            "v_lshrrev_b32 v76, 3, v76\n"
            "v_lshl_add_u32 v74, v71, 1, v74\n"
            "v_lshl_add_u32 v75, v72, 1, v75\n"
            "v_lshl_add_u32 v76, v73, 1, v76\n"
            "s_mov_b64 exec, s[72:73]\n"
            "ds_write_b16 v74, v144 offset:5120\n"
            "s_mov_b64 exec, s[74:75]\n"
            "ds_write_b16 v75, v145 offset:5120\n"
            "s_mov_b64 exec, s[76:77]\n"
            "ds_write_b16 v76, v146 offset:5120\n"
            "s_mov_b64 exec, -1\n"
            "s_cmp_lt_u32 s3, 4\n"
            "s_waitcnt lgkmcnt(0)\n"
            "s_barrier\n"
            "s_cbranch_scc1 Lcol_%=\n"
            "ds_read_u16 v148, v147 offset:4608\n"
            "ds_read_u16 v149, v147 offset:5120\n"
            "ds_read_u16 v150, v147 offset:5632\n"
            "ds_read_u16 v151, v147 offset:6144\n"
            "ds_read_u16 v152, v147 offset:6656\n"
            "s_movk_i32 s72, 4096\n"
            "s_movk_i32 s73, 8192\n"
            "s_movk_i32 s74, 12288\n"
            "s_movk_i32 s75, 16384\n"
            "s_waitcnt lgkmcnt(4)\n"
            "ds_read_b128 v[56:59], v148\n"
            "s_waitcnt lgkmcnt(4)\n"
            "ds_read_b128 v[60:63], v149\n"
            "s_waitcnt lgkmcnt(4)\n"
            "ds_read_b128 v[64:67], v150\n"
            "s_waitcnt lgkmcnt(4)\n"
            "ds_read_b128 v[68:71], v151\n"
            "s_waitcnt lgkmcnt(4)\n"
            "ds_read_b128 v[72:75], v152\n"
            "s_waitcnt lgkmcnt(4)\n"
            "buffer_store_dwordx4 v[56:59], v95, s[4:7], 0 offen sc1\n"
            "s_waitcnt lgkmcnt(3)\n"
            "buffer_store_dwordx4 v[60:63], v95, s[4:7], s72 offen sc1\n"
            "s_waitcnt lgkmcnt(2)\n"
            "buffer_store_dwordx4 v[64:67], v95, s[4:7], s73 offen sc1\n"
            "s_waitcnt lgkmcnt(1)\n"
            "buffer_store_dwordx4 v[68:71], v95, s[4:7], s74 offen sc1\n"
            "s_waitcnt lgkmcnt(0)\n"
            "buffer_store_dwordx4 v[72:75], v95, s[4:7], s75 offen sc1\n"
            "s_setprio 2\n"
            "s_and_b32 s68, s3, 2\n"
            "s_lshl_b32 s68, s68, 13\n"
            "s_and_b32 s76, s3, 1\n"
            "s_lshl_b32 s76, s76, 12\n"
            "s_add_u32 s68, s68, s76\n"
            "v_lshl_add_u32 v81, v195, 10, s68\n"
            "v_add_u32 v81, v81, v214\n"
            "buffer_load_dwordx4 v[128:131], v81, s[44:47], 0 offen offset:0\n"
            "buffer_load_dwordx4 v[132:135], v81, s[44:47], 0 offen offset:256\n"
            "buffer_load_dwordx4 v[136:139], v81, s[44:47], s73 offen offset:0\n"
            "buffer_load_dwordx4 v[140:143], v81, s[44:47], s73 offen offset:256\n"
            "buffer_load_dwordx4 v[144:147], v81, s[44:47], 0 offen offset:512\n"
            "buffer_load_dwordx4 v[148:151], v81, s[44:47], 0 offen offset:768\n"
            "buffer_load_dwordx4 v[152:155], v81, s[44:47], s73 offen offset:512\n"
            "buffer_load_dwordx4 v[156:159], v81, s[44:47], s73 offen offset:768\n"
            "v_lshlrev_b32 v81, 4, v95\n"
            "v_and_b32 v81, 0x1800, v81\n"
            "v_lshrrev_b32 v82, 3, v95\n"
            "v_and_b32 v82, 0x100, v82\n"
            "v_lshlrev_b32 v83, 1, v95\n"
            "v_and_b32 v83, 0xe0, v83\n"
            "v_lshrrev_b32 v84, 7, v95\n"
            "v_and_b32 v84, 12, v84\n"
            "v_or3_b32 v81, v81, v82, v83\n"
            "v_add_u32 v84, 115456, v84\n"
            "v_add_u32 v81, v81, v84\n"
            "s_mov_b32 s78, 0xbfb8aa3b\n"
            "s_waitcnt vmcnt(14)\n"
            "v_lshlrev_b32 v160, 16, v48\n"
            "v_lshlrev_b32 v161, 16, v50\n"
            "v_mul_f32 v160, s78, v160\n"
            "v_mul_f32 v161, s78, v161\n"
            "v_cvt_pk_bf16_f32 v164, v160, v161\n"
            "v_lshlrev_b32 v162, 16, v164\n"
            "v_and_b32 v163, 0xffff0000, v164\n"
            "v_sub_f32 v160, v160, v162\n"
            "v_sub_f32 v161, v161, v163\n"
            "v_cvt_pk_bf16_f32 v165, v160, v161\n"
            "ds_write_b32 v81, v164 offset:0\n"
            "ds_write_b32 v81, v165 offset:512\n"
            "v_and_b32 v160, 0xffff0000, v48\n"
            "v_and_b32 v161, 0xffff0000, v50\n"
            "v_mul_f32 v160, s78, v160\n"
            "v_mul_f32 v161, s78, v161\n"
            "v_cvt_pk_bf16_f32 v166, v160, v161\n"
            "v_lshlrev_b32 v162, 16, v166\n"
            "v_and_b32 v163, 0xffff0000, v166\n"
            "v_sub_f32 v160, v160, v162\n"
            "v_sub_f32 v161, v161, v163\n"
            "v_cvt_pk_bf16_f32 v167, v160, v161\n"
            "ds_write_b32 v81, v166 offset:1024\n"
            "ds_write_b32 v81, v167 offset:1536\n"
            "v_lshlrev_b32 v160, 16, v49\n"
            "v_lshlrev_b32 v161, 16, v51\n"
            "v_mul_f32 v160, s78, v160\n"
            "v_mul_f32 v161, s78, v161\n"
            "v_cvt_pk_bf16_f32 v168, v160, v161\n"
            "v_lshlrev_b32 v162, 16, v168\n"
            "v_and_b32 v163, 0xffff0000, v168\n"
            "v_sub_f32 v160, v160, v162\n"
            "v_sub_f32 v161, v161, v163\n"
            "v_cvt_pk_bf16_f32 v169, v160, v161\n"
            "ds_write_b32 v81, v168 offset:16\n"
            "ds_write_b32 v81, v169 offset:528\n"
            "v_and_b32 v160, 0xffff0000, v49\n"
            "v_and_b32 v161, 0xffff0000, v51\n"
            "v_mul_f32 v160, s78, v160\n"
            "v_mul_f32 v161, s78, v161\n"
            "v_cvt_pk_bf16_f32 v170, v160, v161\n"
            "v_lshlrev_b32 v162, 16, v170\n"
            "v_and_b32 v163, 0xffff0000, v170\n"
            "v_sub_f32 v160, v160, v162\n"
            "v_sub_f32 v161, v161, v163\n"
            "v_cvt_pk_bf16_f32 v171, v160, v161\n"
            "ds_write_b32 v81, v170 offset:1040\n"
            "ds_write_b32 v81, v171 offset:1552\n"
            "s_cmp_lg_u32 s3, 4\n"
            "s_cbranch_scc1 Lncg_%=\n"
            "v_lshlrev_b32 v81, 2, v243\n"
            "s_waitcnt vmcnt(14)\n"
            "ds_write_b32 v81, v52 offset:62464\n"
            "v_lshrrev_b32 v81, 1, v243\n"
            "v_and_b32 v81, 6, v81\n"
            "v_lshl_add_u32 v81, v249, 5, v81\n"
            "ds_read_u16 v160, v81 offset:62464\n"
            "ds_read_u16 v161, v81 offset:62472\n"
            "ds_read_u16 v162, v81 offset:62480\n"
            "ds_read_u16 v163, v81 offset:62488\n"
            "s_mov_b32 s78, 0xbf317218\n"
            "s_waitcnt lgkmcnt(0)\n"
            "v_lshlrev_b32 v160, 16, v160\n"
            "v_lshlrev_b32 v161, 16, v161\n"
            "v_lshlrev_b32 v162, 16, v162\n"
            "v_lshlrev_b32 v163, 16, v163\n"
            "v_mul_f32 v160, s78, v160\n"
            "v_mul_f32 v161, s78, v161\n"
            "v_mul_f32 v162, s78, v162\n"
            "v_mul_f32 v163, s78, v163\n"
            "v_cvt_pk_bf16_f32 v168, v160, v161\n"
            "v_cvt_pk_bf16_f32 v169, v162, v163\n"
            "v_lshlrev_b32 v164, 16, v168\n"
            "v_and_b32 v165, 0xffff0000, v168\n"
            "v_lshlrev_b32 v166, 16, v169\n"
            "v_and_b32 v167, 0xffff0000, v169\n"
            "v_sub_f32 v160, v160, v164\n"
            "v_sub_f32 v161, v161, v165\n"
            "v_sub_f32 v162, v162, v166\n"
            "v_sub_f32 v163, v163, v167\n"
            "v_cvt_pk_bf16_f32 v170, v160, v161\n"
            "v_cvt_pk_bf16_f32 v171, v162, v163\n"
            "ds_write_b128 v192, v[168:171] offset:61440\n"
            "Lncg_%=:\n"
            "s_mov_b64 exec, 1\n"
            "ds_add_u32 v219, v209 offset:64\n"
            "s_mov_b64 exec, -1\n"
            "s_cmp_lg_u32 s3, 7\n"
            "s_cbranch_scc1 Lt2_%=\n"
            "s_add_u32 s68, s69, 0\n"
            "buffer_load_dword v230, v193, s[28:31], s68 offen\n"
            "Lt2_%=:\n"
            "s_mul_i32 s68, s3, 27\n"
            "s_sub_u32 s68, s68, 108\n"
            "v_lshrrev_b32 v81, 1, v243\n"
            "v_add_u32 v81, s68, v81\n"
            "v_min_u32 v81, 79, v81\n"
            "v_lshlrev_b32 v180, 2, v81\n"
            "v_cmp_lt_u32 vcc, 39, v81\n"
            "v_and_b32 v82, 1, v243\n"
            "v_mul_u32_u24 v181, 80, v82\n"
            "v_add_u32 v82, 160, v181\n"
            "v_cndmask_b32 v181, v181, v82, vcc\n"
            "v_cndmask_b32 v82, 1, 0, vcc\n"
            "v_xor_b32 v82, s8, v82\n"
            "v_lshlrev_b32 v182, 8, v82\n"
            "v_mul_u32_u24 v183, 704, v81\n"
            "v_add_u32 v183, 5120, v183\n"
            "v_cmp_gt_u32 vcc, s11, v81\n"
            "v_add_lshl_u32 v184, v81, s10, 10\n"
            "v_bfrev_b32 v82, 1\n"
            "v_cndmask_b32 v184, v82, v184, vcc\n"
            "s_branch Lhpa_%=\n"
            "Lhp_%=:\n"
            "s_sleep 1\n"
            "Lhpa_%=:\n"
            "ds_read_b32 v220, v219 offset:64\n"
            "s_waitcnt lgkmcnt(0)\n"
            "v_readfirstlane_b32 s68, v220\n"
            "s_cmp_lt_u32 s68, 4\n"
            "s_cbranch_scc1 Lhp_%=\n"
            "v_add_u32 v81, 115456, v192\n"
            "ds_read_b128 v[96:99], v81 offset:0\n"
            "ds_read_b128 v[100:103], v81 offset:1024\n"
            "ds_read_b128 v[104:107], v81 offset:2048\n"
            "ds_read_b128 v[108:111], v81 offset:3072\n"
            "ds_read_b128 v[112:115], v81 offset:4096\n"
            "ds_read_b128 v[116:119], v81 offset:5120\n"
            "ds_read_b128 v[120:123], v81 offset:6144\n"
            "ds_read_b128 v[124:127], v81 offset:7168\n"
            "ds_read_b128 v[200:203], v192 offset:61440\n"
            "v_lshrrev_b32 v82, 2, v196\n"
            "s_lshl_b32 s68, s3, 10\n"
            "s_add_u32 s68, s68, 119808\n"
            "v_lshl_add_u32 v82, v82, 13, s68\n"
            "v_lshl_add_u32 v82, v195, 8, v82\n"
            "v_lshl_add_u32 v82, v249, 6, v82\n"
            "s_mov_b32 s78, 0x05040100\n"
            "s_mov_b32 s79, 0x07060302\n"
            "s_waitcnt vmcnt(1)\n"
            "s_cmp_eq_u32 s3, 7\n"
            "s_cbranch_scc1 Lt3_%=\n"
            "s_waitcnt vmcnt(0)\n"
            "Lt3_%=:\n"
            "v_perm_b32 v160, v132, v128, s78\n"
            "v_perm_b32 v161, v140, v136, s78\n"
            "v_perm_b32 v162, v148, v144, s78\n"
            "v_perm_b32 v163, v156, v152, s78\n"
            "ds_write_b128 v82, v[160:163] offset:0\n"
            "v_perm_b32 v164, v132, v128, s79\n"
            "v_perm_b32 v165, v140, v136, s79\n"
            "v_perm_b32 v166, v148, v144, s79\n"
            "v_perm_b32 v167, v156, v152, s79\n"
            "ds_write_b128 v82, v[164:167] offset:4096\n"
            "v_perm_b32 v168, v133, v129, s78\n"
            "v_perm_b32 v169, v141, v137, s78\n"
            "v_perm_b32 v170, v149, v145, s78\n"
            "v_perm_b32 v171, v157, v153, s78\n"
            "ds_write_b128 v82, v[168:171] offset:16\n"
            "v_perm_b32 v172, v133, v129, s79\n"
            "v_perm_b32 v173, v141, v137, s79\n"
            "v_perm_b32 v174, v149, v145, s79\n"
            "v_perm_b32 v175, v157, v153, s79\n"
            "ds_write_b128 v82, v[172:175] offset:4112\n"
            "v_perm_b32 v160, v134, v130, s78\n"
            "v_perm_b32 v161, v142, v138, s78\n"
            "v_perm_b32 v162, v150, v146, s78\n"
            "v_perm_b32 v163, v158, v154, s78\n"
            "ds_write_b128 v82, v[160:163] offset:32\n"
            "v_perm_b32 v164, v134, v130, s79\n"
            "v_perm_b32 v165, v142, v138, s79\n"
            "v_perm_b32 v166, v150, v146, s79\n"
            "v_perm_b32 v167, v158, v154, s79\n"
            "ds_write_b128 v82, v[164:167] offset:4128\n"
            "v_perm_b32 v168, v135, v131, s78\n"
            "v_perm_b32 v169, v143, v139, s78\n"
            "v_perm_b32 v170, v151, v147, s78\n"
            "v_perm_b32 v171, v159, v155, s78\n"
            "ds_write_b128 v82, v[168:171] offset:48\n"
            "v_perm_b32 v172, v135, v131, s79\n"
            "v_perm_b32 v173, v143, v139, s79\n"
            "v_perm_b32 v174, v151, v147, s79\n"
            "v_perm_b32 v175, v159, v155, s79\n"
            "ds_write_b128 v82, v[172:175] offset:4144\n"
            "s_cmp_lg_u32 s3, 7\n"
            "s_cbranch_scc1 Lsort_%=\n"
            "s_mov_b64 exec, 1\n"
            "ds_add_u32 v219, v209 offset:48\n"
            "s_mov_b64 exec, -1\n"
            "Lri_%=:\n"
            "ds_read_b32 v220, v219 offset:60\n"
            "s_waitcnt lgkmcnt(0)\n"
            "v_readfirstlane_b32 s68, v220\n"
            "s_cmp_lt_u32 s68, 4\n"
            "s_cbranch_scc1 Lri_%=\n"
            "s_add_u32 s68, s54, 24576\n"
            "s_nop 1\n"
            "buffer_load_dword v230, v193, s[24:27], s68 offen\n"
            "s_add_u32 s68, s54, 32768\n"
            "buffer_load_dword v230, v193, s[24:27], s68 offen\n"
            "s_and_b32 s68, s9, 31\n"
            "s_add_u32 s68, s68, 0\n"
            "s_lshl_b32 s68, s68, 13\n"
            "s_add_u32 s68, s68, s55\n"
            "buffer_load_dword v230, v193, s[28:31], s68 offen\n"
            "s_and_b32 s68, s9, 31\n"
            "s_add_u32 s68, s68, 32\n"
            "s_lshl_b32 s68, s68, 13\n"
            "s_add_u32 s68, s68, s55\n"
            "buffer_load_dword v230, v193, s[28:31], s68 offen\n"
            "s_add_u32 s68, s69, 1048576\n"
            "buffer_load_dword v230, v193, s[28:31], s68 offen\n"
            "s_add_u32 s68, s69, 2097152\n"
            "buffer_load_dword v230, v193, s[28:31], s68 offen\n"
            "s_branch Lw7a_%=\n"
            "Lw7_%=:\n"
            "s_sleep 1\n"
            "Lw7a_%=:\n"
            "ds_read_b32 v220, v219 offset:44\n"
            "s_waitcnt lgkmcnt(0)\n"
            "v_readfirstlane_b32 s68, v220\n"
            "s_cmp_lt_u32 s68, 4\n"
            "s_cbranch_scc1 Lw7_%=\n"
            "s_setprio 3\n"
            "s_branch Lmain_%=\n"
            "Lsort_%=:\n"
            "s_branch Lc3a_%=\n"
            "Lc3_%=:\n"
            "s_sleep 1\n"
            "Lc3a_%=:\n"
            "ds_read_b32 v220, v219 offset:44\n"
            "s_waitcnt lgkmcnt(0)\n"
            "v_readfirstlane_b32 s68, v220\n"
            "s_cmp_lt_u32 s68, 3\n"
            "s_cbranch_scc1 Lc3_%=\n"
            "s_setprio 3\n"
            "Lc4_%=:\n"
            "ds_read_b32 v220, v219 offset:44\n"
            "s_waitcnt lgkmcnt(0)\n"
            "v_readfirstlane_b32 s68, v220\n"
            "s_cmp_lt_u32 s68, 4\n"
            "s_cbranch_scc1 Lc4_%=\n"
            "ds_read_b32 v160, v180 offset:96\n"
            "ds_read_b128 v[128:131], v181 offset:96\n"
            "ds_read_b128 v[132:135], v181 offset:112\n"
            "ds_read_b128 v[136:139], v181 offset:128\n"
            "ds_read_b128 v[140:143], v181 offset:144\n"
            "ds_read_b128 v[144:147], v181 offset:160\n"
            "s_waitcnt lgkmcnt(4)\n"
            "v_sub_u32 v128, v160, v128\n"
            "v_sub_u32 v129, v160, v129\n"
            "v_sub_u32 v130, v160, v130\n"
            "v_sub_u32 v131, v160, v131\n"
            "v_lshrrev_b32 v161, 31, v128\n"
            "v_lshrrev_b32 v162, 31, v129\n"
            "v_alignbit_b32 v161, v161, v130, 31\n"
            "v_alignbit_b32 v162, v162, v131, 31\n"
            "v_lshrrev_b32 v163, 14, v160\n"
            "v_bfe_u32 v166, v160, 7, 7\n"
            "v_sub_u32 v166, v163, v166\n"
            "s_waitcnt lgkmcnt(3)\n"
            "v_sub_u32 v132, v160, v132\n"
            "v_sub_u32 v133, v160, v133\n"
            "v_sub_u32 v134, v160, v134\n"
            "v_sub_u32 v135, v160, v135\n"
            "v_alignbit_b32 v161, v161, v132, 31\n"
            "v_alignbit_b32 v162, v162, v133, 31\n"
            "v_alignbit_b32 v161, v161, v134, 31\n"
            "v_alignbit_b32 v162, v162, v135, 31\n"
            "v_cmp_lt_u32 vcc, 44, v163\n"
            "v_max_u32 v164, 8, v163\n"
            "s_waitcnt lgkmcnt(2)\n"
            "v_sub_u32 v136, v160, v136\n"
            "v_sub_u32 v137, v160, v137\n"
            "v_sub_u32 v138, v160, v138\n"
            "v_sub_u32 v139, v160, v139\n"
            "v_alignbit_b32 v161, v161, v136, 31\n"
            "v_alignbit_b32 v162, v162, v137, 31\n"
            "v_alignbit_b32 v161, v161, v138, 31\n"
            "v_alignbit_b32 v162, v162, v139, 31\n"
            "v_mov_b32 v165, 255\n"
            "v_lshl_or_b32 v183, v163, 16, v183\n"
            "v_lshl_or_b32 v183, v166, 24, v183\n"
            "s_waitcnt lgkmcnt(1)\n"
            "v_sub_u32 v140, v160, v140\n"
            "v_sub_u32 v141, v160, v141\n"
            "v_sub_u32 v142, v160, v142\n"
            "v_sub_u32 v143, v160, v143\n"
            "v_alignbit_b32 v161, v161, v140, 31\n"
            "v_alignbit_b32 v162, v162, v141, 31\n"
            "v_alignbit_b32 v161, v161, v142, 31\n"
            "v_alignbit_b32 v162, v162, v143, 31\n"
            "v_cndmask_b32 v164, v164, v165, vcc\n"
            "s_waitcnt lgkmcnt(0)\n"
            "v_sub_u32 v144, v160, v144\n"
            "v_sub_u32 v145, v160, v145\n"
            "v_sub_u32 v146, v160, v146\n"
            "v_sub_u32 v147, v160, v147\n"
            "v_alignbit_b32 v161, v161, v144, 31\n"
            "v_alignbit_b32 v162, v162, v145, 31\n"
            "v_alignbit_b32 v161, v161, v146, 31\n"
            "v_alignbit_b32 v162, v162, v147, 31\n"
            "v_bcnt_u32_b32 v161, v161, 0\n"
            "v_bcnt_u32_b32 v161, v162, v161\n"
            "s_nop 1\n"
            "v_add_u32_dpp v161, v161, v161 quad_perm:[1,0,3,2] row_mask:0xf bank_mask:0xf\n"
            "v_lshl_add_u32 v182, v161, 2, v182\n"
            "ds_write2st64_b32 v182, v183, v164 offset0:14 offset1:12\n"
            "ds_write_b32 v182, v184 offset:4096\n"
            "s_mov_b64 exec, 1\n"
            "ds_add_u32 v219, v209 offset:48\n"
            "s_mov_b64 exec, -1\n"
            "s_lshl3_add_u32 s68, s2, s3\n"
            "s_cmp_lg_u32 s68, 4\n"
            "s_cbranch_scc1 Lmain_%=\n"
            "v_mov_b32 v160, s90\n"
            "s_mov_b64 exec, 1\n"
            "buffer_store_dword v160, off, s[20:23], 0 sc1\n"
            "s_mov_b64 exec, -1\n"
            "s_branch Lmain_%=\n"
            "Lcol_%=:\n"
            "s_lshl_b32 s38, s8, 7\n"
            "s_add_u32 s38, s38, s9\n"
            "s_bfe_u32 s68, s38, 0x10001\n"
            "s_lshl_b32 s68, s68, 10\n"
            "s_lshr_b32 s72, s38, 2\n"
            "s_lshl4_add_u32 s68, s72, s68\n"
            "s_and_b32 s72, s38, 1\n"
            "s_lshl3_add_u32 s68, s72, s68\n"
            "s_add_u32 s68, s68, 36864\n"
            "v_lshl_add_u32 v180, v95, 7, s68\n"
            "s_sleep 23\n"
            "buffer_load_dwordx2 v[142:143], v180, s[20:23], 0 offen sc1\n"
            "s_mul_i32 s17, s3, 1792\n"
            "s_add_u32 s17, s17, 76800\n"
            "v_lshl_add_u32 v181, v243, 2, s17\n"
            "s_mul_i32 s15, s3, 1310720\n"
            "s_add_u32 s15, s15, 561152\n"
            "s_add_u32 s16, s15, 4096\n"
            "s_mul_i32 s18, s3, 1280\n"
            "s_add_u32 s18, s18, 84992\n"
            "s_lshl_b32 s19, s10, 3\n"
            "s_lshl_b32 s97, s50, 5\n"
            "s_mul_i32 s98, s97, s3\n"
            "s_lshl_b32 s98, s98, 6\n"
            "v_mul_u32_u24 v182, 704, v243\n"
            "v_add_u32 v182, 5120, v182\n"
            "v_add_u32 v184, 704, v182\n"
            "v_mul_u32_u24 v183, 704, v196\n"
            "v_add_u32 v183, 50176, v183\n"
            "v_add_u32 v185, 704, v183\n"
            "v_add_u32 v187, s18, v192\n"
            "v_add_u32 v188, s18, v214\n"
            "v_lshlrev_b32 v198, 5, v243\n"
            "v_lshlrev_b32 v199, 5, v196\n"
            "s_mov_b32 s68, 16797696\n"
            "v_mul_lo_u32 v189, v243, s68\n"
            "v_mov_b32 v190, 0x4000\n"
            "v_mov_b32 v191, 2.0\n"
            "v_mov_b32 v186, s98\n"
            "v_mov_b32 v197, v80\n"
            "s_branch Lcpa_%=\n"
            "Lcp_%=:\n"
            "s_sleep 1\n"
            "Lcpa_%=:\n"
            "ds_read_b32 v220, v219 offset:64\n"
            "s_waitcnt lgkmcnt(0)\n"
            "v_readfirstlane_b32 s68, v220\n"
            "s_cmp_lt_u32 s68, 4\n"
            "s_cbranch_scc1 Lcp_%=\n"
            "v_add_u32 v81, 115456, v192\n"
            "ds_read_b128 v[96:99], v81 offset:0\n"
            "ds_read_b128 v[100:103], v81 offset:1024\n"
            "ds_read_b128 v[104:107], v81 offset:2048\n"
            "ds_read_b128 v[108:111], v81 offset:3072\n"
            "ds_read_b128 v[112:115], v81 offset:4096\n"
            "ds_read_b128 v[116:119], v81 offset:5120\n"
            "ds_read_b128 v[120:123], v81 offset:6144\n"
            "ds_read_b128 v[124:127], v81 offset:7168\n"
            "ds_read_b128 v[200:203], v192 offset:61440\n"
            "s_mov_b32 s99, 0x400000\n"
            "Lpoll_%=:\n"
            "s_waitcnt vmcnt(0)\n"
            "v_cmp_ne_u32 vcc, s90, v143\n"
            "s_cbranch_vccnz Lpoll2_%=\n"
            "Lpoll3_%=:\n"
            "v_lshrrev_b32 v144, 18, v142\n"
            "v_and_b32 v146, 0xffff, v142\n"
            "v_cmp_ne_u32 s[86:87], 0, v144\n"
            "v_add_u32_dpp v145, v144, v144 row_shr:1 row_mask:0xf bank_mask:0xf bound_ctrl:1\n"
            "v_add_u32 v146, v146, v189\n"
            "v_cmp_lt_u32 s[88:89], 64, v144\n"
            "v_add_u32_dpp v145, v145, v145 row_shr:2 row_mask:0xf bank_mask:0xf bound_ctrl:1\n"
            "v_lshl_add_u32 v146, v144, 2, v146\n"
            "v_sub_u32 v147, s17, v144\n"
            "v_add_u32_dpp v145, v145, v145 row_shr:4 row_mask:0xf bank_mask:0xf bound_ctrl:1\n"
            "s_add_u32 s38, s17, -4\n"
            "s_nop 0\n"
            "v_add_u32_dpp v145, v145, v145 row_shr:8 row_mask:0xf bank_mask:0xf bound_ctrl:1\n"
            "s_nop 1\n"
            "v_add_u32_dpp v145, v145, v145 row_bcast:15 row_mask:0xa bank_mask:0xf\n"
            "s_nop 1\n"
            "v_add_u32_dpp v145, v145, v145 row_bcast:31 row_mask:0xc bank_mask:0xf\n"
            "v_cmp_ge_u32 vcc, 1792, v145\n"
            "v_readlane_b32 s37, v145, 63\n"
            "v_add_u32 v147, v147, v145\n"
            "v_add_u32 v148, s38, v145\n"
            "v_lshlrev_b32 v145, 2, v145\n"
            "v_and_b32 v148, -32, v148\n"
            "v_sub_u32 v146, v146, v145\n"
            "v_add_u32 v149, -32, v148\n"
            "v_max_u32 v149, v149, v147\n"
            "v_max_u32 v148, v148, v147\n"
            "s_and_b64 exec, vcc, s[86:87]\n"
            "ds_write_b32 v147, v146\n"
            "ds_write_b32 v148, v146\n"
            "ds_write_b32 v149, v146\n"
            "s_mov_b64 exec, -1\n"
            "ds_read2st64_b32 v[74:75], v181 offset0:0 offset1:1\n"
            "ds_read2st64_b32 v[76:77], v181 offset0:2 offset1:3\n"
            "ds_read2st64_b32 v[78:79], v181 offset0:4 offset1:5\n"
            "s_cmp_gt_u32 s37, 1792\n"
            "s_cbranch_scc1 Lover_%=\n"
            "Lover2_%=:\n"
            "s_lshr_b32 s37, s37, 2\n"
            "v_cmp_gt_i32 s[72:73], s37, v243\n"
            "s_sub_i32 s38, s37, 64\n"
            "v_cmp_gt_i32 s[74:75], s38, v243\n"
            "s_sub_i32 s38, s37, 128\n"
            "v_cmp_gt_i32 s[76:77], s38, v243\n"
            "s_sub_i32 s38, s37, 192\n"
            "v_cmp_gt_i32 s[78:79], s38, v243\n"
            "s_sub_i32 s38, s37, 256\n"
            "v_cmp_gt_i32 s[80:81], s38, v243\n"
            "s_sub_i32 s38, s37, 320\n"
            "v_cmp_gt_i32 s[82:83], s38, v243\n"
            "v_cndmask_b32 v81, v191, v192, s[72:73]\n"
            "v_cndmask_b32 v82, v191, v192, s[74:75]\n"
            "v_cndmask_b32 v83, v191, v192, s[76:77]\n"
            "v_cndmask_b32 v84, v191, v192, s[78:79]\n"
            "v_cndmask_b32 v85, v191, v192, s[80:81]\n"
            "v_cndmask_b32 v86, v191, v192, s[82:83]\n"
            "s_mov_b64 s[84:85], 0\n"
            "s_cmp_gt_u32 s37, 384\n"
            "s_cmov_b64 s[88:89], -1\n"
            "s_cmp_lg_u64 s[88:89], 0\n"
            "s_waitcnt lgkmcnt(0)\n"
            "v_max_u32_dpp v74, v74, v74 row_shr:1 row_mask:0xf bank_mask:0xf bound_ctrl:1\n"
            "v_max_u32_dpp v75, v75, v75 row_shr:1 row_mask:0xf bank_mask:0xf bound_ctrl:1\n"
            "v_max_u32_dpp v76, v76, v76 row_shr:1 row_mask:0xf bank_mask:0xf bound_ctrl:1\n"
            "v_max_u32_dpp v77, v77, v77 row_shr:1 row_mask:0xf bank_mask:0xf bound_ctrl:1\n"
            "v_max_u32_dpp v78, v78, v78 row_shr:1 row_mask:0xf bank_mask:0xf bound_ctrl:1\n"
            "v_max_u32_dpp v79, v79, v79 row_shr:1 row_mask:0xf bank_mask:0xf bound_ctrl:1\n"
            "v_max_u32_dpp v74, v74, v74 row_shr:2 row_mask:0xf bank_mask:0xf bound_ctrl:1\n"
            "v_max_u32_dpp v75, v75, v75 row_shr:2 row_mask:0xf bank_mask:0xf bound_ctrl:1\n"
            "v_max_u32_dpp v76, v76, v76 row_shr:2 row_mask:0xf bank_mask:0xf bound_ctrl:1\n"
            "v_max_u32_dpp v77, v77, v77 row_shr:2 row_mask:0xf bank_mask:0xf bound_ctrl:1\n"
            "v_max_u32_dpp v78, v78, v78 row_shr:2 row_mask:0xf bank_mask:0xf bound_ctrl:1\n"
            "v_max_u32_dpp v79, v79, v79 row_shr:2 row_mask:0xf bank_mask:0xf bound_ctrl:1\n"
            "v_max_u32_dpp v74, v74, v74 row_shr:4 row_mask:0xf bank_mask:0xf bound_ctrl:1\n"
            "v_max_u32_dpp v75, v75, v75 row_shr:4 row_mask:0xf bank_mask:0xf bound_ctrl:1\n"
            "v_max_u32_dpp v76, v76, v76 row_shr:4 row_mask:0xf bank_mask:0xf bound_ctrl:1\n"
            "v_max_u32_dpp v77, v77, v77 row_shr:4 row_mask:0xf bank_mask:0xf bound_ctrl:1\n"
            "v_max_u32_dpp v78, v78, v78 row_shr:4 row_mask:0xf bank_mask:0xf bound_ctrl:1\n"
            "v_max_u32_dpp v79, v79, v79 row_shr:4 row_mask:0xf bank_mask:0xf bound_ctrl:1\n"
            "s_cbranch_scc1 Llng_%=\n"
            "Llng2_%=:\n"
            "v_mad_u32_u24 v81, v74, 1, v81\n"
            "buffer_load_dwordx4 v[34:37], v81, s[20:23], s15 offen offset:0 sc1\n"
            "v_mad_u32_u24 v82, v75, 1, v82\n"
            "buffer_load_dwordx4 v[40:43], v82, s[20:23], s15 offen offset:1024 sc1\n"
            "v_mad_u32_u24 v83, v76, 1, v83\n"
            "buffer_load_dwordx4 v[46:49], v83, s[20:23], s15 offen offset:2048 sc1\n"
            "v_mad_u32_u24 v84, v77, 1, v84\n"
            "buffer_load_dwordx4 v[52:55], v84, s[20:23], s15 offen offset:3072 sc1\n"
            "v_mad_u32_u24 v85, v78, 1, v85\n"
            "buffer_load_dwordx4 v[58:61], v85, s[20:23], s16 offen offset:0 sc1\n"
            "v_mad_u32_u24 v86, v79, 1, v86\n"
            "buffer_load_dwordx4 v[64:67], v86, s[20:23], s16 offen offset:1024 sc1\n"
            "s_mov_b64 exec, 1\n"
            "ds_add_u32 v219, v209 offset:60\n"
            "s_mov_b64 exec, -1\n"
            "v_lshrrev_b32 v88, 24, v74\n"
            "v_mad_u32_u24 v88, v88, s97, v186\n"
            "v_lshrrev_b32 v89, 24, v75\n"
            "v_mad_u32_u24 v89, v89, s97, v186\n"
            "v_lshrrev_b32 v90, 24, v76\n"
            "v_mad_u32_u24 v90, v90, s97, v186\n"
            "v_lshrrev_b32 v91, 24, v77\n"
            "v_mad_u32_u24 v91, v91, s97, v186\n"
            "v_lshrrev_b32 v92, 24, v78\n"
            "v_mad_u32_u24 v92, v92, s97, v186\n"
            "v_lshrrev_b32 v93, 24, v79\n"
            "v_mad_u32_u24 v93, v93, s97, v186\n"
            "v_lshrrev_b32 v94, 24, v80\n"
            "v_mad_u32_u24 v94, v94, s97, v186\n"
            "s_movk_i32 s38, 0x3fff\n"
            "s_mov_b32 s36, 0x1fffff\n"
            "s_waitcnt vmcnt(5)\n"
            "Lchk0_%=:\n"
            "v_xor_b32 v35, s93, v35\n"
            "v_xor_b32 v37, s94, v37\n"
            "v_cmp_lt_u32 vcc, s38, v35\n"
            "v_cmp_lt_u32 s[86:87], s36, v37\n"
            "v_subrev_u32 v135, s19, v37\n"
            "v_and_b32 v135, 0x3ff, v135\n"
            "s_or_b64 vcc, vcc, s[86:87]\n"
            "s_and_b64 vcc, vcc, s[72:73]\n"
            "s_cbranch_scc1 Lre0_%=\n"
            "Lok0_%=:\n"
            "v_lshlrev_b32 v144, 2, v135\n"
            "v_lshrrev_b32 v145, 1, v135\n"
            "v_and_b32 v145, 0x1fc, v145\n"
            "v_and_b32 v146, 4, v135\n"
            "v_lshl_or_b32 v146, v146, 5, v190\n"
            "s_mov_b64 exec, s[72:73]\n"
            "ds_add_rtn_u32 v128, v144, v197 offset:512\n"
            "ds_add_u32 v145, v146 offset:96\n"
            "s_mov_b64 exec, -1\n"
            "s_waitcnt vmcnt(4)\n"
            "Lchk1_%=:\n"
            "v_xor_b32 v41, s93, v41\n"
            "v_xor_b32 v43, s94, v43\n"
            "v_cmp_lt_u32 vcc, s38, v41\n"
            "v_cmp_lt_u32 s[86:87], s36, v43\n"
            "v_subrev_u32 v136, s19, v43\n"
            "v_and_b32 v136, 0x3ff, v136\n"
            "s_or_b64 vcc, vcc, s[86:87]\n"
            "s_and_b64 vcc, vcc, s[74:75]\n"
            "s_cbranch_scc1 Lre1_%=\n"
            "Lok1_%=:\n"
            "v_lshlrev_b32 v144, 2, v136\n"
            "v_lshrrev_b32 v145, 1, v136\n"
            "v_and_b32 v145, 0x1fc, v145\n"
            "v_and_b32 v146, 4, v136\n"
            "v_lshl_or_b32 v146, v146, 5, v190\n"
            "s_mov_b64 exec, s[74:75]\n"
            "ds_add_rtn_u32 v129, v144, v197 offset:512\n"
            "ds_add_u32 v145, v146 offset:96\n"
            "s_mov_b64 exec, -1\n"
            "s_waitcnt vmcnt(3)\n"
            "Lchk2_%=:\n"
            "v_xor_b32 v47, s93, v47\n"
            "v_xor_b32 v49, s94, v49\n"
            "v_cmp_lt_u32 vcc, s38, v47\n"
            "v_cmp_lt_u32 s[86:87], s36, v49\n"
            "v_subrev_u32 v137, s19, v49\n"
            "v_and_b32 v137, 0x3ff, v137\n"
            "s_or_b64 vcc, vcc, s[86:87]\n"
            "s_and_b64 vcc, vcc, s[76:77]\n"
            "s_cbranch_scc1 Lre2_%=\n"
            "Lok2_%=:\n"
            "v_lshlrev_b32 v144, 2, v137\n"
            "v_lshrrev_b32 v145, 1, v137\n"
            "v_and_b32 v145, 0x1fc, v145\n"
            "v_and_b32 v146, 4, v137\n"
            "v_lshl_or_b32 v146, v146, 5, v190\n"
            "s_mov_b64 exec, s[76:77]\n"
            "ds_add_rtn_u32 v130, v144, v197 offset:512\n"
            "ds_add_u32 v145, v146 offset:96\n"
            "s_mov_b64 exec, -1\n"
            "s_waitcnt vmcnt(2)\n"
            "Lchk3_%=:\n"
            "v_xor_b32 v53, s93, v53\n"
            "v_xor_b32 v55, s94, v55\n"
            "v_cmp_lt_u32 vcc, s38, v53\n"
            "v_cmp_lt_u32 s[86:87], s36, v55\n"
            "v_subrev_u32 v138, s19, v55\n"
            "v_and_b32 v138, 0x3ff, v138\n"
            "s_or_b64 vcc, vcc, s[86:87]\n"
            "s_and_b64 vcc, vcc, s[78:79]\n"
            "s_cbranch_scc1 Lre3_%=\n"
            "Lok3_%=:\n"
            "v_lshlrev_b32 v144, 2, v138\n"
            "v_lshrrev_b32 v145, 1, v138\n"
            "v_and_b32 v145, 0x1fc, v145\n"
            "v_and_b32 v146, 4, v138\n"
            "v_lshl_or_b32 v146, v146, 5, v190\n"
            "s_mov_b64 exec, s[78:79]\n"
            "ds_add_rtn_u32 v131, v144, v197 offset:512\n"
            "ds_add_u32 v145, v146 offset:96\n"
            "s_mov_b64 exec, -1\n"
            "s_waitcnt vmcnt(1)\n"
            "Lchk4_%=:\n"
            "v_xor_b32 v59, s93, v59\n"
            "v_xor_b32 v61, s94, v61\n"
            "v_cmp_lt_u32 vcc, s38, v59\n"
            "v_cmp_lt_u32 s[86:87], s36, v61\n"
            "v_subrev_u32 v139, s19, v61\n"
            "v_and_b32 v139, 0x3ff, v139\n"
            "s_or_b64 vcc, vcc, s[86:87]\n"
            "s_and_b64 vcc, vcc, s[80:81]\n"
            "s_cbranch_scc1 Lre4_%=\n"
            "Lok4_%=:\n"
            "v_lshlrev_b32 v144, 2, v139\n"
            "v_lshrrev_b32 v145, 1, v139\n"
            "v_and_b32 v145, 0x1fc, v145\n"
            "v_and_b32 v146, 4, v139\n"
            "v_lshl_or_b32 v146, v146, 5, v190\n"
            "s_mov_b64 exec, s[80:81]\n"
            "ds_add_rtn_u32 v132, v144, v197 offset:512\n"
            "ds_add_u32 v145, v146 offset:96\n"
            "s_mov_b64 exec, -1\n"
            "s_waitcnt vmcnt(0)\n"
            "Lchk5_%=:\n"
            "v_xor_b32 v65, s93, v65\n"
            "v_xor_b32 v67, s94, v67\n"
            "v_cmp_lt_u32 vcc, s38, v65\n"
            "v_cmp_lt_u32 s[86:87], s36, v67\n"
            "v_subrev_u32 v140, s19, v67\n"
            "v_and_b32 v140, 0x3ff, v140\n"
            "s_or_b64 vcc, vcc, s[86:87]\n"
            "s_and_b64 vcc, vcc, s[82:83]\n"
            "s_cbranch_scc1 Lre5_%=\n"
            "Lok5_%=:\n"
            "v_lshlrev_b32 v144, 2, v140\n"
            "v_lshrrev_b32 v145, 1, v140\n"
            "v_and_b32 v145, 0x1fc, v145\n"
            "v_and_b32 v146, 4, v140\n"
            "v_lshl_or_b32 v146, v146, 5, v190\n"
            "s_mov_b64 exec, s[82:83]\n"
            "ds_add_rtn_u32 v133, v144, v197 offset:512\n"
            "ds_add_u32 v145, v146 offset:96\n"
            "s_mov_b64 exec, -1\n"
            "s_cmp_eq_u64 s[84:85], 0\n"
            "s_cbranch_scc1 Lr7_%=\n"
            "Lchk6_%=:\n"
            "v_xor_b32 v71, s93, v71\n"
            "v_xor_b32 v73, s94, v73\n"
            "v_cmp_lt_u32 vcc, s38, v71\n"
            "v_cmp_lt_u32 s[86:87], s36, v73\n"
            "v_subrev_u32 v141, s19, v73\n"
            "v_and_b32 v141, 0x3ff, v141\n"
            "s_or_b64 vcc, vcc, s[86:87]\n"
            "s_and_b64 vcc, vcc, s[84:85]\n"
            "s_cbranch_scc1 Lre6_%=\n"
            "Lok6_%=:\n"
            "v_lshlrev_b32 v144, 2, v141\n"
            "v_lshrrev_b32 v145, 1, v141\n"
            "v_and_b32 v145, 0x1fc, v145\n"
            "v_and_b32 v146, 4, v141\n"
            "v_lshl_or_b32 v146, v146, 5, v190\n"
            "s_mov_b64 exec, s[84:85]\n"
            "ds_add_rtn_u32 v134, v144, v197 offset:512\n"
            "ds_add_u32 v145, v146 offset:96\n"
            "s_mov_b64 exec, -1\n"
            "Lr7_%=:\n"
            "s_mov_b64 exec, 1\n"
            "ds_add_u32 v219, v209 offset:44\n"
            "s_mov_b64 exec, -1\n"
            "v_lshlrev_b32 v32, 10, v35\n"
            "v_lshrrev_b32 v33, 10, v37\n"
            "v_mov_b32 v35, v36\n"
            "v_lshl_add_u32 v33, v33, 5, v88\n"
            "v_lshl_add_u32 v135, v135, 1, s18\n"
            "v_lshlrev_b32 v38, 10, v41\n"
            "v_lshrrev_b32 v39, 10, v43\n"
            "v_mov_b32 v41, v42\n"
            "v_lshl_add_u32 v39, v39, 5, v89\n"
            "v_lshl_add_u32 v136, v136, 1, s18\n"
            "v_lshlrev_b32 v44, 10, v47\n"
            "v_lshrrev_b32 v45, 10, v49\n"
            "v_mov_b32 v47, v48\n"
            "v_lshl_add_u32 v45, v45, 5, v90\n"
            "v_lshl_add_u32 v137, v137, 1, s18\n"
            "v_lshlrev_b32 v50, 10, v53\n"
            "v_lshrrev_b32 v51, 10, v55\n"
            "v_mov_b32 v53, v54\n"
            "v_lshl_add_u32 v51, v51, 5, v91\n"
            "v_lshl_add_u32 v138, v138, 1, s18\n"
            "v_lshlrev_b32 v56, 10, v59\n"
            "v_lshrrev_b32 v57, 10, v61\n"
            "v_mov_b32 v59, v60\n"
            "v_lshl_add_u32 v57, v57, 5, v92\n"
            "v_lshl_add_u32 v139, v139, 1, s18\n"
            "v_lshlrev_b32 v62, 10, v65\n"
            "v_lshrrev_b32 v63, 10, v67\n"
            "v_mov_b32 v65, v66\n"
            "v_lshl_add_u32 v63, v63, 5, v93\n"
            "v_lshl_add_u32 v140, v140, 1, s18\n"
            "v_lshlrev_b32 v68, 10, v71\n"
            "v_lshrrev_b32 v69, 10, v73\n"
            "v_mov_b32 v71, v72\n"
            "v_lshl_add_u32 v69, v69, 5, v94\n"
            "v_lshl_add_u32 v141, v141, 1, s18\n"
            "Lcc_%=:\n"
            "ds_read_b32 v220, v219 offset:44\n"
            "s_waitcnt lgkmcnt(0)\n"
            "v_readfirstlane_b32 s68, v220\n"
            "s_cmp_lt_u32 s68, 4\n"
            "s_cbranch_scc1 Lcc_%=\n"
            "ds_read_b128 v[144:147], v198 offset:512\n"
            "ds_read_b128 v[148:151], v198 offset:528\n"
            "ds_read_b128 v[152:155], v199 offset:2560\n"
            "ds_read_b128 v[156:159], v199 offset:2576\n"
            "v_bfe_u32 v128, v128, s12, 8\n"
            "v_bfe_u32 v129, v129, s12, 8\n"
            "v_bfe_u32 v130, v130, s12, 8\n"
            "v_bfe_u32 v131, v131, s12, 8\n"
            "v_bfe_u32 v132, v132, s12, 8\n"
            "v_bfe_u32 v133, v133, s12, 8\n"
            "v_bfe_u32 v134, v134, s12, 8\n"
            "s_waitcnt lgkmcnt(3)\n"
            "v_dot4_u32_u8 v160, v144, s13, v182\n"
            "v_dot4_u32_u8 v182, v144, s39, v182\n"
            "v_dot4_u32_u8 v161, v145, s13, v182\n"
            "v_dot4_u32_u8 v182, v145, s39, v182\n"
            "v_dot4_u32_u8 v162, v146, s13, v182\n"
            "v_dot4_u32_u8 v182, v146, s39, v182\n"
            "v_dot4_u32_u8 v163, v147, s13, v182\n"
            "v_dot4_u32_u8 v182, v147, s39, v182\n"
            "s_waitcnt lgkmcnt(2)\n"
            "v_dot4_u32_u8 v164, v148, s13, v182\n"
            "v_dot4_u32_u8 v182, v148, s39, v182\n"
            "v_dot4_u32_u8 v165, v149, s13, v182\n"
            "v_dot4_u32_u8 v182, v149, s39, v182\n"
            "v_dot4_u32_u8 v166, v150, s13, v182\n"
            "v_dot4_u32_u8 v182, v150, s39, v182\n"
            "v_dot4_u32_u8 v167, v151, s13, v182\n"
            "v_dot4_u32_u8 v182, v151, s39, v182\n"
            "s_waitcnt lgkmcnt(0)\n"
            "v_dot4_u32_u8 v168, v152, s13, v183\n"
            "v_dot4_u32_u8 v183, v152, s39, v183\n"
            "v_dot4_u32_u8 v169, v153, s13, v183\n"
            "v_dot4_u32_u8 v183, v153, s39, v183\n"
            "v_lshl_or_b32 v160, v161, 16, v160\n"
            "v_lshl_or_b32 v161, v163, 16, v162\n"
            "v_lshl_or_b32 v162, v165, 16, v164\n"
            "v_lshl_or_b32 v163, v167, 16, v166\n"
            "ds_write_b128 v187, v[160:163]\n"
            "v_dot4_u32_u8 v170, v154, s13, v183\n"
            "v_dot4_u32_u8 v183, v154, s39, v183\n"
            "v_dot4_u32_u8 v171, v155, s13, v183\n"
            "v_dot4_u32_u8 v183, v155, s39, v183\n"
            "v_dot4_u32_u8 v172, v156, s13, v183\n"
            "v_dot4_u32_u8 v183, v156, s39, v183\n"
            "v_dot4_u32_u8 v173, v157, s13, v183\n"
            "v_dot4_u32_u8 v183, v157, s39, v183\n"
            "v_dot4_u32_u8 v174, v158, s13, v183\n"
            "v_dot4_u32_u8 v183, v158, s39, v183\n"
            "v_dot4_u32_u8 v175, v159, s13, v183\n"
            "v_dot4_u32_u8 v183, v159, s39, v183\n"
            "v_cmp_gt_u32 vcc, v182, v184\n"
            "v_lshl_or_b32 v168, v169, 16, v168\n"
            "v_lshl_or_b32 v169, v171, 16, v170\n"
            "v_lshl_or_b32 v170, v173, 16, v172\n"
            "v_lshl_or_b32 v171, v175, 16, v174\n"
            "ds_write_b128 v188, v[168:171] offset:1024\n"
            "ds_read_u16 v135, v135\n"
            "ds_read_u16 v136, v136\n"
            "ds_read_u16 v137, v137\n"
            "ds_read_u16 v138, v138\n"
            "ds_read_u16 v139, v139\n"
            "ds_read_u16 v140, v140\n"
            "ds_read_u16 v141, v141\n"
            "v_cmp_gt_u32 s[86:87], v183, v185\n"
            "s_or_b64 vcc, vcc, s[86:87]\n"
            "s_cbranch_scc1 Lbig_%=\n"
            "s_waitcnt lgkmcnt(6)\n"
            "v_lshl_add_u32 v135, v128, 4, v135\n"
            "s_mov_b64 exec, s[72:73]\n"
            "ds_write_b128 v135, v[32:35]\n"
            "s_waitcnt lgkmcnt(6)\n"
            "v_lshl_add_u32 v136, v129, 4, v136\n"
            "s_mov_b64 exec, s[74:75]\n"
            "ds_write_b128 v136, v[38:41]\n"
            "s_waitcnt lgkmcnt(6)\n"
            "v_lshl_add_u32 v137, v130, 4, v137\n"
            "s_mov_b64 exec, s[76:77]\n"
            "ds_write_b128 v137, v[44:47]\n"
            "s_waitcnt lgkmcnt(6)\n"
            "v_lshl_add_u32 v138, v131, 4, v138\n"
            "s_mov_b64 exec, s[78:79]\n"
            "ds_write_b128 v138, v[50:53]\n"
            "s_waitcnt lgkmcnt(6)\n"
            "v_lshl_add_u32 v139, v132, 4, v139\n"
            "s_mov_b64 exec, s[80:81]\n"
            "ds_write_b128 v139, v[56:59]\n"
            "s_waitcnt lgkmcnt(6)\n"
            "v_lshl_add_u32 v140, v133, 4, v140\n"
            "s_mov_b64 exec, s[82:83]\n"
            "ds_write_b128 v140, v[62:65]\n"
            "s_waitcnt lgkmcnt(6)\n"
            "v_lshl_add_u32 v141, v134, 4, v141\n"
            "s_mov_b64 exec, s[84:85]\n"
            "ds_write_b128 v141, v[68:71]\n"
            "Lbig_%=:\n"
            "s_mov_b64 exec, 1\n"
            "ds_add_u32 v219, v209 offset:56\n"
            "s_mov_b64 exec, -1\n"
            "Lmain_%=:\n"
            "Ls_%=:\n"
            "ds_read_b32 v220, v219 offset:48\n"
            "s_waitcnt lgkmcnt(0)\n"
            "v_readfirstlane_b32 s68, v220\n"
            "s_cmp_lt_u32 s68, 4\n"
            "s_cbranch_scc1 Ls_%=\n"
            "v_mov_b32 v140, s64\n"
            "ds_read_b32 v128, v240\n"
            "ds_read_b32 v129, v241\n"
            "ds_read_b32 v130, v242\n"
            "ds_read_b32 v131, v219\n"
            "ds_read_b32 v138, v248 offset:512\n"
            "ds_read_b32 v139, v244 offset:512\n"
            "ds_read_u8 v140, v140 offset:2\n"
            "ds_read_b32 v217, v244 offset:1024\n"
            "ds_read_b32 v215, v245 offset:1024\n"
            "ds_read_b32 v218, v246 offset:1024\n"
            "s_waitcnt lgkmcnt(6)\n"
            "v_max_u32 v128, 12, v128\n"
            "v_mov_b32_dpp v134, v130 quad_perm:[1,2,3,0] row_mask:0xf bank_mask:0xf\n"
            "v_add_u32 v132, v128, v129\n"
            "v_sub_u32 v133, 0, v132\n"
            "v_lshl_add_u32 v135, v132, 1, v208\n"
            "v_and_b32 v133, 3, v133\n"
            "v_add_lshl_u32 v133, v132, v133, 1\n"
            "v_add3_u32 v135, v135, v134, v130\n"
            "v_readlane_b32 s73, v128, s3\n"
            "s_nop 0\n"
            "v_add_u32_dpp v135, v133, v135 row_shr:4 row_mask:0xf bank_mask:0xf bound_ctrl:1\n"
            "v_readlane_b32 s75, v129, s3\n"
            "v_readlane_b32 s80, v130, s3\n"
            "v_sub_u32_dpp v133, v135, v135 quad_perm:[1,2,3,0] row_mask:0xf bank_mask:0xf\n"
            "v_mov_b32_dpp v136, v135 quad_perm:[3,0,1,2] row_mask:0xf bank_mask:0xf\n"
            "v_lshl_add_u32 v137, v134, 3, 8\n"
            "v_mad_i32_i24 v133, v133, 3, v137\n"
            "v_sub_u32_dpp v136, v135, v136 quad_perm:[2,3,0,1] row_mask:0xf bank_mask:0xf\n"
            "v_add_u32 v134, -4, v134\n"
            "v_add_u32 v133, v133, v136\n"
            "v_ashrrev_i32 v133, 4, v133\n"
            "v_max_i32 v133, 4, v133\n"
            "v_min_i32 v133, v133, v134\n"
            "v_max_i32 v133, 0, v133\n"
            "s_add_i32 s69, s73, -1\n"
            "s_lshr_b32 s69, s69, 1\n"
            "v_mov_b32_dpp v134, v133 quad_perm:[3,0,1,2] row_mask:0xf bank_mask:0xf\n"
            "v_readlane_b32 s77, v133, s3\n"
            "v_sub_u32 v134, v130, v134\n"
            "v_add3_u32 v132, v132, v133, v131\n"
            "v_readlane_b32 s76, v134, s3\n"
            "v_add_u32 v132, v132, v134\n"
            "v_cmp_lt_u32 vcc, 72, v132\n"
            "v_readlane_b32 s73, v132, s3\n"
            "s_cbranch_vccnz Lslow_%=\n"
            "s_sub_u32 s43, 0, s73\n"
            "s_and_b32 s43, s43, 3\n"
            "s_min_u32 s43, s43, 3\n"
            "s_add_u32 s73, s73, s43\n"
            "s_sub_u32 s81, 8, s43\n"
            "s_max_i32 s69, s69, s81\n"
            "s_waitcnt lgkmcnt(3)\n"
            "v_max_u32 v140, 8, v140\n"
            "v_add_u32 v140, s43, v140\n"
            "v_sub_u32_sdwa v134, v140, v138 dst_sel:DWORD dst_unused:UNUSED_PAD src0_sel:DWORD src1_sel:BYTE_2\n"
            "v_sub_u32 v134, v206, v134\n"
            "v_add_u32 v136, 4, v134\n"
            "v_cmp_lt_u32_sdwa vcc, v134, v138 src0_sel:DWORD src1_sel:BYTE_2\n"
            "v_cmp_lt_u32_sdwa s[86:87], v136, v138 src0_sel:DWORD src1_sel:BYTE_2\n"
            "v_and_b32 v138, 0xffff, v138\n"
            "v_lshl_add_u32 v138, v134, 4, v138\n"
            "v_sub_u32_sdwa v135, v140, v139 dst_sel:DWORD dst_unused:UNUSED_PAD src0_sel:DWORD src1_sel:BYTE_2\n"
            "v_sub_u32 v135, v249, v135\n"
            "v_add_u32 v136, 4, v135\n"
            "v_cmp_lt_u32_sdwa s[70:71], v135, v139 src0_sel:DWORD src1_sel:BYTE_2\n"
            "v_cmp_lt_u32_sdwa s[88:89], v136, v139 src0_sel:DWORD src1_sel:BYTE_2\n"
            "v_and_b32 v139, 0xffff, v139\n"
            "v_lshl_add_u32 v139, v135, 4, v139\n"
            "v_cndmask_b32 v232, v251, v138, vcc\n"
            "v_cndmask_b32 v221, v251, v139, s[70:71]\n"
            "v_add_u32 v138, 64, v138\n"
            "v_add_u32 v139, 64, v139\n"
            "v_cndmask_b32 v233, v251, v138, s[86:87]\n"
            "v_cndmask_b32 v210, v251, v139, s[88:89]\n"
            "Lw_%=:\n"
            "ds_read_b32 v220, v219 offset:56\n"
            "s_waitcnt lgkmcnt(0)\n"
            "v_readfirstlane_b32 s68, v220\n"
            "s_cmp_lt_u32 s68, 4\n"
            "s_cbranch_scc1 Lw_%=\n"
            "ds_read_b32 v232, v232 offset:4\n"
            "ds_read_b128 v[196:199], v221\n"
            "ds_read_b32 v233, v233 offset:4\n"
            "s_waitcnt lgkmcnt(2)\n"
            "v_add_u32 v232, v232, v229\n"
            "buffer_load_dwordx4 v[160:163], v232, s[24:27], 0 offen\n"
            "s_waitcnt lgkmcnt(0)\n"
            "v_add_u32 v233, v233, v229\n"
            "v_add_u32_dpp v220, v196, v214 quad_perm:[0,0,0,0] row_mask:0xf bank_mask:0xf\n"
            "v_add_u32_dpp v221, v196, v214 quad_perm:[1,1,1,1] row_mask:0xf bank_mask:0xf\n"
            "v_add_u32_dpp v222, v196, v214 quad_perm:[2,2,2,2] row_mask:0xf bank_mask:0xf\n"
            "v_add_u32_dpp v223, v196, v214 quad_perm:[3,3,3,3] row_mask:0xf bank_mask:0xf\n"
            "buffer_load_dwordx4 v[32:35], v220, s[28:31], 0 offen\n"
            "buffer_load_dwordx4 v[36:39], v220, s[28:31], 0 offen offset:256\n"
            "v_mfma_f32_4x4x4_16b_bf16 v[204:207], v[202:203], v[198:199], 0\n"
            "buffer_load_dwordx4 v[48:51], v221, s[28:31], 0 offen\n"
            "buffer_load_dwordx4 v[52:55], v221, s[28:31], 0 offen offset:256\n"
            "buffer_load_dwordx4 v[232:235], v233, s[24:27], 0 offen\n"
            "buffer_load_dwordx4 v[64:67], v222, s[28:31], 0 offen\n"
            "buffer_load_dwordx4 v[68:71], v222, s[28:31], 0 offen offset:256\n"
            "v_mfma_f32_4x4x4_16b_bf16 v[204:207], v[200:201], v[198:199], v[204:207]\n"
            "buffer_load_dwordx4 v[80:83], v223, s[28:31], 0 offen\n"
            "buffer_load_dwordx4 v[84:87], v223, s[28:31], 0 offen offset:256\n"
            "buffer_load_dwordx4 v[40:43], v220, s[28:31], 0 offen offset:512\n"
            "buffer_load_dwordx4 v[44:47], v220, s[28:31], 0 offen offset:768\n"
            "buffer_load_dwordx4 v[56:59], v221, s[28:31], 0 offen offset:512\n"
            "buffer_load_dwordx4 v[60:63], v221, s[28:31], 0 offen offset:768\n"
            "buffer_load_dwordx4 v[72:75], v222, s[28:31], 0 offen offset:512\n"
            "buffer_load_dwordx4 v[76:79], v222, s[28:31], 0 offen offset:768\n"
            "buffer_load_dwordx4 v[88:91], v223, s[28:31], 0 offen offset:512\n"
            "buffer_load_dwordx4 v[92:95], v223, s[28:31], 0 offen offset:768\n"
            "s_add_u32 s78, s69, s77\n"
            "s_add_u32 s79, s78, s75\n"
            "s_sub_u32 s83, s79, s80\n"
            "s_add_u32 s83, s83, s76\n"
            "s_add_u32 s80, s79, s76\n"
            "s_sub_u32 s84, s80, s69\n"
            "s_pack_ll_b32_b16 s81, s40, s69\n"
            "s_pack_ll_b32_b16 s82, s65, s78\n"
            "s_pack_ll_b32_b16 s83, s66, s83\n"
            "s_pack_ll_b32_b16 s84, s64, s84\n"
            "v_subrev_u32 v128, s43, v243\n"
            "v_mov_b32 v136, s64\n"
            "v_cmpx_le_i32 vcc, s69, v128\n"
            "v_mov_b32 v136, s81\n"
            "v_cmpx_le_i32 vcc, s78, v128\n"
            "v_mov_b32 v136, s82\n"
            "v_cmpx_le_i32 vcc, s79, v128\n"
            "v_mov_b32 v136, s83\n"
            "v_cmpx_le_i32 vcc, s80, v128\n"
            "v_mov_b32 v136, s84\n"
            "s_mov_b64 exec, -1\n"
            "v_and_b32 v137, 0xffff, v136\n"
            "ds_read_b128 v[140:143], v137\n"
            "v_ashrrev_i32 v136, 16, v136\n"
            "v_sub_u32 v136, v128, v136\n"
            "v_cvt_pk_bf16_f32 v204, v204, v205\n"
            "v_cvt_pk_bf16_f32 v205, v206, v207\n"
            "ds_write_b64 v212, v[204:205]\n"
            "ds_read_b128 v[196:199], v210\n"
            "ds_read_b64 v[208:209], v213\n"
            "s_add_u32 s73, s73, 3\n"
            "s_and_b32 s74, s73, 3\n"
            "s_add_u32 s74, s74, 1\n"
            "s_lshr_b32 s73, s73, 2\n"
            "s_mov_b32 s68, 0x05040100\n"
            "v_add_u32 v217, v217, v214\n"
            "v_add3_u32 v215, v215, v214, s63\n"
            "v_add_u32 v218, v218, v214\n"
            "v_not_b32 v223, s90\n"
            "s_waitcnt lgkmcnt(3)\n"
            "v_cmp_eq_u32 vcc, s64, v137\n"
            "v_mov_b32 v132, 60\n"
            "s_nop 0\n"
            "v_cndmask_b32 v132, 0, v132, vcc\n"
            "v_bfe_u32 v137, v140, 16, 8\n"
            "v_max_u32 v137, 8, v137\n"
            "v_lshrrev_b32 v138, 1, v137\n"
            "v_add_u32 v138, v138, v132\n"
            "v_sub_u32_sdwa v132, v138, v140 dst_sel:DWORD dst_unused:UNUSED_PAD src0_sel:DWORD src1_sel:BYTE_3\n"
            "v_sub_u32_sdwa v133, v137, v140 dst_sel:DWORD dst_unused:UNUSED_PAD src0_sel:DWORD src1_sel:BYTE_2\n"
            "v_med3_i32 v132, v132, 0, v133\n"
            "v_sub_u32 v132, v136, v132\n"
            "v_cmp_lt_u32_sdwa vcc, v132, v140 src0_sel:DWORD src1_sel:BYTE_2\n"
            "v_lshl_add_u32 v140, v132, 4, v140\n"
            "v_sub_u32_sdwa v132, v138, v141 dst_sel:DWORD dst_unused:UNUSED_PAD src0_sel:DWORD src1_sel:BYTE_3\n"
            "v_sub_u32_sdwa v133, v137, v141 dst_sel:DWORD dst_unused:UNUSED_PAD src0_sel:DWORD src1_sel:BYTE_2\n"
            "v_med3_i32 v132, v132, 0, v133\n"
            "v_sub_u32 v132, v136, v132\n"
            "v_cmp_lt_u32_sdwa s[70:71], v132, v141 src0_sel:DWORD src1_sel:BYTE_2\n"
            "v_lshl_add_u32 v141, v132, 4, v141\n"
            "v_sub_u32_sdwa v132, v138, v142 dst_sel:DWORD dst_unused:UNUSED_PAD src0_sel:DWORD src1_sel:BYTE_3\n"
            "v_sub_u32_sdwa v133, v137, v142 dst_sel:DWORD dst_unused:UNUSED_PAD src0_sel:DWORD src1_sel:BYTE_2\n"
            "v_med3_i32 v132, v132, 0, v133\n"
            "v_sub_u32 v132, v136, v132\n"
            "v_cmp_lt_u32_sdwa s[86:87], v132, v142 src0_sel:DWORD src1_sel:BYTE_2\n"
            "v_lshl_add_u32 v142, v132, 4, v142\n"
            "v_sub_u32_sdwa v132, v138, v143 dst_sel:DWORD dst_unused:UNUSED_PAD src0_sel:DWORD src1_sel:BYTE_3\n"
            "v_sub_u32_sdwa v133, v137, v143 dst_sel:DWORD dst_unused:UNUSED_PAD src0_sel:DWORD src1_sel:BYTE_2\n"
            "v_med3_i32 v132, v132, 0, v133\n"
            "v_sub_u32 v132, v136, v132\n"
            "v_cmp_lt_u32_sdwa s[88:89], v132, v143 src0_sel:DWORD src1_sel:BYTE_2\n"
            "v_lshl_add_u32 v143, v132, 4, v143\n"
            "v_cndmask_b32 v140, v251, v140, vcc\n"
            "v_cndmask_b32 v141, v251, v141, s[70:71]\n"
            "v_cndmask_b32 v142, v251, v142, s[86:87]\n"
            "v_cndmask_b32 v143, v251, v143, s[88:89]\n"
            "v_perm_b32 v140, v141, v140, s68\n"
            "v_perm_b32 v141, v143, v142, s68\n"
            "ds_write_b64 v247, v[140:141]\n"
            "ds_read_u16 v211, v238\n"
            "s_cmp_gt_u32 s73, 13\n"
            "s_cbranch_scc1 Llong_%=\n"
            "Llong2_%=:\n"
            "s_add_u32 s69, s69, s43\n"
            "v_add_u32 v216, s63, v218\n"
            "s_lshl_b32 s72, s75, 8\n"
            "s_lshl_b32 s76, s76, 16\n"
            "s_add_u32 s72, s72, s76\n"
            "s_add_u32 s72, s72, s77\n"
            "s_add_u32 s72, s72, 0xfefbfbfc\n"
            "s_add_u32 s75, s75, 0xfefc\n"
            "s_cmp_lt_u32 s3, 4\n"
            "s_cselect_b32 s72, s75, s72\n"
            "s_cmp_lt_u32 s3, 6\n"
            "s_cselect_b32 s75, 1, 0\n"
            "v_lshl_or_b32 v218, s75, 31, v218\n"
            "s_mov_b32 s85, 15\n"
            "s_mov_b32 s42, 0\n"
            "s_waitcnt vmcnt(17)\n"
            "ds_bpermute_b32 v192, v253, v160\n"
            "ds_bpermute_b32 v193, v253, v161\n"
            "ds_bpermute_b32 v194, v253, v162\n"
            "ds_bpermute_b32 v195, v253, v163\n"
            "s_waitcnt lgkmcnt(0)\n"
            "v_mfma_f32_16x16x32_bf16 v[176:179], v[192:195], v[96:99], 0\n"
            "v_mfma_f32_16x16x32_bf16 v[180:183], v[192:195], v[100:103], 0\n"
            "v_mfma_f32_16x16x32_bf16 v[184:187], v[192:195], v[104:107], 0\n"
            "v_mfma_f32_16x16x32_bf16 v[188:191], v[192:195], v[108:111], 0\n"
            "v_mfma_f32_16x16x32_bf16 v[128:131], v[192:195], v[112:115], 0\n"
            "v_mfma_f32_16x16x32_bf16 v[132:135], v[192:195], v[116:119], 0\n"
            "s_nop 2\n"
            "v_exp_f32 v144, v176\n"
            "v_exp_f32 v145, v177\n"
            "v_exp_f32 v146, v178\n"
            "v_exp_f32 v147, v179\n"
            "v_exp_f32 v148, v180\n"
            "v_exp_f32 v149, v181\n"
            "v_exp_f32 v150, v182\n"
            "v_exp_f32 v151, v183\n"
            "v_exp_f32 v152, v184\n"
            "v_exp_f32 v153, v185\n"
            "v_exp_f32 v154, v186\n"
            "v_exp_f32 v155, v187\n"
            "v_exp_f32 v156, v188\n"
            "v_exp_f32 v157, v189\n"
            "v_exp_f32 v158, v190\n"
            "v_exp_f32 v159, v191\n"
            "v_pk_add_f32 v[144:145], v[144:145], 1.0 op_sel_hi:[1,0]\n"
            "v_pk_add_f32 v[146:147], v[146:147], 1.0 op_sel_hi:[1,0]\n"
            "v_pk_add_f32 v[148:149], v[148:149], 1.0 op_sel_hi:[1,0]\n"
            "v_pk_add_f32 v[150:151], v[150:151], 1.0 op_sel_hi:[1,0]\n"
            "v_pk_add_f32 v[152:153], v[152:153], 1.0 op_sel_hi:[1,0]\n"
            "v_pk_add_f32 v[154:155], v[154:155], 1.0 op_sel_hi:[1,0]\n"
            "v_pk_add_f32 v[156:157], v[156:157], 1.0 op_sel_hi:[1,0]\n"
            "v_pk_add_f32 v[158:159], v[158:159], 1.0 op_sel_hi:[1,0]\n"
            "v_rcp_f32 v144, v144\n"
            "v_rcp_f32 v145, v145\n"
            "v_rcp_f32 v146, v146\n"
            "v_rcp_f32 v147, v147\n"
            "v_rcp_f32 v148, v148\n"
            "v_rcp_f32 v149, v149\n"
            "v_rcp_f32 v150, v150\n"
            "v_rcp_f32 v151, v151\n"
            "v_rcp_f32 v152, v152\n"
            "v_rcp_f32 v153, v153\n"
            "v_rcp_f32 v154, v154\n"
            "v_rcp_f32 v155, v155\n"
            "v_rcp_f32 v156, v156\n"
            "v_rcp_f32 v157, v157\n"
            "v_rcp_f32 v158, v158\n"
            "v_rcp_f32 v159, v159\n"
            "v_pk_mul_f32 v[144:145], v[144:145], v[176:177]\n"
            "v_pk_mul_f32 v[146:147], v[146:147], v[178:179]\n"
            "v_pk_mul_f32 v[148:149], v[148:149], v[180:181]\n"
            "v_pk_mul_f32 v[150:151], v[150:151], v[182:183]\n"
            "v_pk_mul_f32 v[152:153], v[152:153], v[184:185]\n"
            "v_pk_mul_f32 v[154:155], v[154:155], v[186:187]\n"
            "v_pk_mul_f32 v[156:157], v[156:157], v[188:189]\n"
            "v_pk_mul_f32 v[158:159], v[158:159], v[190:191]\n"
            "s_branch Ldisp_%=\n"
            "Lfast_%=:\n"
            "v_mfma_f32_16x16x32_bf16 v[136:139], v[192:195], v[120:123], 0\n"
            "v_exp_f32 v160, v128\n"
            "v_add_u32 v237, 32, v237\n"
            "v_exp_f32 v161, v129\n"
            "s_waitcnt vmcnt(12) lgkmcnt(0)\n"
            "v_mfma_f32_4x4x4_16b_bf16 v[176:179], v[208:209], v[32:33], 0 cbsz:2\n"
            "v_exp_f32 v162, v130\n"
            "v_mfma_f32_4x4x4_16b_bf16 v[180:183], v[208:209], v[34:35], 0 cbsz:2\n"
            "v_exp_f32 v163, v131\n"
            "v_mfma_f32_4x4x4_16b_bf16 v[184:187], v[208:209], v[36:37], 0 cbsz:2\n"
            "v_exp_f32 v164, v132\n"
            "v_mfma_f32_4x4x4_16b_bf16 v[188:191], v[208:209], v[38:39], 0 cbsz:2\n"
            "v_exp_f32 v165, v133\n"
            "v_add_u32_dpp v219, v196, v214 quad_perm:[0,0,0,0] row_mask:0xf bank_mask:0xf\n"
            "v_mfma_f32_4x4x4_16b_bf16 v[204:207], v[202:203], v[198:199], 0\n"
            "v_exp_f32 v166, v134\n"
            "buffer_load_dwordx4 v[32:35], v219, s[28:31], 0 offen\n"
            "buffer_load_dwordx4 v[36:39], v219, s[28:31], 0 offen offset:256\n"
            "v_exp_f32 v167, v135\n"
            "v_pk_fma_f32 v[0:1], v[144:145], v[176:177], v[0:1] op_sel_hi:[0,1,1]\n"
            "v_pk_fma_f32 v[2:3], v[144:145], v[178:179], v[2:3] op_sel_hi:[0,1,1]\n"
            "v_pk_fma_f32 v[4:5], v[148:149], v[180:181], v[4:5] op_sel_hi:[0,1,1]\n"
            "v_pk_fma_f32 v[6:7], v[148:149], v[182:183], v[6:7] op_sel_hi:[0,1,1]\n"
            "v_pk_fma_f32 v[8:9], v[152:153], v[184:185], v[8:9] op_sel_hi:[0,1,1]\n"
            "v_pk_fma_f32 v[10:11], v[152:153], v[186:187], v[10:11] op_sel_hi:[0,1,1]\n"
            "v_pk_fma_f32 v[12:13], v[156:157], v[188:189], v[12:13] op_sel_hi:[0,1,1]\n"
            "v_pk_fma_f32 v[14:15], v[156:157], v[190:191], v[14:15] op_sel_hi:[0,1,1]\n"
            "v_pk_add_f32 v[160:161], v[160:161], 1.0 op_sel_hi:[1,0]\n"
            "v_pk_add_f32 v[162:163], v[162:163], 1.0 op_sel_hi:[1,0]\n"
            "v_pk_add_f32 v[164:165], v[164:165], 1.0 op_sel_hi:[1,0]\n"
            "v_pk_add_f32 v[166:167], v[166:167], 1.0 op_sel_hi:[1,0]\n"
            "v_mfma_f32_16x16x32_bf16 v[140:143], v[192:195], v[124:127], 0\n"
            "ds_bpermute_b32 v192, v253, v232\n"
            "ds_bpermute_b32 v193, v253, v233\n"
            "ds_bpermute_b32 v194, v253, v234\n"
            "ds_bpermute_b32 v195, v253, v235\n"
            "ds_read_b32 v230, v211 offset:4\n"
            "ds_read_u16 v210, v237\n"
            "v_rcp_f32 v160, v160\n"
            "v_rcp_f32 v161, v161\n"
            "v_mfma_f32_4x4x4_16b_bf16 v[176:179], v[208:209], v[48:49], 0 cbsz:2 abid:1\n"
            "v_rcp_f32 v162, v162\n"
            "v_mfma_f32_4x4x4_16b_bf16 v[180:183], v[208:209], v[50:51], 0 cbsz:2 abid:1\n"
            "v_rcp_f32 v163, v163\n"
            "v_mfma_f32_4x4x4_16b_bf16 v[184:187], v[208:209], v[52:53], 0 cbsz:2 abid:1\n"
            "v_rcp_f32 v164, v164\n"
            "v_mfma_f32_4x4x4_16b_bf16 v[188:191], v[208:209], v[54:55], 0 cbsz:2 abid:1\n"
            "v_rcp_f32 v165, v165\n"
            "v_add_u32_dpp v220, v196, v214 quad_perm:[1,1,1,1] row_mask:0xf bank_mask:0xf\n"
            "v_mfma_f32_4x4x4_16b_bf16 v[204:207], v[200:201], v[198:199], v[204:207]\n"
            "v_rcp_f32 v166, v166\n"
            "buffer_load_dwordx4 v[48:51], v220, s[28:31], 0 offen\n"
            "buffer_load_dwordx4 v[52:55], v220, s[28:31], 0 offen offset:256\n"
            "v_rcp_f32 v167, v167\n"
            "v_pk_fma_f32 v[0:1], v[144:145], v[176:177], v[0:1] op_sel:[1,0,0]\n"
            "v_pk_fma_f32 v[2:3], v[144:145], v[178:179], v[2:3] op_sel:[1,0,0]\n"
            "v_pk_fma_f32 v[4:5], v[148:149], v[180:181], v[4:5] op_sel:[1,0,0]\n"
            "v_pk_fma_f32 v[6:7], v[148:149], v[182:183], v[6:7] op_sel:[1,0,0]\n"
            "v_pk_fma_f32 v[8:9], v[152:153], v[184:185], v[8:9] op_sel:[1,0,0]\n"
            "v_pk_fma_f32 v[10:11], v[152:153], v[186:187], v[10:11] op_sel:[1,0,0]\n"
            "v_pk_fma_f32 v[12:13], v[156:157], v[188:189], v[12:13] op_sel:[1,0,0]\n"
            "v_pk_fma_f32 v[14:15], v[156:157], v[190:191], v[14:15] op_sel:[1,0,0]\n"
            "v_pk_mul_f32 v[160:161], v[160:161], v[128:129]\n"
            "v_pk_mul_f32 v[162:163], v[162:163], v[130:131]\n"
            "v_pk_mul_f32 v[164:165], v[164:165], v[132:133]\n"
            "v_pk_mul_f32 v[166:167], v[166:167], v[134:135]\n"
            "s_waitcnt vmcnt(12) lgkmcnt(0)\n"
            "v_add_u32 v230, v230, v229\n"
            "buffer_load_dwordx4 v[232:235], v230, s[24:27], 0 offen\n"
            "s_bitcmp0_b32 s85, 0\n"
            "s_cbranch_scc1 Lnpf02_%=\n"
            "s_buffer_load_dwordx2 s[86:87], s[28:31], s67 offset:60\n"
            "s_buffer_load_dwordx2 s[86:87], s[28:31], s67 offset:188\n"
            "s_buffer_load_dwordx2 s[86:87], s[28:31], s67 offset:316\n"
            "s_buffer_load_dwordx2 s[86:87], s[28:31], s67 offset:444\n"
            "s_add_u32 s67, s67, 524288\n"
            "Lnpf02_%=:\n"
            "s_lshr_b32 s85, s85, 1\n"
            "v_mfma_f32_16x16x32_bf16 v[128:131], v[192:195], v[96:99], 0\n"
            "v_exp_f32 v168, v136\n"
            "v_exp_f32 v169, v137\n"
            "v_mfma_f32_4x4x4_16b_bf16 v[176:179], v[208:209], v[64:65], 0 cbsz:2 abid:2\n"
            "v_exp_f32 v170, v138\n"
            "v_mfma_f32_4x4x4_16b_bf16 v[180:183], v[208:209], v[66:67], 0 cbsz:2 abid:2\n"
            "v_exp_f32 v171, v139\n"
            "v_mfma_f32_4x4x4_16b_bf16 v[184:187], v[208:209], v[68:69], 0 cbsz:2 abid:2\n"
            "v_exp_f32 v172, v140\n"
            "v_mfma_f32_4x4x4_16b_bf16 v[188:191], v[208:209], v[70:71], 0 cbsz:2 abid:2\n"
            "v_exp_f32 v173, v141\n"
            "v_add_u32_dpp v221, v196, v214 quad_perm:[2,2,2,2] row_mask:0xf bank_mask:0xf\n"
            "v_exp_f32 v174, v142\n"
            "buffer_load_dwordx4 v[64:67], v221, s[28:31], 0 offen\n"
            "buffer_load_dwordx4 v[68:71], v221, s[28:31], 0 offen offset:256\n"
            "v_exp_f32 v175, v143\n"
            "v_pk_fma_f32 v[0:1], v[146:147], v[176:177], v[0:1] op_sel_hi:[0,1,1]\n"
            "v_pk_fma_f32 v[2:3], v[146:147], v[178:179], v[2:3] op_sel_hi:[0,1,1]\n"
            "v_pk_fma_f32 v[4:5], v[150:151], v[180:181], v[4:5] op_sel_hi:[0,1,1]\n"
            "v_pk_fma_f32 v[6:7], v[150:151], v[182:183], v[6:7] op_sel_hi:[0,1,1]\n"
            "v_pk_fma_f32 v[8:9], v[154:155], v[184:185], v[8:9] op_sel_hi:[0,1,1]\n"
            "v_pk_fma_f32 v[10:11], v[154:155], v[186:187], v[10:11] op_sel_hi:[0,1,1]\n"
            "v_pk_fma_f32 v[12:13], v[158:159], v[188:189], v[12:13] op_sel_hi:[0,1,1]\n"
            "v_pk_fma_f32 v[14:15], v[158:159], v[190:191], v[14:15] op_sel_hi:[0,1,1]\n"
            "v_pk_add_f32 v[168:169], v[168:169], 1.0 op_sel_hi:[1,0]\n"
            "v_pk_add_f32 v[170:171], v[170:171], 1.0 op_sel_hi:[1,0]\n"
            "v_pk_add_f32 v[172:173], v[172:173], 1.0 op_sel_hi:[1,0]\n"
            "v_pk_add_f32 v[174:175], v[174:175], 1.0 op_sel_hi:[1,0]\n"
            "v_cvt_pk_bf16_f32 v204, v204, v205\n"
            "v_cvt_pk_bf16_f32 v205, v206, v207\n"
            "ds_write_b64 v212, v[204:205]\n"
            "v_mfma_f32_16x16x32_bf16 v[132:135], v[192:195], v[100:103], 0\n"
            "v_rcp_f32 v168, v168\n"
            "v_rcp_f32 v169, v169\n"
            "v_mfma_f32_4x4x4_16b_bf16 v[176:179], v[208:209], v[80:81], 0 cbsz:2 abid:3\n"
            "v_rcp_f32 v170, v170\n"
            "v_mfma_f32_4x4x4_16b_bf16 v[180:183], v[208:209], v[82:83], 0 cbsz:2 abid:3\n"
            "v_rcp_f32 v171, v171\n"
            "v_mfma_f32_4x4x4_16b_bf16 v[184:187], v[208:209], v[84:85], 0 cbsz:2 abid:3\n"
            "v_rcp_f32 v172, v172\n"
            "v_mfma_f32_4x4x4_16b_bf16 v[188:191], v[208:209], v[86:87], 0 cbsz:2 abid:3\n"
            "v_rcp_f32 v173, v173\n"
            "v_add_u32_dpp v222, v196, v214 quad_perm:[3,3,3,3] row_mask:0xf bank_mask:0xf\n"
            "v_rcp_f32 v174, v174\n"
            "buffer_load_dwordx4 v[80:83], v222, s[28:31], 0 offen\n"
            "buffer_load_dwordx4 v[84:87], v222, s[28:31], 0 offen offset:256\n"
            "v_rcp_f32 v175, v175\n"
            "v_pk_fma_f32 v[0:1], v[146:147], v[176:177], v[0:1] op_sel:[1,0,0]\n"
            "v_pk_fma_f32 v[2:3], v[146:147], v[178:179], v[2:3] op_sel:[1,0,0]\n"
            "v_pk_fma_f32 v[4:5], v[150:151], v[180:181], v[4:5] op_sel:[1,0,0]\n"
            "v_pk_fma_f32 v[6:7], v[150:151], v[182:183], v[6:7] op_sel:[1,0,0]\n"
            "v_pk_fma_f32 v[8:9], v[154:155], v[184:185], v[8:9] op_sel:[1,0,0]\n"
            "v_pk_fma_f32 v[10:11], v[154:155], v[186:187], v[10:11] op_sel:[1,0,0]\n"
            "v_pk_fma_f32 v[12:13], v[158:159], v[188:189], v[12:13] op_sel:[1,0,0]\n"
            "v_pk_fma_f32 v[14:15], v[158:159], v[190:191], v[14:15] op_sel:[1,0,0]\n"
            "v_pk_mul_f32 v[168:169], v[168:169], v[136:137]\n"
            "v_pk_mul_f32 v[170:171], v[170:171], v[138:139]\n"
            "v_pk_mul_f32 v[172:173], v[172:173], v[140:141]\n"
            "v_pk_mul_f32 v[174:175], v[174:175], v[142:143]\n"
            "v_add_u32 v238, 32, v238\n"
            "ds_read_b128 v[196:199], v210\n"
            "ds_read_u16 v211, v238\n"
            "v_mfma_f32_16x16x32_bf16 v[136:139], v[192:195], v[104:107], 0\n"
            "v_exp_f32 v144, v128\n"
            "v_exp_f32 v145, v129\n"
            "s_waitcnt vmcnt(13)\n"
            "v_mfma_f32_4x4x4_16b_bf16 v[176:179], v[208:209], v[40:41], 0 cbsz:2\n"
            "v_exp_f32 v146, v130\n"
            "v_mfma_f32_4x4x4_16b_bf16 v[180:183], v[208:209], v[42:43], 0 cbsz:2\n"
            "v_exp_f32 v147, v131\n"
            "v_mfma_f32_4x4x4_16b_bf16 v[184:187], v[208:209], v[44:45], 0 cbsz:2\n"
            "v_exp_f32 v148, v132\n"
            "v_mfma_f32_4x4x4_16b_bf16 v[188:191], v[208:209], v[46:47], 0 cbsz:2\n"
            "v_exp_f32 v149, v133\n"
            "v_exp_f32 v150, v134\n"
            "buffer_load_dwordx4 v[40:43], v219, s[28:31], 0 offen offset:512\n"
            "buffer_load_dwordx4 v[44:47], v219, s[28:31], 0 offen offset:768\n"
            "v_exp_f32 v151, v135\n"
            "v_pk_fma_f32 v[16:17], v[160:161], v[176:177], v[16:17] op_sel_hi:[0,1,1]\n"
            "v_pk_fma_f32 v[18:19], v[160:161], v[178:179], v[18:19] op_sel_hi:[0,1,1]\n"
            "v_pk_fma_f32 v[20:21], v[164:165], v[180:181], v[20:21] op_sel_hi:[0,1,1]\n"
            "v_pk_fma_f32 v[22:23], v[164:165], v[182:183], v[22:23] op_sel_hi:[0,1,1]\n"
            "v_pk_fma_f32 v[24:25], v[168:169], v[184:185], v[24:25] op_sel_hi:[0,1,1]\n"
            "v_pk_fma_f32 v[26:27], v[168:169], v[186:187], v[26:27] op_sel_hi:[0,1,1]\n"
            "v_pk_fma_f32 v[28:29], v[172:173], v[188:189], v[28:29] op_sel_hi:[0,1,1]\n"
            "v_pk_fma_f32 v[30:31], v[172:173], v[190:191], v[30:31] op_sel_hi:[0,1,1]\n"
            "v_pk_add_f32 v[144:145], v[144:145], 1.0 op_sel_hi:[1,0]\n"
            "v_pk_add_f32 v[146:147], v[146:147], 1.0 op_sel_hi:[1,0]\n"
            "v_pk_add_f32 v[148:149], v[148:149], 1.0 op_sel_hi:[1,0]\n"
            "v_pk_add_f32 v[150:151], v[150:151], 1.0 op_sel_hi:[1,0]\n"
            "v_mfma_f32_16x16x32_bf16 v[140:143], v[192:195], v[108:111], 0\n"
            "v_rcp_f32 v144, v144\n"
            "v_rcp_f32 v145, v145\n"
            "v_mfma_f32_4x4x4_16b_bf16 v[176:179], v[208:209], v[56:57], 0 cbsz:2 abid:1\n"
            "v_rcp_f32 v146, v146\n"
            "v_mfma_f32_4x4x4_16b_bf16 v[180:183], v[208:209], v[58:59], 0 cbsz:2 abid:1\n"
            "v_rcp_f32 v147, v147\n"
            "v_mfma_f32_4x4x4_16b_bf16 v[184:187], v[208:209], v[60:61], 0 cbsz:2 abid:1\n"
            "v_rcp_f32 v148, v148\n"
            "v_mfma_f32_4x4x4_16b_bf16 v[188:191], v[208:209], v[62:63], 0 cbsz:2 abid:1\n"
            "v_rcp_f32 v149, v149\n"
            "v_rcp_f32 v150, v150\n"
            "buffer_load_dwordx4 v[56:59], v220, s[28:31], 0 offen offset:512\n"
            "buffer_load_dwordx4 v[60:63], v220, s[28:31], 0 offen offset:768\n"
            "v_rcp_f32 v151, v151\n"
            "v_pk_fma_f32 v[16:17], v[160:161], v[176:177], v[16:17] op_sel:[1,0,0]\n"
            "v_pk_fma_f32 v[18:19], v[160:161], v[178:179], v[18:19] op_sel:[1,0,0]\n"
            "v_pk_fma_f32 v[20:21], v[164:165], v[180:181], v[20:21] op_sel:[1,0,0]\n"
            "v_pk_fma_f32 v[22:23], v[164:165], v[182:183], v[22:23] op_sel:[1,0,0]\n"
            "v_pk_fma_f32 v[24:25], v[168:169], v[184:185], v[24:25] op_sel:[1,0,0]\n"
            "v_pk_fma_f32 v[26:27], v[168:169], v[186:187], v[26:27] op_sel:[1,0,0]\n"
            "v_pk_fma_f32 v[28:29], v[172:173], v[188:189], v[28:29] op_sel:[1,0,0]\n"
            "v_pk_fma_f32 v[30:31], v[172:173], v[190:191], v[30:31] op_sel:[1,0,0]\n"
            "v_pk_mul_f32 v[144:145], v[144:145], v[128:129]\n"
            "v_pk_mul_f32 v[146:147], v[146:147], v[130:131]\n"
            "v_pk_mul_f32 v[148:149], v[148:149], v[132:133]\n"
            "v_pk_mul_f32 v[150:151], v[150:151], v[134:135]\n"
            "v_mfma_f32_16x16x32_bf16 v[128:131], v[192:195], v[112:115], 0\n"
            "v_exp_f32 v152, v136\n"
            "v_exp_f32 v153, v137\n"
            "s_waitcnt vmcnt(13)\n"
            "v_mfma_f32_4x4x4_16b_bf16 v[176:179], v[208:209], v[72:73], 0 cbsz:2 abid:2\n"
            "v_exp_f32 v154, v138\n"
            "v_mfma_f32_4x4x4_16b_bf16 v[180:183], v[208:209], v[74:75], 0 cbsz:2 abid:2\n"
            "v_exp_f32 v155, v139\n"
            "v_mfma_f32_4x4x4_16b_bf16 v[184:187], v[208:209], v[76:77], 0 cbsz:2 abid:2\n"
            "v_exp_f32 v156, v140\n"
            "v_mfma_f32_4x4x4_16b_bf16 v[188:191], v[208:209], v[78:79], 0 cbsz:2 abid:2\n"
            "v_exp_f32 v157, v141\n"
            "v_exp_f32 v158, v142\n"
            "buffer_load_dwordx4 v[72:75], v221, s[28:31], 0 offen offset:512\n"
            "buffer_load_dwordx4 v[76:79], v221, s[28:31], 0 offen offset:768\n"
            "v_exp_f32 v159, v143\n"
            "v_pk_fma_f32 v[16:17], v[162:163], v[176:177], v[16:17] op_sel_hi:[0,1,1]\n"
            "v_pk_fma_f32 v[18:19], v[162:163], v[178:179], v[18:19] op_sel_hi:[0,1,1]\n"
            "v_pk_fma_f32 v[20:21], v[166:167], v[180:181], v[20:21] op_sel_hi:[0,1,1]\n"
            "v_pk_fma_f32 v[22:23], v[166:167], v[182:183], v[22:23] op_sel_hi:[0,1,1]\n"
            "v_pk_fma_f32 v[24:25], v[170:171], v[184:185], v[24:25] op_sel_hi:[0,1,1]\n"
            "v_pk_fma_f32 v[26:27], v[170:171], v[186:187], v[26:27] op_sel_hi:[0,1,1]\n"
            "v_pk_fma_f32 v[28:29], v[174:175], v[188:189], v[28:29] op_sel_hi:[0,1,1]\n"
            "v_pk_fma_f32 v[30:31], v[174:175], v[190:191], v[30:31] op_sel_hi:[0,1,1]\n"
            "v_pk_add_f32 v[152:153], v[152:153], 1.0 op_sel_hi:[1,0]\n"
            "v_pk_add_f32 v[154:155], v[154:155], 1.0 op_sel_hi:[1,0]\n"
            "v_pk_add_f32 v[156:157], v[156:157], 1.0 op_sel_hi:[1,0]\n"
            "v_pk_add_f32 v[158:159], v[158:159], 1.0 op_sel_hi:[1,0]\n"
            "v_mfma_f32_16x16x32_bf16 v[132:135], v[192:195], v[116:119], 0\n"
            "v_rcp_f32 v152, v152\n"
            "v_rcp_f32 v153, v153\n"
            "v_mfma_f32_4x4x4_16b_bf16 v[176:179], v[208:209], v[88:89], 0 cbsz:2 abid:3\n"
            "v_rcp_f32 v154, v154\n"
            "v_mfma_f32_4x4x4_16b_bf16 v[180:183], v[208:209], v[90:91], 0 cbsz:2 abid:3\n"
            "v_rcp_f32 v155, v155\n"
            "v_mfma_f32_4x4x4_16b_bf16 v[184:187], v[208:209], v[92:93], 0 cbsz:2 abid:3\n"
            "v_rcp_f32 v156, v156\n"
            "v_mfma_f32_4x4x4_16b_bf16 v[188:191], v[208:209], v[94:95], 0 cbsz:2 abid:3\n"
            "ds_read_b64 v[208:209], v213\n"
            "v_rcp_f32 v157, v157\n"
            "v_rcp_f32 v158, v158\n"
            "buffer_load_dwordx4 v[88:91], v222, s[28:31], 0 offen offset:512\n"
            "buffer_load_dwordx4 v[92:95], v222, s[28:31], 0 offen offset:768\n"
            "v_rcp_f32 v159, v159\n"
            "v_pk_fma_f32 v[16:17], v[162:163], v[176:177], v[16:17] op_sel:[1,0,0]\n"
            "v_pk_fma_f32 v[18:19], v[162:163], v[178:179], v[18:19] op_sel:[1,0,0]\n"
            "v_pk_fma_f32 v[20:21], v[166:167], v[180:181], v[20:21] op_sel:[1,0,0]\n"
            "v_pk_fma_f32 v[22:23], v[166:167], v[182:183], v[22:23] op_sel:[1,0,0]\n"
            "v_pk_fma_f32 v[24:25], v[170:171], v[184:185], v[24:25] op_sel:[1,0,0]\n"
            "v_pk_fma_f32 v[26:27], v[170:171], v[186:187], v[26:27] op_sel:[1,0,0]\n"
            "v_pk_fma_f32 v[28:29], v[174:175], v[188:189], v[28:29] op_sel:[1,0,0]\n"
            "v_pk_fma_f32 v[30:31], v[174:175], v[190:191], v[30:31] op_sel:[1,0,0]\n"
            "v_pk_mul_f32 v[152:153], v[152:153], v[136:137]\n"
            "v_pk_mul_f32 v[154:155], v[154:155], v[138:139]\n"
            "v_pk_mul_f32 v[156:157], v[156:157], v[140:141]\n"
            "v_pk_mul_f32 v[158:159], v[158:159], v[142:143]\n"
            "s_sub_u32 s69, s69, 4\n"
            "Lnext_%=:\n"
            "s_sub_u32 s73, s73, 1\n"
            "Ldisp_%=:\n"
            "s_bitcmp1_b32 s60, s73\n"
            "s_cbranch_scc1 Lprio_%=\n"
            "Ld2_%=:\n"
            "s_cmp_gt_u32 s69, 4\n"
            "s_cbranch_scc1 Lfast_%=\n"
            "v_mfma_f32_16x16x32_bf16 v[136:139], v[192:195], v[120:123], 0\n"
            "v_exp_f32 v160, v128\n"
            "v_add_u32 v237, 32, v237\n"
            "v_exp_f32 v161, v129\n"
            "s_waitcnt vmcnt(12) lgkmcnt(0)\n"
            "v_mfma_f32_4x4x4_16b_bf16 v[176:179], v[208:209], v[32:33], 0 cbsz:2\n"
            "v_exp_f32 v162, v130\n"
            "v_mfma_f32_4x4x4_16b_bf16 v[180:183], v[208:209], v[34:35], 0 cbsz:2\n"
            "v_exp_f32 v163, v131\n"
            "v_mfma_f32_4x4x4_16b_bf16 v[184:187], v[208:209], v[36:37], 0 cbsz:2\n"
            "v_exp_f32 v164, v132\n"
            "v_mfma_f32_4x4x4_16b_bf16 v[188:191], v[208:209], v[38:39], 0 cbsz:2\n"
            "v_exp_f32 v165, v133\n"
            "v_add_u32_dpp v219, v196, v214 quad_perm:[0,0,0,0] row_mask:0xf bank_mask:0xf\n"
            "v_mfma_f32_4x4x4_16b_bf16 v[204:207], v[202:203], v[198:199], 0\n"
            "v_exp_f32 v166, v134\n"
            "buffer_load_dwordx4 v[32:35], v219, s[28:31], 0 offen\n"
            "buffer_load_dwordx4 v[36:39], v219, s[28:31], 0 offen offset:256\n"
            "v_exp_f32 v167, v135\n"
            "v_pk_fma_f32 v[0:1], v[144:145], v[176:177], v[0:1] op_sel_hi:[0,1,1]\n"
            "v_pk_fma_f32 v[2:3], v[144:145], v[178:179], v[2:3] op_sel_hi:[0,1,1]\n"
            "v_pk_fma_f32 v[4:5], v[148:149], v[180:181], v[4:5] op_sel_hi:[0,1,1]\n"
            "v_pk_fma_f32 v[6:7], v[148:149], v[182:183], v[6:7] op_sel_hi:[0,1,1]\n"
            "v_pk_fma_f32 v[8:9], v[152:153], v[184:185], v[8:9] op_sel_hi:[0,1,1]\n"
            "v_pk_fma_f32 v[10:11], v[152:153], v[186:187], v[10:11] op_sel_hi:[0,1,1]\n"
            "v_pk_fma_f32 v[12:13], v[156:157], v[188:189], v[12:13] op_sel_hi:[0,1,1]\n"
            "v_pk_fma_f32 v[14:15], v[156:157], v[190:191], v[14:15] op_sel_hi:[0,1,1]\n"
            "v_pk_add_f32 v[160:161], v[160:161], 1.0 op_sel_hi:[1,0]\n"
            "v_pk_add_f32 v[162:163], v[162:163], 1.0 op_sel_hi:[1,0]\n"
            "v_pk_add_f32 v[164:165], v[164:165], 1.0 op_sel_hi:[1,0]\n"
            "v_pk_add_f32 v[166:167], v[166:167], 1.0 op_sel_hi:[1,0]\n"
            "s_cmp_eq_u32 s69, 1\n"
            "s_cbranch_scc1 Lf00_%=\n"
            "Lr00_%=:\n"
            "v_mfma_f32_16x16x32_bf16 v[140:143], v[192:195], v[124:127], 0\n"
            "ds_bpermute_b32 v192, v253, v232\n"
            "ds_bpermute_b32 v193, v253, v233\n"
            "ds_bpermute_b32 v194, v253, v234\n"
            "ds_bpermute_b32 v195, v253, v235\n"
            "ds_read_b32 v230, v211 offset:4\n"
            "ds_read_u16 v210, v237\n"
            "v_rcp_f32 v160, v160\n"
            "v_rcp_f32 v161, v161\n"
            "v_mfma_f32_4x4x4_16b_bf16 v[176:179], v[208:209], v[48:49], 0 cbsz:2 abid:1\n"
            "v_rcp_f32 v162, v162\n"
            "v_mfma_f32_4x4x4_16b_bf16 v[180:183], v[208:209], v[50:51], 0 cbsz:2 abid:1\n"
            "v_rcp_f32 v163, v163\n"
            "v_mfma_f32_4x4x4_16b_bf16 v[184:187], v[208:209], v[52:53], 0 cbsz:2 abid:1\n"
            "v_rcp_f32 v164, v164\n"
            "v_mfma_f32_4x4x4_16b_bf16 v[188:191], v[208:209], v[54:55], 0 cbsz:2 abid:1\n"
            "v_rcp_f32 v165, v165\n"
            "v_add_u32_dpp v220, v196, v214 quad_perm:[1,1,1,1] row_mask:0xf bank_mask:0xf\n"
            "v_mfma_f32_4x4x4_16b_bf16 v[204:207], v[200:201], v[198:199], v[204:207]\n"
            "v_rcp_f32 v166, v166\n"
            "buffer_load_dwordx4 v[48:51], v220, s[28:31], 0 offen\n"
            "buffer_load_dwordx4 v[52:55], v220, s[28:31], 0 offen offset:256\n"
            "v_rcp_f32 v167, v167\n"
            "v_pk_fma_f32 v[0:1], v[144:145], v[176:177], v[0:1] op_sel:[1,0,0]\n"
            "v_pk_fma_f32 v[2:3], v[144:145], v[178:179], v[2:3] op_sel:[1,0,0]\n"
            "v_pk_fma_f32 v[4:5], v[148:149], v[180:181], v[4:5] op_sel:[1,0,0]\n"
            "v_pk_fma_f32 v[6:7], v[148:149], v[182:183], v[6:7] op_sel:[1,0,0]\n"
            "v_pk_fma_f32 v[8:9], v[152:153], v[184:185], v[8:9] op_sel:[1,0,0]\n"
            "v_pk_fma_f32 v[10:11], v[152:153], v[186:187], v[10:11] op_sel:[1,0,0]\n"
            "v_pk_fma_f32 v[12:13], v[156:157], v[188:189], v[12:13] op_sel:[1,0,0]\n"
            "v_pk_fma_f32 v[14:15], v[156:157], v[190:191], v[14:15] op_sel:[1,0,0]\n"
            "v_pk_mul_f32 v[160:161], v[160:161], v[128:129]\n"
            "v_pk_mul_f32 v[162:163], v[162:163], v[130:131]\n"
            "v_pk_mul_f32 v[164:165], v[164:165], v[132:133]\n"
            "v_pk_mul_f32 v[166:167], v[166:167], v[134:135]\n"
            "s_cmp_eq_u32 s69, 2\n"
            "s_cbranch_scc1 Lf01_%=\n"
            "Lr01_%=:\n"
            "s_waitcnt vmcnt(12) lgkmcnt(0)\n"
            "v_add_u32 v230, v230, v229\n"
            "buffer_load_dwordx4 v[232:235], v230, s[24:27], 0 offen\n"
            "s_bitcmp0_b32 s85, 0\n"
            "s_cbranch_scc1 Lnps02_%=\n"
            "s_buffer_load_dwordx2 s[86:87], s[28:31], s67 offset:60\n"
            "s_buffer_load_dwordx2 s[86:87], s[28:31], s67 offset:188\n"
            "s_buffer_load_dwordx2 s[86:87], s[28:31], s67 offset:316\n"
            "s_buffer_load_dwordx2 s[86:87], s[28:31], s67 offset:444\n"
            "s_add_u32 s67, s67, 524288\n"
            "Lnps02_%=:\n"
            "s_lshr_b32 s85, s85, 1\n"
            "v_mfma_f32_16x16x32_bf16 v[128:131], v[192:195], v[96:99], 0\n"
            "v_exp_f32 v168, v136\n"
            "v_exp_f32 v169, v137\n"
            "v_mfma_f32_4x4x4_16b_bf16 v[176:179], v[208:209], v[64:65], 0 cbsz:2 abid:2\n"
            "v_exp_f32 v170, v138\n"
            "v_mfma_f32_4x4x4_16b_bf16 v[180:183], v[208:209], v[66:67], 0 cbsz:2 abid:2\n"
            "v_exp_f32 v171, v139\n"
            "v_mfma_f32_4x4x4_16b_bf16 v[184:187], v[208:209], v[68:69], 0 cbsz:2 abid:2\n"
            "v_exp_f32 v172, v140\n"
            "v_mfma_f32_4x4x4_16b_bf16 v[188:191], v[208:209], v[70:71], 0 cbsz:2 abid:2\n"
            "v_exp_f32 v173, v141\n"
            "v_add_u32_dpp v221, v196, v214 quad_perm:[2,2,2,2] row_mask:0xf bank_mask:0xf\n"
            "v_exp_f32 v174, v142\n"
            "buffer_load_dwordx4 v[64:67], v221, s[28:31], 0 offen\n"
            "buffer_load_dwordx4 v[68:71], v221, s[28:31], 0 offen offset:256\n"
            "v_exp_f32 v175, v143\n"
            "v_pk_fma_f32 v[0:1], v[146:147], v[176:177], v[0:1] op_sel_hi:[0,1,1]\n"
            "v_pk_fma_f32 v[2:3], v[146:147], v[178:179], v[2:3] op_sel_hi:[0,1,1]\n"
            "v_pk_fma_f32 v[4:5], v[150:151], v[180:181], v[4:5] op_sel_hi:[0,1,1]\n"
            "v_pk_fma_f32 v[6:7], v[150:151], v[182:183], v[6:7] op_sel_hi:[0,1,1]\n"
            "v_pk_fma_f32 v[8:9], v[154:155], v[184:185], v[8:9] op_sel_hi:[0,1,1]\n"
            "v_pk_fma_f32 v[10:11], v[154:155], v[186:187], v[10:11] op_sel_hi:[0,1,1]\n"
            "v_pk_fma_f32 v[12:13], v[158:159], v[188:189], v[12:13] op_sel_hi:[0,1,1]\n"
            "v_pk_fma_f32 v[14:15], v[158:159], v[190:191], v[14:15] op_sel_hi:[0,1,1]\n"
            "v_pk_add_f32 v[168:169], v[168:169], 1.0 op_sel_hi:[1,0]\n"
            "v_pk_add_f32 v[170:171], v[170:171], 1.0 op_sel_hi:[1,0]\n"
            "v_pk_add_f32 v[172:173], v[172:173], 1.0 op_sel_hi:[1,0]\n"
            "v_pk_add_f32 v[174:175], v[174:175], 1.0 op_sel_hi:[1,0]\n"
            "v_cvt_pk_bf16_f32 v204, v204, v205\n"
            "v_cvt_pk_bf16_f32 v205, v206, v207\n"
            "ds_write_b64 v212, v[204:205]\n"
            "s_cmp_eq_u32 s69, 3\n"
            "s_cbranch_scc1 Lf02_%=\n"
            "Lr02_%=:\n"
            "v_mfma_f32_16x16x32_bf16 v[132:135], v[192:195], v[100:103], 0\n"
            "v_rcp_f32 v168, v168\n"
            "v_rcp_f32 v169, v169\n"
            "v_mfma_f32_4x4x4_16b_bf16 v[176:179], v[208:209], v[80:81], 0 cbsz:2 abid:3\n"
            "v_rcp_f32 v170, v170\n"
            "v_mfma_f32_4x4x4_16b_bf16 v[180:183], v[208:209], v[82:83], 0 cbsz:2 abid:3\n"
            "v_rcp_f32 v171, v171\n"
            "v_mfma_f32_4x4x4_16b_bf16 v[184:187], v[208:209], v[84:85], 0 cbsz:2 abid:3\n"
            "v_rcp_f32 v172, v172\n"
            "v_mfma_f32_4x4x4_16b_bf16 v[188:191], v[208:209], v[86:87], 0 cbsz:2 abid:3\n"
            "v_rcp_f32 v173, v173\n"
            "v_add_u32_dpp v222, v196, v214 quad_perm:[3,3,3,3] row_mask:0xf bank_mask:0xf\n"
            "v_rcp_f32 v174, v174\n"
            "buffer_load_dwordx4 v[80:83], v222, s[28:31], 0 offen\n"
            "buffer_load_dwordx4 v[84:87], v222, s[28:31], 0 offen offset:256\n"
            "v_rcp_f32 v175, v175\n"
            "v_pk_fma_f32 v[0:1], v[146:147], v[176:177], v[0:1] op_sel:[1,0,0]\n"
            "v_pk_fma_f32 v[2:3], v[146:147], v[178:179], v[2:3] op_sel:[1,0,0]\n"
            "v_pk_fma_f32 v[4:5], v[150:151], v[180:181], v[4:5] op_sel:[1,0,0]\n"
            "v_pk_fma_f32 v[6:7], v[150:151], v[182:183], v[6:7] op_sel:[1,0,0]\n"
            "v_pk_fma_f32 v[8:9], v[154:155], v[184:185], v[8:9] op_sel:[1,0,0]\n"
            "v_pk_fma_f32 v[10:11], v[154:155], v[186:187], v[10:11] op_sel:[1,0,0]\n"
            "v_pk_fma_f32 v[12:13], v[158:159], v[188:189], v[12:13] op_sel:[1,0,0]\n"
            "v_pk_fma_f32 v[14:15], v[158:159], v[190:191], v[14:15] op_sel:[1,0,0]\n"
            "v_pk_mul_f32 v[168:169], v[168:169], v[136:137]\n"
            "v_pk_mul_f32 v[170:171], v[170:171], v[138:139]\n"
            "v_pk_mul_f32 v[172:173], v[172:173], v[140:141]\n"
            "v_pk_mul_f32 v[174:175], v[174:175], v[142:143]\n"
            "s_cmp_eq_u32 s69, 4\n"
            "s_cbranch_scc1 Lf03_%=\n"
            "Lr03_%=:\n"
            "v_add_u32 v238, 32, v238\n"
            "ds_read_b128 v[196:199], v210\n"
            "ds_read_u16 v211, v238\n"
            "v_mfma_f32_16x16x32_bf16 v[136:139], v[192:195], v[104:107], 0\n"
            "v_exp_f32 v144, v128\n"
            "v_exp_f32 v145, v129\n"
            "s_waitcnt vmcnt(13)\n"
            "v_mfma_f32_4x4x4_16b_bf16 v[176:179], v[208:209], v[40:41], 0 cbsz:2\n"
            "v_exp_f32 v146, v130\n"
            "v_mfma_f32_4x4x4_16b_bf16 v[180:183], v[208:209], v[42:43], 0 cbsz:2\n"
            "v_exp_f32 v147, v131\n"
            "v_mfma_f32_4x4x4_16b_bf16 v[184:187], v[208:209], v[44:45], 0 cbsz:2\n"
            "v_exp_f32 v148, v132\n"
            "v_mfma_f32_4x4x4_16b_bf16 v[188:191], v[208:209], v[46:47], 0 cbsz:2\n"
            "v_exp_f32 v149, v133\n"
            "v_exp_f32 v150, v134\n"
            "buffer_load_dwordx4 v[40:43], v219, s[28:31], 0 offen offset:512\n"
            "buffer_load_dwordx4 v[44:47], v219, s[28:31], 0 offen offset:768\n"
            "v_exp_f32 v151, v135\n"
            "v_pk_fma_f32 v[16:17], v[160:161], v[176:177], v[16:17] op_sel_hi:[0,1,1]\n"
            "v_pk_fma_f32 v[18:19], v[160:161], v[178:179], v[18:19] op_sel_hi:[0,1,1]\n"
            "v_pk_fma_f32 v[20:21], v[164:165], v[180:181], v[20:21] op_sel_hi:[0,1,1]\n"
            "v_pk_fma_f32 v[22:23], v[164:165], v[182:183], v[22:23] op_sel_hi:[0,1,1]\n"
            "v_pk_fma_f32 v[24:25], v[168:169], v[184:185], v[24:25] op_sel_hi:[0,1,1]\n"
            "v_pk_fma_f32 v[26:27], v[168:169], v[186:187], v[26:27] op_sel_hi:[0,1,1]\n"
            "v_pk_fma_f32 v[28:29], v[172:173], v[188:189], v[28:29] op_sel_hi:[0,1,1]\n"
            "v_pk_fma_f32 v[30:31], v[172:173], v[190:191], v[30:31] op_sel_hi:[0,1,1]\n"
            "v_pk_add_f32 v[144:145], v[144:145], 1.0 op_sel_hi:[1,0]\n"
            "v_pk_add_f32 v[146:147], v[146:147], 1.0 op_sel_hi:[1,0]\n"
            "v_pk_add_f32 v[148:149], v[148:149], 1.0 op_sel_hi:[1,0]\n"
            "v_pk_add_f32 v[150:151], v[150:151], 1.0 op_sel_hi:[1,0]\n"
            "s_cmp_eq_u32 s69, 1\n"
            "s_cbranch_scc1 Lf10_%=\n"
            "Lr10_%=:\n"
            "v_mfma_f32_16x16x32_bf16 v[140:143], v[192:195], v[108:111], 0\n"
            "v_rcp_f32 v144, v144\n"
            "v_rcp_f32 v145, v145\n"
            "v_mfma_f32_4x4x4_16b_bf16 v[176:179], v[208:209], v[56:57], 0 cbsz:2 abid:1\n"
            "v_rcp_f32 v146, v146\n"
            "v_mfma_f32_4x4x4_16b_bf16 v[180:183], v[208:209], v[58:59], 0 cbsz:2 abid:1\n"
            "v_rcp_f32 v147, v147\n"
            "v_mfma_f32_4x4x4_16b_bf16 v[184:187], v[208:209], v[60:61], 0 cbsz:2 abid:1\n"
            "v_rcp_f32 v148, v148\n"
            "v_mfma_f32_4x4x4_16b_bf16 v[188:191], v[208:209], v[62:63], 0 cbsz:2 abid:1\n"
            "v_rcp_f32 v149, v149\n"
            "v_rcp_f32 v150, v150\n"
            "buffer_load_dwordx4 v[56:59], v220, s[28:31], 0 offen offset:512\n"
            "buffer_load_dwordx4 v[60:63], v220, s[28:31], 0 offen offset:768\n"
            "v_rcp_f32 v151, v151\n"
            "v_pk_fma_f32 v[16:17], v[160:161], v[176:177], v[16:17] op_sel:[1,0,0]\n"
            "v_pk_fma_f32 v[18:19], v[160:161], v[178:179], v[18:19] op_sel:[1,0,0]\n"
            "v_pk_fma_f32 v[20:21], v[164:165], v[180:181], v[20:21] op_sel:[1,0,0]\n"
            "v_pk_fma_f32 v[22:23], v[164:165], v[182:183], v[22:23] op_sel:[1,0,0]\n"
            "v_pk_fma_f32 v[24:25], v[168:169], v[184:185], v[24:25] op_sel:[1,0,0]\n"
            "v_pk_fma_f32 v[26:27], v[168:169], v[186:187], v[26:27] op_sel:[1,0,0]\n"
            "v_pk_fma_f32 v[28:29], v[172:173], v[188:189], v[28:29] op_sel:[1,0,0]\n"
            "v_pk_fma_f32 v[30:31], v[172:173], v[190:191], v[30:31] op_sel:[1,0,0]\n"
            "v_pk_mul_f32 v[144:145], v[144:145], v[128:129]\n"
            "v_pk_mul_f32 v[146:147], v[146:147], v[130:131]\n"
            "v_pk_mul_f32 v[148:149], v[148:149], v[132:133]\n"
            "v_pk_mul_f32 v[150:151], v[150:151], v[134:135]\n"
            "s_cmp_eq_u32 s69, 2\n"
            "s_cbranch_scc1 Lf11_%=\n"
            "Lr11_%=:\n"
            "v_mfma_f32_16x16x32_bf16 v[128:131], v[192:195], v[112:115], 0\n"
            "v_exp_f32 v152, v136\n"
            "v_exp_f32 v153, v137\n"
            "s_waitcnt vmcnt(13)\n"
            "v_mfma_f32_4x4x4_16b_bf16 v[176:179], v[208:209], v[72:73], 0 cbsz:2 abid:2\n"
            "v_exp_f32 v154, v138\n"
            "v_mfma_f32_4x4x4_16b_bf16 v[180:183], v[208:209], v[74:75], 0 cbsz:2 abid:2\n"
            "v_exp_f32 v155, v139\n"
            "v_mfma_f32_4x4x4_16b_bf16 v[184:187], v[208:209], v[76:77], 0 cbsz:2 abid:2\n"
            "v_exp_f32 v156, v140\n"
            "v_mfma_f32_4x4x4_16b_bf16 v[188:191], v[208:209], v[78:79], 0 cbsz:2 abid:2\n"
            "v_exp_f32 v157, v141\n"
            "v_exp_f32 v158, v142\n"
            "buffer_load_dwordx4 v[72:75], v221, s[28:31], 0 offen offset:512\n"
            "buffer_load_dwordx4 v[76:79], v221, s[28:31], 0 offen offset:768\n"
            "v_exp_f32 v159, v143\n"
            "v_pk_fma_f32 v[16:17], v[162:163], v[176:177], v[16:17] op_sel_hi:[0,1,1]\n"
            "v_pk_fma_f32 v[18:19], v[162:163], v[178:179], v[18:19] op_sel_hi:[0,1,1]\n"
            "v_pk_fma_f32 v[20:21], v[166:167], v[180:181], v[20:21] op_sel_hi:[0,1,1]\n"
            "v_pk_fma_f32 v[22:23], v[166:167], v[182:183], v[22:23] op_sel_hi:[0,1,1]\n"
            "v_pk_fma_f32 v[24:25], v[170:171], v[184:185], v[24:25] op_sel_hi:[0,1,1]\n"
            "v_pk_fma_f32 v[26:27], v[170:171], v[186:187], v[26:27] op_sel_hi:[0,1,1]\n"
            "v_pk_fma_f32 v[28:29], v[174:175], v[188:189], v[28:29] op_sel_hi:[0,1,1]\n"
            "v_pk_fma_f32 v[30:31], v[174:175], v[190:191], v[30:31] op_sel_hi:[0,1,1]\n"
            "v_pk_add_f32 v[152:153], v[152:153], 1.0 op_sel_hi:[1,0]\n"
            "v_pk_add_f32 v[154:155], v[154:155], 1.0 op_sel_hi:[1,0]\n"
            "v_pk_add_f32 v[156:157], v[156:157], 1.0 op_sel_hi:[1,0]\n"
            "v_pk_add_f32 v[158:159], v[158:159], 1.0 op_sel_hi:[1,0]\n"
            "s_cmp_eq_u32 s69, 3\n"
            "s_cbranch_scc1 Lf12_%=\n"
            "Lr12_%=:\n"
            "v_mfma_f32_16x16x32_bf16 v[132:135], v[192:195], v[116:119], 0\n"
            "v_rcp_f32 v152, v152\n"
            "v_rcp_f32 v153, v153\n"
            "v_mfma_f32_4x4x4_16b_bf16 v[176:179], v[208:209], v[88:89], 0 cbsz:2 abid:3\n"
            "v_rcp_f32 v154, v154\n"
            "v_mfma_f32_4x4x4_16b_bf16 v[180:183], v[208:209], v[90:91], 0 cbsz:2 abid:3\n"
            "v_rcp_f32 v155, v155\n"
            "v_mfma_f32_4x4x4_16b_bf16 v[184:187], v[208:209], v[92:93], 0 cbsz:2 abid:3\n"
            "v_rcp_f32 v156, v156\n"
            "v_mfma_f32_4x4x4_16b_bf16 v[188:191], v[208:209], v[94:95], 0 cbsz:2 abid:3\n"
            "ds_read_b64 v[208:209], v213\n"
            "v_rcp_f32 v157, v157\n"
            "v_rcp_f32 v158, v158\n"
            "buffer_load_dwordx4 v[88:91], v222, s[28:31], 0 offen offset:512\n"
            "buffer_load_dwordx4 v[92:95], v222, s[28:31], 0 offen offset:768\n"
            "v_rcp_f32 v159, v159\n"
            "v_pk_fma_f32 v[16:17], v[162:163], v[176:177], v[16:17] op_sel:[1,0,0]\n"
            "v_pk_fma_f32 v[18:19], v[162:163], v[178:179], v[18:19] op_sel:[1,0,0]\n"
            "v_pk_fma_f32 v[20:21], v[166:167], v[180:181], v[20:21] op_sel:[1,0,0]\n"
            "v_pk_fma_f32 v[22:23], v[166:167], v[182:183], v[22:23] op_sel:[1,0,0]\n"
            "v_pk_fma_f32 v[24:25], v[170:171], v[184:185], v[24:25] op_sel:[1,0,0]\n"
            "v_pk_fma_f32 v[26:27], v[170:171], v[186:187], v[26:27] op_sel:[1,0,0]\n"
            "v_pk_fma_f32 v[28:29], v[174:175], v[188:189], v[28:29] op_sel:[1,0,0]\n"
            "v_pk_fma_f32 v[30:31], v[174:175], v[190:191], v[30:31] op_sel:[1,0,0]\n"
            "v_pk_mul_f32 v[152:153], v[152:153], v[136:137]\n"
            "v_pk_mul_f32 v[154:155], v[154:155], v[138:139]\n"
            "v_pk_mul_f32 v[156:157], v[156:157], v[140:141]\n"
            "v_pk_mul_f32 v[158:159], v[158:159], v[142:143]\n"
            "s_cmp_eq_u32 s69, 4\n"
            "s_cbranch_scc1 Lf13_%=\n"
            "Lr13_%=:\n"
            "s_mov_b32 s81, s69\n"
            "s_and_b32 s68, s72, 0xff\n"
            "s_add_u32 s69, s69, s68\n"
            "s_lshr_b32 s72, s72, 8\n"
            "v_mov_b32 v231, v239\n"
            "v_mov_b32 v239, v252\n"
            "s_and_b32 s68, s62, 14\n"
            "s_lshr_b32 s62, s62, 4\n"
            "s_cmp_lg_u32 s68, 0\n"
            "s_cbranch_scc1 Lb1_%=\n"
            "v_mov_b32 v215, v216\n"
            "s_branch Lnext_%=\n"
            "Lb1_%=:\n"
            "s_cmp_lg_u32 s68, 6\n"
            "s_cbranch_scc1 Lb2_%=\n"
            "s_lshl_b32 s68, s3, 2\n"
            "v_mov_b32 v176, s68\n"
            "ds_write_b32 v176, v176\n"
            "s_branch Lnext_%=\n"
            "Lb2_%=:\n"
            "s_branch Lnext_%=\n"
            "Lpub_%=:\n"
            "s_waitcnt vmcnt(0)\n"
            "v_mov_b32 v176, 4\n"
            "v_mov_b32 v177, 1\n"
            "s_mov_b64 exec, 1\n"
            "ds_add_rtn_u32 v177, v176, v177\n"
            "s_waitcnt lgkmcnt(0)\n"
            "v_readfirstlane_b32 s68, v177\n"
            "s_mov_b64 exec, 0xff\n"
            "v_mov_b32 v176, s90\n"
            "v_mov_b32 v177, s90\n"
            "v_mov_b32 v178, s90\n"
            "v_mov_b32 v179, s90\n"
            "s_cmp_lg_u32 s68, 7\n"
            "s_cbranch_scc1 Lpub2_%=\n"
            "global_store_dwordx4 v214, v[176:179], s[56:57] sc1\n"
            "Lpub2_%=:\n"
            "s_mov_b64 exec, -1\n"
            "s_nop 0\n"
            "s_setpc_b64 s[70:71]\n"
            "Lprio_%=:\n"
            "s_cmp_eq_u32 s73, 1\n"
            "s_cbranch_scc1 Llast_%=\n"
            "s_lshl_b32 s68, s73, 1\n"
            "s_lshr_b32 s68, s61, s68\n"
            "s_bitcmp1_b32 s68, 1\n"
            "s_cbranch_scc1 Lq2_%=\n"
            "s_bitcmp1_b32 s68, 0\n"
            "s_setprio 0\n"
            "s_cbranch_scc0 Lq9_%=\n"
            "s_setprio 1\n"
            "s_branch Lq9_%=\n"
            "Lq2_%=:\n"
            "s_bitcmp1_b32 s68, 0\n"
            "s_setprio 2\n"
            "s_cbranch_scc0 Lq9_%=\n"
            "s_setprio 3\n"
            "Lq9_%=:\n"
            "s_cmp_lg_u32 s73, 2\n"
            "s_cbranch_scc1 Ld2_%=\n"
            "v_mov_b32 v223, 0\n"
            "global_load_dword v223, v223, s[58:59] sc1\n"
            "s_branch Ld2_%=\n"
            "Lf00_%=:\n"
            "s_call_b64 s[70:71], Lflush0_%=\n"
            "s_branch Lr00_%=\n"
            "Lf01_%=:\n"
            "s_call_b64 s[70:71], Lflush0_%=\n"
            "s_branch Lr01_%=\n"
            "Lf02_%=:\n"
            "s_call_b64 s[70:71], Lflush0_%=\n"
            "s_branch Lr02_%=\n"
            "Lf03_%=:\n"
            "s_call_b64 s[70:71], Lflush0_%=\n"
            "s_branch Lr03_%=\n"
            "Lflush0_%=:\n"
            "s_bitcmp0_b32 s62, 0\n"
            "s_cbranch_scc1 Lfa0_%=\n"
            "Lfw0_%=:\n"
            "v_mov_b32 v184, s41\n"
            "ds_read_b32 v184, v184\n"
            "ds_read_b128 v[176:179], v231 offset:0\n"
            "ds_read_b128 v[180:183], v231 offset:1024\n"
            "s_waitcnt lgkmcnt(0)\n"
            "v_readfirstlane_b32 s68, v184\n"
            "s_cmp_eq_u32 s68, 0\n"
            "s_cbranch_scc1 Lfw0_%=\n"
            "v_mfma_f32_4x4x4_16b_bf16 v[0:3], v[224:225], v[176:177], v[0:3]\n"
            "v_mfma_f32_4x4x4_16b_bf16 v[4:7], v[224:225], v[178:179], v[4:7]\n"
            "v_mfma_f32_4x4x4_16b_bf16 v[8:11], v[224:225], v[180:181], v[8:11]\n"
            "v_mfma_f32_4x4x4_16b_bf16 v[12:15], v[224:225], v[182:183], v[12:15]\n"
            "s_nop 1\n"
            "Lfa0_%=:\n"
            "s_cmp_lg_u32 s72, 0xff\n"
            "s_cbranch_scc1 Lfb0_%=\n"
            "ds_read_b128 v[184:187], v236 offset:0\n"
            "ds_read_b128 v[188:191], v236 offset:1024\n"
            "Lfb0_%=:\n"
            "v_cvt_pk_bf16_f32 v176, v0, v1\n"
            "v_cvt_pk_bf16_f32 v177, v2, v3\n"
            "v_cvt_pk_bf16_f32 v178, v4, v5\n"
            "v_cvt_pk_bf16_f32 v179, v6, v7\n"
            "v_cvt_pk_bf16_f32 v180, v8, v9\n"
            "v_cvt_pk_bf16_f32 v181, v10, v11\n"
            "v_cvt_pk_bf16_f32 v182, v12, v13\n"
            "v_cvt_pk_bf16_f32 v183, v14, v15\n"
            "s_bitcmp1_b32 s62, 1\n"
            "s_cbranch_scc1 Lpark0_%=\n"
            "buffer_store_dwordx4 v[176:179], v215, s[20:23], 0 offen offset:0 sc1\n"
            "buffer_store_dwordx4 v[180:183], v215, s[20:23], 0 offen offset:256 sc1\n"
            "s_branch Lzero0_%=\n"
            "Lpark0_%=:\n"
            "ds_write_b128 v231, v[176:179] offset:0\n"
            "ds_write_b128 v231, v[180:183] offset:1024\n"
            "Lzero0_%=:\n"
            "s_cmp_lg_u32 s72, 0xff\n"
            "s_cbranch_scc1 Lzz0_%=\n"
            "s_waitcnt lgkmcnt(0)\n"
            "v_mfma_f32_4x4x4_16b_bf16 v[0:3], v[224:225], v[184:185], 0\n"
            "v_mfma_f32_4x4x4_16b_bf16 v[4:7], v[224:225], v[186:187], 0\n"
            "v_mfma_f32_4x4x4_16b_bf16 v[8:11], v[224:225], v[188:189], 0\n"
            "v_mfma_f32_4x4x4_16b_bf16 v[12:15], v[224:225], v[190:191], 0\n"
            "s_setpc_b64 s[70:71]\n"
            "Lzz0_%=:\n"
            "v_mov_b64 v[0:1], 0\n"
            "v_mov_b64 v[2:3], 0\n"
            "v_mov_b64 v[4:5], 0\n"
            "v_mov_b64 v[6:7], 0\n"
            "v_mov_b64 v[8:9], 0\n"
            "v_mov_b64 v[10:11], 0\n"
            "v_mov_b64 v[12:13], 0\n"
            "v_mov_b64 v[14:15], 0\n"
            "s_setpc_b64 s[70:71]\n"
            "Lf10_%=:\n"
            "s_call_b64 s[70:71], Lflush1_%=\n"
            "s_branch Lr10_%=\n"
            "Lf11_%=:\n"
            "s_call_b64 s[70:71], Lflush1_%=\n"
            "s_branch Lr11_%=\n"
            "Lf12_%=:\n"
            "s_call_b64 s[70:71], Lflush1_%=\n"
            "s_branch Lr12_%=\n"
            "Lf13_%=:\n"
            "s_call_b64 s[70:71], Lflush1_%=\n"
            "s_branch Lr13_%=\n"
            "Lflush1_%=:\n"
            "s_bitcmp0_b32 s62, 0\n"
            "s_cbranch_scc1 Lfa1_%=\n"
            "Lfw1_%=:\n"
            "v_mov_b32 v184, s41\n"
            "ds_read_b32 v184, v184\n"
            "ds_read_b128 v[176:179], v231 offset:2048\n"
            "ds_read_b128 v[180:183], v231 offset:3072\n"
            "s_waitcnt lgkmcnt(0)\n"
            "v_readfirstlane_b32 s68, v184\n"
            "s_cmp_eq_u32 s68, 0\n"
            "s_cbranch_scc1 Lfw1_%=\n"
            "v_mfma_f32_4x4x4_16b_bf16 v[16:19], v[224:225], v[176:177], v[16:19]\n"
            "v_mfma_f32_4x4x4_16b_bf16 v[20:23], v[224:225], v[178:179], v[20:23]\n"
            "v_mfma_f32_4x4x4_16b_bf16 v[24:27], v[224:225], v[180:181], v[24:27]\n"
            "v_mfma_f32_4x4x4_16b_bf16 v[28:31], v[224:225], v[182:183], v[28:31]\n"
            "s_nop 1\n"
            "Lfa1_%=:\n"
            "s_cmp_lg_u32 s72, 0xff\n"
            "s_cbranch_scc1 Lfb1_%=\n"
            "ds_read_b128 v[184:187], v236 offset:2048\n"
            "ds_read_b128 v[188:191], v236 offset:3072\n"
            "Lfb1_%=:\n"
            "v_cvt_pk_bf16_f32 v176, v16, v17\n"
            "v_cvt_pk_bf16_f32 v177, v18, v19\n"
            "v_cvt_pk_bf16_f32 v178, v20, v21\n"
            "v_cvt_pk_bf16_f32 v179, v22, v23\n"
            "v_cvt_pk_bf16_f32 v180, v24, v25\n"
            "v_cvt_pk_bf16_f32 v181, v26, v27\n"
            "v_cvt_pk_bf16_f32 v182, v28, v29\n"
            "v_cvt_pk_bf16_f32 v183, v30, v31\n"
            "s_bitcmp1_b32 s62, 1\n"
            "s_cbranch_scc1 Lpark1_%=\n"
            "buffer_store_dwordx4 v[176:179], v215, s[20:23], 0 offen offset:512 sc1\n"
            "buffer_store_dwordx4 v[180:183], v215, s[20:23], 0 offen offset:768 sc1\n"
            "s_branch Lzero1_%=\n"
            "Lpark1_%=:\n"
            "ds_write_b128 v231, v[176:179] offset:2048\n"
            "ds_write_b128 v231, v[180:183] offset:3072\n"
            "Lzero1_%=:\n"
            "s_cmp_lg_u32 s72, 0xff\n"
            "s_cbranch_scc1 Lzz1_%=\n"
            "s_waitcnt lgkmcnt(0)\n"
            "v_mfma_f32_4x4x4_16b_bf16 v[16:19], v[224:225], v[184:185], 0\n"
            "v_mfma_f32_4x4x4_16b_bf16 v[20:23], v[224:225], v[186:187], 0\n"
            "v_mfma_f32_4x4x4_16b_bf16 v[24:27], v[224:225], v[188:189], 0\n"
            "v_mfma_f32_4x4x4_16b_bf16 v[28:31], v[224:225], v[190:191], 0\n"
            "s_bitcmp1_b32 s62, 3\n"
            "s_cbranch_scc1 Lpub_%=\n"
            "s_setpc_b64 s[70:71]\n"
            "Lzz1_%=:\n"
            "v_mov_b64 v[16:17], 0\n"
            "v_mov_b64 v[18:19], 0\n"
            "v_mov_b64 v[20:21], 0\n"
            "v_mov_b64 v[22:23], 0\n"
            "v_mov_b64 v[24:25], 0\n"
            "v_mov_b64 v[26:27], 0\n"
            "v_mov_b64 v[28:29], 0\n"
            "v_mov_b64 v[30:31], 0\n"
            "s_bitcmp1_b32 s62, 3\n"
            "s_cbranch_scc1 Lpub_%=\n"
            "s_setpc_b64 s[70:71]\n"
            "Llk_%=:\n"
            "s_waitcnt vmcnt(4)\n"
            "v_readfirstlane_b32 s73, v223\n"
            "s_cmp_lg_u32 s73, s90\n"
            "s_cbranch_scc1 Llk2_%=\n"
            "buffer_load_dwordx4 v[32:35], v217, s[20:23], s63 offen offset:0 sc1\n"
            "buffer_load_dwordx4 v[36:39], v217, s[20:23], s63 offen offset:256 sc1\n"
            "buffer_load_dwordx4 v[48:51], v217, s[20:23], s63 offen offset:512 sc1\n"
            "buffer_load_dwordx4 v[52:55], v217, s[20:23], s63 offen offset:768 sc1\n"
            "s_branch Llkr_%=\n"
            "Llk2_%=:\n"
            "v_mov_b32 v223, 0\n"
            "global_load_dword v223, v223, s[58:59] sc1\n"
            "s_branch Llkr_%=\n"
            "Lspin_%=:\n"
            "s_waitcnt vmcnt(0)\n"
            "s_mov_b32 s72, 0x400000\n"
            "Lsp1_%=:\n"
            "v_readfirstlane_b32 s73, v223\n"
            "s_sub_u32 s72, s72, 1\n"
            "s_cmp_eq_u32 s72, 0\n"
            "s_cbranch_scc1 Lsp2_%=\n"
            "s_cmp_eq_u32 s73, s90\n"
            "s_cbranch_scc1 Lsp2_%=\n"
            "s_sleep 1\n"
            "v_mov_b32 v223, 0\n"
            "global_load_dword v223, v223, s[58:59] sc1\n"
            "s_waitcnt vmcnt(0)\n"
            "s_branch Lsp1_%=\n"
            "Lsp2_%=:\n"
            "buffer_load_dwordx4 v[32:35], v217, s[20:23], s63 offen offset:0 sc1\n"
            "buffer_load_dwordx4 v[36:39], v217, s[20:23], s63 offen offset:256 sc1\n"
            "buffer_load_dwordx4 v[48:51], v217, s[20:23], s63 offen offset:512 sc1\n"
            "buffer_load_dwordx4 v[52:55], v217, s[20:23], s63 offen offset:768 sc1\n"
            "s_branch Lgo_%=\n"
            "Llast_%=:\n"
            "s_waitcnt vmcnt(16)\n"
            "v_readfirstlane_b32 s73, v223\n"
            "v_mov_b32 v222, v217\n"
            "s_cmp_eq_u32 s73, s90\n"
            "s_cbranch_scc1 Lp4_%=\n"
            "v_bfrev_b32 v222, 1\n"
            "v_mov_b32 v223, 0\n"
            "global_load_dword v223, v223, s[58:59] sc1\n"
            "Lp4_%=:\n"
            "v_mfma_f32_16x16x32_bf16 v[136:139], v[192:195], v[120:123], 0\n"
            "v_exp_f32 v160, v128\n"
            "v_exp_f32 v161, v129\n"
            "s_waitcnt vmcnt(13) lgkmcnt(0)\n"
            "v_mfma_f32_4x4x4_16b_bf16 v[176:179], v[208:209], v[32:33], 0 cbsz:2\n"
            "v_exp_f32 v162, v130\n"
            "v_mfma_f32_4x4x4_16b_bf16 v[180:183], v[208:209], v[34:35], 0 cbsz:2\n"
            "v_exp_f32 v163, v131\n"
            "v_mfma_f32_4x4x4_16b_bf16 v[184:187], v[208:209], v[36:37], 0 cbsz:2\n"
            "v_exp_f32 v164, v132\n"
            "v_mfma_f32_4x4x4_16b_bf16 v[188:191], v[208:209], v[38:39], 0 cbsz:2\n"
            "v_exp_f32 v165, v133\n"
            "v_exp_f32 v166, v134\n"
            "buffer_load_dwordx4 v[32:35], v222, s[20:23], s63 offen offset:0 sc1\n"
            "buffer_load_dwordx4 v[36:39], v222, s[20:23], s63 offen offset:256 sc1\n"
            "ds_read_b128 v[236:239], v228 offset:0\n"
            "ds_read_b128 v[240:243], v228 offset:1024\n"
            "ds_read_b128 v[244:247], v228 offset:2048\n"
            "ds_read_b128 v[248:251], v228 offset:3072\n"
            "ds_read_b128 v[252:255], v228 offset:4096\n"
            "ds_read_b128 v[96:99], v228 offset:5120\n"
            "v_exp_f32 v167, v135\n"
            "v_pk_fma_f32 v[0:1], v[144:145], v[176:177], v[0:1] op_sel_hi:[0,1,1]\n"
            "v_pk_fma_f32 v[2:3], v[144:145], v[178:179], v[2:3] op_sel_hi:[0,1,1]\n"
            "v_pk_fma_f32 v[4:5], v[148:149], v[180:181], v[4:5] op_sel_hi:[0,1,1]\n"
            "v_pk_fma_f32 v[6:7], v[148:149], v[182:183], v[6:7] op_sel_hi:[0,1,1]\n"
            "v_pk_fma_f32 v[8:9], v[152:153], v[184:185], v[8:9] op_sel_hi:[0,1,1]\n"
            "v_pk_fma_f32 v[10:11], v[152:153], v[186:187], v[10:11] op_sel_hi:[0,1,1]\n"
            "v_pk_fma_f32 v[12:13], v[156:157], v[188:189], v[12:13] op_sel_hi:[0,1,1]\n"
            "v_pk_fma_f32 v[14:15], v[156:157], v[190:191], v[14:15] op_sel_hi:[0,1,1]\n"
            "v_pk_add_f32 v[160:161], v[160:161], 1.0 op_sel_hi:[1,0]\n"
            "v_pk_add_f32 v[162:163], v[162:163], 1.0 op_sel_hi:[1,0]\n"
            "v_pk_add_f32 v[164:165], v[164:165], 1.0 op_sel_hi:[1,0]\n"
            "v_pk_add_f32 v[166:167], v[166:167], 1.0 op_sel_hi:[1,0]\n"
            "v_mfma_f32_16x16x32_bf16 v[140:143], v[192:195], v[124:127], 0\n"
            "v_rcp_f32 v160, v160\n"
            "v_rcp_f32 v161, v161\n"
            "v_mfma_f32_4x4x4_16b_bf16 v[176:179], v[208:209], v[48:49], 0 cbsz:2 abid:1\n"
            "v_rcp_f32 v162, v162\n"
            "v_mfma_f32_4x4x4_16b_bf16 v[180:183], v[208:209], v[50:51], 0 cbsz:2 abid:1\n"
            "v_rcp_f32 v163, v163\n"
            "v_mfma_f32_4x4x4_16b_bf16 v[184:187], v[208:209], v[52:53], 0 cbsz:2 abid:1\n"
            "v_rcp_f32 v164, v164\n"
            "v_mfma_f32_4x4x4_16b_bf16 v[188:191], v[208:209], v[54:55], 0 cbsz:2 abid:1\n"
            "v_rcp_f32 v165, v165\n"
            "v_rcp_f32 v166, v166\n"
            "buffer_load_dwordx4 v[48:51], v222, s[20:23], s63 offen offset:512 sc1\n"
            "buffer_load_dwordx4 v[52:55], v222, s[20:23], s63 offen offset:768 sc1\n"
            "ds_read_b128 v[100:103], v228 offset:6144\n"
            "ds_read_b128 v[104:107], v228 offset:7168\n"
            "ds_read_b128 v[108:111], v228 offset:8192\n"
            "ds_read_b128 v[112:115], v228 offset:9216\n"
            "ds_read_b128 v[116:119], v228 offset:10240\n"
            "ds_read_b128 v[196:199], v228 offset:11264\n"
            "v_rcp_f32 v167, v167\n"
            "v_pk_fma_f32 v[0:1], v[144:145], v[176:177], v[0:1] op_sel:[1,0,0]\n"
            "v_pk_fma_f32 v[2:3], v[144:145], v[178:179], v[2:3] op_sel:[1,0,0]\n"
            "v_pk_fma_f32 v[4:5], v[148:149], v[180:181], v[4:5] op_sel:[1,0,0]\n"
            "v_pk_fma_f32 v[6:7], v[148:149], v[182:183], v[6:7] op_sel:[1,0,0]\n"
            "v_pk_fma_f32 v[8:9], v[152:153], v[184:185], v[8:9] op_sel:[1,0,0]\n"
            "v_pk_fma_f32 v[10:11], v[152:153], v[186:187], v[10:11] op_sel:[1,0,0]\n"
            "v_pk_fma_f32 v[12:13], v[156:157], v[188:189], v[12:13] op_sel:[1,0,0]\n"
            "v_pk_fma_f32 v[14:15], v[156:157], v[190:191], v[14:15] op_sel:[1,0,0]\n"
            "v_pk_mul_f32 v[160:161], v[160:161], v[128:129]\n"
            "v_pk_mul_f32 v[162:163], v[162:163], v[130:131]\n"
            "v_pk_mul_f32 v[164:165], v[164:165], v[132:133]\n"
            "v_pk_mul_f32 v[166:167], v[166:167], v[134:135]\n"
            "v_exp_f32 v168, v136\n"
            "v_exp_f32 v169, v137\n"
            "s_waitcnt vmcnt(12)\n"
            "v_mfma_f32_4x4x4_16b_bf16 v[176:179], v[208:209], v[64:65], 0 cbsz:2 abid:2\n"
            "v_exp_f32 v170, v138\n"
            "v_mfma_f32_4x4x4_16b_bf16 v[180:183], v[208:209], v[66:67], 0 cbsz:2 abid:2\n"
            "v_exp_f32 v171, v139\n"
            "v_mfma_f32_4x4x4_16b_bf16 v[184:187], v[208:209], v[68:69], 0 cbsz:2 abid:2\n"
            "v_exp_f32 v172, v140\n"
            "v_mfma_f32_4x4x4_16b_bf16 v[188:191], v[208:209], v[70:71], 0 cbsz:2 abid:2\n"
            "v_exp_f32 v173, v141\n"
            "v_exp_f32 v174, v142\n"
            "ds_read_b128 v[200:203], v228 offset:12288\n"
            "ds_read_b128 v[204:207], v228 offset:13312\n"
            "ds_read_b128 v[232:235], v228 offset:14336\n"
            "ds_read_b128 v[120:123], v228 offset:15360\n"
            "ds_read_b128 v[64:67], v228 offset:16384\n"
            "ds_read_b128 v[68:71], v228 offset:17408\n"
            "v_exp_f32 v175, v143\n"
            "v_pk_fma_f32 v[0:1], v[146:147], v[176:177], v[0:1] op_sel_hi:[0,1,1]\n"
            "v_pk_fma_f32 v[2:3], v[146:147], v[178:179], v[2:3] op_sel_hi:[0,1,1]\n"
            "v_pk_fma_f32 v[4:5], v[150:151], v[180:181], v[4:5] op_sel_hi:[0,1,1]\n"
            "v_pk_fma_f32 v[6:7], v[150:151], v[182:183], v[6:7] op_sel_hi:[0,1,1]\n"
            "v_pk_fma_f32 v[8:9], v[154:155], v[184:185], v[8:9] op_sel_hi:[0,1,1]\n"
            "v_pk_fma_f32 v[10:11], v[154:155], v[186:187], v[10:11] op_sel_hi:[0,1,1]\n"
            "v_pk_fma_f32 v[12:13], v[158:159], v[188:189], v[12:13] op_sel_hi:[0,1,1]\n"
            "v_pk_fma_f32 v[14:15], v[158:159], v[190:191], v[14:15] op_sel_hi:[0,1,1]\n"
            "v_pk_add_f32 v[168:169], v[168:169], 1.0 op_sel_hi:[1,0]\n"
            "v_pk_add_f32 v[170:171], v[170:171], 1.0 op_sel_hi:[1,0]\n"
            "v_pk_add_f32 v[172:173], v[172:173], 1.0 op_sel_hi:[1,0]\n"
            "v_pk_add_f32 v[174:175], v[174:175], 1.0 op_sel_hi:[1,0]\n"
            "v_rcp_f32 v168, v168\n"
            "v_rcp_f32 v169, v169\n"
            "v_mfma_f32_4x4x4_16b_bf16 v[176:179], v[208:209], v[80:81], 0 cbsz:2 abid:3\n"
            "v_rcp_f32 v170, v170\n"
            "v_mfma_f32_4x4x4_16b_bf16 v[180:183], v[208:209], v[82:83], 0 cbsz:2 abid:3\n"
            "v_rcp_f32 v171, v171\n"
            "v_mfma_f32_4x4x4_16b_bf16 v[184:187], v[208:209], v[84:85], 0 cbsz:2 abid:3\n"
            "v_rcp_f32 v172, v172\n"
            "v_mfma_f32_4x4x4_16b_bf16 v[188:191], v[208:209], v[86:87], 0 cbsz:2 abid:3\n"
            "v_rcp_f32 v173, v173\n"
            "v_rcp_f32 v174, v174\n"
            "ds_read_b128 v[124:127], v228 offset:18432\n"
            "ds_read_b128 v[192:195], v228 offset:19456\n"
            "ds_read_b128 v[128:131], v228 offset:20480\n"
            "ds_read_b128 v[132:135], v228 offset:21504\n"
            "ds_read_b128 v[80:83], v228 offset:22528\n"
            "ds_read_b128 v[84:87], v228 offset:23552\n"
            "v_rcp_f32 v175, v175\n"
            "v_pk_fma_f32 v[0:1], v[146:147], v[176:177], v[0:1] op_sel:[1,0,0]\n"
            "v_pk_fma_f32 v[2:3], v[146:147], v[178:179], v[2:3] op_sel:[1,0,0]\n"
            "v_pk_fma_f32 v[4:5], v[150:151], v[180:181], v[4:5] op_sel:[1,0,0]\n"
            "v_pk_fma_f32 v[6:7], v[150:151], v[182:183], v[6:7] op_sel:[1,0,0]\n"
            "v_pk_fma_f32 v[8:9], v[154:155], v[184:185], v[8:9] op_sel:[1,0,0]\n"
            "v_pk_fma_f32 v[10:11], v[154:155], v[186:187], v[10:11] op_sel:[1,0,0]\n"
            "v_pk_fma_f32 v[12:13], v[158:159], v[188:189], v[12:13] op_sel:[1,0,0]\n"
            "v_pk_fma_f32 v[14:15], v[158:159], v[190:191], v[14:15] op_sel:[1,0,0]\n"
            "v_pk_mul_f32 v[168:169], v[168:169], v[136:137]\n"
            "v_pk_mul_f32 v[170:171], v[170:171], v[138:139]\n"
            "v_pk_mul_f32 v[172:173], v[172:173], v[140:141]\n"
            "v_pk_mul_f32 v[174:175], v[174:175], v[142:143]\n"
            "s_cmp_lg_u32 s73, s90\n"
            "s_cbranch_scc1 Llk_%=\n"
            "Llkr_%=:\n"
            "s_waitcnt vmcnt(8)\n"
            "v_mfma_f32_4x4x4_16b_bf16 v[176:179], v[208:209], v[40:41], 0 cbsz:2\n"
            "v_mfma_f32_4x4x4_16b_bf16 v[180:183], v[208:209], v[42:43], 0 cbsz:2\n"
            "v_mfma_f32_4x4x4_16b_bf16 v[184:187], v[208:209], v[44:45], 0 cbsz:2\n"
            "v_mfma_f32_4x4x4_16b_bf16 v[188:191], v[208:209], v[46:47], 0 cbsz:2\n"
            "ds_read_b128 v[136:139], v228 offset:24576\n"
            "ds_read_b128 v[140:143], v228 offset:25600\n"
            "ds_read_b128 v[144:147], v228 offset:26624\n"
            "ds_read_b128 v[148:151], v228 offset:27648\n"
            "ds_read_b128 v[152:155], v228 offset:28672\n"
            "ds_read_b128 v[156:159], v228 offset:29696\n"
            "ds_read_b128 v[40:43], v228 offset:30720\n"
            "ds_read_b128 v[44:47], v228 offset:31744\n"
            "s_nop 1\n"
            "v_pk_fma_f32 v[16:17], v[160:161], v[176:177], v[16:17] op_sel_hi:[0,1,1]\n"
            "v_pk_fma_f32 v[18:19], v[160:161], v[178:179], v[18:19] op_sel_hi:[0,1,1]\n"
            "v_pk_fma_f32 v[20:21], v[164:165], v[180:181], v[20:21] op_sel_hi:[0,1,1]\n"
            "v_pk_fma_f32 v[22:23], v[164:165], v[182:183], v[22:23] op_sel_hi:[0,1,1]\n"
            "v_pk_fma_f32 v[24:25], v[168:169], v[184:185], v[24:25] op_sel_hi:[0,1,1]\n"
            "v_pk_fma_f32 v[26:27], v[168:169], v[186:187], v[26:27] op_sel_hi:[0,1,1]\n"
            "v_pk_fma_f32 v[28:29], v[172:173], v[188:189], v[28:29] op_sel_hi:[0,1,1]\n"
            "v_pk_fma_f32 v[30:31], v[172:173], v[190:191], v[30:31] op_sel_hi:[0,1,1]\n"
            "s_cmp_le_u32 s74, 1\n"
            "s_cbranch_scc1 Ltail_%=\n"
            "v_mfma_f32_4x4x4_16b_bf16 v[176:179], v[208:209], v[56:57], 0 cbsz:2 abid:1\n"
            "v_mfma_f32_4x4x4_16b_bf16 v[180:183], v[208:209], v[58:59], 0 cbsz:2 abid:1\n"
            "v_mfma_f32_4x4x4_16b_bf16 v[184:187], v[208:209], v[60:61], 0 cbsz:2 abid:1\n"
            "v_mfma_f32_4x4x4_16b_bf16 v[188:191], v[208:209], v[62:63], 0 cbsz:2 abid:1\n"
            "s_nop 1\n"
            "v_pk_fma_f32 v[16:17], v[160:161], v[176:177], v[16:17] op_sel:[1,0,0]\n"
            "v_pk_fma_f32 v[18:19], v[160:161], v[178:179], v[18:19] op_sel:[1,0,0]\n"
            "v_pk_fma_f32 v[20:21], v[164:165], v[180:181], v[20:21] op_sel:[1,0,0]\n"
            "v_pk_fma_f32 v[22:23], v[164:165], v[182:183], v[22:23] op_sel:[1,0,0]\n"
            "v_pk_fma_f32 v[24:25], v[168:169], v[184:185], v[24:25] op_sel:[1,0,0]\n"
            "v_pk_fma_f32 v[26:27], v[168:169], v[186:187], v[26:27] op_sel:[1,0,0]\n"
            "v_pk_fma_f32 v[28:29], v[172:173], v[188:189], v[28:29] op_sel:[1,0,0]\n"
            "v_pk_fma_f32 v[30:31], v[172:173], v[190:191], v[30:31] op_sel:[1,0,0]\n"
            "s_cmp_le_u32 s74, 2\n"
            "s_cbranch_scc1 Ltail_%=\n"
            "s_waitcnt vmcnt(4)\n"
            "v_mfma_f32_4x4x4_16b_bf16 v[176:179], v[208:209], v[72:73], 0 cbsz:2 abid:2\n"
            "v_mfma_f32_4x4x4_16b_bf16 v[180:183], v[208:209], v[74:75], 0 cbsz:2 abid:2\n"
            "v_mfma_f32_4x4x4_16b_bf16 v[184:187], v[208:209], v[76:77], 0 cbsz:2 abid:2\n"
            "v_mfma_f32_4x4x4_16b_bf16 v[188:191], v[208:209], v[78:79], 0 cbsz:2 abid:2\n"
            "s_nop 1\n"
            "v_pk_fma_f32 v[16:17], v[162:163], v[176:177], v[16:17] op_sel_hi:[0,1,1]\n"
            "v_pk_fma_f32 v[18:19], v[162:163], v[178:179], v[18:19] op_sel_hi:[0,1,1]\n"
            "v_pk_fma_f32 v[20:21], v[166:167], v[180:181], v[20:21] op_sel_hi:[0,1,1]\n"
            "v_pk_fma_f32 v[22:23], v[166:167], v[182:183], v[22:23] op_sel_hi:[0,1,1]\n"
            "v_pk_fma_f32 v[24:25], v[170:171], v[184:185], v[24:25] op_sel_hi:[0,1,1]\n"
            "v_pk_fma_f32 v[26:27], v[170:171], v[186:187], v[26:27] op_sel_hi:[0,1,1]\n"
            "v_pk_fma_f32 v[28:29], v[174:175], v[188:189], v[28:29] op_sel_hi:[0,1,1]\n"
            "v_pk_fma_f32 v[30:31], v[174:175], v[190:191], v[30:31] op_sel_hi:[0,1,1]\n"
            "s_cmp_le_u32 s74, 3\n"
            "s_cbranch_scc1 Ltail_%=\n"
            "v_mfma_f32_4x4x4_16b_bf16 v[176:179], v[208:209], v[88:89], 0 cbsz:2 abid:3\n"
            "v_mfma_f32_4x4x4_16b_bf16 v[180:183], v[208:209], v[90:91], 0 cbsz:2 abid:3\n"
            "v_mfma_f32_4x4x4_16b_bf16 v[184:187], v[208:209], v[92:93], 0 cbsz:2 abid:3\n"
            "v_mfma_f32_4x4x4_16b_bf16 v[188:191], v[208:209], v[94:95], 0 cbsz:2 abid:3\n"
            "s_nop 1\n"
            "v_pk_fma_f32 v[16:17], v[162:163], v[176:177], v[16:17] op_sel:[1,0,0]\n"
            "v_pk_fma_f32 v[18:19], v[162:163], v[178:179], v[18:19] op_sel:[1,0,0]\n"
            "v_pk_fma_f32 v[20:21], v[166:167], v[180:181], v[20:21] op_sel:[1,0,0]\n"
            "v_pk_fma_f32 v[22:23], v[166:167], v[182:183], v[22:23] op_sel:[1,0,0]\n"
            "v_pk_fma_f32 v[24:25], v[170:171], v[184:185], v[24:25] op_sel:[1,0,0]\n"
            "v_pk_fma_f32 v[26:27], v[170:171], v[186:187], v[26:27] op_sel:[1,0,0]\n"
            "v_pk_fma_f32 v[28:29], v[174:175], v[188:189], v[28:29] op_sel:[1,0,0]\n"
            "v_pk_fma_f32 v[30:31], v[174:175], v[190:191], v[30:31] op_sel:[1,0,0]\n"
            "Ltail_%=:\n"
            "s_setprio 3\n"
            "s_cmp_lg_u32 s73, s90\n"
            "s_cbranch_scc1 Lspin_%=\n"
            "Lgo_%=:\n"
            "s_waitcnt vmcnt(0)\n"
            "v_mfma_f32_4x4x4_16b_bf16 v[0:3], v[224:225], v[32:33], v[0:3]\n"
            "v_mfma_f32_4x4x4_16b_bf16 v[4:7], v[224:225], v[34:35], v[4:7]\n"
            "v_mfma_f32_4x4x4_16b_bf16 v[8:11], v[224:225], v[36:37], v[8:11]\n"
            "v_mfma_f32_4x4x4_16b_bf16 v[12:15], v[224:225], v[38:39], v[12:15]\n"
            "v_mfma_f32_4x4x4_16b_bf16 v[16:19], v[224:225], v[48:49], v[16:19]\n"
            "v_mfma_f32_4x4x4_16b_bf16 v[20:23], v[224:225], v[50:51], v[20:23]\n"
            "v_mfma_f32_4x4x4_16b_bf16 v[24:27], v[224:225], v[52:53], v[24:27]\n"
            "v_mfma_f32_4x4x4_16b_bf16 v[28:31], v[224:225], v[54:55], v[28:31]\n"
            "v_cvt_pk_bf16_f32 v176, v0, v4\n"
            "v_cvt_pk_bf16_f32 v177, v8, v12\n"
            "ds_write_b64 v226, v[176:177] offset:0\n"
            "v_cvt_pk_bf16_f32 v178, v1, v5\n"
            "v_cvt_pk_bf16_f32 v179, v9, v13\n"
            "ds_write_b64 v226, v[178:179] offset:272\n"
            "v_cvt_pk_bf16_f32 v180, v2, v6\n"
            "v_cvt_pk_bf16_f32 v181, v10, v14\n"
            "ds_write_b64 v226, v[180:181] offset:544\n"
            "v_cvt_pk_bf16_f32 v182, v3, v7\n"
            "v_cvt_pk_bf16_f32 v183, v11, v15\n"
            "ds_write_b64 v226, v[182:183] offset:816\n"
            "ds_read_b128 v[176:179], v227 offset:0\n"
            "ds_read_b128 v[180:183], v227 offset:64\n"
            "v_cvt_pk_bf16_f32 v184, v16, v20\n"
            "v_cvt_pk_bf16_f32 v185, v24, v28\n"
            "ds_write_b64 v226, v[184:185] offset:128\n"
            "v_cvt_pk_bf16_f32 v186, v17, v21\n"
            "v_cvt_pk_bf16_f32 v187, v25, v29\n"
            "ds_write_b64 v226, v[186:187] offset:400\n"
            "v_cvt_pk_bf16_f32 v188, v18, v22\n"
            "v_cvt_pk_bf16_f32 v189, v26, v30\n"
            "ds_write_b64 v226, v[188:189] offset:672\n"
            "v_cvt_pk_bf16_f32 v190, v19, v23\n"
            "v_cvt_pk_bf16_f32 v191, v27, v31\n"
            "ds_write_b64 v226, v[190:191] offset:944\n"
            "ds_read_b128 v[184:187], v227 offset:128\n"
            "ds_read_b128 v[188:191], v227 offset:192\n"
            "s_waitcnt lgkmcnt(7)\n"
            "v_mfma_f32_16x16x32_bf16 v[0:3], v[176:179], v[236:239], 0\n"
            "v_mfma_f32_16x16x32_bf16 v[4:7], v[176:179], v[252:255], 0\n"
            "s_waitcnt lgkmcnt(6)\n"
            "v_mfma_f32_16x16x32_bf16 v[0:3], v[180:183], v[240:243], v[0:3]\n"
            "v_mfma_f32_16x16x32_bf16 v[4:7], v[180:183], v[96:99], v[4:7]\n"
            "buffer_load_dwordx4 v[72:75], v218, s[20:23], s63 offen offset:0 sc1\n"
            "v_mfma_f32_16x16x32_bf16 v[8:11], v[176:179], v[108:111], 0\n"
            "v_mfma_f32_16x16x32_bf16 v[12:15], v[176:179], v[200:203], 0\n"
            "v_mfma_f32_16x16x32_bf16 v[8:11], v[180:183], v[112:115], v[8:11]\n"
            "v_mfma_f32_16x16x32_bf16 v[12:15], v[180:183], v[204:207], v[12:15]\n"
            "buffer_load_dwordx4 v[76:79], v218, s[20:23], s63 offen offset:256 sc1\n"
            "v_mfma_f32_16x16x32_bf16 v[16:19], v[176:179], v[64:67], 0\n"
            "v_mfma_f32_16x16x32_bf16 v[20:23], v[176:179], v[128:131], 0\n"
            "v_mfma_f32_16x16x32_bf16 v[16:19], v[180:183], v[68:71], v[16:19]\n"
            "v_mfma_f32_16x16x32_bf16 v[20:23], v[180:183], v[132:135], v[20:23]\n"
            "buffer_load_dwordx4 v[88:91], v218, s[20:23], s63 offen offset:512 sc1\n"
            "v_mfma_f32_16x16x32_bf16 v[24:27], v[176:179], v[136:139], 0\n"
            "v_mfma_f32_16x16x32_bf16 v[28:31], v[176:179], v[152:155], 0\n"
            "v_mfma_f32_16x16x32_bf16 v[24:27], v[180:183], v[140:143], v[24:27]\n"
            "v_mfma_f32_16x16x32_bf16 v[28:31], v[180:183], v[156:159], v[28:31]\n"
            "buffer_load_dwordx4 v[92:95], v218, s[20:23], s63 offen offset:768 sc1\n"
            "s_waitcnt lgkmcnt(1)\n"
            "v_mfma_f32_16x16x32_bf16 v[0:3], v[184:187], v[244:247], v[0:3]\n"
            "v_mfma_f32_16x16x32_bf16 v[4:7], v[184:187], v[100:103], v[4:7]\n"
            "s_waitcnt lgkmcnt(0)\n"
            "v_mfma_f32_16x16x32_bf16 v[0:3], v[188:191], v[248:251], v[0:3]\n"
            "v_mfma_f32_16x16x32_bf16 v[4:7], v[188:191], v[104:107], v[4:7]\n"
            "v_mfma_f32_16x16x32_bf16 v[8:11], v[184:187], v[116:119], v[8:11]\n"
            "v_mfma_f32_16x16x32_bf16 v[12:15], v[184:187], v[232:235], v[12:15]\n"
            "v_mfma_f32_16x16x32_bf16 v[8:11], v[188:191], v[196:199], v[8:11]\n"
            "v_mfma_f32_16x16x32_bf16 v[12:15], v[188:191], v[120:123], v[12:15]\n"
            "v_mfma_f32_16x16x32_bf16 v[16:19], v[184:187], v[124:127], v[16:19]\n"
            "v_mfma_f32_16x16x32_bf16 v[20:23], v[184:187], v[80:83], v[20:23]\n"
            "v_mfma_f32_16x16x32_bf16 v[16:19], v[188:191], v[192:195], v[16:19]\n"
            "v_mfma_f32_16x16x32_bf16 v[20:23], v[188:191], v[84:87], v[20:23]\n"
            "v_cvt_pk_bf16_f32 v0, v0, v1\n"
            "v_cvt_pk_bf16_f32 v1, v2, v3\n"
            "v_cvt_pk_bf16_f32 v2, v4, v5\n"
            "v_cvt_pk_bf16_f32 v3, v6, v7\n"
            "buffer_store_dwordx4 v[0:3], v217, s[32:35], 0 offen offset:0 sc1\n"
            "v_mfma_f32_16x16x32_bf16 v[24:27], v[184:187], v[144:147], v[24:27]\n"
            "v_mfma_f32_16x16x32_bf16 v[28:31], v[184:187], v[40:43], v[28:31]\n"
            "v_mfma_f32_16x16x32_bf16 v[24:27], v[188:191], v[148:151], v[24:27]\n"
            "v_mfma_f32_16x16x32_bf16 v[28:31], v[188:191], v[44:47], v[28:31]\n"
            "v_cvt_pk_bf16_f32 v8, v8, v9\n"
            "v_cvt_pk_bf16_f32 v9, v10, v11\n"
            "v_cvt_pk_bf16_f32 v10, v12, v13\n"
            "v_cvt_pk_bf16_f32 v11, v14, v15\n"
            "buffer_store_dwordx4 v[8:11], v217, s[32:35], 0 offen offset:256 sc1\n"
            "v_cvt_pk_bf16_f32 v16, v16, v17\n"
            "v_cvt_pk_bf16_f32 v17, v18, v19\n"
            "v_cvt_pk_bf16_f32 v18, v20, v21\n"
            "v_cvt_pk_bf16_f32 v19, v22, v23\n"
            "buffer_store_dwordx4 v[16:19], v217, s[32:35], 0 offen offset:512 sc1\n"
            "v_cvt_pk_bf16_f32 v24, v24, v25\n"
            "v_cvt_pk_bf16_f32 v25, v26, v27\n"
            "v_cvt_pk_bf16_f32 v26, v28, v29\n"
            "v_cvt_pk_bf16_f32 v27, v30, v31\n"
            "buffer_store_dwordx4 v[24:27], v217, s[32:35], 0 offen offset:768 sc1\n"
            "s_cmp_lt_u32 s3, 6\n"
            "s_cbranch_scc0 Ltwo_%=\n"
            "s_endpgm\n"
            "Ltwo_%=:\n"
            "ds_read_b128 v[32:35], v231 offset:0\n"
            "ds_read_b128 v[36:39], v231 offset:1024\n"
            "ds_read_b128 v[48:51], v231 offset:2048\n"
            "ds_read_b128 v[52:55], v231 offset:3072\n"
            "s_waitcnt vmcnt(4)\n"
            "v_mfma_f32_4x4x4_16b_bf16 v[0:3], v[224:225], v[72:73], 0\n"
            "v_mfma_f32_4x4x4_16b_bf16 v[4:7], v[224:225], v[74:75], 0\n"
            "v_mfma_f32_4x4x4_16b_bf16 v[8:11], v[224:225], v[76:77], 0\n"
            "v_mfma_f32_4x4x4_16b_bf16 v[12:15], v[224:225], v[78:79], 0\n"
            "v_mfma_f32_4x4x4_16b_bf16 v[16:19], v[224:225], v[88:89], 0\n"
            "v_mfma_f32_4x4x4_16b_bf16 v[20:23], v[224:225], v[90:91], 0\n"
            "v_mfma_f32_4x4x4_16b_bf16 v[24:27], v[224:225], v[92:93], 0\n"
            "v_mfma_f32_4x4x4_16b_bf16 v[28:31], v[224:225], v[94:95], 0\n"
            "s_waitcnt lgkmcnt(0)\n"
            "v_mfma_f32_4x4x4_16b_bf16 v[0:3], v[224:225], v[32:33], v[0:3]\n"
            "v_mfma_f32_4x4x4_16b_bf16 v[4:7], v[224:225], v[34:35], v[4:7]\n"
            "v_mfma_f32_4x4x4_16b_bf16 v[8:11], v[224:225], v[36:37], v[8:11]\n"
            "v_mfma_f32_4x4x4_16b_bf16 v[12:15], v[224:225], v[38:39], v[12:15]\n"
            "v_mfma_f32_4x4x4_16b_bf16 v[16:19], v[224:225], v[48:49], v[16:19]\n"
            "v_mfma_f32_4x4x4_16b_bf16 v[20:23], v[224:225], v[50:51], v[20:23]\n"
            "v_mfma_f32_4x4x4_16b_bf16 v[24:27], v[224:225], v[52:53], v[24:27]\n"
            "v_mfma_f32_4x4x4_16b_bf16 v[28:31], v[224:225], v[54:55], v[28:31]\n"
            "v_cvt_pk_bf16_f32 v176, v0, v4\n"
            "v_cvt_pk_bf16_f32 v177, v8, v12\n"
            "ds_write_b64 v226, v[176:177] offset:0\n"
            "v_cvt_pk_bf16_f32 v178, v1, v5\n"
            "v_cvt_pk_bf16_f32 v179, v9, v13\n"
            "ds_write_b64 v226, v[178:179] offset:272\n"
            "v_cvt_pk_bf16_f32 v180, v2, v6\n"
            "v_cvt_pk_bf16_f32 v181, v10, v14\n"
            "ds_write_b64 v226, v[180:181] offset:544\n"
            "v_cvt_pk_bf16_f32 v182, v3, v7\n"
            "v_cvt_pk_bf16_f32 v183, v11, v15\n"
            "ds_write_b64 v226, v[182:183] offset:816\n"
            "ds_read_b128 v[176:179], v227 offset:0\n"
            "ds_read_b128 v[180:183], v227 offset:64\n"
            "v_cvt_pk_bf16_f32 v184, v16, v20\n"
            "v_cvt_pk_bf16_f32 v185, v24, v28\n"
            "ds_write_b64 v226, v[184:185] offset:128\n"
            "v_cvt_pk_bf16_f32 v186, v17, v21\n"
            "v_cvt_pk_bf16_f32 v187, v25, v29\n"
            "ds_write_b64 v226, v[186:187] offset:400\n"
            "v_cvt_pk_bf16_f32 v188, v18, v22\n"
            "v_cvt_pk_bf16_f32 v189, v26, v30\n"
            "ds_write_b64 v226, v[188:189] offset:672\n"
            "v_cvt_pk_bf16_f32 v190, v19, v23\n"
            "v_cvt_pk_bf16_f32 v191, v27, v31\n"
            "ds_write_b64 v226, v[190:191] offset:944\n"
            "ds_read_b128 v[184:187], v227 offset:128\n"
            "ds_read_b128 v[188:191], v227 offset:192\n"
            "s_waitcnt lgkmcnt(7)\n"
            "v_mfma_f32_16x16x32_bf16 v[0:3], v[176:179], v[236:239], 0\n"
            "v_mfma_f32_16x16x32_bf16 v[4:7], v[176:179], v[252:255], 0\n"
            "s_waitcnt lgkmcnt(6)\n"
            "v_mfma_f32_16x16x32_bf16 v[0:3], v[180:183], v[240:243], v[0:3]\n"
            "v_mfma_f32_16x16x32_bf16 v[4:7], v[180:183], v[96:99], v[4:7]\n"
            "v_mfma_f32_16x16x32_bf16 v[8:11], v[176:179], v[108:111], 0\n"
            "v_mfma_f32_16x16x32_bf16 v[12:15], v[176:179], v[200:203], 0\n"
            "v_mfma_f32_16x16x32_bf16 v[8:11], v[180:183], v[112:115], v[8:11]\n"
            "v_mfma_f32_16x16x32_bf16 v[12:15], v[180:183], v[204:207], v[12:15]\n"
            "v_mfma_f32_16x16x32_bf16 v[16:19], v[176:179], v[64:67], 0\n"
            "v_mfma_f32_16x16x32_bf16 v[20:23], v[176:179], v[128:131], 0\n"
            "v_mfma_f32_16x16x32_bf16 v[16:19], v[180:183], v[68:71], v[16:19]\n"
            "v_mfma_f32_16x16x32_bf16 v[20:23], v[180:183], v[132:135], v[20:23]\n"
            "v_mfma_f32_16x16x32_bf16 v[24:27], v[176:179], v[136:139], 0\n"
            "v_mfma_f32_16x16x32_bf16 v[28:31], v[176:179], v[152:155], 0\n"
            "v_mfma_f32_16x16x32_bf16 v[24:27], v[180:183], v[140:143], v[24:27]\n"
            "v_mfma_f32_16x16x32_bf16 v[28:31], v[180:183], v[156:159], v[28:31]\n"
            "s_waitcnt lgkmcnt(1)\n"
            "v_mfma_f32_16x16x32_bf16 v[0:3], v[184:187], v[244:247], v[0:3]\n"
            "v_mfma_f32_16x16x32_bf16 v[4:7], v[184:187], v[100:103], v[4:7]\n"
            "s_waitcnt lgkmcnt(0)\n"
            "v_mfma_f32_16x16x32_bf16 v[0:3], v[188:191], v[248:251], v[0:3]\n"
            "v_mfma_f32_16x16x32_bf16 v[4:7], v[188:191], v[104:107], v[4:7]\n"
            "v_mfma_f32_16x16x32_bf16 v[8:11], v[184:187], v[116:119], v[8:11]\n"
            "v_mfma_f32_16x16x32_bf16 v[12:15], v[184:187], v[232:235], v[12:15]\n"
            "v_mfma_f32_16x16x32_bf16 v[8:11], v[188:191], v[196:199], v[8:11]\n"
            "v_mfma_f32_16x16x32_bf16 v[12:15], v[188:191], v[120:123], v[12:15]\n"
            "v_mfma_f32_16x16x32_bf16 v[16:19], v[184:187], v[124:127], v[16:19]\n"
            "v_mfma_f32_16x16x32_bf16 v[20:23], v[184:187], v[80:83], v[20:23]\n"
            "v_mfma_f32_16x16x32_bf16 v[16:19], v[188:191], v[192:195], v[16:19]\n"
            "v_mfma_f32_16x16x32_bf16 v[20:23], v[188:191], v[84:87], v[20:23]\n"
            "v_cvt_pk_bf16_f32 v0, v0, v1\n"
            "v_cvt_pk_bf16_f32 v1, v2, v3\n"
            "v_cvt_pk_bf16_f32 v2, v4, v5\n"
            "v_cvt_pk_bf16_f32 v3, v6, v7\n"
            "buffer_store_dwordx4 v[0:3], v218, s[32:35], 0 offen offset:0 sc1\n"
            "v_mfma_f32_16x16x32_bf16 v[24:27], v[184:187], v[144:147], v[24:27]\n"
            "v_mfma_f32_16x16x32_bf16 v[28:31], v[184:187], v[40:43], v[28:31]\n"
            "v_mfma_f32_16x16x32_bf16 v[24:27], v[188:191], v[148:151], v[24:27]\n"
            "v_mfma_f32_16x16x32_bf16 v[28:31], v[188:191], v[44:47], v[28:31]\n"
            "v_cvt_pk_bf16_f32 v8, v8, v9\n"
            "v_cvt_pk_bf16_f32 v9, v10, v11\n"
            "v_cvt_pk_bf16_f32 v10, v12, v13\n"
            "v_cvt_pk_bf16_f32 v11, v14, v15\n"
            "buffer_store_dwordx4 v[8:11], v218, s[32:35], 0 offen offset:256 sc1\n"
            "v_cvt_pk_bf16_f32 v16, v16, v17\n"
            "v_cvt_pk_bf16_f32 v17, v18, v19\n"
            "v_cvt_pk_bf16_f32 v18, v20, v21\n"
            "v_cvt_pk_bf16_f32 v19, v22, v23\n"
            "buffer_store_dwordx4 v[16:19], v218, s[32:35], 0 offen offset:512 sc1\n"
            "v_cvt_pk_bf16_f32 v24, v24, v25\n"
            "v_cvt_pk_bf16_f32 v25, v26, v27\n"
            "v_cvt_pk_bf16_f32 v26, v28, v29\n"
            "v_cvt_pk_bf16_f32 v27, v30, v31\n"
            "buffer_store_dwordx4 v[24:27], v218, s[32:35], 0 offen offset:768 sc1\n"
            "Lend_%=:\n"
            "s_endpgm\n"
            "s_mov_b32 s68, 0\n"
            "s_branch Lout_%=\n"
            "Lpoll2_%=:\n"
            "s_sub_u32 s99, s99, 1\n"
            "s_cmp_lt_i32 s99, 0\n"
            "s_cbranch_scc1 Lpoll3_%=\n"
            "s_sleep 2\n"
            "buffer_load_dwordx2 v[142:143], v180, s[20:23], 0 offen sc1\n"
            "s_branch Lpoll_%=\n"
            "Lover_%=:\n"
            "s_mov_b32 s37, 0\n"
            "v_mov_b32 v144, 255\n"
            "ds_write_b32 v219, v144\n"
            "s_branch Lover2_%=\n"
            "Llng_%=:\n"
            "ds_read_b32 v80, v181 offset:1536\n"
            "s_sub_i32 s38, s37, 384\n"
            "v_cmp_gt_i32 s[84:85], s38, v243\n"
            "s_nop 1\n"
            "v_cndmask_b32 v87, v191, v192, s[84:85]\n"
            "s_waitcnt lgkmcnt(0)\n"
            "v_max_u32_dpp v80, v80, v80 row_shr:1 row_mask:0xf bank_mask:0xf bound_ctrl:1\n"
            "s_nop 1\n"
            "v_max_u32_dpp v80, v80, v80 row_shr:2 row_mask:0xf bank_mask:0xf bound_ctrl:1\n"
            "s_nop 1\n"
            "v_max_u32_dpp v80, v80, v80 row_shr:4 row_mask:0xf bank_mask:0xf bound_ctrl:1\n"
            "s_mov_b32 s38, 0\n"
            "v_max_u32_dpp v74, v74, v74 row_shr:8 row_mask:0xf bank_mask:0xf bound_ctrl:1\n"
            "s_nop 1\n"
            "v_max_u32_dpp v74, v74, v74 row_bcast:15 row_mask:0xa bank_mask:0xf\n"
            "s_nop 1\n"
            "v_max_u32_dpp v74, v74, v74 row_bcast:31 row_mask:0xc bank_mask:0xf\n"
            "v_max_u32 v74, s38, v74\n"
            "s_nop 0\n"
            "v_readlane_b32 s38, v74, 63\n"
            "v_max_u32_dpp v75, v75, v75 row_shr:8 row_mask:0xf bank_mask:0xf bound_ctrl:1\n"
            "s_nop 1\n"
            "v_max_u32_dpp v75, v75, v75 row_bcast:15 row_mask:0xa bank_mask:0xf\n"
            "s_nop 1\n"
            "v_max_u32_dpp v75, v75, v75 row_bcast:31 row_mask:0xc bank_mask:0xf\n"
            "v_max_u32 v75, s38, v75\n"
            "s_nop 0\n"
            "v_readlane_b32 s38, v75, 63\n"
            "v_max_u32_dpp v76, v76, v76 row_shr:8 row_mask:0xf bank_mask:0xf bound_ctrl:1\n"
            "s_nop 1\n"
            "v_max_u32_dpp v76, v76, v76 row_bcast:15 row_mask:0xa bank_mask:0xf\n"
            "s_nop 1\n"
            "v_max_u32_dpp v76, v76, v76 row_bcast:31 row_mask:0xc bank_mask:0xf\n"
            "v_max_u32 v76, s38, v76\n"
            "s_nop 0\n"
            "v_readlane_b32 s38, v76, 63\n"
            "v_max_u32_dpp v77, v77, v77 row_shr:8 row_mask:0xf bank_mask:0xf bound_ctrl:1\n"
            "s_nop 1\n"
            "v_max_u32_dpp v77, v77, v77 row_bcast:15 row_mask:0xa bank_mask:0xf\n"
            "s_nop 1\n"
            "v_max_u32_dpp v77, v77, v77 row_bcast:31 row_mask:0xc bank_mask:0xf\n"
            "v_max_u32 v77, s38, v77\n"
            "s_nop 0\n"
            "v_readlane_b32 s38, v77, 63\n"
            "v_max_u32_dpp v78, v78, v78 row_shr:8 row_mask:0xf bank_mask:0xf bound_ctrl:1\n"
            "s_nop 1\n"
            "v_max_u32_dpp v78, v78, v78 row_bcast:15 row_mask:0xa bank_mask:0xf\n"
            "s_nop 1\n"
            "v_max_u32_dpp v78, v78, v78 row_bcast:31 row_mask:0xc bank_mask:0xf\n"
            "v_max_u32 v78, s38, v78\n"
            "s_nop 0\n"
            "v_readlane_b32 s38, v78, 63\n"
            "v_max_u32_dpp v79, v79, v79 row_shr:8 row_mask:0xf bank_mask:0xf bound_ctrl:1\n"
            "s_nop 1\n"
            "v_max_u32_dpp v79, v79, v79 row_bcast:15 row_mask:0xa bank_mask:0xf\n"
            "s_nop 1\n"
            "v_max_u32_dpp v79, v79, v79 row_bcast:31 row_mask:0xc bank_mask:0xf\n"
            "v_max_u32 v79, s38, v79\n"
            "s_nop 0\n"
            "v_readlane_b32 s38, v79, 63\n"
            "v_max_u32_dpp v80, v80, v80 row_shr:8 row_mask:0xf bank_mask:0xf bound_ctrl:1\n"
            "s_nop 1\n"
            "v_max_u32_dpp v80, v80, v80 row_bcast:15 row_mask:0xa bank_mask:0xf\n"
            "s_nop 1\n"
            "v_max_u32_dpp v80, v80, v80 row_bcast:31 row_mask:0xc bank_mask:0xf\n"
            "v_max_u32 v80, s38, v80\n"
            "s_nop 0\n"
            "v_readlane_b32 s38, v80, 63\n"
            "v_mad_u32_u24 v87, v80, 1, v87\n"
            "buffer_load_dwordx4 v[70:73], v87, s[20:23], s16 offen offset:2048 sc1\n"
            "s_branch Llng2_%=\n"
            "Lre0_%=:\n"
            "s_sub_u32 s99, s99, 1\n"
            "s_cmp_lt_i32 s99, 0\n"
            "s_cbranch_scc1 Lok0_%=\n"
            "s_sleep 1\n"
            "buffer_load_dwordx4 v[34:37], v81, s[20:23], s15 offen offset:0 sc1\n"
            "s_waitcnt vmcnt(0)\n"
            "s_branch Lchk0_%=\n"
            "Lre1_%=:\n"
            "s_sub_u32 s99, s99, 1\n"
            "s_cmp_lt_i32 s99, 0\n"
            "s_cbranch_scc1 Lok1_%=\n"
            "s_sleep 1\n"
            "buffer_load_dwordx4 v[40:43], v82, s[20:23], s15 offen offset:1024 sc1\n"
            "s_waitcnt vmcnt(0)\n"
            "s_branch Lchk1_%=\n"
            "Lre2_%=:\n"
            "s_sub_u32 s99, s99, 1\n"
            "s_cmp_lt_i32 s99, 0\n"
            "s_cbranch_scc1 Lok2_%=\n"
            "s_sleep 1\n"
            "buffer_load_dwordx4 v[46:49], v83, s[20:23], s15 offen offset:2048 sc1\n"
            "s_waitcnt vmcnt(0)\n"
            "s_branch Lchk2_%=\n"
            "Lre3_%=:\n"
            "s_sub_u32 s99, s99, 1\n"
            "s_cmp_lt_i32 s99, 0\n"
            "s_cbranch_scc1 Lok3_%=\n"
            "s_sleep 1\n"
            "buffer_load_dwordx4 v[52:55], v84, s[20:23], s15 offen offset:3072 sc1\n"
            "s_waitcnt vmcnt(0)\n"
            "s_branch Lchk3_%=\n"
            "Lre4_%=:\n"
            "s_sub_u32 s99, s99, 1\n"
            "s_cmp_lt_i32 s99, 0\n"
            "s_cbranch_scc1 Lok4_%=\n"
            "s_sleep 1\n"
            "buffer_load_dwordx4 v[58:61], v85, s[20:23], s16 offen offset:0 sc1\n"
            "s_waitcnt vmcnt(0)\n"
            "s_branch Lchk4_%=\n"
            "Lre5_%=:\n"
            "s_sub_u32 s99, s99, 1\n"
            "s_cmp_lt_i32 s99, 0\n"
            "s_cbranch_scc1 Lok5_%=\n"
            "s_sleep 1\n"
            "buffer_load_dwordx4 v[64:67], v86, s[20:23], s16 offen offset:1024 sc1\n"
            "s_waitcnt vmcnt(0)\n"
            "s_branch Lchk5_%=\n"
            "Lre6_%=:\n"
            "s_sub_u32 s99, s99, 1\n"
            "s_cmp_lt_i32 s99, 0\n"
            "s_cbranch_scc1 Lok6_%=\n"
            "s_sleep 1\n"
            "buffer_load_dwordx4 v[70:73], v87, s[20:23], s16 offen offset:2048 sc1\n"
            "s_waitcnt vmcnt(0)\n"
            "s_branch Lchk6_%=\n"
            "Llong_%=:\n"
            "v_and_b32 v243, 31, v243\n"
            "v_readfirstlane_b32 s86, v247\n"
            "v_add_u32 v243, 64, v243\n"
            "s_nop 0\n"
            "v_lshl_add_u32 v247, v243, 3, s86\n"
            "v_subrev_u32 v128, s43, v243\n"
            "v_mov_b32 v136, s64\n"
            "v_cmpx_le_i32 vcc, s69, v128\n"
            "v_mov_b32 v136, s81\n"
            "v_cmpx_le_i32 vcc, s78, v128\n"
            "v_mov_b32 v136, s82\n"
            "v_cmpx_le_i32 vcc, s79, v128\n"
            "v_mov_b32 v136, s83\n"
            "v_cmpx_le_i32 vcc, s80, v128\n"
            "v_mov_b32 v136, s84\n"
            "s_mov_b64 exec, -1\n"
            "v_and_b32 v137, 0xffff, v136\n"
            "ds_read_b128 v[140:143], v137\n"
            "v_ashrrev_i32 v136, 16, v136\n"
            "v_sub_u32 v136, v128, v136\n"
            "s_waitcnt lgkmcnt(0)\n"
            "v_cmp_eq_u32 vcc, s64, v137\n"
            "v_mov_b32 v132, 60\n"
            "s_nop 0\n"
            "v_cndmask_b32 v132, 0, v132, vcc\n"
            "v_bfe_u32 v137, v140, 16, 8\n"
            "v_max_u32 v137, 8, v137\n"
            "v_lshrrev_b32 v138, 1, v137\n"
            "v_add_u32 v138, v138, v132\n"
            "v_sub_u32_sdwa v132, v138, v140 dst_sel:DWORD dst_unused:UNUSED_PAD src0_sel:DWORD src1_sel:BYTE_3\n"
            "v_sub_u32_sdwa v133, v137, v140 dst_sel:DWORD dst_unused:UNUSED_PAD src0_sel:DWORD src1_sel:BYTE_2\n"
            "v_med3_i32 v132, v132, 0, v133\n"
            "v_sub_u32 v132, v136, v132\n"
            "v_cmp_lt_u32_sdwa vcc, v132, v140 src0_sel:DWORD src1_sel:BYTE_2\n"
            "v_lshl_add_u32 v140, v132, 4, v140\n"
            "v_sub_u32_sdwa v132, v138, v141 dst_sel:DWORD dst_unused:UNUSED_PAD src0_sel:DWORD src1_sel:BYTE_3\n"
            "v_sub_u32_sdwa v133, v137, v141 dst_sel:DWORD dst_unused:UNUSED_PAD src0_sel:DWORD src1_sel:BYTE_2\n"
            "v_med3_i32 v132, v132, 0, v133\n"
            "v_sub_u32 v132, v136, v132\n"
            "v_cmp_lt_u32_sdwa s[70:71], v132, v141 src0_sel:DWORD src1_sel:BYTE_2\n"
            "v_lshl_add_u32 v141, v132, 4, v141\n"
            "v_sub_u32_sdwa v132, v138, v142 dst_sel:DWORD dst_unused:UNUSED_PAD src0_sel:DWORD src1_sel:BYTE_3\n"
            "v_sub_u32_sdwa v133, v137, v142 dst_sel:DWORD dst_unused:UNUSED_PAD src0_sel:DWORD src1_sel:BYTE_2\n"
            "v_med3_i32 v132, v132, 0, v133\n"
            "v_sub_u32 v132, v136, v132\n"
            "v_cmp_lt_u32_sdwa s[86:87], v132, v142 src0_sel:DWORD src1_sel:BYTE_2\n"
            "v_lshl_add_u32 v142, v132, 4, v142\n"
            "v_sub_u32_sdwa v132, v138, v143 dst_sel:DWORD dst_unused:UNUSED_PAD src0_sel:DWORD src1_sel:BYTE_3\n"
            "v_sub_u32_sdwa v133, v137, v143 dst_sel:DWORD dst_unused:UNUSED_PAD src0_sel:DWORD src1_sel:BYTE_2\n"
            "v_med3_i32 v132, v132, 0, v133\n"
            "v_sub_u32 v132, v136, v132\n"
            "v_cmp_lt_u32_sdwa s[88:89], v132, v143 src0_sel:DWORD src1_sel:BYTE_2\n"
            "v_lshl_add_u32 v143, v132, 4, v143\n"
            "v_cndmask_b32 v140, v251, v140, vcc\n"
            "v_cndmask_b32 v141, v251, v141, s[70:71]\n"
            "v_cndmask_b32 v142, v251, v142, s[86:87]\n"
            "v_cndmask_b32 v143, v251, v143, s[88:89]\n"
            "v_perm_b32 v140, v141, v140, s68\n"
            "v_perm_b32 v141, v143, v142, s68\n"
            "ds_write_b64 v247, v[140:141]\n"
            "s_branch Llong2_%=\n"
            "Lslow_%=:\n"
            "s_waitcnt vmcnt(0) lgkmcnt(0)\n"
            "s_mov_b32 s68, -1\n"
            "Lout_%=:\n"
            : "+{v0}"(tid), "={s68}"(slow), "={s90}"(tag), "={s3}"(wq)
            : "{s[0:1]}"(ka), "{s2}"(bx)
            : "memory", "vcc", "scc", "v1", "v2", "v3", "v4", "v5", "v6", "v7", "v8", "v9", "v10", "v11", "v12", "v13", "v14", "v15", "v16", "v17", "v18", "v19", "v20", "v21", "v22", "v23", "v24", "v25", "v26", "v27", "v28", "v29", "v30", "v31", "v32", "v33", "v34", "v35", "v36", "v37", "v38", "v39", "v40", "v41", "v42", "v43", "v44", "v45", "v46", "v47", "v48", "v49", "v50", "v51", "v52", "v53", "v54", "v55", "v56", "v57", "v58", "v59", "v60", "v61", "v62", "v63", "v64", "v65", "v66", "v67", "v68", "v69", "v70", "v71", "v72", "v73", "v74", "v75", "v76", "v77", "v78", "v79", "v80", "v81", "v82", "v83", "v84", "v85", "v86", "v87", "v88", "v89", "v90", "v91", "v92", "v93", "v94", "v95", "v96", "v97", "v98", "v99", "v100", "v101", "v102", "v103", "v104", "v105", "v106", "v107", "v108", "v109", "v110", "v111", "v112", "v113", "v114", "v115", "v116", "v117", "v118", "v119", "v120", "v121", "v122", "v123", "v124", "v125", "v126", "v127", "v128", "v129", "v130", "v131", "v132", "v133", "v134", "v135", "v136", "v137", "v138", "v139", "v140", "v141", "v142", "v143", "v144", "v145", "v146", "v147", "v148", "v149", "v150", "v151", "v152", "v153", "v154", "v155", "v156", "v157", "v158", "v159", "v160", "v161", "v162", "v163", "v164", "v165", "v166", "v167", "v168", "v169", "v170", "v171", "v172", "v173", "v174", "v175", "v176", "v177", "v178", "v179", "v180", "v181", "v182", "v183", "v184", "v185", "v186", "v187", "v188", "v189", "v190", "v191", "v192", "v193", "v194", "v195", "v196", "v197", "v198", "v199", "v200", "v201", "v202", "v203", "v204", "v205", "v206", "v207", "v208", "v209", "v210", "v211", "v212", "v213", "v214", "v215", "v216", "v217", "v218", "v219", "v220", "v221", "v222", "v223", "v224", "v225", "v226", "v227", "v228", "v229", "v230", "v231", "v232", "v233", "v234", "v235", "v236", "v237", "v238", "v239", "v240", "v241", "v242", "v243", "v244", "v245", "v246", "v247", "v248", "v249", "v250", "v251", "v252", "v253", "v254", "v255", "s4", "s5", "s6", "s7", "s8", "s9", "s10", "s11", "s12", "s13", "s14", "s15", "s16", "s17", "s18", "s19", "s20", "s21", "s22", "s23", "s24", "s25", "s26", "s27", "s28", "s29", "s30", "s31", "s33", "s34", "s35", "s36", "s37", "s38", "s39", "s40", "s41", "s42", "s43", "s44", "s45", "s46", "s47", "s48", "s49", "s50", "s51", "s52", "s53", "s54", "s55", "s56", "s57", "s58", "s59", "s60", "s61", "s62", "s63", "s64", "s65", "s66", "s67", "s69", "s70", "s71", "s72", "s73", "s74", "s75", "s76", "s77", "s78", "s79", "s80", "s81", "s82", "s83", "s84", "s85", "s86", "s87", "s88", "s89", "s91", "s92", "s93", "s94", "s95", "s96", "s97", "s98", "s99");
    const int w = __builtin_amdgcn_readfirstlane((int)wq);
    const unsigned lds0 = (unsigned)(uintptr_t)(__attribute__((address_space(3))) unsigned char*)smem;
    asm volatile("" : "+s"(ka));
    unsigned char* ws = (unsigned char*)ka[6];
    if (__builtin_amdgcn_readfirstlane((int)slow) == -1) {
        const int lane = lane_opaque();
        tag = (unsigned)__builtin_amdgcn_readfirstlane((int)tag);
        const int* dst = (const int*)ka[0]; const int* src = (const int*)ka[2];
        const unsigned short *Y = (const unsigned short*)ka[4], *ef = (const unsigned short*)ka[8], *x = (const unsigned short*)ka[10], *w_rad = (const unsigned short*)ka[14],
                             *cg = (const unsigned short*)ka[16], *w_mix = (const unsigned short*)ka[18];
        unsigned short* out = (unsigned short*)ka[12];
        const int N = (int)ka[20], E = (int)(ka[20] >> 32), nh = (int)ka[23];
        const int half = (bx >> 2) & 1, b = (bx & 3) * 32 + (bx >> 3), sS = 40 * (1 - half), sK = 40 * half;
        const int node0 = (b * N + 127) >> 7, npb = (((b + 1) * N + 127) >> 7) - node0;
        const __amdgpu_buffer_rsrc_t rsw = __builtin_amdgcn_make_buffer_rsrc((void*)ws, 0, W_END, 0x00020000);
        lds_wait(lds0 + SY_C3, 4);
        const __amdgpu_buffer_rsrc_t rso = __builtin_amdgcn_make_buffer_rsrc((void*)out, 0, N * 1024, 0x00020000);
#pragma unroll 1
        for (int it = 0; it < 10; ++it) {
            const int nl = (it < 5 ? sS + 8 * it : sK + 8 * (it - 5)) + w, node = node0 + nl;
            const bool nv = nl < npb;
            const unsigned ro = nv ? (unsigned)node * 1024u + (unsigned)lane * 16u : OOB_OFF;
            float a8[8] = {0.f, 0.f, 0.f, 0.f, 0.f, 0.f, 0.f, 0.f};
#pragma unroll 1
            for (int e0 = 0; e0 < (nv ? E : 0); e0 += 64) {
                const int ec = min(e0 + lane, E - 1);
                u64 mm = __ballot(e0 + lane < E && dst[ec] == node && (src[ec] >= nh) == (half != 0));
#pragma unroll 1
                while (mm) {
                    const int e = e0 + __builtin_ctzll(mm);
                    mm &= mm - 1;
                    const unsigned short* fe = (const unsigned short*)ef + (size_t)e * DE_DIM;
                    const unsigned short* ye = (const unsigned short*)Y + (size_t)e * M_DIM;
                    const unsigned short* xp = x + (size_t)src[e] * (C_DIM * M_DIM) + lane * 8;
                    float z0 = 0.f, z1 = 0.f;
#pragma unroll 1
                    for (int f = 0; f < DE_DIM; ++f) { z0 += bf2f(fe[f]) * bf2f(w_rad[f * C_DIM + 2 * lane]); z1 += bf2f(fe[f]) * bf2f(w_rad[f * C_DIM + 2 * lane + 1]); }
                    const float r0 = z0 / (1.f + __expf(-z0)), r1 = z1 / (1.f + __expf(-z1));
#pragma unroll 1
                    for (int i = 0; i < 4; ++i) {
                        const float x0 = bf2f(xp[i]) * r0, x1 = bf2f(xp[4 + i]) * r1;
#pragma unroll
                        for (int o = 0; o < 4; ++o) {
                            float wv4 = 0.f;
#pragma unroll
                            for (int f = 0; f < 4; ++f) wv4 += bf2f(cg[(i * 4 + f) * 4 + o]) * bf2f(ye[f]);
                            a8[o] += x0 * wv4; a8[4 + o] += x1 * wv4;
                        }
                    }
                }
            }
            if (it >= 5) {
                const u32x4 pv = __builtin_amdgcn_raw_buffer_load_b128(rsw, ro, W_AGG, SC1);
#pragma unroll
                for (int i = 0; i < 4; ++i) { a8[2 * i] += __uint_as_float(pv[i] << 16); a8[2 * i + 1] += __uint_as_float(pv[i] & 0xffff0000u); }
            }
            const u32x4 rv = (u32x4){pack2bf(a8[0], a8[1]), pack2bf(a8[2], a8[3]), pack2bf(a8[4], a8[5]), pack2bf(a8[6], a8[7])};
            if (it < 5) {
                __builtin_amdgcn_raw_buffer_store_b128(rv, rsw, ro, W_AGG, SC1);
                if (it == 4) {
                    asm volatile("s_waitcnt vmcnt(0)" ::: "memory");
                    if (lds_arrive_rtn(lds0 + 4) == 7u) flag_store(ws + W_FLG + bx * 128, tag);
                    poll_tag(ws + W_FLG + (bx ^ 4) * 128, tag);
                }
            } else {
                *(u32x4*)(smem + L_LST + w * 1024 + lane * 16) = rv;
#pragma unroll
                for (int i = 0; i < 8; ++i) a8[i] = 0.f;
#pragma unroll 1
                for (int c = 0; c < C_DIM; ++c) {
                    const unsigned short* ap = (const unsigned short*)(smem + L_LST + w * 1024 + c * 8);
                    const float w0 = bf2f(w_mix[c * C_DIM + 2 * lane]), w1 = bf2f(w_mix[c * C_DIM + 2 * lane + 1]);
#pragma unroll
                    for (int m = 0; m < 4; ++m) { a8[m] += bf2f(ap[m]) * w0; a8[4 + m] += bf2f(ap[m]) * w1; }
                }
                __builtin_amdgcn_raw_buffer_store_b128((u32x4){pack2bf(a8[0], a8[1]), pack2bf(a8[2], a8[3]), pack2bf(a8[4], a8[5]), pack2bf(a8[6], a8[7])}, rso, ro, 0, 0);
            }
        }
    }
}

__global__ void kf_zero(float* __restrict__ agg, size_t n) {
    for (size_t i = (size_t)blockIdx.x * blockDim.x + threadIdx.x; i < n; i += (size_t)gridDim.x * blockDim.x) agg[i] = 0.f;
}
__global__ void kf_edges(const unsigned short* __restrict__ x, const int* __restrict__ src, const int* __restrict__ dst,
                         const unsigned short* __restrict__ Y, const unsigned short* __restrict__ ef,
                         const unsigned short* __restrict__ w_rad, const unsigned short* __restrict__ cg,
                         float* __restrict__ agg, int E) {
    const int ch = threadIdx.x;
    for (int e = blockIdx.x; e < E; e += gridDim.x) {
        float z = 0.f;
        for (int f = 0; f < DE_DIM; ++f) z += bf2f(ef[(size_t)e * DE_DIM + f]) * bf2f(w_rad[f * C_DIM + ch]);
        const float r = z / (1.f + __expf(-z));
        float a4[4] = {0.f, 0.f, 0.f, 0.f};
        for (int ii = 0; ii < 4; ++ii) {
            const float xv = bf2f(x[((size_t)src[e] * C_DIM + ch) * M_DIM + ii]) * r;
            for (int o = 0; o < 4; ++o) {
                float wv = 0.f;
                for (int f = 0; f < 4; ++f) wv += bf2f(cg[(ii * 4 + f) * 4 + o]) * bf2f(Y[(size_t)e * 4 + f]);
                a4[o] += xv * wv;
            }
        }
        for (int o = 0; o < 4; ++o) atomicAdd(&agg[((size_t)dst[e] * C_DIM + ch) * M_DIM + o], a4[o]);
    }
}
__global__ void kf_mix(const float* __restrict__ agg, const unsigned short* __restrict__ w_mix, unsigned short* __restrict__ out, int N) {
    const int d = threadIdx.x;
    for (int n = blockIdx.x; n < N; n += gridDim.x) {
        float a4[4] = {0.f, 0.f, 0.f, 0.f};
        for (int c = 0; c < C_DIM; ++c) {
            const float wv = bf2f(w_mix[c * C_DIM + d]);
            for (int m = 0; m < 4; ++m) a4[m] += bf2f(f2bf(agg[((size_t)n * C_DIM + c) * M_DIM + m])) * wv;
        }
        for (int m = 0; m < 4; ++m) out[((size_t)n * C_DIM + d) * M_DIM + m] = f2bf(a4[m]);
    }
}

extern "C" void kernel_launch(void* const* d_in, const int* in_sizes, int n_in,
                              void* d_out, int out_size, void* d_ws, size_t ws_size,
                              hipStream_t stream) {
    const unsigned short* x = (const unsigned short*)d_in[0];
    const int* src = (const int*)d_in[1];
    const int* dst = (const int*)d_in[2];
    const unsigned short* Y = (const unsigned short*)d_in[3];
    const unsigned short* ef = (const unsigned short*)d_in[4];
    const unsigned short* w_rad = (const unsigned short*)d_in[5];
    const unsigned short* cg = (const unsigned short*)d_in[6];
    const unsigned short* w_mix = (const unsigned short*)d_in[7];
    unsigned short* out = (unsigned short*)d_out;
    const int N = in_sizes[0] / (C_DIM * M_DIM);
    const int E = in_sizes[1];
    const int epb = (E + 255) / 256;
    const int nh = (N + 1) / 2;
    u64 hi = (u64)(uintptr_t)d_out | (u64)(uintptr_t)d_ws;
    for (int i = 0; i < 8; ++i) hi |= (u64)(uintptr_t)d_in[i];
    const bool fast = (hi >> 48) == 0 && N >= 256 && N <= NB_MAX * NPB_MAX && E > 0 && epb <= EPB_MAX && ws_size >= (size_t)W_END;
    if (!fast) {
        float* agg = (float*)d_ws;
        if (N <= 0) return;
        kf_zero<<<1024, 256, 0, stream>>>(agg, (size_t)N * C_DIM * M_DIM);
        if (E > 0) kf_edges<<<4096, C_DIM, 0, stream>>>(x, src, dst, Y, ef, w_rad, cg, agg, E);
        kf_mix<<<2048, C_DIM, 0, stream>>>(agg, w_mix, out, N);
        return;
    }
    int k = 0;
    while ((N << k) <= 32768) ++k;
    const unsigned magic = (unsigned)(((1ull << (39 - k)) + N - 1) / N);
    const unsigned F = 0x00020000u;
    k_fused<<<256, 512, 0, stream>>>(dst, E * 4u, F, src, E * 4u, F, Y, E * 8u, F, d_ws, W_END, F, ef, E * 32u, F, x, N * 1024u, F, out, N * 1024u, F, w_rad, DE_DIM * C_DIM * 2u, F,
                                     cg, 128u, F, w_mix, C_DIM * C_DIM * 2u, F, N, E, epb, magic, k, nh * 1024u, nh, 0u);
}
```
